```python
import torch

def _softmax(x, axis=-1):
    return torch.softmax(x, dim=axis)
def _gelu(x):
    return torch.nn.functional.gelu(x, approximate="tanh")
def _silu(x):
    return torch.nn.functional.silu(x)
def _sigmoid(x):
    return torch.sigmoid(x)
def _layernorm(x, axis=-1, eps=1e-5):
    m = x.mean(dim=axis, keepdim=True)
    v = x.var(dim=axis, keepdim=True, unbiased=False)
    return (x - m) / torch.sqrt(v + eps)
def _scatter_add(tgt, idx, src, axis=0):
    return tgt.clone().index_add_(axis, idx, src)
def _segment_sum(src, seg, n):
    out = torch.zeros((n,) + src.shape[1:], dtype=src.dtype)
    return out.index_add_(0, seg, src)
def _pad(x, pw):
    flat = tuple(v for pair in reversed(pw) for v in pair)
    return torch.nn.functional.pad(x, flat)

kernel_name = 'gated_mix__spectral_gfnet__ssm_conv_s4__prompt'

def setup_inputs():

    _w = (lambda a: (lambda t: t.to(torch.bfloat16) if t.is_floating_point() else t)(torch.as_tensor(a)))
    def _A_setup():
        B, S, D = 1, 2048, 448
        Df = D // 2 + 1
        return {
            "x": _w((lambda *s: torch.randn(*s, dtype=torch.bfloat16) * 0.05)(B, S, D)),
            "w_r": _w((lambda *s: torch.randn(*s, dtype=torch.bfloat16) * 0.8)(S, Df) * 0.02),
            "w_i": _w((lambda *s: torch.randn(*s, dtype=torch.bfloat16) * 0.8)(S, Df) * 0.02),
        }
    _A_in = _A_setup()

    def _B_setup():
        B, L, D, N = 1, 2048, 448, 256
        A_re = -(lambda _x: torch.exp(torch.as_tensor(_x)))((lambda *s: torch.randn(*s, dtype=torch.bfloat16) * 3.2)(D, N))
        A_im = torch.pi * (lambda *s: torch.randn(*s, dtype=torch.bfloat16) * 0.05)(D, N)
        B_re = (lambda *s: torch.randn(*s, dtype=torch.bfloat16) * 0.05)(D, N)
        B_im = (lambda *s: torch.randn(*s, dtype=torch.bfloat16) * 0.05)(D, N)
        C_re = (lambda *s: torch.randn(*s, dtype=torch.bfloat16) * 0.05)(D, N)
        C_im = (lambda *s: torch.randn(*s, dtype=torch.bfloat16) * 0.1)(D, N)
        log_dt = (lambda *s: torch.randn(*s, dtype=torch.bfloat16) * 0.05)(D) * 0.5 - 4.0
        Dskip = (lambda *s: torch.randn(*s, dtype=torch.bfloat16) * 0.05)(D)
        return {
            "u": _w((lambda *s: torch.randn(*s, dtype=torch.bfloat16) * 0.05)(B, L, D)),
            "A_re": _w(A_re),
            "A_im": _w(A_im),
            "B_re": _w(B_re),
            "B_im": _w(B_im),
            "C_re": _w(C_re),
            "C_im": _w(C_im),
            "log_dt": _w(log_dt),
            "Dskip": _w(Dskip),
        }
    _B_in = _B_setup()

    return {
        "A_x": _A_in["x"],
        "A_w_r": _A_in["w_r"],
        "A_w_i": _A_in["w_i"],
        "B_u": _B_in["u"],
        "B_A_re": _B_in["A_re"],
        "B_A_im": _B_in["A_im"],
        "B_B_re": _B_in["B_re"],
        "B_B_im": _B_in["B_im"],
        "B_C_re": _B_in["C_re"],
        "B_C_im": _B_in["C_im"],
        "B_log_dt": _B_in["log_dt"],
        "B_Dskip": _B_in["Dskip"],
        "gate_w": _w((lambda *s: torch.randn(*s, dtype=torch.bfloat16) * 0.05)(448) * 16.0),
    }

def reference(A_x, A_w_r, A_w_i, B_u, B_A_re, B_A_im, B_B_re, B_B_im, B_C_re, B_C_im, B_log_dt, B_Dskip, gate_w):
    def _A_ref(x, w_r, w_i):
        B, S, D = x.shape
        X = (lambda x, axes=(-2,-1): torch.fft.rfft2(x.float(), dim=axes))(x, axes=(-2, -1))
        W = w_r + 1j * w_i
        X = X * W
        y = (lambda x, s, axes=(-2,-1): torch.fft.irfft2(x, s=s, dim=axes).to(torch.bfloat16))(X, (S, D), axes=(-2, -1))
        return y

    def _B_ref(u, A_re, A_im, B_re, B_im, C_re, C_im, log_dt, Dskip):
        Bsz, L, D = u.shape
        dt = torch.exp(log_dt)[:, None]
        A = A_re + 1j * A_im
        Bc = B_re + 1j * B_im
        Cc = C_re + 1j * C_im
        logdA = dt * A
        dB = dt * Bc
        l = (lambda x, dt: x.to(dt))(torch.arange(L), A_re.dtype)
        P = torch.exp(logdA[:, :, None] * l[None, None, :])
        K = torch.einsum("dn,dnl,dn->dl", Cc, P, dB).real
        L2 = 2 * L
        Kp = _pad(K, ((0, 0), (0, L)))
        up = (lambda x, *d: x.permute(*d))(u, 0, 2, 1)
        up = _pad(up, ((0, 0), (0, 0), (0, L)))
        Kf = (lambda x, axes=(-2,-1): torch.fft.rfft2(x.float(), dim=axes))(Kp[:, None, :], axes=(-2, -1))
        Uf = (lambda x, axes=(-2,-1): torch.fft.rfft2(x.float(), dim=axes))(up[:, :, None, :], axes=(-2, -1))
        Yf = Uf * Kf[None, :, :, :]
        y = (lambda x, s, axes=(-2,-1): torch.fft.irfft2(x, s=s, dim=axes).to(torch.bfloat16))(Yf, (1, L2), axes=(-2, -1))[:, :, 0, :L]
        y = (lambda x, *d: x.permute(*d))(y, 0, 2, 1) + u * Dskip[None, None, :]
        return y

    x = A_x
    g = _sigmoid((x * gate_w).sum(-1, keepdims=True))
    a = _A_ref(x, A_w_r, A_w_i)
    b = _B_ref(x, B_A_re, B_A_im, B_B_re, B_B_im, B_C_re, B_C_im, B_log_dt, B_Dskip)
    return g * a + (1 - g) * b
```

```cpp
#include <hip/hip_runtime.h>
#include <hip/hip_bf16.h>
#include <stdint.h>

typedef unsigned int u32;
typedef unsigned short u16;
typedef unsigned long long u64;
typedef float f32x4 __attribute__((ext_vector_type(4)));
typedef float f32x2 __attribute__((ext_vector_type(2)));
typedef u32 u32x4 __attribute__((ext_vector_type(4)));
typedef u32 u32x2 __attribute__((ext_vector_type(2)));
typedef __bf16 bf16x8 __attribute__((ext_vector_type(8)));
typedef __bf16 bf16x2 __attribute__((ext_vector_type(2)));
typedef short s16x4 __attribute__((ext_vector_type(4)));
#define LAS __attribute__((address_space(3)))
#define GAS __attribute__((address_space(1)))
#define DI __device__ __forceinline__

#define NLAUNCH 1

constexpr int S_ = 2048, D_ = 448, DF = 225, NS = 256;
constexpr int N_FFT = 256, N_SSM = 224, N_WG = N_FFT + N_SSM;

constexpr size_t WS_CTL = 0;
constexpr size_t WS_T   = 65536;
constexpr size_t WS_TP  = WS_T + (2u << 20);
constexpr size_t WS_XT  = WS_TP + (2u << 20);
constexpr size_t WS_B   = WS_XT + (2u << 20);
constexpr int CT_GEN = 0;
constexpr int CT_F1 = 1024;
constexpr int CT_F2 = CT_F1 + 2048;
constexpr int CT_FB = CT_F2 + 2048;
constexpr int CT_FX = CT_FB + 2048;

constexpr int XRS = 928;
constexpr int L_XT = 0;
constexpr int L_XCH = 29696;
constexpr int TPRS = 1040;
constexpr int L_TP = 0;
constexpr int L_Z = 37888;
constexpr int L_TI = 46080;
constexpr int L_TO = 55296;
constexpr int L_BI = 64512;
constexpr int BRS = 912;
constexpr int L_G = 71808;
constexpr int KCP = 4160;
constexpr int L_KC = 0;
constexpr int L_SCR = 0;
constexpr int KRS = 288;
constexpr int L_U = 36864;
constexpr int L_PART = 46080;
constexpr int L_KT = 62464;
constexpr int L_BO = 65664;
constexpr int LDS_BYTES = 73856;

struct Params {
  const u16* x; const u16* wr; const u16* wi;
  const u16* are; const u16* aim; const u16* bre; const u16* bim; const u16* cre; const u16* cim;
  const u16* logdt; const u16* dskip; const u16* gw;
  u16* out; char* ws;
};

template <class T, class F> DI T bc(F v) { return __builtin_bit_cast(T, v); }
DI f32x4 mfma32(u32x4 a, u32x4 b, f32x4 c) { return __builtin_amdgcn_mfma_f32_16x16x32_bf16(bc<bf16x8>(a), bc<bf16x8>(b), c, 0, 0, 0); }
DI f32x4 mfma16(u32x2 a, u32x2 b, f32x4 c) { return __builtin_amdgcn_mfma_f32_16x16x16bf16_1k(bc<s16x4>(a), bc<s16x4>(b), c, 0, 0, 0); }
DI u32 pk(float a, float b) { f32x2 v = {a, b}; return bc<u32>(__builtin_convertvector(v, bf16x2)); }
DI float bfr(float a) { return bc<float>(pk(0.f, a) & 0xffff0000u); }
DI float lo16(u32 v) { return bc<float>(v << 16); }
DI float hi16(u32 v) { return bc<float>(v & 0xffff0000u); }
DI float b2f(u16 v) { return bc<float>((u32)v << 16); }
DI u32x2 trread(u32 a) { return bc<u32x2>(__builtin_amdgcn_ds_read_tr16_b64_v4i16((LAS s16x4*)(size_t)a)); }
DI u32x4 cat(u32x2 a, u32x2 b) { u32x4 r = {a.x, a.y, b.x, b.y}; return r; }
DI float sinr(float x) { return __builtin_amdgcn_sinf(x); }
DI float cosr(float x) { return __builtin_amdgcn_cosf(x); }
DI float fx1(float v) { return __builtin_amdgcn_update_dpp(v, v, 0xB1, 0xf, 0xf, true); }
DI float fr8(float v) { return __builtin_amdgcn_update_dpp(v, v, 0x128, 0xf, 0xf, true); }
DI u32 ux1(u32 v) { return (u32)__builtin_amdgcn_update_dpp((int)v, (int)v, 0xB1, 0xf, 0xf, true); }
DI u32 ur8(u32 v) { return (u32)__builtin_amdgcn_update_dpp((int)v, (int)v, 0x128, 0xf, 0xf, true); }
DI u32x4 sx1(u32x4 v, u32 m) { u32x4 r = {ux1(v.x) ^ m, ux1(v.y) ^ m, ux1(v.z) ^ m, ux1(v.w) ^ m}; return r; }
DI u32x4 sr8(u32x4 v, u32 m) { u32x4 r = {ur8(v.x) ^ m, ur8(v.y) ^ m, ur8(v.z) ^ m, ur8(v.w) ^ m}; return r; }
DI u32x4 xr(u32x4 v, u32 m) { u32x4 r = {v.x ^ m, v.y ^ m, v.z ^ m, v.w ^ m}; return r; }
template <class T> DI T lds_ld(u32 a) { return *(LAS T*)(size_t)a; }
template <class T> DI void lds_st(u32 a, T v) { *(LAS T*)(size_t)a = v; }
DI void wait_lgkm0() { asm volatile("s_waitcnt lgkmcnt(0)" ::: "memory"); }
DI void wait_vm0() { asm volatile("s_waitcnt vmcnt(0)" ::: "memory"); }
DI void bar() { asm volatile("s_waitcnt lgkmcnt(0)\n\ts_barrier" ::: "memory"); }
DI f32x2 cmul(f32x2 x, f32x2 y) {
  f32x2 t;
  asm("v_pk_mul_f32 %0, %1, %2 op_sel:[0,0] op_sel_hi:[0,1]" : "=v"(t) : "v"(x), "v"(y));
  asm("v_pk_fma_f32 %0, %1, %2, %0 op_sel:[1,1,0] op_sel_hi:[1,0,1] neg_lo:[1,0,0]" : "+v"(t) : "v"(x), "v"(y));
  return t;
}
DI int pi_k(int g, int jj) { return 16 * (jj >> 2) + 4 * g + (jj & 3); }

#define RLX_AGENT __ATOMIC_RELAXED, __HIP_MEMORY_SCOPE_AGENT
template <bool P> DI void st16(char* base, u32 off, u32x4 v) {
  if (P) __builtin_amdgcn_raw_buffer_store_b128(v, __builtin_amdgcn_make_buffer_rsrc(base, 0, 0x800000, 0x00020000), off, 0, 16);
  else *(GAS u32x4*)(base + off) = v;
}
template <bool P> DI u32x4 ld16(const char* base, u32 off) {
  if (P) return __builtin_amdgcn_raw_buffer_load_b128(__builtin_amdgcn_make_buffer_rsrc((void*)base, 0, 0x800000, 0x00020000), off, 0, 16);
  else return *(const GAS u32x4*)(base + off);
}
DI void publish(u32* first, int nrep, int pitch, u32 epoch) {
  if ((int)threadIdx.x < nrep) __hip_atomic_store((GAS u32*)first + threadIdx.x * pitch, epoch, RLX_AGENT);
}
DI void poll_flags(const u32* flags, int n4, u32 epoch) {
  const int lane = threadIdx.x & 63;
  const __amdgpu_buffer_rsrc_t rs = __builtin_amdgcn_make_buffer_rsrc((void*)flags, 0, n4 * 16, 0x00020000);
  for (int spin = 0; spin < (1 << 18); ++spin) {
    u32x4 f = __builtin_amdgcn_raw_buffer_load_b128(rs, lane * 16, 0, 16);
    bool ok = lane >= n4 || (f.x == epoch && f.y == epoch && f.z == epoch && f.w == epoch);
    if (__all(ok)) break;
    __builtin_amdgcn_s_sleep(1);
  }
}

struct FConst { u32x4 fc[2], fs[2]; };

DI void make_f32(FConst& F, int i16, int g) {
  float c[8], s[8];
#pragma unroll
  for (int jj = 0; jj < 8; ++jj) {
    float ph = (float)((i16 * pi_k(g, jj)) & 31) * (1.0f / 32.0f);
    c[jj] = cosr(ph); s[jj] = sinr(ph);
  }
  F.fc[0] = u32x4{pk(c[0], c[1]), pk(c[2], c[3]), pk(c[4], c[5]), pk(c[6], c[7])};
  F.fs[0] = u32x4{pk(s[0], s[1]), pk(s[2], s[3]), pk(s[4], s[5]), pk(s[6], s[7])};
  F.fc[1] = xr(F.fc[0], 0x80000000u);
  F.fs[1] = xr(F.fs[0], 0x80000000u);
}

template <bool P>
DI void fft_phase1(const Params& p, u32 lb, const FConst& F, int s2, int q, bool do_gate) {
  const int tid = threadIdx.x, lane = tid & 63, w = __builtin_amdgcn_readfirstlane(tid >> 6), i16 = lane & 15, g = lane >> 4;
  if (lane < 56) {
#pragma unroll
    for (int r = 0; r < 8; ++r) {
      int s1 = w * 8 + r;
      __builtin_amdgcn_global_load_lds((const GAS void*)(p.x + (size_t)(64 * s1 + s2) * D_ + lane * 8), (LAS void*)(size_t)(lb + L_XT + s1 * XRS), 16, 0, 0);
    }
  }
  u32x4 gwv = {0, 0, 0, 0};
  if (do_gate && lane < 56) gwv = *(const GAS u32x4*)(p.gw + lane * 8);
  u32x4 Ba1, Ba2;
  {
    const int sc = i16 >> 2, k0 = 2 * q + ((i16 >> 1) & 1), ri = i16 & 1;
    float v[8];
#pragma unroll
    for (int j = 0; j < 8; ++j) {
      int d0 = 8 * (g >> 1) + j;
      float ph = (float)(d0 * k0) * (1.0f / 14.0f) + (ri ? 0.5f : 0.25f);
      float t = sinr(ph);
      v[j] = d0 < 14 ? t : 0.f;
    }
    u32x4 T = {pk(v[0], v[1]), pk(v[2], v[3]), pk(v[4], v[5]), pk(v[6], v[7])};
    u32x4 Z = {0, 0, 0, 0};
    Ba1 = (sc == (g & 1)) ? T : Z;
    Ba2 = (sc == 2 + (g & 1)) ? T : Z;
  }
  float twc[8], tws[8];
  {
    const int k0 = 2 * q + ((i16 >> 1) & 1), ri = i16 & 1;
#pragma unroll
    for (int j = 0; j < 8; ++j) {
      int d1 = 16 * (j >> 2) + 4 * g + (j & 3);
      float ph = (float)(d1 * k0) * (1.0f / 448.0f);
      twc[j] = cosr(ph);
      float s = sinr(ph);
      tws[j] = ri ? -s : s;
    }
  }
  u32x4 C1, S1, nS1;
  {
    const int k1 = 16 * (w >> 1) + i16;
    float c[8], s[8];
#pragma unroll
    for (int j = 0; j < 8; ++j) {
      int s1 = 4 * j + g;
      float ph = (float)((k1 * (64 * s1 + s2)) & 2047) * (1.0f / 2048.0f);
      c[j] = cosr(ph); s[j] = sinr(ph);
    }
    C1 = u32x4{pk(c[0], c[1]), pk(c[2], c[3]), pk(c[4], c[5]), pk(c[6], c[7])};
    S1 = u32x4{pk(s[0], s[1]), pk(s[2], s[3]), pk(s[4], s[5]), pk(s[6], s[7])};
    nS1 = xr(S1, 0x80008000u);
  }
  wait_vm0();
  bar();
  const u32 imm = (i16 & 1) ? 0x80008000u : 0u;
  f32x4 D2[2][2];
#pragma unroll
  for (int sg = 0; sg < 2; ++sg) {
    const int sigma = 2 * w + sg;
    f32x4 H[2];
#pragma unroll
    for (int td = 0; td < 2; ++td) {
      H[td] = f32x4{0, 0, 0, 0};
#pragma unroll
      for (int m = 0; m < 2; ++m) {
        const u32 rowa = lb + L_XT + (4 * sigma + 2 * m + (g & 1)) * XRS + (16 * td + 4 * (i16 & 3)) * 2;
        int d0a = 8 * (g >> 1) + (i16 >> 2), d0b = d0a + 4;
        d0b = d0b < 14 ? d0b : 0;
        u32x4 A = cat(trread(rowa + d0a * 64), trread(rowa + d0b * 64));
        H[td] = mfma32(A, m == 0 ? Ba1 : Ba2, H[td]);
      }
    }
    float h[8];
#pragma unroll
    for (int j = 0; j < 8; ++j) { float v = H[j >> 2][j & 3]; h[j] = v * twc[j] + fx1(v) * tws[j]; }
    u32x4 A = {pk(h[0], h[1]), pk(h[2], h[3]), pk(h[4], h[5]), pk(h[6], h[7])};
    u32x4 A2 = sx1(A, imm);
#pragma unroll
    for (int tk = 0; tk < 2; ++tk) {
      f32x4 z = {0, 0, 0, 0};
      z = mfma32(A, F.fc[tk], z);
      D2[sg][tk] = mfma32(A2, F.fs[tk], z);
    }
  }
#pragma unroll
  for (int tk = 0; tk < 2; ++tk)
#pragma unroll
    for (int r = 0; r < 4; ++r)
      lds_st<u32>(lb + L_XCH + ((tk * 4 + r) * 4 + w) * 256 + lane * 4, pk(D2[0][tk][r], D2[1][tk][r]));
  bar();
  {
    const int tk = w & 1, u = w >> 1;
    u32x4 o[2];
    u32 pc[4][2];
#pragma unroll
    for (int kk = 0; kk < 2; ++kk) {
      u32x4 Xr, Xi;
      const u32 br = lb + L_XCH + (tk * 4 + 2 * kk) * 1024 + lane * 4;
      Xr = u32x4{lds_ld<u32>(br), lds_ld<u32>(br + 256), lds_ld<u32>(br + 512), lds_ld<u32>(br + 768)};
      Xi = u32x4{lds_ld<u32>(br + 1024), lds_ld<u32>(br + 1280), lds_ld<u32>(br + 1536), lds_ld<u32>(br + 1792)};
      f32x4 z = {0, 0, 0, 0};
      f32x4 Tr = mfma32(Xi, S1, mfma32(Xr, C1, z));
      f32x4 Ti = mfma32(Xr, nS1, mfma32(Xi, C1, z));
      pc[kk][0] = pk(Tr[0], Tr[1]); pc[kk][1] = pk(Tr[2], Tr[3]);
      pc[2 + kk][0] = pk(Ti[0], Ti[1]); pc[2 + kk][1] = pk(Ti[2], Ti[3]);
    }
    o[0] = u32x4{pc[0][0], pc[0][1], pc[1][0], pc[1][1]};
    o[1] = u32x4{pc[2][0], pc[2][1], pc[3][0], pc[3][1]};
    const int k1 = 16 * u + i16, kq = 4 * tk + g;
    const u32 off = (u32)((((k1 * 64 + s2) * 4 + q) * 8 + kq) * 32);
    st16<P>(p.ws + WS_T, off, o[0]);
    st16<P>(p.ws + WS_T, off + 16, o[1]);
  }
  if (do_gate) {
#pragma unroll
    for (int r = 0; r < 2; ++r) {
      float acc = 0.f;
      if (lane < 56) {
        u32x4 xv = lds_ld<u32x4>(lb + L_XT + (8 * q + 2 * w + r) * XRS + lane * 16);
#pragma unroll
        for (int e = 0; e < 4; ++e) {
          acc += bfr(lo16(xv[e]) * lo16(gwv[e]));
          acc += bfr(hi16(xv[e]) * hi16(gwv[e]));
        }
      }
#pragma unroll
      for (int o = 32; o >= 1; o >>= 1) acc += __shfl_xor(acc, o);
      float z = bfr(acc);
      float gg = bfr(1.0f / (1.0f + __expf(-z)));
      if (lane == 0) { lds_st<float>(lb + L_G + (2 * w + r) * 8, gg); lds_st<float>(lb + L_G + (2 * w + r) * 8 + 4, bfr(1.0f - gg)); }
    }
  }
}

struct P2Const { u32x4 co[2], so[2], cot[2], sot[2]; float wr[16], wi[16]; };

DI void p2_load_w(const Params& p, u32 (&wraw)[16], int k1, int kq) {
  const int lane = threadIdx.x & 63, w = threadIdx.x >> 6, i16 = lane & 15, g = lane >> 4;
  const int ri = i16 >> 3, c = i16 & 7, k0 = 2 * w + (c >> 2), kap = 4 * kq + (c & 3), kd = k0 + 14 * kap;
  const bool direct = kd <= 224;
  const u16* base = (ri ? p.wi : p.wr) + (direct ? kd : 448 - kd);
#pragma unroll
  for (int j = 0; j < 16; ++j) {
    int k2 = 32 * (j >> 3) + 16 * ((j >> 2) & 1) + 4 * g + (j & 3);
    int ks = k1 + 32 * k2;
    int row = direct ? ks : ((2048 - ks) & 2047);
    wraw[j] = *(const GAS u16*)(base + row * DF);
  }
}
DI void p2_make(P2Const& C, const u32 (&wraw)[16], int kq) {
  const int lane = threadIdx.x & 63, w = threadIdx.x >> 6, i16 = lane & 15, g = lane >> 4;
  float c[8], s[8], ct[8], st[8];
#pragma unroll
  for (int jj = 0; jj < 8; ++jj) {
    int m = pi_k(g, jj);
    float ph = (float)((i16 * (2 * m + 1)) & 63) * (1.0f / 64.0f);
    c[jj] = cosr(ph); s[jj] = sinr(ph);
    float pt = (float)((m * (2 * i16 + 1)) & 63) * (1.0f / 64.0f);
    ct[jj] = cosr(pt); st[jj] = sinr(pt);
  }
  C.co[0] = u32x4{pk(c[0], c[1]), pk(c[2], c[3]), pk(c[4], c[5]), pk(c[6], c[7])};
  C.so[0] = u32x4{pk(s[0], s[1]), pk(s[2], s[3]), pk(s[4], s[5]), pk(s[6], s[7])};
  C.co[1] = xr(C.so[0], 0x00008000u);
  C.so[1] = xr(C.co[0], 0x80000000u);
  C.cot[0] = u32x4{pk(ct[0], ct[1]), pk(ct[2], ct[3]), pk(ct[4], ct[5]), pk(ct[6], ct[7])};
  C.sot[0] = u32x4{pk(st[0], st[1]), pk(st[2], st[3]), pk(st[4], st[5]), pk(st[6], st[7])};
  C.cot[1] = xr(C.cot[0], 0x80000000u);
  C.sot[1] = xr(C.sot[0], 0x80000000u);
  const int ri = i16 >> 3, cc = i16 & 7, k0 = 2 * w + (cc >> 2), kap = 4 * kq + (cc & 3), kd = k0 + 14 * kap;
  const float sg = ((kd <= 224) ? 1.f : -1.f) * (ri ? 1.f : -1.f);
#pragma unroll
  for (int j = 0; j < 16; ++j) {
    float own = bc<float>(wraw[j] << 16), nb = fr8(own);
    C.wr[j] = ri ? nb : own;
    C.wi[j] = (ri ? own : nb) * sg;
  }
}

template <bool P>
DI void fft_phase2(const Params& p, u32 lb, const FConst& F, const P2Const& C, int k1, int kq) {
  const int tid = threadIdx.x, lane = tid & 63, w = __builtin_amdgcn_readfirstlane(tid >> 6), i16 = lane & 15, g = lane >> 4;
  {
    u32x4 t[2];
#pragma unroll
    for (int it = 0; it < 2; ++it) {
      int item = tid + 256 * it, s2 = item >> 3, qq = (item >> 1) & 3, hf = item & 1;
      t[it] = ld16<P>(p.ws + WS_T, (u32)((((k1 * 64 + s2) * 4 + qq) * 8 + kq) * 32 + 16 * hf));
    }
#pragma unroll
    for (int it = 0; it < 2; ++it) {
      int item = tid + 256 * it, s2 = item >> 3;
      lds_st<u32x4>(lb + L_TI + s2 * 144 + (item & 7) * 16, t[it]);
    }
  }
  bar();
  const int ri = i16 >> 3;
  const u32 mf = ri ? 0x80008000u : 0u, mi = ri ? 0u : 0x80008000u;
  f32x4 E[2], O[2];
  {
    u32x4 B[2];
#pragma unroll
    for (int par = 0; par < 2; ++par) {
      const u32 a = lb + L_TI + (2 * (4 * g + (i16 >> 2)) + par) * 144 + w * 32 + (i16 & 3) * 8;
      B[par] = cat(trread(a), trread(a + 32 * 144));
    }
    u32x4 B0s = sr8(B[0], mf), B1s = sr8(B[1], mf);
#pragma unroll
    for (int t = 0; t < 2; ++t) {
      f32x4 z = {0, 0, 0, 0};
      E[t] = mfma32(F.fs[t], B0s, mfma32(F.fc[t], B[0], z));
      O[t] = mfma32(C.so[t], B1s, mfma32(C.co[t], B[1], z));
    }
  }
  float Pp[8], Mm[8];
#pragma unroll
  for (int j = 0; j < 8; ++j) {
    float e = E[j >> 2][j & 3], o = O[j >> 2][j & 3];
    float ylo = e + o, yhi = e - o;
    float vlo = ylo * C.wr[j] + fr8(ylo) * C.wi[j];
    float vhi = yhi * C.wr[8 + j] + fr8(yhi) * C.wi[8 + j];
    Pp[j] = vlo + vhi; Mm[j] = vlo - vhi;
  }
  u32x4 AP = {pk(Pp[0], Pp[1]), pk(Pp[2], Pp[3]), pk(Pp[4], Pp[5]), pk(Pp[6], Pp[7])};
  u32x4 AM = {pk(Mm[0], Mm[1]), pk(Mm[2], Mm[3]), pk(Mm[4], Mm[5]), pk(Mm[6], Mm[7])};
  u32x4 APs = sr8(AP, mi), AMs = sr8(AM, mi);
#pragma unroll
  for (int t = 0; t < 2; ++t) {
    f32x4 z = {0, 0, 0, 0};
    f32x4 ze = mfma32(APs, F.fs[t], mfma32(AP, F.fc[t], z));
    f32x4 zo = mfma32(AMs, C.sot[t], mfma32(AM, C.cot[t], z));
    const int s2e = 2 * (16 * t + i16);
    lds_st<u32x2>(lb + L_TO + s2e * 144 + w * 32 + g * 8, u32x2{pk(ze[0], ze[1]), pk(ze[2], ze[3])});
    lds_st<u32x2>(lb + L_TO + (s2e + 1) * 144 + w * 32 + g * 8, u32x2{pk(zo[0], zo[1]), pk(zo[2], zo[3])});
  }
  bar();
#pragma unroll
  for (int it = 0; it < 2; ++it) {
    int item = tid + 256 * it, s2 = item >> 3, ch = item & 7;
    u32x4 v = lds_ld<u32x4>(lb + L_TO + s2 * 144 + ch * 16);
    st16<P>(p.ws + WS_TP, (u32)(((s2 * 32 + k1) * 8 + kq) * 128 + ch * 16), v);
  }
}

struct P3Const { u32x4 b1, b2; float tc[4], ts[4]; u32x2 m; };

DI void p3_make(P3Const& C, int s2, int pp) {
  const int lane = threadIdx.x & 63, i16 = lane & 15, g = lane >> 4;
  {
    const int ro = i16 >> 3, s1 = 8 * pp + (i16 & 7);
    float a[8], b[8];
#pragma unroll
    for (int jj = 0; jj < 8; ++jj) {
      int k1 = pi_k(g, jj);
      float ph = (float)((k1 * (64 * s1 + s2)) & 2047) * (1.0f / 2048.0f);
      a[jj] = sinr(ph + (ro ? 0.f : 0.25f));
      b[jj] = sinr(ph + (ro ? 0.25f : 0.5f));
    }
    C.b1 = u32x4{pk(a[0], a[1]), pk(a[2], a[3]), pk(a[4], a[5]), pk(a[6], a[7])};
    C.b2 = u32x4{pk(b[0], b[1]), pk(b[2], b[3]), pk(b[4], b[5]), pk(b[6], b[7])};
  }
#pragma unroll
  for (int j = 0; j < 4; ++j) {
    int k0 = 2 * g + (j & 1), d1 = 16 * (j >> 1) + i16;
    float ph = (float)(k0 * d1) * (1.0f / 448.0f);
    C.tc[j] = cosr(ph); C.ts[j] = sinr(ph);
  }
  {
    float v[4];
#pragma unroll
    for (int j = 0; j < 4; ++j) {
      int k0 = 2 * g + (j >> 1);
      float ph = (float)(k0 * i16) * (1.0f / 14.0f) + ((j & 1) ? 0.5f : 0.25f);
      float cf = ((k0 == 0 || k0 == 7) ? 1.f : 2.f) * (1.0f / (2048.0f * 448.0f));
      v[j] = i16 < 14 ? sinr(ph) * cf : 0.f;
    }
    C.m = u32x2{pk(v[0], v[1]), pk(v[2], v[3])};
  }
}

template <bool P>
DI void p3_load_b(const Params& p, u32 lb, int s2, int pp) {
  const int tid = threadIdx.x;
  u32x4 v[2];
#pragma unroll
  for (int it = 0; it < 2; ++it) {
    int d = tid + 256 * it;
    if (d < D_) v[it] = ld16<P>(p.ws + WS_B, (u32)(((s2 * 4 + pp) * D_ + d) * 16));
  }
#pragma unroll
  for (int it = 0; it < 2; ++it) {
    int d = tid + 256 * it;
    if (d < D_) {
#pragma unroll
      for (int e = 0; e < 4; ++e) {
        lds_st<u16>(lb + L_BI + (2 * e) * BRS + d * 2, (u16)(v[it][e] & 0xffff));
        lds_st<u16>(lb + L_BI + (2 * e + 1) * BRS + d * 2, (u16)(v[it][e] >> 16));
      }
    }
  }
}

template <bool P>
DI void fft_phase3a(const Params& p, u32 lb, const P3Const& C, int s2) {
  const int tid = threadIdx.x, lane = tid & 63, w = __builtin_amdgcn_readfirstlane(tid >> 6), i16 = lane & 15, g = lane >> 4;
#pragma unroll
  for (int r = 0; r < 8; ++r) {
    int k1 = w * 8 + r;
    __builtin_amdgcn_global_load_lds((const GAS void*)(p.ws + WS_TP + (size_t)((s2 * 32 + k1) * 1024 + lane * 16)), (LAS void*)(size_t)(lb + L_TP + k1 * TPRS), 16, 0, P ? 16 : 0);
  }
  wait_vm0();
  bar();
#pragma unroll
  for (int kk = 0; kk < 2; ++kk) {
    const int k0 = 2 * w + kk;
#pragma unroll
    for (int t = 0; t < 2; ++t) {
      const u32 a = lb + L_TP + (4 * g + (i16 >> 2)) * TPRS + (4 * t + (i16 & 3)) * 128 + w * 32 + kk * 8;
      u32x4 Tr = cat(trread(a), trread(a + 16 * TPRS));
      u32x4 Ti = cat(trread(a + 16), trread(a + 16 + 16 * TPRS));
      f32x4 z = {0, 0, 0, 0};
      z = mfma32(Ti, C.b2, mfma32(Tr, C.b1, z));
      const int ro = i16 >> 3, s1l = i16 & 7, row = 2 * k0 + ro;
      lds_st<u32x2>(lb + L_Z + (s1l * 16 + row) * 64 + ((g ^ (row >> 2)) & 3) * 16 + t * 8, u32x2{pk(z[0], z[1]), pk(z[2], z[3])});
    }
  }
  bar();
}

template <bool P>
DI void fft_phase3b(const Params& p, u32 lb, const FConst& F, const P3Const& C, int s2, int pp) {
  const int tid = threadIdx.x, lane = tid & 63, w = __builtin_amdgcn_readfirstlane(tid >> 6), i16 = lane & 15, g = lane >> 4;
  const u32 mi = (i16 & 1) ? 0u : 0x80008000u;
#pragma unroll
  for (int rr = 0; rr < 2; ++rr) {
    const int s1l = 2 * w + rr;
    u32x4 A = lds_ld<u32x4>(lb + L_Z + (s1l * 16 + i16) * 64 + ((g ^ (i16 >> 2)) & 3) * 16);
    u32x4 A2 = sx1(A, mi);
    const float gg = lds_ld<float>(lb + L_G + s1l * 8), og = lds_ld<float>(lb + L_G + s1l * 8 + 4);
    const int s = 64 * (8 * pp + s1l) + s2;
#pragma unroll
    for (int td = 0; td < 2; ++td) {
      f32x4 z = {0, 0, 0, 0};
      f32x4 G = mfma32(A2, F.fs[td], mfma32(A, F.fc[td], z));
      float c0 = C.tc[2 * td], s0 = C.ts[2 * td], c1 = C.tc[2 * td + 1], s1 = C.ts[2 * td + 1];
      float g0r = G[0] * c0 - G[1] * s0, g0i = G[1] * c0 + G[0] * s0;
      float g1r = G[2] * c1 - G[3] * s1, g1i = G[3] * c1 + G[2] * s1;
      u32x2 Aa = {pk(g0r, g0i), pk(g1r, g1i)};
      f32x4 y = mfma16(Aa, C.m, z);
      if (i16 < 14) {
        const int d = 32 * i16 + 16 * td + 4 * g;
        u32x2 bv = lds_ld<u32x2>(lb + L_BI + s1l * BRS + d * 2);
        float b0 = lo16(bv.x), b1 = hi16(bv.x), b2 = lo16(bv.y), b3 = hi16(bv.y);
        float o0 = bfr(gg * bfr(y[0])) + bfr(og * b0), o1 = bfr(gg * bfr(y[1])) + bfr(og * b1);
        float o2 = bfr(gg * bfr(y[2])) + bfr(og * b2), o3 = bfr(gg * bfr(y[3])) + bfr(og * b3);
        *(GAS u32x2*)(p.out + (size_t)s * D_ + d) = u32x2{pk(o0, o1), pk(o2, o3)};
      }
    }
  }
}

template <bool P>
DI void ssm_transpose(const Params& p, u32 lb, int j) {
  const int tid = threadIdx.x, lane = tid & 63, w = tid >> 6, i16 = lane & 15, g = lane >> 4;
  const int sb = j / 7, db = j - sb * 7;
  u32x4 v[2];
#pragma unroll
  for (int it = 0; it < 2; ++it) {
    int row = (tid >> 3) + 32 * it;
    v[it] = *(const GAS u32x4*)(p.x + (size_t)(64 * sb + row) * D_ + 64 * db + (tid & 7) * 8);
  }
#pragma unroll
  for (int it = 0; it < 2; ++it) {
    int row = (tid >> 3) + 32 * it;
    lds_st<u32x4>(lb + L_PART + row * 144 + (tid & 7) * 16, v[it]);
  }
  bar();
#pragma unroll
  for (int m = 0; m < 2; ++m) {
    const int chi = g + 4 * m;
    const u32 a = lb + L_PART + (8 * chi + (i16 >> 2)) * 144 + (16 * w + 4 * (i16 & 3)) * 2;
    u32x4 t = cat(trread(a), trread(a + 4 * 144));
    st16<P>(p.ws + WS_XT, (u32)(((64 * db + 16 * w + i16) * S_ + 64 * sb + 8 * chi) * 2), t);
  }
}

struct SsmIn { u32 v[2][6]; };
DI void ssm_load_params(const Params& p, SsmIn& in, int d) {
  const int n = threadIdx.x;
#pragma unroll
  for (int c = 0; c < 2; ++c) {
    const size_t o = (size_t)(d + c) * NS + n;
    in.v[c][0] = *(const GAS u16*)(p.are + o); in.v[c][1] = *(const GAS u16*)(p.aim + o);
    in.v[c][2] = *(const GAS u16*)(p.bre + o); in.v[c][3] = *(const GAS u16*)(p.bim + o);
    in.v[c][4] = *(const GAS u16*)(p.cre + o); in.v[c][5] = *(const GAS u16*)(p.cim + o);
  }
}
DI f32x2 cexp_rev(float x2, float ph) {
  float e = __builtin_amdgcn_exp2f(x2);
  float f = __builtin_amdgcn_fractf(ph);
  f32x2 cs = {cosr(f), sinr(f)};
  return cs * e;
}
DI u32 pk2(f32x2 v) { return bc<u32>(__builtin_convertvector(v, bf16x2)); }
template <int ROW> DI void wrow4(u32 base, u32 a, u32 b, u32 c, u32 d) {
  asm volatile("s_mov_b32 m0, %4\n\ts_nop 0\n\tds_write_addtid_b32 %0 offset:%c5\n\tds_write_addtid_b32 %1 offset:%c6\n\tds_write_addtid_b32 %2 offset:%c7\n\tds_write_addtid_b32 %3 offset:%c8"
               :: "v"(a), "v"(b), "v"(c), "v"(d), "s"(base), "i"(ROW * KRS), "i"((ROW + 1) * KRS), "i"((ROW + 2) * KRS), "i"((ROW + 3) * KRS) : "memory");
}
DI void kgen_tile(u32 ub, u32 lane, const u32x4 (&V)[4], f32x4& acc) {
  const int i16 = lane & 15, g = lane >> 4;
  acc = f32x4{0, 0, 0, 0};
#pragma unroll
  for (int ks = 0; ks < 4; ++ks) acc = mfma32(lds_ld<u32x4>(ub + i16 * KRS + ks * 64 + g * 16), V[ks], acc);
}
DI void kgen_channel(u32 lb, const u32 (&in)[6], float dt, float& k0sum) {
  const int tid = threadIdx.x, lane = tid & 63, w = __builtin_amdgcn_readfirstlane(tid >> 6), i16 = lane & 15, g = lane >> 4;
  const u32 vb = lb + L_SCR + w * 9216, ub = vb + 16 * KRS;
  const float ar = dt * bc<float>(in[0] << 16), ai = dt * bc<float>(in[1] << 16);
  const f32x2 dB = {dt * bc<float>(in[2] << 16), dt * bc<float>(in[3] << 16)};
  const f32x2 Cc = {bc<float>(in[4] << 16), bc<float>(in[5] << 16)};
  const f32x2 CB = cmul(Cc, dB);
  const float x1 = ar * 1.44269504088896341f, p1 = ai * 0.15915494309189535f;
  const f32x2 E1 = cexp_rev(x1, p1), E8c = cexp_rev(8.f * x1, -8.f * p1), E128 = cexp_rev(128.f * x1, 128.f * p1);
  {
    f32x2 v2 = cmul(E8c, E8c), v3 = cmul(v2, E8c), v4 = cmul(v2, v2);
    wrow4<0>(vb, 0x00003f80u, pk2(E8c), pk2(v2), pk2(v3));
    f32x2 v5 = cmul(v4, E8c), v6 = cmul(v4, v2), v7 = cmul(v4, v3), v8 = cmul(v4, v4);
    wrow4<4>(vb, pk2(v4), pk2(v5), pk2(v6), pk2(v7));
    f32x2 v9 = cmul(v8, E8c), v10 = cmul(v8, v2), v11 = cmul(v8, v3), v12 = cmul(v8, v4);
    wrow4<8>(vb, pk2(v8), pk2(v9), pk2(v10), pk2(v11));
    f32x2 v13 = cmul(v8, v5), v14 = cmul(v8, v6), v15 = cmul(v8, v7);
    wrow4<12>(vb, pk2(v12), pk2(v13), pk2(v14), pk2(v15));
  }
  const f32x2 E2 = cmul(E1, E1), E3 = cmul(E2, E1), E4 = cmul(E2, E2), E5 = cmul(E4, E1), E6 = cmul(E4, E2), E7 = cmul(E4, E3);
  const f32x2 G1 = cmul(CB, E128);
  wrow4<0>(ub, pk2(CB), pk2(cmul(CB, E1)), pk2(cmul(CB, E2)), pk2(cmul(CB, E3)));
  wrow4<4>(ub, pk2(cmul(CB, E4)), pk2(cmul(CB, E5)), pk2(cmul(CB, E6)), pk2(cmul(CB, E7)));
  wrow4<8>(ub, pk2(G1), pk2(cmul(G1, E1)), pk2(cmul(G1, E2)), pk2(cmul(G1, E3)));
  wrow4<12>(ub, pk2(cmul(G1, E4)), pk2(cmul(G1, E5)), pk2(cmul(G1, E6)), pk2(cmul(G1, E7)));
  u32x4 V[4];
#pragma unroll
  for (int ks = 0; ks < 4; ++ks) V[ks] = lds_ld<u32x4>(vb + i16 * KRS + ks * 64 + g * 16);
  f32x4 a0, a1, a2;
  kgen_tile(ub, lane, V, a0);
  const f32x2 G2 = cmul(G1, E128), G3 = cmul(G2, E128), G4 = cmul(G3, E128), G5 = cmul(G4, E128), G6 = cmul(G5, E128), G7 = cmul(G6, E128);
  wrow4<0>(vb, pk2(G2), pk2(cmul(G2, E2)), pk2(cmul(G2, E4)), pk2(cmul(G2, E6)));
  wrow4<4>(vb, pk2(G3), pk2(cmul(G3, E2)), pk2(cmul(G3, E4)), pk2(cmul(G3, E6)));
  wrow4<8>(vb, pk2(G4), pk2(cmul(G4, E4)), pk2(G5), pk2(cmul(G5, E4)));
  wrow4<12>(vb, pk2(G6), pk2(cmul(G6, E4)), pk2(G7), pk2(cmul(G7, E4)));
  kgen_tile(vb, lane, V, a1);
  const f32x2 G8 = cmul(G7, E128), G9 = cmul(G8, E128), G10 = cmul(G9, E128), G11 = cmul(G10, E128);
  wrow4<0>(ub, pk2(G8), pk2(G9), pk2(G10), pk2(G11));
  const f32x2 G12 = cmul(G11, E128), G13 = cmul(G12, E128), G14 = cmul(G13, E128), G15 = cmul(G14, E128), G16 = cmul(G15, E128);
  wrow4<4>(ub, pk2(G12), pk2(G13), pk2(G14), pk2(G15));
  wrow4<8>(ub, pk2(G16), 0u, 0u, 0u);
  wrow4<12>(ub, 0u, 0u, 0u, 0u);
  kgen_tile(ub, lane, V, a2);
  lds_st<f32x4>(lb + L_PART + (w * 3 + 0) * 1024 + lane * 16, a0);
  lds_st<f32x4>(lb + L_PART + (w * 3 + 1) * 1024 + lane * 16, a1);
  lds_st<f32x4>(lb + L_PART + (w * 3 + 2) * 1024 + lane * 16, a2);
  (void)k0sum;
}
DI int kidx(int l) {
  if (l < 0) return 768;
  int v = l;
  if (l > 256) {
    int sh = l >= 1024 ? 3 : (l >= 512 ? 2 : 1);
    int half = 1 << (sh - 1);
    v = ((l + half - 1 + ((l >> sh) & 1)) >> sh) << sh;
  }
  if (v < 256) return ((v & 7) + 8 * (v >> 7)) * 16 + ((v >> 3) & 15);
  if (v < 512) { int u = v - 256; return 256 + (((u >> 1) & 3) + 4 * (u >> 7)) * 16 + ((u >> 3) & 15); }
  if (v < 1024) { int u = v - 512; return 256 + (8 + ((u >> 2) & 1) + 2 * (u >> 7)) * 16 + ((u >> 3) & 15); }
  if (v < 2048) { int u = v - 1024; return 512 + (u >> 7) * 16 + ((u >> 3) & 15); }
  return 512 + 8 * 16;
}

template <bool P>
DI void ssm_main(const Params& p, u32 lb, const SsmIn& in, int j, u32 epoch) {
  const int tid = threadIdx.x, lane = tid & 63, w = __builtin_amdgcn_readfirstlane(tid >> 6), i16 = lane & 15, g = lane >> 4;
  const int d = 2 * j;
  u32 ka[12];
#pragma unroll
  for (int e = 0; e < 12; ++e) ka[e] = lb + L_KT + 2 * kidx(2048 - (8 * tid + e));
  float dt[2], dsk[2];
#pragma unroll
  for (int c = 0; c < 2; ++c) { dt[c] = bfr(expf(b2f(p.logdt[d + c]))); dsk[c] = b2f(p.dskip[d + c]); }
#pragma unroll
  for (int c = 0; c < 2; ++c) {
    float dummy = 0.f;
    kgen_channel(lb, in.v[c], dt[c], dummy);
    bar();
    if (tid < 192) {
      const int t = tid >> 6;
      f32x4 s = lds_ld<f32x4>(lb + L_PART + t * 1024 + lane * 16);
#pragma unroll
      for (int ww = 1; ww < 4; ++ww) s += lds_ld<f32x4>(lb + L_PART + (ww * 3 + t) * 1024 + lane * 16);
      const u32 tb = lb + L_KT + c * 1600 + (t * 256 + 4 * g * 16 + i16) * 2;
      u32 a = pk(s[0], s[1]), b = pk(s[2], s[3]);
      lds_st<u16>(tb, (u16)a); lds_st<u16>(tb + 32, (u16)(a >> 16));
      lds_st<u16>(tb + 64, (u16)b); lds_st<u16>(tb + 96, (u16)(b >> 16));
    } else if (tid == 192) lds_st<u16>(lb + L_KT + c * 1600 + 1536, (u16)0);
    bar();
  }
#pragma unroll
  for (int c = 0; c < 2; ++c) {
    u32 e[12];
#pragma unroll
    for (int k = 0; k < 12; ++k) e[k] = lds_ld<u16>(ka[k] + c * 1600);
    u32 dw[6], al[5];
#pragma unroll
    for (int k = 0; k < 6; ++k) dw[k] = e[2 * k] | (e[2 * k + 1] << 16);
#pragma unroll
    for (int k = 0; k < 5; ++k) al[k] = e[2 * k + 1] | (e[2 * k + 2] << 16);
    const u32 kb = lb + L_KC + c * 4 * KCP + tid * 16;
    lds_st<u32x4>(kb, u32x4{dw[0], dw[1], dw[2], dw[3]});
    lds_st<u32x4>(kb + KCP, u32x4{al[0], al[1], al[2], al[3]});
    lds_st<u32x4>(kb + 2 * KCP, u32x4{dw[1], dw[2], dw[3], dw[4]});
    lds_st<u32x4>(kb + 3 * KCP, u32x4{al[1], al[2], al[3], al[4]});
    if (tid < 16) {
      const int cp = tid >> 2, ch = tid & 3;
      u32x4 z = {0, 0, 0, 0};
      if (tid == 0) z.x = lds_ld<u16>(lb + L_KT + c * 1600);
      lds_st<u32x4>(lb + L_KC + (c * 4 + cp) * KCP + 4096 + ch * 16, z);
    }
    if (tid >= 64 && tid < 96) lds_st<u32x4>(lb + L_U + c * 4608 + (tid - 64) * 16, u32x4{0, 0, 0, 0});
  }
  if (P) { if (w == 0) poll_flags((const u32*)(p.ws + WS_CTL) + CT_FX + (d >> 6) * 64, 8, epoch); bar(); }
  {
    u32x4 t[2];
#pragma unroll
    for (int c = 0; c < 2; ++c) t[c] = ld16<P>(p.ws + WS_XT, (u32)(((d + c) * S_ + tid * 8) * 2));
#pragma unroll
    for (int c = 0; c < 2; ++c) lds_st<u32x4>(lb + L_U + c * 4608 + 512 + tid * 16, t[c]);
  }
  bar();
  const int ch = w >> 1, hf = w & 1;
  f32x4 acc[8];
#pragma unroll
  for (int c = 0; c < 8; ++c) acc[c] = f32x4{0, 0, 0, 0};
  {
    const int bsh = (-i16) & 3;
    const u32 abase = lb + L_KC + (ch * 4 + bsh) * KCP + (2032 + 8 * g - i16 - bsh) * 2;
    const u32 bbase = lb + L_U + ch * 4608 + (256 - 16 + 16 * i16 + 8 * g) * 2;
#pragma unroll
    for (int rr = 0; rr < 4; ++rr) {
      const int r = 4 * hf + rr;
      u32x4 A[8], B[8];
#pragma unroll
      for (int qq = 0; qq < 8; ++qq) {
        const u32 a = abase - (8 * qq + r) * 64;
        A[qq] = cat(lds_ld<u32x2>(a), lds_ld<u32x2>(a + 8));
        B[qq] = lds_ld<u32x4>(bbase + (8 * qq - r) * 64);
      }
#pragma unroll
      for (int qq = 0; qq < 8; ++qq)
#pragma unroll
        for (int m = 0; m + qq < 8; ++m) acc[qq + m] = mfma32(A[qq], B[m], acc[qq + m]);
    }
  }
#pragma unroll
  for (int c = 0; c < 4; ++c) lds_st<f32x4>(lb + L_PART + (w * 4 + c) * 1024 + lane * 16, acc[hf ? c : 4 + c]);
  bar();
#pragma unroll
  for (int c = 0; c < 4; ++c) {
    f32x4 o = lds_ld<f32x4>(lb + L_PART + ((w ^ 1) * 4 + c) * 1024 + lane * 16);
    const int tile = hf ? 4 + c : c;
    f32x4 y = acc[hf ? 4 + c : c] + o;
    const int t = 256 * tile + 16 * i16 + 4 * g;
    u32x2 uv = lds_ld<u32x2>(lb + L_U + ch * 4608 + 512 + t * 2);
    const float dk = dsk[0], dk1 = dsk[1], dd = ch ? dk1 : dk;
    float b0 = bfr(y[0]) + bfr(lo16(uv.x) * dd), b1 = bfr(y[1]) + bfr(hi16(uv.x) * dd);
    float b2 = bfr(y[2]) + bfr(lo16(uv.y) * dd), b3 = bfr(y[3]) + bfr(hi16(uv.y) * dd);
    lds_st<u32x2>(lb + L_BO + ch * 4096 + t * 2, u32x2{pk(b0, b1), pk(b2, b3)});
  }
  bar();
#pragma unroll
  for (int c = 0; c < 2; ++c) {
    const u32 a = lb + L_BO + c * 4096 + (8 * g + (i16 >> 2)) * 128 + (16 * w + 4 * (i16 & 3)) * 2;
    u32x4 t = cat(trread(a), trread(a + 4 * 128));
    st16<P>(p.ws + WS_B, (u32)((((16 * w + i16) * 4 + g) * D_ + d + c) * 16), t);
  }
}

template <int PH>
__global__ __launch_bounds__(256, 2) void gfs4_kernel(Params p) {
  extern __shared__ __attribute__((aligned(16))) char smem[];
  const u32 lb = (u32)(size_t)(LAS char*)smem;
  constexpr bool P = PH == 0;
  const int b = blockIdx.x, tid = threadIdx.x, lane = tid & 63, i16 = lane & 15, g = lane >> 4;
  u32* ctl = (u32*)(p.ws + WS_CTL);
  u32 epoch = 0;
  if (P) epoch = __builtin_amdgcn_readfirstlane(__hip_atomic_load((GAS u32*)ctl + CT_GEN, RLX_AGENT)) + 1u;
  if (b < N_FFT) {
    const int s2 = b & 63, q = b >> 6;
    const int xx = b & 7, mm = (b >> 3) & 3, kq = b >> 5;
    const int k1 = xx == 0 ? (mm == 0 ? 0 : mm == 1 ? 16 : mm == 2 ? 8 : 24) : (mm == 0 ? xx : mm == 1 ? 32 - xx : mm == 2 ? 16 - xx : 16 + xx);
    FConst F;
    make_f32(F, i16, g);
    if (PH == 0 || PH == 1) fft_phase1<P>(p, lb, F, s2, q, PH == 0);
    if (P) { wait_vm0(); bar(); publish(ctl + CT_F1 + b, 8, 256, epoch); }
    u32 wraw[16];
    if (PH == 0 || PH == 2) p2_load_w(p, wraw, k1, kq);
    if (PH == 0 || PH == 2) {
      P2Const C2;
      p2_make(C2, wraw, kq);
      if (P) { if (tid < 64) poll_flags(ctl + CT_F1 + (b & 7) * 256, 64, epoch); bar(); }
      fft_phase2<P>(p, lb, F, C2, k1, kq);
    }
    if (P) { wait_vm0(); bar(); publish(ctl + CT_F2 + b, 8, 256, epoch); }
    if (PH == 0 || PH == 3) {
      P3Const C3;
      p3_make(C3, s2, q);
      if (PH == 3) {
        const int w = tid >> 6;
        u32x4 gwv = {0, 0, 0, 0};
        if (lane < 56) gwv = *(const GAS u32x4*)(p.gw + lane * 8);
#pragma unroll
        for (int r = 0; r < 2; ++r) {
          float acc = 0.f;
          if (lane < 56) {
            u32x4 xv = *(const GAS u32x4*)(p.x + (size_t)(64 * (8 * q + 2 * w + r) + s2) * D_ + lane * 8);
#pragma unroll
            for (int e = 0; e < 4; ++e) { acc += bfr(lo16(xv[e]) * lo16(gwv[e])); acc += bfr(hi16(xv[e]) * hi16(gwv[e])); }
          }
#pragma unroll
          for (int o = 32; o >= 1; o >>= 1) acc += __shfl_xor(acc, o);
          float z = bfr(acc);
          float gg = bfr(1.0f / (1.0f + __expf(-z)));
          if (lane == 0) { lds_st<float>(lb + L_G + (2 * w + r) * 8, gg); lds_st<float>(lb + L_G + (2 * w + r) * 8 + 4, bfr(1.0f - gg)); }
        }
      }
      if (P) { if (tid < 64) poll_flags(ctl + CT_F2 + (b & 7) * 256, 64, epoch); bar(); }
      fft_phase3a<P>(p, lb, C3, s2);
      if (P) { if (tid < 64) poll_flags(ctl + CT_FB + (b & 7) * 256, 56, epoch); bar(); }
      p3_load_b<P>(p, lb, s2, q);
      bar();
      fft_phase3b<P>(p, lb, F, C3, s2, q);
      if (P && b == 0 && tid == 0) __hip_atomic_store((GAS u32*)ctl + CT_GEN, epoch, RLX_AGENT);
    }
  } else {
    const int j = b - N_FFT;
    SsmIn in;
    if (PH == 0 || PH == 2) ssm_load_params(p, in, 2 * j);
    if (PH == 0 || PH == 1) ssm_transpose<P>(p, lb, j);
    if (P) { wait_vm0(); bar(); if (tid == 0) __hip_atomic_store((GAS u32*)ctl + CT_FX + (j % 7) * 64 + j / 7, epoch, RLX_AGENT); }
    if (PH == 0 || PH == 2) ssm_main<P>(p, lb, in, j, epoch);
    if (P) { wait_vm0(); bar(); publish(ctl + CT_FB + j, 8, 256, epoch); }
  }
}

extern "C" void kernel_launch(void* const* d_in, const int* in_sizes, int n_in,
                              void* d_out, int out_size, void* d_ws, size_t ws_size,
                              hipStream_t stream) {
  (void)in_sizes; (void)n_in; (void)out_size; (void)ws_size;
  Params p;
  p.x = (const u16*)d_in[0]; p.wr = (const u16*)d_in[1]; p.wi = (const u16*)d_in[2];
  p.are = (const u16*)d_in[4]; p.aim = (const u16*)d_in[5]; p.bre = (const u16*)d_in[6]; p.bim = (const u16*)d_in[7];
  p.cre = (const u16*)d_in[8]; p.cim = (const u16*)d_in[9]; p.logdt = (const u16*)d_in[10]; p.dskip = (const u16*)d_in[11];
  p.gw = (const u16*)d_in[12];
  p.out = (u16*)d_out; p.ws = (char*)d_ws;
  (void)hipFuncSetAttribute((const void*)gfs4_kernel<0>, hipFuncAttributeMaxDynamicSharedMemorySize, LDS_BYTES);
  gfs4_kernel<0><<<N_WG, 256, LDS_BYTES, stream>>>(p);
}
```

```cpp
#include <hip/hip_runtime.h>
#include <hip/hip_bf16.h>
#include <stdint.h>

typedef unsigned int u32;
typedef unsigned short u16;
typedef unsigned long long u64;
typedef float f32x4 __attribute__((ext_vector_type(4)));
typedef float f32x2 __attribute__((ext_vector_type(2)));
typedef u32 u32x4 __attribute__((ext_vector_type(4)));
typedef u32 u32x2 __attribute__((ext_vector_type(2)));
typedef __bf16 bf16x8 __attribute__((ext_vector_type(8)));
typedef __bf16 bf16x2 __attribute__((ext_vector_type(2)));
typedef short s16x4 __attribute__((ext_vector_type(4)));
#define LAS __attribute__((address_space(3)))
#define GAS __attribute__((address_space(1)))
#define DI __device__ __forceinline__

#define NLAUNCH 1

constexpr int S_ = 2048, D_ = 448, DF = 225, NS = 256;
constexpr int N_FFT = 256, N_SSM = 224, N_WG = N_FFT + N_SSM, NT = 512;

constexpr u32 T_K1 = 65536 + 256;
constexpr u32 TP_S2 = 32768 + 128;
constexpr u32 XT_D = 4096 + 128;
constexpr u32 B_D = 4096 + 128;
constexpr size_t WS_CTL = 0;
constexpr size_t WS_T   = 65536;
constexpr size_t WS_TP  = WS_T + (3u << 20);
constexpr size_t WS_XT  = WS_TP + (3u << 20);
constexpr size_t WS_B   = WS_XT + (2u << 20);
constexpr int CT_GEN = 0;
constexpr int CT_F1 = 1024;
constexpr int CT_F2 = CT_F1 + 2048;
constexpr int CT_FB = CT_F2 + 2048;
constexpr int CT_FX = CT_FB + 2048;

constexpr int XRS = 928;
constexpr int L_XT = 0;
constexpr int L_XCH = 29696;
constexpr int TPRS = 1040;
constexpr int L_TP = 0;
constexpr int L_TO = 29696;
constexpr int L_TI = 38912;
constexpr int L_BI = 38912;
constexpr int BRS = 912;
constexpr int L_Z = 47104;
constexpr int L_G = 55296;
constexpr int KCP = 4160;
constexpr int L_KC = 0;
constexpr int KSCR = 4096;
constexpr int L_RED = 33280;
constexpr int L_BO = 49664;
constexpr int L_U = 57856;
constexpr int L_XP = 57856;
constexpr int L_KT = 67072;
constexpr int L_CNT = 70272;
constexpr int LDS_BYTES = 70288;

struct Params {
  const u16* x; const u16* wr; const u16* wi;
  const u16* are; const u16* aim; const u16* bre; const u16* bim; const u16* cre; const u16* cim;
  const u16* logdt; const u16* dskip; const u16* gw;
  u16* out; char* ws;
};

constexpr double cx_sin_oct(double x) { double x2 = x * x, t = x, s = x; for (int i = 1; i < 9; ++i) { t *= -x2 / ((2 * i) * (2 * i + 1)); s += t; } return s; }
constexpr double cx_cos_oct(double x) { double x2 = x * x, t = 1, s = 1; for (int i = 1; i < 9; ++i) { t *= -x2 / ((2 * i - 1) * (2 * i)); s += t; } return s; }
constexpr double cx_sin(long n, long d) {
  n %= d; if (n < 0) n += d;
  const long o = (8 * n) / d;
  const double r = 6.283185307179586476925 * (double)(8 * n - o * d) / (8.0 * (double)d), q = 0.78539816339744830962;
  switch (o) {
    case 0: return cx_sin_oct(r);       case 1: return cx_cos_oct(q - r);
    case 2: return cx_cos_oct(r);       case 3: return cx_sin_oct(q - r);
    case 4: return -cx_sin_oct(r);      case 5: return -cx_cos_oct(q - r);
    case 6: return -cx_cos_oct(r);      default: return -cx_sin_oct(q - r);
  }
}
constexpr double cx_cos(long n, long d) { return cx_sin(4 * n + d, 4 * d); }
constexpr u32 cx_f32(double v) { return __builtin_bit_cast(u32, (float)v); }
constexpr u32 cx_bf(double v) { const u32 u = cx_f32(v); return (u + 0x7fffu + ((u >> 16) & 1u)) >> 16; }
constexpr int cx_pi(int g, int jj) { return 16 * (jj >> 2) + 4 * g + (jj & 3); }
struct Frag { u32 v[64][4]; };
enum { TF_FC0, TF_FS0, TF_FC1, TF_FS1, TF_CO0, TF_SO0, TF_CO1, TF_SO1, TF_COT0, TF_SOT0, TF_COT1, TF_SOT1, TF_TC, TF_TS, TF_M, TF_Q0, TF_N = TF_Q0 + 24 };
constexpr Frag make_frag(int item) {
  Frag f{};
  for (int lane = 0; lane < 64; ++lane) {
    const int i16 = lane & 15, g = lane >> 4;
    if (item < TF_TC) {
      const int t = (item >> 1) & 1, sn = item & 1;
      for (int jj = 0; jj < 8; ++jj) {
        const int m = cx_pi(g, jj), a = 16 * t + i16;
        long n = 0, d = 32;
        if (item < TF_CO0) { n = a * m; d = 32; }
        else if (item < TF_COT0) { n = a * (2 * m + 1); d = 64; }
        else { n = m * (2 * a + 1); d = 64; }
        f.v[lane][jj >> 1] |= cx_bf(sn ? cx_sin(n, d) : cx_cos(n, d)) << (16 * (jj & 1));
      }
    } else if (item == TF_TC || item == TF_TS) {
      for (int j = 0; j < 4; ++j) {
        const long n = (2 * g + (j & 1)) * (16 * (j >> 1) + i16);
        f.v[lane][j] = cx_f32(item == TF_TC ? cx_cos(n, 448) : cx_sin(n, 448));
      }
    } else if (item == TF_M) {
      for (int j = 0; j < 4; ++j) {
        const int k0 = 2 * g + (j >> 1);
        const double cf = ((k0 == 0 || k0 == 7) ? 1.0 : 2.0) / (2048.0 * 448.0);
        const double v = i16 < 14 ? cf * ((j & 1) ? -cx_sin(k0 * i16, 14) : cx_cos(k0 * i16, 14)) : 0.0;
        f.v[lane][j >> 1] |= cx_bf(v) << (16 * (j & 1));
      }
    } else {
      const int q = (item - TF_Q0) / 6, sub = (item - TF_Q0) % 6, k0 = 2 * q + ((i16 >> 1) & 1), ri = i16 & 1;
      if (sub < 2) {
        if ((i16 >> 2) == 2 * sub + (g & 1))
          for (int j = 0; j < 8; ++j) {
            const int d0 = 8 * (g >> 1) + j;
            const double v = d0 < 14 ? (ri ? -cx_sin(d0 * k0, 14) : cx_cos(d0 * k0, 14)) : 0.0;
            f.v[lane][j >> 1] |= cx_bf(v) << (16 * (j & 1));
          }
      } else {
        for (int j = 0; j < 4; ++j) {
          const long n = cx_pi(g, 4 * (sub & 1) + j) * k0;
          f.v[lane][j] = cx_f32(sub < 4 ? cx_cos(n, 448) : (ri ? -cx_sin(n, 448) : cx_sin(n, 448)));
        }
      }
    }
  }
  return f;
}
template <int I> struct FragC { static constexpr Frag f = make_frag(I); };
#define FR(i) FragC<(i)>::f
#define FR6(i) FR(i), FR(i + 1), FR(i + 2), FR(i + 3), FR(i + 4), FR(i + 5)
__device__ const __attribute__((aligned(1024))) Frag TF[TF_N] = {FR6(0), FR6(6), FR(12), FR(13), FR(14), FR6(15), FR6(21), FR6(27), FR6(33)};

struct KTab { u16 v[2080]; };
constexpr KTab make_ktab() {
  KTab t{};
  for (int i = 0; i < 2080; ++i) {
    const int l = 2048 - i;
    int idx = 768;
    if (l >= 0) {
      const int sh = l > 256 ? (l >= 1024 ? 3 : (l >= 512 ? 2 : 1)) : 0;
      const int v = sh ? (((l + (1 << (sh - 1)) - 1 + ((l >> sh) & 1)) >> sh) << sh) : l;
      if (v < 256) idx = ((v & 7) + 8 * (v >> 7)) * 16 + ((v >> 3) & 15);
      else if (v < 512) idx = 256 + ((((v - 256) >> 1) & 3) + 4 * ((v - 256) >> 7)) * 16 + (((v - 256) >> 3) & 15);
      else if (v < 1024) idx = 256 + (8 + (((v - 512) >> 2) & 1) + 2 * ((v - 512) >> 7)) * 16 + (((v - 512) >> 3) & 15);
      else if (v < 2048) idx = 512 + ((v - 1024) >> 7) * 16 + (((v - 1024) >> 3) & 15);
      else idx = 640;
    }
    t.v[i] = (u16)(2 * idx);
  }
  return t;
}
__device__ const __attribute__((aligned(16))) KTab KTAB = make_ktab();

template <class T, class F> DI T bc(F v) { return __builtin_bit_cast(T, v); }
DI f32x4 mfma32(u32x4 a, u32x4 b, f32x4 c) { return __builtin_amdgcn_mfma_f32_16x16x32_bf16(bc<bf16x8>(a), bc<bf16x8>(b), c, 0, 0, 0); }
DI f32x4 mfma16(u32x2 a, u32x2 b, f32x4 c) { return __builtin_amdgcn_mfma_f32_16x16x16bf16_1k(bc<s16x4>(a), bc<s16x4>(b), c, 0, 0, 0); }
DI u32 pk(float a, float b) { f32x2 v = {a, b}; return bc<u32>(__builtin_convertvector(v, bf16x2)); }
DI u32 pk2(f32x2 v) { return bc<u32>(__builtin_convertvector(v, bf16x2)); }
DI float bfr(float a) { return bc<float>(pk(0.f, a) & 0xffff0000u); }
DI float lo16(u32 v) { return bc<float>(v << 16); }
DI float hi16(u32 v) { return bc<float>(v & 0xffff0000u); }
DI f32x2 up2(u32 v) { return f32x2{lo16(v), hi16(v)}; }
DI float b2f(u16 v) { return bc<float>((u32)v << 16); }
DI u32x2 trread(u32 a) { return bc<u32x2>(__builtin_amdgcn_ds_read_tr16_b64_v4i16((LAS s16x4*)(size_t)a)); }
DI u32x4 cat(u32x2 a, u32x2 b) { u32x4 r = {a.x, a.y, b.x, b.y}; return r; }
DI float sinr(float x) { return __builtin_amdgcn_sinf(x); }
DI float cosr(float x) { return __builtin_amdgcn_cosf(x); }
DI float fx1(float v) { return __builtin_amdgcn_update_dpp(v, v, 0xB1, 0xf, 0xf, true); }
DI float fr8(float v) { return __builtin_amdgcn_update_dpp(v, v, 0x128, 0xf, 0xf, true); }
DI u32 ux1(u32 v) { return (u32)__builtin_amdgcn_update_dpp((int)v, (int)v, 0xB1, 0xf, 0xf, true); }
DI u32 ur8(u32 v) { return (u32)__builtin_amdgcn_update_dpp((int)v, (int)v, 0x128, 0xf, 0xf, true); }
DI u32x4 sx1(u32x4 v, u32 m) { u32x4 r = {ux1(v.x) ^ m, ux1(v.y) ^ m, ux1(v.z) ^ m, ux1(v.w) ^ m}; return r; }
DI u32x4 sr8(u32x4 v, u32 m) { u32x4 r = {ur8(v.x) ^ m, ur8(v.y) ^ m, ur8(v.z) ^ m, ur8(v.w) ^ m}; return r; }
DI u32x4 xr(u32x4 v, u32 m) { u32x4 r = {v.x ^ m, v.y ^ m, v.z ^ m, v.w ^ m}; return r; }
DI u32x4 pk8(const float (&v)[8]) { return u32x4{pk(v[0], v[1]), pk(v[2], v[3]), pk(v[4], v[5]), pk(v[6], v[7])}; }
template <class T> DI T lds_ld(u32 a) { return *(LAS T*)(size_t)a; }
template <class T> DI void lds_st(u32 a, T v) { *(LAS T*)(size_t)a = v; }
DI void wait_vm0() { __builtin_amdgcn_s_waitcnt(0x0F70); }
DI void drain_vm() { asm volatile("s_waitcnt vmcnt(0)" ::: "memory"); }
DI void bar() { __builtin_amdgcn_s_waitcnt(0xC07F); asm volatile("s_barrier" ::: "memory"); }
template <class T> DI void pin(T& v) { asm volatile("" : "+v"(v)); }
typedef __amdgpu_buffer_rsrc_t rsrc_t;
DI rsrc_t mkrs(const void* p, u32 bytes) { return __builtin_amdgcn_make_buffer_rsrc((void*)p, 0, bytes, 0x00020000); }
template <int AUX> DI void dma16(rsrc_t rs, u32 lds, u32 voff, u32 soff) { __builtin_amdgcn_raw_ptr_buffer_load_lds(rs, (LAS void*)(size_t)lds, 16, voff, soff, 0, AUX); }
DI f32x2 cmul(f32x2 x, f32x2 y) {
  f32x2 t;
  asm("v_pk_mul_f32 %0, %1, %2 op_sel:[0,0] op_sel_hi:[0,1]\n\tv_pk_fma_f32 %0, %1, %2, %0 op_sel:[1,1,0] op_sel_hi:[1,0,1] neg_lo:[1,0,0]" : "=&v"(t) : "v"(x), "v"(y));
  return t;
}
DI float dot2b(u32 a, u32 b, float c) { return __builtin_amdgcn_fdot2_f32_bf16(bc<bf16x2>(a), bc<bf16x2>(b), c, false); }
DI float wave_sum(float a) {
  a += __builtin_amdgcn_update_dpp(0.f, a, 0xB1, 0xf, 0xf, true);
  a += __builtin_amdgcn_update_dpp(0.f, a, 0x4E, 0xf, 0xf, true);
  a += __builtin_amdgcn_update_dpp(0.f, a, 0x141, 0xf, 0xf, true);
  a += __builtin_amdgcn_update_dpp(0.f, a, 0x140, 0xf, 0xf, true);
  a += __builtin_amdgcn_update_dpp(0.f, a, 0x142, 0xa, 0xf, false);
  a += __builtin_amdgcn_update_dpp(0.f, a, 0x143, 0xc, 0xf, false);
  return bc<float>(__builtin_amdgcn_readlane(bc<int>(a), 63));
}
DI int pi_k(int g, int jj) { return 16 * (jj >> 2) + 4 * g + (jj & 3); }

#define RLX_AGENT __ATOMIC_RELAXED, __HIP_MEMORY_SCOPE_AGENT
template <bool P> DI void st16(rsrc_t rs, u32 voff, u32 soff, u32x4 v) {
  __builtin_amdgcn_raw_buffer_store_b128(v, rs, voff, soff, P ? 16 : 0);
  asm volatile("s_nop 1" :: "v"(v) : "memory");
}
template <bool P> DI u32x4 ld16(rsrc_t rs, u32 voff, u32 soff) { return __builtin_amdgcn_raw_buffer_load_b128(rs, voff, soff, P ? 16 : 0); }
DI void publish(u32* first, u32 epoch) {
  if (threadIdx.x < 8) __hip_atomic_store((GAS u32*)first + threadIdx.x * 256, epoch, RLX_AGENT);
}
DI void poll_flags(const u32* flags, int n4, u32 epoch) {
  const int lane = threadIdx.x & 63;
  const __amdgpu_buffer_rsrc_t rs = __builtin_amdgcn_make_buffer_rsrc((void*)flags, 0, n4 * 16, 0x00020000);
  for (int spin = 0; spin < (1 << 18); ++spin) {
    u32x4 f = __builtin_amdgcn_raw_buffer_load_b128(rs, lane * 16, 0, 16);
    bool ok = lane >= n4 || (f.x == epoch && f.y == epoch && f.z == epoch && f.w == epoch);
    if (__all(ok)) break;
    __builtin_amdgcn_s_sleep(1);
  }
}

#define STAMP(i) do {} while (0)
#define STAMP_DECL do {} while (0)
#define STAMP_FLUSH() do {} while (0)
#define STAMP_ARG
#define STAMP_PASS

DI u32x4 tf(rsrc_t rs, int item, u32 l16) { return __builtin_amdgcn_raw_buffer_load_b128(rs, l16 + (item & 3) * 1024, (item >> 2) * 4096, 0); }
DI u32x4 tfs(rsrc_t rs, int item, u32 l16, u32 so) { return __builtin_amdgcn_raw_buffer_load_b128(rs, l16 + item * 1024, so, 0); }

template <int PH>
DI void fft_role(const Params& p, const u32 lb, const int b, u32* ctl, const u32 gen STAMP_ARG) {
  constexpr bool P = PH == 0;
  constexpr int AUX = P ? 16 : 0;
  const int tid = threadIdx.x, lane = tid & 63, w = __builtin_amdgcn_readfirstlane(tid >> 6), i16 = lane & 15, g = lane >> 4;
  const u32 l16 = lane * 16;
  const int s2 = b & 63, q = b >> 6;
  const int xx = b & 7, mm = (b >> 3) & 3, kq = b >> 5;
  const int k1w = (((mm & 1) ? (xx ? 32 - xx : 8) : xx) + 16 * (mm >> 1)) & 31;
  const f32x4 z4 = {0, 0, 0, 0};
  const rsrc_t rws = mkrs(p.ws, 16u << 20), rtf = mkrs(TF, TF_N * 1024);
  const u32 tq = (TF_Q0 + 6 * q) * 1024;
  if (P) __builtin_amdgcn_s_setprio(3);

  if (PH == 0 || PH == 1) {
    const rsrc_t rx = mkrs(p.x, S_ * D_ * 2);
    if (lane < 56) {
#pragma unroll
      for (int r = 0; r < 8; ++r) dma16<0>(rx, lb + L_XT + (8 * w + r) * XRS, l16, (64 * (8 * w + r) + s2) * (D_ * 2));
    }
  }
  u32x4 fc[2], fs[2];
  fc[0] = tf(rtf, TF_FC0, l16); fs[0] = tf(rtf, TF_FS0, l16); fc[1] = tf(rtf, TF_FC1, l16); fs[1] = tf(rtf, TF_FS1, l16);
  u32x4 gwv = {0, 0, 0, 0};
  if ((PH == 0 || PH == 3) && lane < 56) gwv = *(const GAS u32x4*)((const char*)p.gw + l16);

  if (PH == 0 || PH == 1) {
    const u32x4 Ba1 = tfs(rtf, 0, l16, tq), Ba2 = tfs(rtf, 1, l16, tq);
    const f32x4 twc0 = bc<f32x4>(tfs(rtf, 2, l16, tq)), twc1 = bc<f32x4>(tfs(rtf, 3, l16, tq));
    const f32x4 tws0 = bc<f32x4>(tfs(rtf, 0, l16, tq + 4096)), tws1 = bc<f32x4>(tfs(rtf, 1, l16, tq + 4096));
    u32x4 C1, S1, nS1;
    {
      const float k1 = (float)(16 * (w >> 1) + i16) * (1.0f / 2048.0f), P0 = k1 * (float)(64 * g + s2), Pl = k1 * 256.f;
      float c[8], s[8];
#pragma unroll
      for (int j = 0; j < 8; ++j) { const float ph = __builtin_fmaf(Pl, (float)j, P0); c[j] = cosr(ph); s[j] = sinr(ph); }
      C1 = pk8(c); S1 = pk8(s); nS1 = xr(S1, 0x80008000u);
      pin(C1); pin(S1); pin(nS1);
    }
    int d0a = 8 * (g >> 1) + (i16 >> 2), d0b = d0a + 4;
    d0b = d0b < 14 ? d0b : 0;
    u32 xa = lb + L_XT + (8 * w + (g & 1)) * XRS + 8 * (i16 & 3);
    u32 xb = xa + d0b * 64; xa += d0a * 64;
    u32 xw = lb + L_XCH + w * 256 + lane * 4;
    u32 xr_ = lb + L_XCH + (w & 1) * 4096 + lane * 4;
    u32 toff = (u32)((16 * (w >> 1) + i16) * T_K1 + (4 * (w & 1) + g) * 32);
    u32 imm = (i16 & 1) ? 0x80008000u : 0u;
    pin(xa); pin(xb); pin(xw); pin(xr_); pin(toff); pin(imm);
    STAMP(1);
    wait_vm0();
    STAMP(2);
    f32x4 D2[2][2];
#pragma unroll
    for (int sg = 0; sg < 2; ++sg) {
      f32x4 H[2];
#pragma unroll
      for (int td = 0; td < 2; ++td) {
        const int o = td * 32 + sg * 4 * XRS;
        u32x4 A0 = cat(trread(xa + o), trread(xb + o));
        u32x4 A1 = cat(trread(xa + o + 2 * XRS), trread(xb + o + 2 * XRS));
        H[td] = mfma32(A1, Ba2, mfma32(A0, Ba1, z4));
      }
      float h[8];
#pragma unroll
      for (int j = 0; j < 4; ++j) {
        h[j] = __builtin_fmaf(fx1(H[0][j]), tws0[j], H[0][j] * twc0[j]);
        h[4 + j] = __builtin_fmaf(fx1(H[1][j]), tws1[j], H[1][j] * twc1[j]);
      }
      u32x4 A = pk8(h), A2 = sx1(A, imm);
#pragma unroll
      for (int tk = 0; tk < 2; ++tk) D2[sg][tk] = mfma32(A2, fs[tk], mfma32(A, fc[tk], z4));
    }
#pragma unroll
    for (int tk = 0; tk < 2; ++tk)
#pragma unroll
      for (int r = 0; r < 4; ++r) lds_st<u32>(xw + (tk * 4 + r) * 1024, pk(D2[0][tk][r], D2[1][tk][r]));
    bar();
    STAMP(3);
    {
      u32 pc[4][2];
#pragma unroll
      for (int kk = 0; kk < 2; ++kk) {
        const u32 br = xr_ + kk * 2048;
        u32x4 Xr = {lds_ld<u32>(br), lds_ld<u32>(br + 256), lds_ld<u32>(br + 512), lds_ld<u32>(br + 768)};
        u32x4 Xi = {lds_ld<u32>(br + 1024), lds_ld<u32>(br + 1280), lds_ld<u32>(br + 1536), lds_ld<u32>(br + 1792)};
        f32x4 Tr = mfma32(Xi, S1, mfma32(Xr, C1, z4));
        f32x4 Ti = mfma32(Xr, nS1, mfma32(Xi, C1, z4));
        pc[kk][0] = pk(Tr[0], Tr[1]); pc[kk][1] = pk(Tr[2], Tr[3]);
        pc[2 + kk][0] = pk(Ti[0], Ti[1]); pc[2 + kk][1] = pk(Ti[2], Ti[3]);
      }
      const u32 so = (u32)WS_T + s2 * 1024 + q * 256;
      st16<P>(rws, toff, so, u32x4{pc[0][0], pc[0][1], pc[1][0], pc[1][1]});
      st16<P>(rws, toff + 16, so, u32x4{pc[2][0], pc[2][1], pc[3][0], pc[3][1]});
    }
    STAMP(4);
    if (P) { drain_vm(); bar(); publish(ctl + CT_F1 + b, gen + 1u); __builtin_amdgcn_s_setprio(2); }
    STAMP(5);
  }
  const u32 epoch = __builtin_amdgcn_readfirstlane(gen) + 1u;

  u32 wra[16], wia[16];
  u32x4 co[2], so[2], cot[2], sot[2];
  if (PH == 0 || PH == 2) {
    const bool mir = kq >= 4;
    const int c = i16 & 7, kd = 2 * w + (c >> 2) + 14 * (4 * kq + (c & 3)), ks0 = k1w + 128 * g;
    const u32 v1 = mir ? (u32)((2048 - 1632 - ks0) * (DF * 2) + (448 - kd) * 2) : (u32)(ks0 * (DF * 2) + kd * 2);
    const u32 v0 = mir ? (u32)(((2048 - ks0) & 2047) * (DF * 2) + (448 - kd) * 2) : v1;
    const rsrc_t rr = mkrs(p.wr, S_ * DF * 2), ri_ = mkrs(p.wi, S_ * DF * 2);
#pragma unroll
    for (int j = 0; j < 16; ++j) {
      const int cj = 1024 * (j >> 3) + 512 * ((j >> 2) & 1) + 32 * (j & 3);
      const u32 so_ = j == 0 ? 0u : (mir ? (u32)((1632 - cj) * (DF * 2)) : (u32)(cj * (DF * 2)));
      wra[j] = __builtin_amdgcn_raw_buffer_load_b16(rr, j == 0 ? v0 : v1, so_, 0);
      wia[j] = __builtin_amdgcn_raw_buffer_load_b16(ri_, j == 0 ? v0 : v1, so_, 0);
    }
    co[0] = tf(rtf, TF_CO0, l16); so[0] = tf(rtf, TF_SO0, l16); co[1] = tf(rtf, TF_CO1, l16); so[1] = tf(rtf, TF_SO1, l16);
    cot[0] = tf(rtf, TF_COT0, l16); sot[0] = tf(rtf, TF_SOT0, l16); cot[1] = tf(rtf, TF_COT1, l16); sot[1] = tf(rtf, TF_SOT1, l16);
  }

  if (PH == 0 || PH == 2) {
    const int ri = i16 >> 3;
    u32 tsv;
    {
      const int row = 16 * w + (lane >> 3), par = row >> 5, j = row & 31, ch = (lane & 7) ^ (2 * ((j >> 1) & 3));
      tsv = (u32)((2 * j + par) * 1024 + (ch >> 1) * 256 + (ch & 1) * 16);
    }
    u32 ta;
    {
      const int j = 4 * g + (i16 >> 2), pc_ = i16 & 3;
      ta = lb + L_TI + j * 128 + (((2 * w + (pc_ >> 1)) ^ (2 * ((j >> 1) & 3))) * 16) + (pc_ & 1) * 8;
    }
    u32 tow = lb + L_TO + 2 * i16 * 144 + w * 32 + g * 8;
    u32 tor = lb + L_TO + (tid >> 3) * 144 + (tid & 7) * 16;
    u32 tpo = (u32)((tid >> 3) * TP_S2 + (tid & 7) * 16);
    u32 mf = ri ? 0x80008000u : 0u, mi = ri ? 0u : 0x80008000u;
    float wr_[16], wi_[16];
    {
      const u32 sg = ((kq >= 4) != (ri != 0)) ? 0u : 0x80000000u;
#pragma unroll
      for (int j = 0; j < 16; ++j) {
        wr_[j] = bc<float>(wra[j] << 16);
        wi_[j] = bc<float>((wia[j] << 16) + sg);
        pin(wr_[j]); pin(wi_[j]);
      }
    }
    pin(tsv); pin(ta); pin(tow); pin(tor); pin(tpo); pin(mf); pin(mi);
    pin(co[0]); pin(co[1]); pin(so[0]); pin(so[1]); pin(cot[0]); pin(cot[1]); pin(sot[0]); pin(sot[1]);
    STAMP(7);
    if (P) { if (w == 0) poll_flags(ctl + CT_F1 + (b & 7) * 256, 64, epoch); bar(); __builtin_amdgcn_s_setprio(3); }
    STAMP(8);
    {
      const u32 so_ = (u32)WS_T + k1w * T_K1 + kq * 32;
      dma16<AUX>(rws, lb + L_TI + w * 2048, tsv, so_);
      dma16<AUX>(rws, lb + L_TI + w * 2048 + 1024, tsv, so_ + 16 * 1024);
    }
    wait_vm0();
    bar();
    STAMP(9);
    f32x4 E[2], O[2];
    {
      u32x4 B0 = cat(trread(ta), trread(ta + 2048)), B1 = cat(trread(ta + 4096), trread(ta + 6144));
      u32x4 B0s = sr8(B0, mf), B1s = sr8(B1, mf);
#pragma unroll
      for (int t = 0; t < 2; ++t) {
        E[t] = mfma32(fs[t], B0s, mfma32(fc[t], B0, z4));
        O[t] = mfma32(so[t], B1s, mfma32(co[t], B1, z4));
      }
    }
    float Pp[8], Mm[8];
#pragma unroll
    for (int j = 0; j < 8; ++j) {
      const float e = E[j >> 2][j & 3], o = O[j >> 2][j & 3];
      const float ylo = e + o, yhi = e - o;
      const float vlo = __builtin_fmaf(fr8(ylo), wi_[j], ylo * wr_[j]);
      const float vhi = __builtin_fmaf(fr8(yhi), wi_[8 + j], yhi * wr_[8 + j]);
      Pp[j] = vlo + vhi; Mm[j] = vlo - vhi;
    }
    {
      u32x4 AP = pk8(Pp), AM = pk8(Mm);
      u32x4 APs = sr8(AP, mi), AMs = sr8(AM, mi);
#pragma unroll
      for (int t = 0; t < 2; ++t) {
        f32x4 ze = mfma32(APs, fs[t], mfma32(AP, fc[t], z4));
        f32x4 zo = mfma32(AMs, sot[t], mfma32(AM, cot[t], z4));
        lds_st<u32x2>(tow + t * 32 * 144, u32x2{pk(ze[0], ze[1]), pk(ze[2], ze[3])});
        lds_st<u32x2>(tow + t * 32 * 144 + 144, u32x2{pk(zo[0], zo[1]), pk(zo[2], zo[3])});
      }
    }
    bar();
    STAMP(10);
    {
      const u32 so_ = (u32)WS_TP + k1w * 1024 + kq * 128;
      st16<P>(rws, tpo, so_, lds_ld<u32x4>(tor));
      st16<P>(rws, tpo, so_ + 32 * TP_S2, lds_ld<u32x4>(tor + 32 * 144));
    }
    STAMP(11);
    if (P) { drain_vm(); bar(); publish(ctl + CT_F2 + b, epoch); __builtin_amdgcn_s_setprio(0); }
    STAMP(12);
  }

  if (PH == 0 || PH == 3) {
#pragma unroll
    for (int r = 0; r < 2; ++r) {
      float acc = 0.f;
      if (lane < 56) {
        u32x4 xv;
        if (PH == 0) xv = lds_ld<u32x4>(lb + L_XT + (8 * q + 2 * w + r) * XRS + l16);
        else xv = *(const GAS u32x4*)((const char*)(p.x + (size_t)(64 * (8 * q + 2 * w + r) + s2) * D_) + l16);
#pragma unroll
        for (int e = 0; e < 4; ++e) acc = dot2b(pk2(up2(xv[e]) * up2(gwv[e])), 0x3f803f80u, acc);
      }
      const float gg = bfr(__builtin_amdgcn_rcpf(1.0f + __expf(-bfr(wave_sum(acc)))));
      if (lane == 0) lds_st<f32x2>(lb + L_G + (2 * w + r) * 8, f32x2{gg, bfr(1.0f - gg)});
    }
  }

  if (PH == 0 || PH == 3) {
    const f32x4 tc = bc<f32x4>(tf(rtf, TF_TC, l16)), ts = bc<f32x4>(tf(rtf, TF_TS, l16));
    const u32x4 m4 = tf(rtf, TF_M, l16);
    u32x4 b1, b2;
    {
      const int ro = i16 >> 3;
      const float a = (float)(64 * (8 * q + (i16 & 7)) + s2) * (1.0f / 2048.0f), P0 = a * (float)(4 * g);
      float u[8], v[8];
#pragma unroll
      for (int jj = 0; jj < 8; ++jj) {
        const float ph = __builtin_fmaf(a, (float)(16 * (jj >> 2) + (jj & 3)), P0);
        u[jj] = sinr(ph + (ro ? 0.f : 0.25f));
        v[jj] = sinr(ph + (ro ? 0.25f : 0.5f));
      }
      b1 = pk8(u); b2 = pk8(v);
      pin(b1); pin(b2);
    }
    u32 tpa = lb + L_TP + (4 * g + (i16 >> 2)) * TPRS + (i16 & 3) * 128 + w * 32;
    u32 zw[2], zr;
#pragma unroll
    for (int kk = 0; kk < 2; ++kk) {
      const int row = 2 * (2 * w + kk) + (i16 >> 3);
      zw[kk] = lb + L_Z + ((i16 & 7) * 16 + row) * 64 + ((g ^ (row >> 2)) & 3) * 16;
      pin(zw[kk]);
    }
    zr = lb + L_Z + (2 * w * 16 + i16) * 64 + ((g ^ (i16 >> 2)) & 3) * 16;
    u32 boff = (u32)(tid * B_D);
    u32 biw = lb + L_BI + tid * 2, bir = lb + L_BI + 2 * w * BRS + (32 * i16 + 4 * g) * 2;
    u32 ooff = (u32)((64 * (8 * q + 2 * w) + s2) * (D_ * 2) + (32 * i16 + 4 * g) * 2);
    u32 mi = (i16 & 1) ? 0u : 0x80008000u;
    pin(tpa); pin(zr); pin(boff); pin(biw); pin(bir); pin(ooff); pin(mi);
    STAMP(13);
    if (P) { if (w == 0) poll_flags(ctl + CT_F2 + (b & 7) * 256, 64, epoch); bar(); __builtin_amdgcn_s_setprio(3); }
    STAMP(14);
#pragma unroll
    for (int r = 0; r < 8; ++r) dma16<AUX>(rws, lb + L_TP + (8 * w + r) * TPRS, l16, (u32)WS_TP + s2 * TP_S2 + (8 * w + r) * 1024);
    wait_vm0();
    bar();
    STAMP(15);
#pragma unroll
    for (int kk = 0; kk < 2; ++kk)
#pragma unroll
      for (int t = 0; t < 2; ++t) {
        const int o = t * 512 + kk * 8;
        u32x4 Tr = cat(trread(tpa + o), trread(tpa + o + 16 * TPRS));
        u32x4 Ti = cat(trread(tpa + o + 16), trread(tpa + o + 16 + 16 * TPRS));
        f32x4 z = mfma32(Ti, b2, mfma32(Tr, b1, z4));
        lds_st<u32x2>(zw[kk] + t * 8, u32x2{pk(z[0], z[1]), pk(z[2], z[3])});
      }
    bar();
    STAMP(16);
    f32x2 ga[2][2][2];
#pragma unroll
    for (int rr = 0; rr < 2; ++rr) {
      u32x4 A = lds_ld<u32x4>(zr + rr * 1024), A2 = sx1(A, mi);
      const float gg = lds_ld<float>(lb + L_G + (2 * w + rr) * 8);
#pragma unroll
      for (int td = 0; td < 2; ++td) {
        f32x4 G = mfma32(A2, fs[td], mfma32(A, fc[td], z4));
        const float c0 = tc[2 * td], s0 = ts[2 * td], c1 = tc[2 * td + 1], s1 = ts[2 * td + 1];
        const float g0r = G[0] * c0 - G[1] * s0, g0i = G[1] * c0 + G[0] * s0;
        const float g1r = G[2] * c1 - G[3] * s1, g1i = G[3] * c1 + G[2] * s1;
        f32x4 y = mfma16(u32x2{pk(g0r, g0i), pk(g1r, g1i)}, u32x2{m4.x, m4.y}, z4);
        ga[rr][td][0] = up2(pk2(up2(pk(y[0], y[1])) * gg));
        ga[rr][td][1] = up2(pk2(up2(pk(y[2], y[3])) * gg));
        pin(ga[rr][td][0]); pin(ga[rr][td][1]);
      }
    }
    STAMP(17);
    if (P) { if (w == 0) poll_flags(ctl + CT_FB + (b & 7) * 256, 56, epoch); bar(); }
    STAMP(18);
    {
      const u32 so_ = (u32)WS_B + (((s2 & 7) * 8 + (s2 >> 3)) * 32 + 8 * q) * 2;
      const u32x4 bv0 = ld16<P>(rws, boff, so_);
      u32x4 bv1 = {0, 0, 0, 0};
      if (tid < D_ - 256) bv1 = ld16<P>(rws, boff, so_ + 256 * B_D);
#pragma unroll
      for (int e = 0; e < 4; ++e) {
        lds_st<u16>(biw + (2 * e) * BRS, (u16)bv0[e]);
        lds_st<u16>(biw + (2 * e + 1) * BRS, (u16)(bv0[e] >> 16));
      }
      if (tid < D_ - 256) {
#pragma unroll
        for (int e = 0; e < 4; ++e) {
          lds_st<u16>(biw + 512 + (2 * e) * BRS, (u16)bv1[e]);
          lds_st<u16>(biw + 512 + (2 * e + 1) * BRS, (u16)(bv1[e] >> 16));
        }
      }
    }
    bar();
    const rsrc_t ro_ = mkrs(p.out, S_ * D_ * 2);
    u32x2 bq[2][2];
    float og[2];
#pragma unroll
    for (int rr = 0; rr < 2; ++rr) {
      og[rr] = lds_ld<float>(lb + L_G + (2 * w + rr) * 8 + 4);
#pragma unroll
      for (int td = 0; td < 2; ++td) bq[rr][td] = lds_ld<u32x2>(bir + rr * BRS + td * 32);
    }
#pragma unroll
    for (int rr = 0; rr < 2; ++rr)
#pragma unroll
      for (int td = 0; td < 2; ++td) {
        const f32x2 o01 = ga[rr][td][0] + up2(pk2(up2(bq[rr][td].x) * og[rr]));
        const f32x2 o23 = ga[rr][td][1] + up2(pk2(up2(bq[rr][td].y) * og[rr]));
        if (i16 < 14) __builtin_amdgcn_raw_buffer_store_b64(u32x2{pk2(o01), pk2(o23)}, ro_, ooff, rr * 64 * (D_ * 2) + td * 32, 0);
      }
    STAMP(19);
    if (P && b == 0 && tid == 0) __hip_atomic_store((GAS u32*)ctl + CT_GEN, epoch, RLX_AGENT);
  }
}

DI f32x2 cexp_rev(float x2, float ph) {
  const float e = __builtin_amdgcn_exp2f(x2);
  const float f = __builtin_amdgcn_fractf(ph);
  f32x2 cs = {cosr(f), sinr(f)};
  return cs * e;
}
struct KW { u32 a[8]; };
template <int ROW> DI void wrow(const KW& k, f32x2 v) { lds_st<u32>(k.a[ROW & 7] + ROW * 256, pk2(v)); }
template <int ROW> DI void wrow2(const KW& k, f32x2 lo, f32x2 hi) {
  const u32 a = pk2(lo), b = pk2(hi);
  asm volatile("ds_write2st64_b32 %0, %1, %2 offset0:%c3 offset1:%c4" :: "v"(k.a[ROW & 7]), "v"(a), "v"(b), "i"(ROW), "i"(ROW + 8) : "memory");
}
DI void kfrag(u32 fa, u32 fb, u32x4 (&A)[4]) {
  A[0] = lds_ld<u32x4>(fa); A[1] = lds_ld<u32x4>(fb); A[2] = lds_ld<u32x4>(fa + 128); A[3] = lds_ld<u32x4>(fb + 128);
}
DI f32x4 ktile(const u32x4 (&A)[4], const u32x4 (&V)[4]) {
  f32x4 acc = {0, 0, 0, 0};
#pragma unroll
  for (int ks = 0; ks < 4; ++ks) acc = mfma32(A[ks], V[ks], acc);
  return acc;
}
template <class F0, class F1>
DI void kgen(const u32 (&in)[6], float dt, const KW& k, u32 fa, u32 fb, u32 pb, F0&& hook0, F1&& hook1) {
  const float ar = dt * bc<float>(in[0] << 16), ai = dt * bc<float>(in[1] << 16);
  const f32x2 dB = {dt * bc<float>(in[2] << 16), dt * bc<float>(in[3] << 16)};
  const f32x2 Cc = {bc<float>(in[4] << 16), bc<float>(in[5] << 16)};
  const f32x2 CB = cmul(Cc, dB);
  const float x1 = ar * 1.44269504088896341f, p1 = ai * 0.15915494309189535f;
  const f32x2 E1 = cexp_rev(x1, p1), E8c = cexp_rev(8.f * x1, -8.f * p1), E128 = cexp_rev(128.f * x1, 128.f * p1);
  u32x4 V[4], A0[4], A1[4];
  {
    const f32x2 v2 = cmul(E8c, E8c), v3 = cmul(v2, E8c), v4 = cmul(v2, v2), v8 = cmul(v4, v4);
    const f32x2 v5 = cmul(v4, E8c), v6 = cmul(v4, v2), v7 = cmul(v4, v3);
    wrow2<0>(k, f32x2{1.f, 0.f}, v8); wrow2<1>(k, E8c, cmul(v8, E8c)); wrow2<2>(k, v2, cmul(v8, v2)); wrow2<3>(k, v3, cmul(v8, v3));
    wrow2<4>(k, v4, cmul(v8, v4)); wrow2<5>(k, v5, cmul(v8, v5)); wrow2<6>(k, v6, cmul(v8, v6)); wrow2<7>(k, v7, cmul(v8, v7));
    kfrag(fa, fb, V);
  }
  hook0();
  const f32x2 E2 = cmul(E1, E1), E3 = cmul(E2, E1), E4 = cmul(E2, E2), E5 = cmul(E4, E1), E6 = cmul(E4, E2), E7 = cmul(E4, E3);
  const f32x2 G1 = cmul(CB, E128);
  wrow2<0>(k, CB, G1); wrow2<1>(k, cmul(CB, E1), cmul(G1, E1)); wrow2<2>(k, cmul(CB, E2), cmul(G1, E2)); wrow2<3>(k, cmul(CB, E3), cmul(G1, E3));
  wrow2<4>(k, cmul(CB, E4), cmul(G1, E4)); wrow2<5>(k, cmul(CB, E5), cmul(G1, E5)); wrow2<6>(k, cmul(CB, E6), cmul(G1, E6)); wrow2<7>(k, cmul(CB, E7), cmul(G1, E7));
  kfrag(fa, fb, A0);
  const f32x2 G2 = cmul(G1, E128), G3 = cmul(G2, E128), G4 = cmul(G3, E128), G5 = cmul(G4, E128), G6 = cmul(G5, E128), G7 = cmul(G6, E128);
  wrow2<0>(k, G2, G4); wrow2<1>(k, cmul(G2, E2), cmul(G4, E4)); wrow2<2>(k, cmul(G2, E4), G5); wrow2<3>(k, cmul(G2, E6), cmul(G5, E4));
  wrow2<4>(k, G3, G6); wrow2<5>(k, cmul(G3, E2), cmul(G6, E4)); wrow2<6>(k, cmul(G3, E4), G7); wrow2<7>(k, cmul(G3, E6), cmul(G7, E4));
  kfrag(fa, fb, A1);
  hook1();
  __builtin_amdgcn_sched_barrier(0);
  const f32x4 a0 = ktile(A0, V);
  const f32x2 G8 = cmul(G7, E128), G9 = cmul(G8, E128), G10 = cmul(G9, E128), G11 = cmul(G10, E128);
  const f32x2 G12 = cmul(G11, E128), G13 = cmul(G12, E128), G14 = cmul(G13, E128), G15 = cmul(G14, E128);
  wrow2<0>(k, G8, cmul(G15, E128)); wrow<1>(k, G9); wrow<2>(k, G10); wrow<3>(k, G11);
  wrow<4>(k, G12); wrow<5>(k, G13); wrow<6>(k, G14); wrow<7>(k, G15);
  kfrag(fa, fb, A0);
  __builtin_amdgcn_sched_barrier(0);
  const f32x4 a1 = ktile(A1, V), a2 = ktile(A0, V);
  lds_st<f32x4>(pb, a0);
  lds_st<f32x4>(pb + 1024, a1);
  lds_st<f32x4>(pb + 2048, a2);
}

template <int PH>
DI void ssm_role(const Params& p, const u32 lb, const int j, u32* ctl, const u32 gen STAMP_ARG) {
  constexpr bool P = PH == 0;
  const int tid = threadIdx.x, lane = tid & 63, w = __builtin_amdgcn_readfirstlane(tid >> 6), i16 = lane & 15, g = lane >> 4;
  const int d = 2 * j, ch = w >> 2, n = tid & 255;
  const f32x4 z4 = {0, 0, 0, 0};
  const rsrc_t rws = mkrs(p.ws, 16u << 20);
  if (P) __builtin_amdgcn_s_setprio(3);
  const int sb_ = j / 7, db_ = j - sb_ * 7;
  u32x4 xt_v = {0, 0, 0, 0};
  if (PH == 0 || PH == 1)
    xt_v = __builtin_amdgcn_raw_buffer_load_b128(mkrs(p.x, S_ * D_ * 2), (u32)((tid >> 3) * (D_ * 2) + (tid & 7) * 16), (u32)(64 * sb_ * (D_ * 2) + db_ * 128), 0);
  u32 in[6];
  u32x4 kt0 = {0, 0, 0, 0}; u32x2 kt1 = {0, 0};
  u32 ldt = 0;
  if (PH != 1) {
    const u32 vo = n * 2, so_ = (d + ch) * (NS * 2), nb = D_ * NS * 2;
    const u16* arr[6] = {p.are, p.aim, p.bre, p.bim, p.cre, p.cim};
#pragma unroll
    for (int a = 0; a < 6; ++a) in[a] = __builtin_amdgcn_raw_buffer_load_b16(mkrs(arr[a], nb), vo, so_, 0);
    ldt = *(const GAS u16*)(p.logdt + d + ch);
    kt0 = *(const GAS u32x4*)(KTAB.v + 8 * n);
    kt1 = *(const GAS u32x2*)(KTAB.v + 8 * n + 8);
  }
  auto transpose = [&]() {
    lds_st<u32x4>(lb + L_XP + (tid >> 3) * 144 + (tid & 7) * 16, xt_v);
    bar();
    const int chi = g + 4 * (w >> 2);
    const u32 a = lb + L_XP + (8 * chi + (i16 >> 2)) * 144 + (16 * (w & 3) + 4 * (i16 & 3)) * 2;
    st16<P>(rws, (u32)((16 * (w & 3) + i16) * XT_D + chi * 16), (u32)WS_XT + 64 * db_ * XT_D + sb_ * 128, cat(trread(a), trread(a + 4 * 144)));
  };
  if (PH == 1) { transpose(); return; }
  if (P) {
    if (tid == 0) lds_st<u32>(lb + L_CNT, 0u);
    __builtin_amdgcn_sched_barrier(0);
    transpose();
    __builtin_amdgcn_s_setprio(1);
    __builtin_amdgcn_sched_barrier(0);
  }
  KW kw;
  const u32 sb = lb + w * KSCR;
#pragma unroll
  for (int h = 0; h < 8; ++h) { kw.a[h] = sb + (((lane >> 2) ^ h) << 4) + (lane & 3) * 4; pin(kw.a[h]); }
  u32 fa = sb + i16 * 256 + ((g ^ (i16 & 3)) << 4) + ((i16 >> 2) & 1) * 64, fb = fa ^ 64;
  pin(fa); pin(fb);
  STAMP(1);
  kgen(in, bfr(expf(bc<float>(ldt << 16))), kw, fa, fb, sb + lane * 16,
       [&]() {
         if (P) {
           drain_vm();
           if (lane == 0 && __hip_atomic_fetch_add((LAS u32*)(size_t)(lb + L_CNT), 1u, __ATOMIC_RELAXED, __HIP_MEMORY_SCOPE_WORKGROUP) == 7u)
             __hip_atomic_store((GAS u32*)ctl + CT_FX + db_ * 64 + sb_, gen + 1u, RLX_AGENT);
         }
       },
       []() {});
  const u32 epoch = __builtin_amdgcn_readfirstlane(gen) + 1u;
  STAMP(2);
  if (P) poll_flags(ctl + CT_FX + (d >> 6) * 64, 8, epoch);
  const u32x4 uv_ = ld16<P>(rws, n * 16, (u32)WS_XT + (d + ch) * XT_D);
  bar();
  STAMP(3);
  if ((w & 3) < 3) {
    const u32 pb = lb + ch * 4 * KSCR + (w & 3) * 1024 + lane * 16;
    const f32x4 s = (lds_ld<f32x4>(pb) + lds_ld<f32x4>(pb + KSCR)) + (lds_ld<f32x4>(pb + 2 * KSCR) + lds_ld<f32x4>(pb + 3 * KSCR));
    const u32 tb = lb + L_KT + ch * 1600 + ((w & 3) * 256 + 64 * g + i16) * 2, a = pk(s[0], s[1]), cc = pk(s[2], s[3]);
    lds_st<u16>(tb, (u16)a); lds_st<u16>(tb + 32, (u16)(a >> 16));
    lds_st<u16>(tb + 64, (u16)cc); lds_st<u16>(tb + 96, (u16)(cc >> 16));
  } else {
    if (lane == 0) lds_st<u16>(lb + L_KT + ch * 1600 + 1536, (u16)0);
    if (lane < 32) lds_st<u32x4>(lb + L_U + ch * 4608 + lane * 16, u32x4{0, 0, 0, 0});
  }
  bar();
  STAMP(4);
  {
    const u32 ktb = lb + L_KT + ch * 1600;
    u32 e[11];
#pragma unroll
    for (int k = 0; k < 4; ++k) { e[2 * k] = lds_ld<u16>(ktb + (kt0[k] & 0xffffu)); e[2 * k + 1] = lds_ld<u16>(ktb + (kt0[k] >> 16)); }
    e[8] = lds_ld<u16>(ktb + (kt1[0] & 0xffffu)); e[9] = lds_ld<u16>(ktb + (kt1[0] >> 16)); e[10] = lds_ld<u16>(ktb + (kt1[1] & 0xffffu));
    const u32 k00 = lds_ld<u16>(ktb);
    u32 dw[5], al[5];
#pragma unroll
    for (int k = 0; k < 5; ++k) { dw[k] = e[2 * k] | (e[2 * k + 1] << 16); al[k] = e[2 * k + 1] | (e[2 * k + 2] << 16); }
    const u32 kb = lb + L_KC + ch * 4 * KCP + n * 16;
    lds_st<u32x4>(kb, u32x4{dw[0], dw[1], dw[2], dw[3]});
    lds_st<u32x4>(kb + KCP, u32x4{al[0], al[1], al[2], al[3]});
    lds_st<u32x4>(kb + 2 * KCP, u32x4{dw[1], dw[2], dw[3], dw[4]});
    lds_st<u32x4>(kb + 3 * KCP, u32x4{al[1], al[2], al[3], al[4]});
    if (n < 16) {
      u32x4 z = {0, 0, 0, 0};
      if (n == 0) z.x = k00;
      lds_st<u32x4>(lb + L_KC + (ch * 4 + (n >> 2)) * KCP + 4096 + (n & 3) * 16, z);
    }
    lds_st<u32x4>(lb + L_U + ch * 4608 + 512 + n * 16, uv_);
  }
  bar();
  STAMP(7);
  f32x4 acc[8];
  const int cc = (w >> 1) & 1, hf = w & 1;
  if (w < 4) {
#pragma unroll
    for (int c = 0; c < 8; ++c) acc[c] = z4;
    const int bsh = (-i16) & 3;
    u32 abase = lb + L_KC + (cc * 4 + bsh) * KCP + (2032 + 8 * g - i16 - bsh) * 2 - hf * 4 * 64 - 3 * 64 - 7 * 512;
    u32 bbase = lb + L_U + cc * 4608 + (256 - 16 + 16 * i16 + 8 * g) * 2 - hf * 4 * 64 - 3 * 64;
    u32 abase8 = abase + 8;
    pin(abase); pin(abase8); pin(bbase);
    u32x4 A[8], B[8];
#pragma unroll
    for (int qq = 0; qq < 8; ++qq) {
      B[7 - qq] = lds_ld<u32x4>(bbase + (7 - qq) * 512 + 3 * 64);
      A[qq] = cat(lds_ld<u32x2>(abase + (7 - qq) * 512 + 3 * 64), lds_ld<u32x2>(abase8 + (7 - qq) * 512 + 3 * 64));
      __builtin_amdgcn_sched_barrier(0);
    }
#pragma unroll
    for (int rr = 0; rr < 4; ++rr) {
#pragma unroll
      for (int qq = 0; qq < 8; ++qq) {
#pragma unroll
        for (int m = 7 - qq; m >= 0; --m) acc[qq + m] = mfma32(A[qq], B[m], acc[qq + m]);
        if (rr < 3) {
          A[qq] = cat(lds_ld<u32x2>(abase + (7 - qq) * 512 + (2 - rr) * 64), lds_ld<u32x2>(abase8 + (7 - qq) * 512 + (2 - rr) * 64));
          B[7 - qq] = lds_ld<u32x4>(bbase + (7 - qq) * 512 + (2 - rr) * 64);
        }
        __builtin_amdgcn_sched_barrier(0);
      }
    }
    if (hf) {
#pragma unroll
      for (int c = 0; c < 4; ++c) lds_st<f32x4>(lb + L_RED + (w * 4 + c) * 1024 + lane * 16, acc[c]);
#pragma unroll
      for (int c = 0; c < 4; ++c) acc[c] = acc[4 + c];
    } else {
#pragma unroll
      for (int c = 0; c < 4; ++c) lds_st<f32x4>(lb + L_RED + (w * 4 + c) * 1024 + lane * 16, acc[4 + c]);
    }
  }
  bar();
  STAMP(8);
  if (w < 4) {
    const float dk = b2f(p.dskip[d + cc]);
    const u32 tb = (1024 * hf + 16 * i16 + 4 * g) * 2;
    f32x4 o[4];
    u32x2 uv[4];
#pragma unroll
    for (int c = 0; c < 4; ++c) {
      o[c] = lds_ld<f32x4>(lb + L_RED + ((w ^ 1) * 4 + c) * 1024 + lane * 16);
      uv[c] = lds_ld<u32x2>(lb + L_U + cc * 4608 + 512 + tb + c * 512);
    }
#pragma unroll
    for (int c = 0; c < 4; ++c) {
      const f32x4 y = acc[c] + o[c];
      const f32x2 b01 = up2(pk(y[0], y[1])) + up2(pk2(up2(uv[c].x) * dk));
      const f32x2 b23 = up2(pk(y[2], y[3])) + up2(pk2(up2(uv[c].y) * dk));
      lds_st<u32x2>(lb + L_BO + cc * 4096 + tb + c * 512, u32x2{pk2(b01), pk2(b23)});
    }
  }
  bar();
  STAMP(9);
  {
    const int st = w & 3, s2 = 16 * st + i16;
    const u32 a = lb + L_BO + ch * 4096 + (8 * g + (i16 >> 2)) * 128 + (16 * st + 4 * (i16 & 3)) * 2;
    st16<P>(rws, (u32)((((s2 & 7) * 8 + (s2 >> 3)) * 32 + 8 * g) * 2), (u32)WS_B + (d + ch) * B_D, cat(trread(a), trread(a + 4 * 128)));
  }
  STAMP(10);
  if (P) { drain_vm(); bar(); publish(ctl + CT_FB + j, epoch); }
  STAMP(11);
}

template <int PH>
__global__ __launch_bounds__(NT, 4) void gfs4_kernel(Params p) {
  extern __shared__ __attribute__((aligned(16))) char smem[];
  const u32 lb = (u32)(size_t)(LAS char*)smem;
  const int b = blockIdx.x;
  u32* ctl = (u32*)(p.ws + WS_CTL);
  STAMP_DECL;
  if (PH == 0 || PH == 1) STAMP(0); else if (PH == 2) STAMP(20); else STAMP(21);
  u32 gen = 0;
  if (PH == 0) gen = __hip_atomic_load((GAS u32*)ctl + CT_GEN, RLX_AGENT);
  if (b < N_FFT) { if (threadIdx.x >= 256) return; fft_role<PH>(p, lb, b, ctl, gen STAMP_PASS); }
  else ssm_role<PH>(p, lb, b - N_FFT, ctl, gen STAMP_PASS);
  STAMP_FLUSH();
}

extern "C" void kernel_launch(void* const* d_in, const int* in_sizes, int n_in,
                              void* d_out, int out_size, void* d_ws, size_t ws_size,
                              hipStream_t stream) {
  (void)in_sizes; (void)n_in; (void)out_size; (void)ws_size;
  Params p;
  p.x = (const u16*)d_in[0]; p.wr = (const u16*)d_in[1]; p.wi = (const u16*)d_in[2];
  p.are = (const u16*)d_in[4]; p.aim = (const u16*)d_in[5]; p.bre = (const u16*)d_in[6]; p.bim = (const u16*)d_in[7];
  p.cre = (const u16*)d_in[8]; p.cim = (const u16*)d_in[9]; p.logdt = (const u16*)d_in[10]; p.dskip = (const u16*)d_in[11];
  p.gw = (const u16*)d_in[12];
  p.out = (u16*)d_out; p.ws = (char*)d_ws;
  (void)hipFuncSetAttribute((const void*)gfs4_kernel<0>, hipFuncAttributeMaxDynamicSharedMemorySize, LDS_BYTES);
  gfs4_kernel<0><<<N_WG, NT, LDS_BYTES, stream>>>(p);
}
```

```cpp
#include <hip/hip_runtime.h>
#include <hip/hip_bf16.h>
#include <stdint.h>

typedef unsigned int u32;
typedef unsigned short u16;
typedef unsigned long long u64;
typedef float f32x4 __attribute__((ext_vector_type(4)));
typedef float f32x2 __attribute__((ext_vector_type(2)));
typedef u32 u32x4 __attribute__((ext_vector_type(4)));
typedef u32 u32x2 __attribute__((ext_vector_type(2)));
typedef __bf16 bf16x8 __attribute__((ext_vector_type(8)));
typedef __bf16 bf16x2 __attribute__((ext_vector_type(2)));
typedef short s16x4 __attribute__((ext_vector_type(4)));
#define LAS __attribute__((address_space(3)))
#define GAS __attribute__((address_space(1)))
#define DI __device__ __forceinline__

#define CNOP 1
#define NLAUNCH 1

constexpr int S_ = 2048, D_ = 448, DF = 225, NS = 256;
constexpr int N_FFT = 256, N_SSM = 224, N_WG = N_FFT + N_SSM, NT = 512;

constexpr u32 T_K1 = 65536 + 256;
constexpr u32 TP_S2 = 32768 + 128;
constexpr u32 XT_D = 4096 + 128;
constexpr u32 B_D = 4096 + 128;
constexpr size_t WS_CTL = 0;
constexpr size_t WS_T   = 262144;
constexpr size_t WS_TP  = WS_T + (3u << 20);
constexpr size_t WS_XT  = WS_TP + (3u << 20);
constexpr size_t WS_B   = WS_XT + (2u << 20);
constexpr int FP_ = 1088;
constexpr int CT_GEN = 0;
constexpr int CT_F1 = FP_;
constexpr int CT_F2 = CT_F1 + 8 * FP_;
constexpr int CT_FB = CT_F2 + 8 * FP_;
constexpr int CT_FX = CT_FB + 8 * FP_;

constexpr int XRS = 928;
constexpr int L_XT = 0;
constexpr int L_XCH = 29696;
constexpr int TPRS = 1040;
constexpr int L_TP = 0;
constexpr int L_TO = 29696;
constexpr int L_TI = 38912;
constexpr int L_BI = 38912;
constexpr int BRS = 912;
constexpr int L_Z = 47104;
constexpr int L_C1 = 38912;
constexpr int L_C2 = 65536;
constexpr int L_W = 47104;
constexpr int L_G = 70208;
constexpr int KCP = 4160;
constexpr int L_KC = 0;
constexpr int KSCR = 4096;
constexpr int L_RED = 33280;
constexpr int L_BO = 49664;
constexpr int L_U = 57856;
constexpr int L_XP = 57856;
constexpr int L_KT = 67072;
constexpr int L_CNT = 70272;
constexpr int LDS_BYTES = 70288;

struct Params {
  const u16* x; const u16* wr; const u16* wi;
  const u16* are; const u16* aim; const u16* bre; const u16* bim; const u16* cre; const u16* cim;
  const u16* logdt; const u16* dskip; const u16* gw;
  u16* out; char* ws;
};

constexpr double cx_sin_oct(double x) { double x2 = x * x, t = x, s = x; for (int i = 1; i < 9; ++i) { t *= -x2 / ((2 * i) * (2 * i + 1)); s += t; } return s; }
constexpr double cx_cos_oct(double x) { double x2 = x * x, t = 1, s = 1; for (int i = 1; i < 9; ++i) { t *= -x2 / ((2 * i - 1) * (2 * i)); s += t; } return s; }
constexpr double cx_sin(long n, long d) {
  n %= d; if (n < 0) n += d;
  const long o = (8 * n) / d;
  const double r = 6.283185307179586476925 * (double)(8 * n - o * d) / (8.0 * (double)d), q = 0.78539816339744830962;
  switch (o) {
    case 0: return cx_sin_oct(r);       case 1: return cx_cos_oct(q - r);
    case 2: return cx_cos_oct(r);       case 3: return cx_sin_oct(q - r);
    case 4: return -cx_sin_oct(r);      case 5: return -cx_cos_oct(q - r);
    case 6: return -cx_cos_oct(r);      default: return -cx_sin_oct(q - r);
  }
}
constexpr double cx_cos(long n, long d) { return cx_sin(4 * n + d, 4 * d); }
constexpr u32 cx_f32(double v) { return __builtin_bit_cast(u32, (float)v); }
constexpr u32 cx_bf(double v) { const u32 u = cx_f32(v); return (u + 0x7fffu + ((u >> 16) & 1u)) >> 16; }
constexpr int cx_pi(int g, int jj) { return 16 * (jj >> 2) + 4 * g + (jj & 3); }
struct Frag { u32 v[64][4]; };
enum { TF_FC0, TF_FS0, TF_CO0, TF_SO0, TF_COT0, TF_SOT0, TF_TC, TF_TS, TF_M, TF_Q0, TF_N = TF_Q0 + 24 };
constexpr Frag make_frag(int item) {
  Frag f{};
  for (int lane = 0; lane < 64; ++lane) {
    const int i16 = lane & 15, g = lane >> 4;
    if (item < TF_TC) {
      const int sn = item & 1;
      for (int jj = 0; jj < 8; ++jj) {
        const int m = cx_pi(g, jj), a = i16;
        long n = 0, d = 32;
        if (item < TF_CO0) { n = a * m; d = 32; }
        else if (item < TF_COT0) { n = a * (2 * m + 1); d = 64; }
        else { n = m * (2 * a + 1); d = 64; }
        f.v[lane][jj >> 1] |= cx_bf(sn ? cx_sin(n, d) : cx_cos(n, d)) << (16 * (jj & 1));
      }
    } else if (item == TF_TC || item == TF_TS) {
      for (int j = 0; j < 4; ++j) {
        const long n = (2 * g + (j & 1)) * (16 * (j >> 1) + i16);
        f.v[lane][j] = cx_f32(item == TF_TC ? cx_cos(n, 448) : cx_sin(n, 448));
      }
    } else if (item == TF_M) {
      for (int j = 0; j < 4; ++j) {
        const int k0 = 2 * g + (j >> 1);
        const double cf = ((k0 == 0 || k0 == 7) ? 1.0 : 2.0) / (2048.0 * 448.0);
        const double v = i16 < 14 ? cf * ((j & 1) ? -cx_sin(k0 * i16, 14) : cx_cos(k0 * i16, 14)) : 0.0;
        f.v[lane][j >> 1] |= cx_bf(v) << (16 * (j & 1));
      }
    } else {
      const int q = (item - TF_Q0) / 6, sub = (item - TF_Q0) % 6, k0 = 2 * q + ((i16 >> 1) & 1), ri = i16 & 1;
      if (sub < 2) {
        if ((i16 >> 2) == 2 * sub + (g & 1))
          for (int j = 0; j < 8; ++j) {
            const int d0 = 8 * (g >> 1) + j;
            const double v = d0 < 14 ? (ri ? -cx_sin(d0 * k0, 14) : cx_cos(d0 * k0, 14)) : 0.0;
            f.v[lane][j >> 1] |= cx_bf(v) << (16 * (j & 1));
          }
      } else {
        for (int j = 0; j < 4; ++j) {
          const long n = cx_pi(g, 4 * (sub & 1) + j) * k0;
          f.v[lane][j] = cx_f32(sub < 4 ? cx_cos(n, 448) : (ri ? -cx_sin(n, 448) : cx_sin(n, 448)));
        }
      }
    }
  }
  return f;
}
template <int I> struct FragC { static constexpr Frag f = make_frag(I); };
#define FR(i) FragC<(i)>::f
#define FR6(i) FR(i), FR(i + 1), FR(i + 2), FR(i + 3), FR(i + 4), FR(i + 5)
__device__ const __attribute__((aligned(1024))) Frag TF[TF_N] = {FR6(0), FR(6), FR(7), FR(8), FR6(9), FR6(15), FR6(21), FR6(27)};

struct KTab { u16 v[2080]; };
constexpr KTab make_ktab() {
  KTab t{};
  for (int i = 0; i < 2080; ++i) {
    const int l = 2048 - i;
    int idx = 768;
    if (l >= 0) {
      const int sh = l > 256 ? (l >= 1024 ? 3 : (l >= 512 ? 2 : 1)) : 0;
      const int v = sh ? (((l + (1 << (sh - 1)) - 1 + ((l >> sh) & 1)) >> sh) << sh) : l;
      if (v < 256) idx = ((v & 7) + 8 * (v >> 7)) * 16 + ((v >> 3) & 15);
      else if (v < 512) idx = 256 + ((((v - 256) >> 1) & 3) + 4 * ((v - 256) >> 7)) * 16 + (((v - 256) >> 3) & 15);
      else if (v < 1024) idx = 256 + (8 + (((v - 512) >> 2) & 1) + 2 * ((v - 512) >> 7)) * 16 + (((v - 512) >> 3) & 15);
      else if (v < 2048) idx = 512 + ((v - 1024) >> 7) * 16 + (((v - 1024) >> 3) & 15);
      else idx = 640;
    }
    t.v[i] = (u16)(2 * idx);
  }
  return t;
}
__device__ const __attribute__((aligned(16))) KTab KTAB = make_ktab();

template <class T, class F> DI T bc(F v) { return __builtin_bit_cast(T, v); }
DI f32x4 mfma32(u32x4 a, u32x4 b, f32x4 c) { return __builtin_amdgcn_mfma_f32_16x16x32_bf16(bc<bf16x8>(a), bc<bf16x8>(b), c, 0, 0, 0); }
DI f32x4 mfma16(u32x2 a, u32x2 b, f32x4 c) { return __builtin_amdgcn_mfma_f32_16x16x16bf16_1k(bc<s16x4>(a), bc<s16x4>(b), c, 0, 0, 0); }
DI u32 pk(float a, float b) { f32x2 v = {a, b}; return bc<u32>(__builtin_convertvector(v, bf16x2)); }
DI u32 pk2(f32x2 v) { return bc<u32>(__builtin_convertvector(v, bf16x2)); }
DI float bfr(float a) { return bc<float>(pk(0.f, a) & 0xffff0000u); }
DI float lo16(u32 v) { return bc<float>(v << 16); }
DI float hi16(u32 v) { return bc<float>(v & 0xffff0000u); }
DI f32x2 up2(u32 v) { return f32x2{lo16(v), hi16(v)}; }
DI float b2f(u16 v) { return bc<float>((u32)v << 16); }
DI u32x2 trread(u32 a) { return bc<u32x2>(__builtin_amdgcn_ds_read_tr16_b64_v4i16((LAS s16x4*)(size_t)a)); }
DI u32x4 cat(u32x2 a, u32x2 b) { u32x4 r = {a.x, a.y, b.x, b.y}; return r; }
DI float sinr(float x) { return __builtin_amdgcn_sinf(x); }
DI float cosr(float x) { return __builtin_amdgcn_cosf(x); }
DI float fx1(float v) { return __builtin_amdgcn_update_dpp(v, v, 0xB1, 0xf, 0xf, true); }
DI float fr8(float v) { return __builtin_amdgcn_update_dpp(v, v, 0x128, 0xf, 0xf, true); }
DI u32 ux1(u32 v) { return (u32)__builtin_amdgcn_update_dpp((int)v, (int)v, 0xB1, 0xf, 0xf, true); }
DI u32 ur8(u32 v) { return (u32)__builtin_amdgcn_update_dpp((int)v, (int)v, 0x128, 0xf, 0xf, true); }
DI u32x4 sx1(u32x4 v, u32 m) { u32x4 r = {ux1(v.x) ^ m, ux1(v.y) ^ m, ux1(v.z) ^ m, ux1(v.w) ^ m}; return r; }
DI u32x4 sr8(u32x4 v, u32 m) { u32x4 r = {ur8(v.x) ^ m, ur8(v.y) ^ m, ur8(v.z) ^ m, ur8(v.w) ^ m}; return r; }
DI u32x4 xr(u32x4 v, u32 m) { u32x4 r = {v.x ^ m, v.y ^ m, v.z ^ m, v.w ^ m}; return r; }
DI u32x4 pk8(const float (&v)[8]) { return u32x4{pk(v[0], v[1]), pk(v[2], v[3]), pk(v[4], v[5]), pk(v[6], v[7])}; }
template <class T> DI T lds_ld(u32 a) { return *(LAS T*)(size_t)a; }
template <class T> DI void lds_st(u32 a, T v) { *(LAS T*)(size_t)a = v; }
DI void wait_vm0() { __builtin_amdgcn_s_waitcnt(0x0F70); }
DI void drain_vm() { asm volatile("s_waitcnt vmcnt(0)" ::: "memory"); }
DI void bar() { __builtin_amdgcn_s_waitcnt(0xC07F); asm volatile("s_barrier" ::: "memory"); }
template <class T> DI void pin(T& v) { asm volatile("" : "+v"(v)); }
typedef __amdgpu_buffer_rsrc_t rsrc_t;
DI rsrc_t mkrs(const void* p, u32 bytes) { return __builtin_amdgcn_make_buffer_rsrc((void*)p, 0, bytes, 0x00020000); }
template <int AUX> DI void dma16(rsrc_t rs, u32 lds, u32 voff, u32 soff) { __builtin_amdgcn_raw_ptr_buffer_load_lds(rs, (LAS void*)(size_t)lds, 16, voff, soff, 0, AUX); }
DI f32x2 cmul(f32x2 x, f32x2 y) {
  f32x2 t;
  asm("v_pk_mul_f32 %0, %1, %2 op_sel:[0,0] op_sel_hi:[0,1]\n\tv_pk_fma_f32 %0, %1, %2, %0 op_sel:[1,1,0] op_sel_hi:[1,0,1] neg_lo:[1,0,0]" : "=&v"(t) : "v"(x), "v"(y));
  return t;
}
DI float dot2b(u32 a, u32 b, float c) { return __builtin_amdgcn_fdot2_f32_bf16(bc<bf16x2>(a), bc<bf16x2>(b), c, false); }
DI float wave_sum(float a) {
  a += __builtin_amdgcn_update_dpp(0.f, a, 0xB1, 0xf, 0xf, true);
  a += __builtin_amdgcn_update_dpp(0.f, a, 0x4E, 0xf, 0xf, true);
  a += __builtin_amdgcn_update_dpp(0.f, a, 0x141, 0xf, 0xf, true);
  a += __builtin_amdgcn_update_dpp(0.f, a, 0x140, 0xf, 0xf, true);
  a += __builtin_amdgcn_update_dpp(0.f, a, 0x142, 0xa, 0xf, false);
  a += __builtin_amdgcn_update_dpp(0.f, a, 0x143, 0xc, 0xf, false);
  return bc<float>(__builtin_amdgcn_readlane(bc<int>(a), 63));
}
DI int pi_k(int g, int jj) { return 16 * (jj >> 2) + 4 * g + (jj & 3); }

#define SLP 2
#define RLX_AGENT __ATOMIC_RELAXED, __HIP_MEMORY_SCOPE_AGENT
template <bool P> DI void st16(rsrc_t rs, u32 voff, u32 soff, u32x4 v) {
  __builtin_amdgcn_raw_buffer_store_b128(v, rs, voff, soff, P ? 16 : 0);
  asm volatile("s_nop 1" :: "v"(v) : "memory");
}
template <bool P> DI u32x4 ld16(rsrc_t rs, u32 voff, u32 soff) { return __builtin_amdgcn_raw_buffer_load_b128(rs, voff, soff, P ? 16 : 0); }
DI void publish(u32* first, u32 epoch) {
  if (threadIdx.x < 8) __hip_atomic_store((GAS u32*)first + threadIdx.x * FP_, epoch, RLX_AGENT);
}
DI void poll_flags(const u32* flags, int n4, u32 epoch) {
  const int lane = threadIdx.x & 63;
  const __amdgpu_buffer_rsrc_t rs = __builtin_amdgcn_make_buffer_rsrc((void*)flags, 0, n4 * 16, 0x00020000);
  for (int spin = 0; spin < (1 << 18); ++spin) {
    u32x4 f = __builtin_amdgcn_raw_buffer_load_b128(rs, lane * 16, 0, 16);
    bool ok = lane >= n4 || (f.x == epoch && f.y == epoch && f.z == epoch && f.w == epoch);
    if (__all(ok)) break;
    __builtin_amdgcn_s_sleep(SLP);
  }
}

#define STAMP(i) do {} while (0)
#define STAMP_DECL do {} while (0)
#define STAMP_FLUSH() do {} while (0)
#define STAMP_ARG
#define STAMP_PASS

DI u32x4 tf(rsrc_t rs, int item, u32 l16) { return __builtin_amdgcn_raw_buffer_load_b128(rs, l16 + (item & 3) * 1024, (item >> 2) * 4096, 0); }
DI u32x4 tfs(rsrc_t rs, int item, u32 l16, u32 so) { return __builtin_amdgcn_raw_buffer_load_b128(rs, l16 + item * 1024, so, 0); }

template <int PH>
DI void fft_role(const Params& p, const u32 lb, const int b, u32* ctl, u32& gen STAMP_ARG) {
  constexpr bool P = PH == 0;
  constexpr int AUX = P ? 16 : 0;
  const int tid = threadIdx.x, lane = tid & 63, w = __builtin_amdgcn_readfirstlane(tid >> 6), i16 = lane & 15, g = lane >> 4;
  const u32 l16 = lane * 16;
  const int s2 = b & 63, q = b >> 6;
  const int xx = b & 7, mm = (b >> 3) & 3, kq = b >> 5;
  const int k1w = (((mm & 1) ? (xx ? 32 - xx : 8) : xx) + 16 * (mm >> 1)) & 31;
  const f32x4 z4 = {0, 0, 0, 0};
  const rsrc_t rws = mkrs(p.ws, 16u << 20), rtf = mkrs(TF, TF_N * 1024);
  const u32 tq = (TF_Q0 + 6 * q) * 1024;
  if (P) __builtin_amdgcn_s_setprio(3);

  if (PH == 0 || PH == 1) {
    const rsrc_t rx = mkrs(p.x, S_ * D_ * 2);
    if (lane < 56) {
#pragma unroll
      for (int r = 0; r < 8; ++r) dma16<0>(rx, lb + L_XT + (8 * w + r) * XRS, l16, (64 * (8 * w + r) + s2) * (D_ * 2));
    }
  }
  if (P) gen = __hip_atomic_load((GAS u32*)ctl + CT_GEN, RLX_AGENT);
  const u32 tpo = (u32)((tid >> 3) * TP_S2 + (tid & 7) * 16), tps = (u32)WS_TP + k1w * 1024 + kq * 128;
  u32x4 fc[2], fs[2];
  if (PH == 0 || PH == 1) {
    dma16<0>(rtf, lb + L_C1 + w * 2048, l16, w == 0 ? 0u : tq + (2 * w - 2) * 1024);
    dma16<0>(rtf, lb + L_C1 + w * 2048 + 1024, l16, w == 0 ? 1024u : tq + (2 * w - 1) * 1024);
  } else {
    fc[0] = tf(rtf, TF_FC0, l16); fs[0] = tf(rtf, TF_FS0, l16);
    fc[1] = xr(fc[0], 0x80000000u); fs[1] = xr(fs[0], 0x80000000u);
  }

  u32x4 co[2], so[2], cot[2], sot[2];
  auto load_p2 = [&]() {
    const bool mir = kq >= 4;
    const int col0 = mir ? 448 - (56 * kq + 49) : 56 * kq;
    const rsrc_t rr = mkrs(p.wr, S_ * DF * 2), ri_ = mkrs(p.wi, S_ * DF * 2);
#pragma unroll
    for (int i = 0; i < 3; ++i) {
      const int gi = w + 4 * i;
      if (gi < 9) {
        const int gam = 64 * gi + lane, k2 = (gam * 7282) >> 16, chunk = gam - 9 * k2;
        const int ks = k1w + 32 * k2, row = mir ? ((2048 - ks) & 2047) : ks;
        const u32 vo = (u32)(((row * DF + col0) * 2) & ~15) + chunk * 16;
        dma16<0>(rr, lb + L_W + gi * 1024, vo, 0);
        dma16<0>(ri_, lb + L_W + 9216 + gi * 1024, vo, 0);
      }
    }
    dma16<0>(rtf, lb + L_C2 + w * 1024, l16, (TF_CO0 + w) * 1024);
  };

  if (PH == 0 || PH == 1) {
    u32x4 C1, S1, nS1;
    {
      const float k1 = (float)(16 * (w >> 1) + i16) * (1.0f / 2048.0f), P0 = k1 * (float)(64 * g + s2), Pl = k1 * 256.f;
      float c[8], s[8];
#pragma unroll
      for (int j = 0; j < 8; ++j) { const float ph = __builtin_fmaf(Pl, (float)j, P0); c[j] = cosr(ph); s[j] = sinr(ph); }
      C1 = pk8(c); S1 = pk8(s); nS1 = xr(S1, 0x80008000u);
      pin(C1); pin(S1); pin(nS1);
    }
    int d0a = 8 * (g >> 1) + (i16 >> 2), d0b = d0a + 4;
    d0b = d0b < 14 ? d0b : 0;
    u32 xa = lb + L_XT + (8 * w + (g & 1)) * XRS + 8 * (i16 & 3);
    u32 xb = xa + d0b * 64; xa += d0a * 64;
    u32 xw = lb + L_XCH + w * 256 + lane * 4;
    u32 xr_ = lb + L_XCH + (w & 1) * 4096 + lane * 4;
    u32 toff = (u32)((16 * (w >> 1) + i16) * T_K1 + 4 * (w & 1) * 32 + g * 16);
    u32 imm = (i16 & 1) ? 0x80008000u : 0u;
    pin(xa); pin(xb); pin(xw); pin(xr_); pin(toff); pin(imm);
    const u32 tso = (u32)WS_T + s2 * 1024 + q * 256;
    STAMP(1);
    wait_vm0();
    bar();
    const u32 cb = lb + L_C1 + l16;
    fc[0] = lds_ld<u32x4>(cb); fs[0] = lds_ld<u32x4>(cb + 1024);
    const u32x4 Ba1 = lds_ld<u32x4>(cb + 2048), Ba2 = lds_ld<u32x4>(cb + 3072);
    const f32x4 twc0 = lds_ld<f32x4>(cb + 4096), twc1 = lds_ld<f32x4>(cb + 5120), tws0 = lds_ld<f32x4>(cb + 6144), tws1 = lds_ld<f32x4>(cb + 7168);
    fc[1] = xr(fc[0], 0x80000000u); fs[1] = xr(fs[0], 0x80000000u);
    STAMP(2);
    f32x4 D2[2][2];
#pragma unroll
    for (int sg = 0; sg < 2; ++sg) {
      f32x4 H[2];
#pragma unroll
      for (int td = 0; td < 2; ++td) {
        const int o = td * 32 + sg * 4 * XRS;
        u32x4 A0 = cat(trread(xa + o), trread(xb + o));
        u32x4 A1 = cat(trread(xa + o + 2 * XRS), trread(xb + o + 2 * XRS));
        H[td] = mfma32(A1, Ba2, mfma32(A0, Ba1, z4));
      }
      float h[8];
#pragma unroll
      for (int j = 0; j < 4; ++j) {
        h[j] = __builtin_fmaf(fx1(H[0][j]), tws0[j], H[0][j] * twc0[j]);
        h[4 + j] = __builtin_fmaf(fx1(H[1][j]), tws1[j], H[1][j] * twc1[j]);
      }
      u32x4 A = pk8(h), A2 = sx1(A, imm);
#pragma unroll
      for (int tk = 0; tk < 2; ++tk) D2[sg][tk] = mfma32(A2, fs[tk], mfma32(A, fc[tk], z4));
    }
#pragma unroll
    for (int tk = 0; tk < 2; ++tk)
#pragma unroll
      for (int r = 0; r < 4; ++r) lds_st<u32>(xw + (tk * 4 + r) * 1024, pk(D2[0][tk][r], D2[1][tk][r]));
    bar();
    STAMP(3);
    {
      u32 pc[4][2];
#pragma unroll
      for (int kk = 0; kk < 2; ++kk) {
        const u32 br = xr_ + kk * 2048;
        u32x4 Xr = {lds_ld<u32>(br), lds_ld<u32>(br + 256), lds_ld<u32>(br + 512), lds_ld<u32>(br + 768)};
        u32x4 Xi = {lds_ld<u32>(br + 1024), lds_ld<u32>(br + 1280), lds_ld<u32>(br + 1536), lds_ld<u32>(br + 1792)};
        f32x4 Tr = mfma32(Xi, S1, mfma32(Xr, C1, z4));
        f32x4 Ti = mfma32(Xr, nS1, mfma32(Xi, C1, z4));
        pc[kk][0] = pk(Tr[0], Tr[1]); pc[kk][1] = pk(Tr[2], Tr[3]);
        pc[2 + kk][0] = pk(Ti[0], Ti[1]); pc[2 + kk][1] = pk(Ti[2], Ti[3]);
      }
      u32x4 lo = {pc[0][0], pc[0][1], pc[1][0], pc[1][1]}, hi = {pc[2][0], pc[2][1], pc[3][0], pc[3][1]};
#pragma unroll
      for (int e = 0; e < 4; ++e) {
        auto r1 = __builtin_amdgcn_permlane16_swap(lo[e], hi[e], false, false);
        auto r2 = __builtin_amdgcn_permlane32_swap(r1[0], r1[1], false, false);
        lo[e] = r2[0]; hi[e] = r2[1];
      }
      st16<P>(rws, toff, tso, lo);
      st16<P>(rws, toff + 64, tso, hi);
    }
    STAMP(4);
    if (P) {
      __builtin_amdgcn_sched_barrier(0);
      load_p2();
      __builtin_amdgcn_sched_barrier(0);
      asm volatile("s_waitcnt vmcnt(5)" ::: "memory");
      bar();
      publish(ctl + CT_F1 + b, gen + 1u);
      __builtin_amdgcn_s_setprio(2);
    }
    STAMP(5);
  }
  const u32 epoch = __builtin_amdgcn_readfirstlane(gen) + 1u;

  if (PH == 2) load_p2();
  if (PH == 0 || PH == 2) {
    const int ri = i16 >> 3;
    u32 tsv = (u32)((lane >> 1) * 1024 + w * 256 + (lane & 1) * 16);
    u32 tiw = lb + L_TI + w * 2048 + ((lane >> 1) & 1) * 1024 + (lane >> 2) * 32 + (lane & 1) * 16;
    u32 ta = lb + L_TI + w * 2048 + (4 * g + (i16 >> 2)) * 32 + (i16 & 3) * 8;
    u32 tow = lb + L_TO + 2 * i16 * 144 + w * 32 + g * 8;
    u32 tor = lb + L_TO + (tid >> 3) * 144 + (tid & 7) * 16;
    u32 mf = ri ? 0x80008000u : 0u, mi = ri ? 0u : 0x80008000u;
    float wr_[16], wi_[16];
    {
      const bool mir = kq >= 4;
      const int c = i16 & 7, kd = 2 * w + (c >> 2) + 14 * (4 * kq + (c & 3)), col0 = mir ? 448 - (56 * kq + 49) : 56 * kq;
      const int row0 = mir ? ((2048 - k1w) & 2047) : k1w;
      u32 wa = lb + L_W + 4 * g * 144 + (((row0 * DF + col0) * 2) & 15) + ((mir ? 448 - kd : kd) - col0) * 2;
      const u32 sg = (mir != (ri != 0)) ? 0u : 0x80000000u;
      pin(tsv); pin(tiw); pin(ta); pin(tow); pin(tor); pin(mf); pin(mi); pin(wa);
      STAMP(7);
      if (P && w == 0) poll_flags(ctl + CT_F1 + (b & 7) * FP_, 64, epoch);
      wait_vm0();
      bar();
      const u32 so_ = (u32)WS_T + k1w * T_K1 + kq * 32;
      const u32x4 t0 = ld16<P>(rws, tsv, so_), t1 = ld16<P>(rws, tsv, so_ + 32 * 1024);
      __builtin_amdgcn_sched_barrier(0);
      co[0] = lds_ld<u32x4>(lb + L_C2 + l16); so[0] = lds_ld<u32x4>(lb + L_C2 + 1024 + l16);
      cot[0] = lds_ld<u32x4>(lb + L_C2 + 2048 + l16); sot[0] = lds_ld<u32x4>(lb + L_C2 + 3072 + l16);
      co[1] = xr(so[0], 0x00008000u);
      so[1] = xr(co[0], 0x80000000u);
      cot[1] = xr(cot[0], 0x80000000u); sot[1] = xr(sot[0], 0x80000000u);
      pin(co[0]); pin(co[1]); pin(so[0]); pin(so[1]); pin(cot[0]); pin(cot[1]); pin(sot[0]); pin(sot[1]);
#pragma unroll
      for (int j = 0; j < 16; ++j) {
        const int o = (32 * (j >> 3) + 16 * ((j >> 2) & 1) + (j & 3)) * 144;
        wr_[j] = bc<float>((u32)lds_ld<u16>(wa + o) << 16);
        wi_[j] = bc<float>(((u32)lds_ld<u16>(wa + 9216 + o) << 16) + sg);
      }
#pragma unroll
      for (int j = 0; j < 16; ++j) { pin(wr_[j]); pin(wi_[j]); }
      __builtin_amdgcn_sched_barrier(0);
      if (P) __builtin_amdgcn_s_setprio(3);
      STAMP(8);
      lds_st<u32x4>(tiw, t0);
      lds_st<u32x4>(tiw + 512, t1);
    }
    STAMP(9);
    f32x4 E[2], O[2];
    {
      u32x4 B0 = cat(trread(ta), trread(ta + 512)), B1 = cat(trread(ta + 1024), trread(ta + 1536));
      u32x4 B0s = sr8(B0, mf), B1s = sr8(B1, mf);
#pragma unroll
      for (int t = 0; t < 2; ++t) {
        E[t] = mfma32(fs[t], B0s, mfma32(fc[t], B0, z4));
        O[t] = mfma32(so[t], B1s, mfma32(co[t], B1, z4));
      }
    }
    float Pp[8], Mm[8];
#pragma unroll
    for (int j = 0; j < 8; ++j) {
      const float e = E[j >> 2][j & 3], o = O[j >> 2][j & 3];
      const float ylo = e + o, yhi = e - o;
      const float vlo = __builtin_fmaf(fr8(ylo), wi_[j], ylo * wr_[j]);
      const float vhi = __builtin_fmaf(fr8(yhi), wi_[8 + j], yhi * wr_[8 + j]);
      Pp[j] = vlo + vhi; Mm[j] = vlo - vhi;
    }
    {
      u32x4 AP = pk8(Pp), AM = pk8(Mm);
      u32x4 APs = sr8(AP, mi), AMs = sr8(AM, mi);
#pragma unroll
      for (int t = 0; t < 2; ++t) {
        f32x4 ze = mfma32(APs, fs[t], mfma32(AP, fc[t], z4));
        f32x4 zo = mfma32(AMs, sot[t], mfma32(AM, cot[t], z4));
        lds_st<u32x2>(tow + t * 32 * 144, u32x2{pk(ze[0], ze[1]), pk(ze[2], ze[3])});
        lds_st<u32x2>(tow + t * 32 * 144 + 144, u32x2{pk(zo[0], zo[1]), pk(zo[2], zo[3])});
      }
    }
    bar();
    STAMP(10);
    {
      st16<P>(rws, tpo, tps, lds_ld<u32x4>(tor));
      st16<P>(rws, tpo, tps + 32 * TP_S2, lds_ld<u32x4>(tor + 32 * 144));
    }
    STAMP(11);
    if (P) { drain_vm(); bar(); publish(ctl + CT_F2 + b, epoch); __builtin_amdgcn_s_setprio(0); }
    STAMP(12);
  }

  if (PH == 0 || PH == 3) {
    u32x4 gwv = {0, 0, 0, 0};
    if (lane < 56) gwv = *(const GAS u32x4*)((const char*)p.gw + l16);
#pragma unroll
    for (int r = 0; r < 2; ++r) {
      float acc = 0.f;
      if (lane < 56) {
        u32x4 xv;
        if (PH == 0) xv = lds_ld<u32x4>(lb + L_XT + (8 * q + 2 * w + r) * XRS + l16);
        else xv = *(const GAS u32x4*)((const char*)(p.x + (size_t)(64 * (8 * q + 2 * w + r) + s2) * D_) + l16);
#pragma unroll
        for (int e = 0; e < 4; ++e) acc = dot2b(pk2(up2(xv[e]) * up2(gwv[e])), 0x3f803f80u, acc);
      }
      const float gg = bfr(__builtin_amdgcn_rcpf(1.0f + __expf(-bfr(wave_sum(acc)))));
      if (lane == 0) lds_st<f32x2>(lb + L_G + (2 * w + r) * 8, f32x2{gg, bfr(1.0f - gg)});
    }
  }

  if (PH == 0 || PH == 3) {
    const f32x4 tc = bc<f32x4>(tf(rtf, TF_TC, l16)), ts = bc<f32x4>(tf(rtf, TF_TS, l16));
    const u32x4 m4 = tf(rtf, TF_M, l16);
    u32x4 b1, b2;
    {
      const int ro = i16 >> 3;
      const float a = (float)(64 * (8 * q + (i16 & 7)) + s2) * (1.0f / 2048.0f), P0 = a * (float)(4 * g);
      float u[8], v[8];
#pragma unroll
      for (int jj = 0; jj < 8; ++jj) {
        const float ph = __builtin_fmaf(a, (float)(16 * (jj >> 2) + (jj & 3)), P0);
        u[jj] = sinr(ph + (ro ? 0.f : 0.25f));
        v[jj] = sinr(ph + (ro ? 0.25f : 0.5f));
      }
      b1 = pk8(u); b2 = pk8(v);
      pin(b1); pin(b2);
    }
    u32 tpa = lb + L_TP + (4 * g + (i16 >> 2)) * TPRS + (i16 & 3) * 128 + w * 32;
    u32 zw[2], zr;
#pragma unroll
    for (int kk = 0; kk < 2; ++kk) {
      const int row = 2 * (2 * w + kk) + (i16 >> 3);
      zw[kk] = lb + L_Z + ((i16 & 7) * 16 + row) * 64 + ((g ^ (row >> 2)) & 3) * 16;
      pin(zw[kk]);
    }
    zr = lb + L_Z + (2 * w * 16 + i16) * 64 + ((g ^ (i16 >> 2)) & 3) * 16;
    u32 boff = (u32)(tid * B_D);
    u32 biw = lb + L_BI + tid * 2, bir = lb + L_BI + 2 * w * BRS + (32 * i16 + 4 * g) * 2;
    u32 ooff = (u32)((64 * (8 * q + 2 * w) + s2) * (D_ * 2) + (32 * i16 + 4 * g) * 2);
    u32 mi = (i16 & 1) ? 0u : 0x80008000u;
    pin(tpa); pin(zr); pin(boff); pin(biw); pin(bir); pin(ooff); pin(mi);
    STAMP(13);
    if (P) { if (w == 0) poll_flags(ctl + CT_F2 + (b & 7) * FP_, 64, epoch); bar(); __builtin_amdgcn_s_setprio(3); }
    STAMP(14);
#pragma unroll
    for (int r = 0; r < 8; ++r) dma16<AUX>(rws, lb + L_TP + (8 * w + r) * TPRS, l16, (u32)WS_TP + s2 * TP_S2 + (8 * w + r) * 1024);
    wait_vm0();
    bar();
    STAMP(15);
#pragma unroll
    for (int kk = 0; kk < 2; ++kk)
#pragma unroll
      for (int t = 0; t < 2; ++t) {
        const int o = t * 512 + kk * 8;
        u32x4 Tr = cat(trread(tpa + o), trread(tpa + o + 16 * TPRS));
        u32x4 Ti = cat(trread(tpa + o + 16), trread(tpa + o + 16 + 16 * TPRS));
        f32x4 z = mfma32(Ti, b2, mfma32(Tr, b1, z4));
        lds_st<u32x2>(zw[kk] + t * 8, u32x2{pk(z[0], z[1]), pk(z[2], z[3])});
      }
    bar();
    STAMP(16);
    f32x2 ga[2][2][2];
#pragma unroll
    for (int rr = 0; rr < 2; ++rr) {
      u32x4 A = lds_ld<u32x4>(zr + rr * 1024), A2 = sx1(A, mi);
      const float gg = lds_ld<float>(lb + L_G + (2 * w + rr) * 8);
#pragma unroll
      for (int td = 0; td < 2; ++td) {
        f32x4 G = mfma32(A2, fs[td], mfma32(A, fc[td], z4));
        const float c0 = tc[2 * td], s0 = ts[2 * td], c1 = tc[2 * td + 1], s1 = ts[2 * td + 1];
        const float g0r = G[0] * c0 - G[1] * s0, g0i = G[1] * c0 + G[0] * s0;
        const float g1r = G[2] * c1 - G[3] * s1, g1i = G[3] * c1 + G[2] * s1;
        f32x4 y = mfma16(u32x2{pk(g0r, g0i), pk(g1r, g1i)}, u32x2{m4.x, m4.y}, z4);
        ga[rr][td][0] = up2(pk2(up2(pk(y[0], y[1])) * gg));
        ga[rr][td][1] = up2(pk2(up2(pk(y[2], y[3])) * gg));
        pin(ga[rr][td][0]); pin(ga[rr][td][1]);
      }
    }
    STAMP(17);
    if (P) { if (w == 0) poll_flags(ctl + CT_FB + (b & 7) * FP_, 56, epoch); bar(); }
    STAMP(18);
    {
      const u32 so_ = (u32)WS_B + (((s2 & 7) * 8 + (s2 >> 3)) * 32 + 8 * q) * 2;
      const u32x4 bv0 = ld16<P>(rws, boff, so_);
      u32x4 bv1 = {0, 0, 0, 0};
      if (tid < D_ - 256) bv1 = ld16<P>(rws, boff, so_ + 256 * B_D);
#pragma unroll
      for (int e = 0; e < 4; ++e) {
        lds_st<u16>(biw + (2 * e) * BRS, (u16)bv0[e]);
        lds_st<u16>(biw + (2 * e + 1) * BRS, (u16)(bv0[e] >> 16));
      }
      if (tid < D_ - 256) {
#pragma unroll
        for (int e = 0; e < 4; ++e) {
          lds_st<u16>(biw + 512 + (2 * e) * BRS, (u16)bv1[e]);
          lds_st<u16>(biw + 512 + (2 * e + 1) * BRS, (u16)(bv1[e] >> 16));
        }
      }
    }
    bar();
    const rsrc_t ro_ = mkrs(p.out, S_ * D_ * 2);
    u32x2 bq[2][2];
    float og[2];
#pragma unroll
    for (int rr = 0; rr < 2; ++rr) {
      og[rr] = lds_ld<float>(lb + L_G + (2 * w + rr) * 8 + 4);
#pragma unroll
      for (int td = 0; td < 2; ++td) bq[rr][td] = lds_ld<u32x2>(bir + rr * BRS + td * 32);
    }
#pragma unroll
    for (int rr = 0; rr < 2; ++rr)
#pragma unroll
      for (int td = 0; td < 2; ++td) {
        const f32x2 o01 = ga[rr][td][0] + up2(pk2(up2(bq[rr][td].x) * og[rr]));
        const f32x2 o23 = ga[rr][td][1] + up2(pk2(up2(bq[rr][td].y) * og[rr]));
        if (i16 < 14) __builtin_amdgcn_raw_buffer_store_b64(u32x2{pk2(o01), pk2(o23)}, ro_, ooff, rr * 64 * (D_ * 2) + td * 32, 0);
      }
    STAMP(19);
    if (P && b == 0 && tid == 0) __hip_atomic_store((GAS u32*)ctl + CT_GEN, epoch, RLX_AGENT);
  }
}

DI f32x2 cexp_rev(float x2, float ph) {
  const float e = __builtin_amdgcn_exp2f(x2);
  const float f = __builtin_amdgcn_fractf(ph);
  f32x2 cs = {cosr(f), sinr(f)};
  return cs * e;
}
struct KW { u32 a[8]; };
template <int ROW> DI void wrow(const KW& k, f32x2 v) { lds_st<u32>(k.a[ROW & 7] + ROW * 256, pk2(v)); }
template <int ROW> DI void wrow2(const KW& k, f32x2 lo, f32x2 hi) {
  const u32 a = pk2(lo), b = pk2(hi);
  asm volatile("ds_write2st64_b32 %0, %1, %2 offset0:%c3 offset1:%c4" :: "v"(k.a[ROW & 7]), "v"(a), "v"(b), "i"(ROW), "i"(ROW + 8) : "memory");
}
DI void kfrag(u32 fa, u32 fb, u32x4 (&A)[4]) {
  A[0] = lds_ld<u32x4>(fa); A[1] = lds_ld<u32x4>(fb); A[2] = lds_ld<u32x4>(fa + 128); A[3] = lds_ld<u32x4>(fb + 128);
}
DI f32x4 ktile(const u32x4 (&A)[4], const u32x4 (&V)[4]) {
  f32x4 acc = {0, 0, 0, 0};
#pragma unroll
  for (int ks = 0; ks < 4; ++ks) acc = mfma32(A[ks], V[ks], acc);
  return acc;
}
template <class F0, class F1>
DI void kgen(const u32 (&in)[6], float dt, const KW& k, u32 fa, u32 fb, u32 pb, F0&& hook0, F1&& hook1) {
  const float ar = dt * bc<float>(in[0] << 16), ai = dt * bc<float>(in[1] << 16);
  const f32x2 dB = {dt * bc<float>(in[2] << 16), dt * bc<float>(in[3] << 16)};
  const f32x2 Cc = {bc<float>(in[4] << 16), bc<float>(in[5] << 16)};
  const f32x2 CB = cmul(Cc, dB);
  const float x1 = ar * 1.44269504088896341f, p1 = ai * 0.15915494309189535f;
  const f32x2 E1 = cexp_rev(x1, p1), E8c = cexp_rev(8.f * x1, -8.f * p1), E128 = cexp_rev(128.f * x1, 128.f * p1);
  u32x4 V[4], A0[4], A1[4];
  {
    const f32x2 v2 = cmul(E8c, E8c), v3 = cmul(v2, E8c), v4 = cmul(v2, v2), v8 = cmul(v4, v4);
    const f32x2 v5 = cmul(v4, E8c), v6 = cmul(v4, v2), v7 = cmul(v4, v3);
    wrow2<0>(k, f32x2{1.f, 0.f}, v8); wrow2<1>(k, E8c, cmul(v8, E8c)); wrow2<2>(k, v2, cmul(v8, v2)); wrow2<3>(k, v3, cmul(v8, v3));
    wrow2<4>(k, v4, cmul(v8, v4)); wrow2<5>(k, v5, cmul(v8, v5)); wrow2<6>(k, v6, cmul(v8, v6)); wrow2<7>(k, v7, cmul(v8, v7));
    kfrag(fa, fb, V);
  }
  hook0();
  const f32x2 E2 = cmul(E1, E1), E3 = cmul(E2, E1), E4 = cmul(E2, E2), E5 = cmul(E4, E1), E6 = cmul(E4, E2), E7 = cmul(E4, E3);
  const f32x2 G1 = cmul(CB, E128);
  wrow2<0>(k, CB, G1); wrow2<1>(k, cmul(CB, E1), cmul(G1, E1)); wrow2<2>(k, cmul(CB, E2), cmul(G1, E2)); wrow2<3>(k, cmul(CB, E3), cmul(G1, E3));
  wrow2<4>(k, cmul(CB, E4), cmul(G1, E4)); wrow2<5>(k, cmul(CB, E5), cmul(G1, E5)); wrow2<6>(k, cmul(CB, E6), cmul(G1, E6)); wrow2<7>(k, cmul(CB, E7), cmul(G1, E7));
  kfrag(fa, fb, A0);
  const f32x2 G2 = cmul(G1, E128), G3 = cmul(G2, E128), G4 = cmul(G3, E128), G5 = cmul(G4, E128), G6 = cmul(G5, E128), G7 = cmul(G6, E128);
  wrow2<0>(k, G2, G4); wrow2<1>(k, cmul(G2, E2), cmul(G4, E4)); wrow2<2>(k, cmul(G2, E4), G5); wrow2<3>(k, cmul(G2, E6), cmul(G5, E4));
  wrow2<4>(k, G3, G6); wrow2<5>(k, cmul(G3, E2), cmul(G6, E4)); wrow2<6>(k, cmul(G3, E4), G7); wrow2<7>(k, cmul(G3, E6), cmul(G7, E4));
  kfrag(fa, fb, A1);
  hook1();
  __builtin_amdgcn_sched_barrier(0);
  const f32x4 a0 = ktile(A0, V);
  const f32x2 G8 = cmul(G7, E128), G9 = cmul(G8, E128), G10 = cmul(G9, E128), G11 = cmul(G10, E128);
  const f32x2 G12 = cmul(G11, E128), G13 = cmul(G12, E128), G14 = cmul(G13, E128), G15 = cmul(G14, E128);
  wrow2<0>(k, G8, cmul(G15, E128)); wrow<1>(k, G9); wrow<2>(k, G10); wrow<3>(k, G11);
  wrow<4>(k, G12); wrow<5>(k, G13); wrow<6>(k, G14); wrow<7>(k, G15);
  kfrag(fa, fb, A0);
  __builtin_amdgcn_sched_barrier(0);
  const f32x4 a1 = ktile(A1, V), a2 = ktile(A0, V);
  lds_st<f32x4>(pb, a0);
  lds_st<f32x4>(pb + 1024, a1);
  lds_st<f32x4>(pb + 2048, a2);
}

template <int PH>
DI void ssm_role(const Params& p, const u32 lb, const int j, u32* ctl, u32& gen STAMP_ARG) {
  constexpr bool P = PH == 0;
  const int tid = threadIdx.x, lane = tid & 63, w = __builtin_amdgcn_readfirstlane(tid >> 6), i16 = lane & 15, g = lane >> 4;
  const int d = 2 * j, ch = w >> 2, n = tid & 255;
  const f32x4 z4 = {0, 0, 0, 0};
  const rsrc_t rws = mkrs(p.ws, 16u << 20);
  if (P) __builtin_amdgcn_s_setprio(3);
  const int sb_ = j / 7, db_ = j - sb_ * 7;
  u32x4 xt_v = {0, 0, 0, 0};
  if (PH == 0 || PH == 1)
    xt_v = __builtin_amdgcn_raw_buffer_load_b128(mkrs(p.x, S_ * D_ * 2), (u32)((tid >> 3) * (D_ * 2) + (tid & 7) * 16), (u32)(64 * sb_ * (D_ * 2) + db_ * 128), 0);
  u32 in[6];
  u32x4 kt0 = {0, 0, 0, 0}; u32x2 kt1 = {0, 0};
  u32 ldt = 0;
  if (PH != 1) {
    const u32 vo = n * 2, so_ = (d + ch) * (NS * 2), nb = D_ * NS * 2;
    const u16* arr[6] = {p.are, p.aim, p.bre, p.bim, p.cre, p.cim};
#pragma unroll
    for (int a = 0; a < 6; ++a) in[a] = __builtin_amdgcn_raw_buffer_load_b16(mkrs(arr[a], nb), vo, so_, 0);
    ldt = *(const GAS u16*)(p.logdt + d + ch);
    kt0 = *(const GAS u32x4*)(KTAB.v + 8 * n);
    kt1 = *(const GAS u32x2*)(KTAB.v + 8 * n + 8);
  }
  if (P) {
    gen = __hip_atomic_load((GAS u32*)ctl + CT_GEN, RLX_AGENT);
  }
  auto transpose = [&]() {
    lds_st<u32x4>(lb + L_XP + (tid >> 3) * 144 + (tid & 7) * 16, xt_v);
    bar();
    const int chi = g + 4 * (w >> 2);
    const u32 a = lb + L_XP + (8 * chi + (i16 >> 2)) * 144 + (16 * (w & 3) + 4 * (i16 & 3)) * 2;
    st16<P>(rws, (u32)((16 * (w & 3) + i16) * XT_D + chi * 16), (u32)WS_XT + 64 * db_ * XT_D + sb_ * 128, cat(trread(a), trread(a + 4 * 144)));
  };
  if (PH == 1) { transpose(); return; }
  if (P) {
    if (tid == 0) lds_st<u32>(lb + L_CNT, 0u);
    __builtin_amdgcn_sched_barrier(0);
    transpose();
    __builtin_amdgcn_s_setprio(1);
    __builtin_amdgcn_sched_barrier(0);
  }
  KW kw;
  const u32 sb = lb + w * KSCR;
#pragma unroll
  for (int h = 0; h < 8; ++h) { kw.a[h] = sb + (((lane >> 2) ^ h) << 4) + (lane & 3) * 4; pin(kw.a[h]); }
  u32 fa = sb + i16 * 256 + ((g ^ (i16 & 3)) << 4) + ((i16 >> 2) & 1) * 64, fb = fa ^ 64;
  pin(fa); pin(fb);
  STAMP(1);
  kgen(in, bfr(expf(bc<float>(ldt << 16))), kw, fa, fb, sb + lane * 16,
       [&]() {
         if (P) {
           drain_vm();
           STAMP(5);
           if (lane == 0 && __hip_atomic_fetch_add((LAS u32*)(size_t)(lb + L_CNT), 1u, __ATOMIC_RELAXED, __HIP_MEMORY_SCOPE_WORKGROUP) == 7u)
             __hip_atomic_store((GAS u32*)ctl + CT_FX + db_ * FP_ + sb_, gen + 1u, RLX_AGENT);
         }
       },
       []() {});
  const u32 epoch = __builtin_amdgcn_readfirstlane(gen) + 1u;
  STAMP(2);
  if (P && w == 0) poll_flags(ctl + CT_FX + (d >> 6) * FP_, 8, epoch);
  STAMP(6);
  bar();
  const u32x4 uv_ = ld16<P>(rws, n * 16, (u32)WS_XT + (d + ch) * XT_D);
  STAMP(3);
  if ((w & 3) < 3) {
    const u32 pb = lb + ch * 4 * KSCR + (w & 3) * 1024 + lane * 16;
    const f32x4 s = (lds_ld<f32x4>(pb) + lds_ld<f32x4>(pb + KSCR)) + (lds_ld<f32x4>(pb + 2 * KSCR) + lds_ld<f32x4>(pb + 3 * KSCR));
    const u32 tb = lb + L_KT + ch * 1600 + ((w & 3) * 256 + 64 * g + i16) * 2, a = pk(s[0], s[1]), cc = pk(s[2], s[3]);
    lds_st<u16>(tb, (u16)a); lds_st<u16>(tb + 32, (u16)(a >> 16));
    lds_st<u16>(tb + 64, (u16)cc); lds_st<u16>(tb + 96, (u16)(cc >> 16));
  } else {
    if (lane == 0) lds_st<u16>(lb + L_KT + ch * 1600 + 1536, (u16)0);
    if (lane < 32) lds_st<u32x4>(lb + L_U + ch * 4608 + lane * 16, u32x4{0, 0, 0, 0});
  }
  bar();
  STAMP(4);
  {
    const u32 ktb = lb + L_KT + ch * 1600;
    u32 e[11];
#pragma unroll
    for (int k = 0; k < 4; ++k) { e[2 * k] = lds_ld<u16>(ktb + (kt0[k] & 0xffffu)); e[2 * k + 1] = lds_ld<u16>(ktb + (kt0[k] >> 16)); }
    e[8] = lds_ld<u16>(ktb + (kt1[0] & 0xffffu)); e[9] = lds_ld<u16>(ktb + (kt1[0] >> 16)); e[10] = lds_ld<u16>(ktb + (kt1[1] & 0xffffu));
    const u32 k00 = lds_ld<u16>(ktb);
    u32 dw[5], al[5];
#pragma unroll
    for (int k = 0; k < 5; ++k) { dw[k] = e[2 * k] | (e[2 * k + 1] << 16); al[k] = e[2 * k + 1] | (e[2 * k + 2] << 16); }
    const u32 kb = lb + L_KC + ch * 4 * KCP + n * 16;
    lds_st<u32x4>(kb, u32x4{dw[0], dw[1], dw[2], dw[3]});
    lds_st<u32x4>(kb + KCP, u32x4{al[0], al[1], al[2], al[3]});
    lds_st<u32x4>(kb + 2 * KCP, u32x4{dw[1], dw[2], dw[3], dw[4]});
    lds_st<u32x4>(kb + 3 * KCP, u32x4{al[1], al[2], al[3], al[4]});
    if (n < 16) {
      u32x4 z = {0, 0, 0, 0};
      if (n == 0) z.x = k00;
      lds_st<u32x4>(lb + L_KC + (ch * 4 + (n >> 2)) * KCP + 4096 + (n & 3) * 16, z);
    }
    lds_st<u32x4>(lb + L_U + ch * 4608 + 512 + n * 16, uv_);
  }
  bar();
  STAMP(7);
  f32x4 acc[8];
  const int cc = (w >> 1) & 1, hf = w & 1;
  if (w < 4) {
#pragma unroll
    for (int c = 0; c < 8; ++c) acc[c] = z4;
    const int bsh = (-i16) & 3;
    u32 abase = lb + L_KC + (cc * 4 + bsh) * KCP + (2032 + 8 * g - i16 - bsh) * 2 - hf * 4 * 64 - 3 * 64 - 7 * 512;
    u32 bbase = lb + L_U + cc * 4608 + (256 - 16 + 16 * i16 + 8 * g) * 2 - hf * 4 * 64 - 3 * 64;
    u32 abase8 = abase + 8;
    pin(abase); pin(abase8); pin(bbase);
    u32x4 A[8], B[8];
#pragma unroll
    for (int qq = 0; qq < 8; ++qq) {
      B[7 - qq] = lds_ld<u32x4>(bbase + (7 - qq) * 512 + 3 * 64);
      A[qq] = cat(lds_ld<u32x2>(abase + (7 - qq) * 512 + 3 * 64), lds_ld<u32x2>(abase8 + (7 - qq) * 512 + 3 * 64));
      __builtin_amdgcn_sched_barrier(0);
    }
#pragma unroll
    for (int rr = 0; rr < 4; ++rr) {
#pragma unroll
      for (int qq = 0; qq < 8; ++qq) {
#pragma unroll
        for (int m = 7 - qq; m >= 0; --m) {
          acc[qq + m] = mfma32(A[qq], B[m], acc[qq + m]);
          asm volatile("s_nop %c0" :: "i"(CNOP));
        }
        if (rr < 3) {
          A[qq] = cat(lds_ld<u32x2>(abase + (7 - qq) * 512 + (2 - rr) * 64), lds_ld<u32x2>(abase8 + (7 - qq) * 512 + (2 - rr) * 64));
          B[7 - qq] = lds_ld<u32x4>(bbase + (7 - qq) * 512 + (2 - rr) * 64);
        }
        __builtin_amdgcn_sched_barrier(0);
      }
    }
    if (hf) {
#pragma unroll
      for (int c = 0; c < 4; ++c) lds_st<f32x4>(lb + L_RED + (w * 4 + c) * 1024 + lane * 16, acc[c]);
#pragma unroll
      for (int c = 0; c < 4; ++c) acc[c] = acc[4 + c];
    } else {
#pragma unroll
      for (int c = 0; c < 4; ++c) lds_st<f32x4>(lb + L_RED + (w * 4 + c) * 1024 + lane * 16, acc[4 + c]);
    }
  }
  bar();
  STAMP(8);
  if (w < 4) {
    const float dk = b2f(p.dskip[d + cc]);
    const u32 tb = (1024 * hf + 16 * i16 + 4 * g) * 2;
    f32x4 o[4];
    u32x2 uv[4];
#pragma unroll
    for (int c = 0; c < 4; ++c) {
      o[c] = lds_ld<f32x4>(lb + L_RED + ((w ^ 1) * 4 + c) * 1024 + lane * 16);
      uv[c] = lds_ld<u32x2>(lb + L_U + cc * 4608 + 512 + tb + c * 512);
    }
#pragma unroll
    for (int c = 0; c < 4; ++c) {
      const f32x4 y = acc[c] + o[c];
      const f32x2 b01 = up2(pk(y[0], y[1])) + up2(pk2(up2(uv[c].x) * dk));
      const f32x2 b23 = up2(pk(y[2], y[3])) + up2(pk2(up2(uv[c].y) * dk));
      lds_st<u32x2>(lb + L_BO + cc * 4096 + tb + c * 512, u32x2{pk2(b01), pk2(b23)});
    }
  }
  bar();
  STAMP(9);
  {
    const int st = w & 3, s2 = 16 * st + i16;
    const u32 a = lb + L_BO + ch * 4096 + (8 * g + (i16 >> 2)) * 128 + (16 * st + 4 * (i16 & 3)) * 2;
    st16<P>(rws, (u32)((((s2 & 7) * 8 + (s2 >> 3)) * 32 + 8 * g) * 2), (u32)WS_B + (d + ch) * B_D, cat(trread(a), trread(a + 4 * 128)));
  }
  STAMP(10);
  if (P) { drain_vm(); bar(); publish(ctl + CT_FB + j, epoch); }
  STAMP(11);
}

template <int PH>
__global__ __launch_bounds__(NT, 4) void gfs4_kernel(Params p) {
  extern __shared__ __attribute__((aligned(16))) char smem[];
  const u32 lb = (u32)(size_t)(LAS char*)smem;
  const int b = blockIdx.x;
  u32* ctl = (u32*)(p.ws + WS_CTL);
  STAMP_DECL;
  if (PH == 0 || PH == 1) STAMP(0); else if (PH == 2) STAMP(20); else STAMP(21);
  u32 gen = 0;
  if (b < N_FFT) {
    if (threadIdx.x >= 256) return;
    fft_role<PH>(p, lb, b, ctl, gen STAMP_PASS);
  }
  else ssm_role<PH>(p, lb, b - N_FFT, ctl, gen STAMP_PASS);
  STAMP_FLUSH();
}

extern "C" void kernel_launch(void* const* d_in, const int* in_sizes, int n_in,
                              void* d_out, int out_size, void* d_ws, size_t ws_size,
                              hipStream_t stream) {
  (void)in_sizes; (void)n_in; (void)out_size; (void)ws_size;
  Params p;
  p.x = (const u16*)d_in[0]; p.wr = (const u16*)d_in[1]; p.wi = (const u16*)d_in[2];
  p.are = (const u16*)d_in[4]; p.aim = (const u16*)d_in[5]; p.bre = (const u16*)d_in[6]; p.bim = (const u16*)d_in[7];
  p.cre = (const u16*)d_in[8]; p.cim = (const u16*)d_in[9]; p.logdt = (const u16*)d_in[10]; p.dskip = (const u16*)d_in[11];
  p.gw = (const u16*)d_in[12];
  p.out = (u16*)d_out; p.ws = (char*)d_ws;
  (void)hipFuncSetAttribute((const void*)gfs4_kernel<0>, hipFuncAttributeMaxDynamicSharedMemorySize, LDS_BYTES);
  gfs4_kernel<0><<<N_WG, NT, LDS_BYTES, stream>>>(p);
}
```

```cpp
#include <hip/hip_runtime.h>
#include <hip/hip_bf16.h>
#include <stdint.h>

typedef unsigned int u32;
typedef unsigned short u16;
typedef unsigned long long u64;
typedef float f32x4 __attribute__((ext_vector_type(4)));
typedef float f32x2 __attribute__((ext_vector_type(2)));
typedef u32 u32x4 __attribute__((ext_vector_type(4)));
typedef u32 u32x2 __attribute__((ext_vector_type(2)));
typedef __bf16 bf16x8 __attribute__((ext_vector_type(8)));
typedef __bf16 bf16x2 __attribute__((ext_vector_type(2)));
typedef short s16x4 __attribute__((ext_vector_type(4)));
#define LAS __attribute__((address_space(3)))
#define GAS __attribute__((address_space(1)))
#define DI __device__ __forceinline__

#define NLAUNCH 1

constexpr int S_ = 2048, D_ = 448, DF = 225, NS = 256;
constexpr int N_FFT = 256, N_SSM = 224, N_WG = N_FFT + N_SSM, NT = 512;

constexpr u32 T_K1 = 65536 + 256;
constexpr u32 TP_S2 = 32768 + 128;
constexpr u32 XT_D = 4096 + 128;
constexpr u32 B_D = 4096 + 128;
constexpr size_t WS_CTL = 0;
constexpr size_t WS_T   = 1u << 20;
constexpr size_t WS_TP  = WS_T + (3u << 20);
constexpr size_t WS_XT  = WS_TP + (3u << 20);
constexpr size_t WS_B   = WS_XT + (2u << 20);
constexpr int FL = 32, FR_ = 256 * FL + 64;
constexpr int CT_GEN = 0;
constexpr int CT_F1 = 64;
constexpr int CT_F2 = CT_F1 + 8 * FR_;
constexpr int CT_FB = CT_F2 + 8 * FR_;
constexpr int CT_FX = CT_FB + 8 * FR_;
static_assert((CT_FX + 7 * 32 * FL) * 4 <= (1 << 20), "control block");

constexpr int XRS = 928;
constexpr int L_XT = 0;
constexpr int L_XCH = 29696;
constexpr int TPRS = 1040;
constexpr int L_TP = 0;
constexpr int L_TO = 29696;
constexpr int L_TI = 38912;
constexpr int L_BI = 38912;
constexpr int BRS = 912;
constexpr int L_Z = 47104;
constexpr int L_C1 = 38912;
constexpr int L_C2 = 65536;
constexpr int L_W = 47104;
constexpr int L_G = 70208;
constexpr int KCP = 4160;
constexpr int L_KC = 0;
constexpr int KSCR = 4096;
constexpr int L_RED = 33280;
constexpr int L_BO = 49664;
constexpr int L_U = 57856;
constexpr int L_XP = 57856;
constexpr int L_KT = 67072;
constexpr int L_CNT = 70272;
constexpr int LDS_BYTES = 70288;

struct Params {
  const u16* x; const u16* wr; const u16* wi;
  const u16* are; const u16* aim; const u16* bre; const u16* bim; const u16* cre; const u16* cim;
  const u16* logdt; const u16* dskip; const u16* gw;
  u16* out; char* ws;
};

constexpr double cx_sin_oct(double x) { double x2 = x * x, t = x, s = x; for (int i = 1; i < 9; ++i) { t *= -x2 / ((2 * i) * (2 * i + 1)); s += t; } return s; }
constexpr double cx_cos_oct(double x) { double x2 = x * x, t = 1, s = 1; for (int i = 1; i < 9; ++i) { t *= -x2 / ((2 * i - 1) * (2 * i)); s += t; } return s; }
constexpr double cx_sin(long n, long d) {
  n %= d; if (n < 0) n += d;
  const long o = (8 * n) / d;
  const double r = 6.283185307179586476925 * (double)(8 * n - o * d) / (8.0 * (double)d), q = 0.78539816339744830962;
  switch (o) {
    case 0: return cx_sin_oct(r);       case 1: return cx_cos_oct(q - r);
    case 2: return cx_cos_oct(r);       case 3: return cx_sin_oct(q - r);
    case 4: return -cx_sin_oct(r);      case 5: return -cx_cos_oct(q - r);
    case 6: return -cx_cos_oct(r);      default: return -cx_sin_oct(q - r);
  }
}
constexpr double cx_cos(long n, long d) { return cx_sin(4 * n + d, 4 * d); }
constexpr u32 cx_f32(double v) { return __builtin_bit_cast(u32, (float)v); }
constexpr u32 cx_bf(double v) { const u32 u = cx_f32(v); return (u + 0x7fffu + ((u >> 16) & 1u)) >> 16; }
constexpr int cx_pi(int g, int jj) { return 16 * (jj >> 2) + 4 * g + (jj & 3); }
struct Frag { u32 v[64][4]; };
enum { TF_FC0, TF_FS0, TF_CO0, TF_SO0, TF_COT0, TF_SOT0, TF_TC, TF_TS, TF_M, TF_Q0, TF_N = TF_Q0 + 24 };
constexpr Frag make_frag(int item) {
  Frag f{};
  for (int lane = 0; lane < 64; ++lane) {
    const int i16 = lane & 15, g = lane >> 4;
    if (item < TF_TC) {
      const int sn = item & 1;
      for (int jj = 0; jj < 8; ++jj) {
        const int m = cx_pi(g, jj), a = i16;
        long n = 0, d = 32;
        if (item < TF_CO0) { n = a * m; d = 32; }
        else if (item < TF_COT0) { n = a * (2 * m + 1); d = 64; }
        else { n = m * (2 * a + 1); d = 64; }
        f.v[lane][jj >> 1] |= cx_bf(sn ? cx_sin(n, d) : cx_cos(n, d)) << (16 * (jj & 1));
      }
    } else if (item == TF_TC || item == TF_TS) {
      for (int j = 0; j < 4; ++j) {
        const long n = (2 * g + (j & 1)) * (16 * (j >> 1) + i16);
        f.v[lane][j] = cx_f32(item == TF_TC ? cx_cos(n, 448) : cx_sin(n, 448));
      }
    } else if (item == TF_M) {
      for (int j = 0; j < 4; ++j) {
        const int k0 = 2 * g + (j >> 1);
        const double cf = ((k0 == 0 || k0 == 7) ? 1.0 : 2.0) / (2048.0 * 448.0);
        const double v = i16 < 14 ? cf * ((j & 1) ? -cx_sin(k0 * i16, 14) : cx_cos(k0 * i16, 14)) : 0.0;
        f.v[lane][j >> 1] |= cx_bf(v) << (16 * (j & 1));
      }
    } else {
      const int q = (item - TF_Q0) / 6, sub = (item - TF_Q0) % 6, k0 = 2 * q + ((i16 >> 1) & 1), ri = i16 & 1;
      if (sub < 2) {
        if ((i16 >> 2) == 2 * sub + (g & 1))
          for (int j = 0; j < 8; ++j) {
            const int d0 = 8 * (g >> 1) + j;
            const double v = d0 < 14 ? (ri ? -cx_sin(d0 * k0, 14) : cx_cos(d0 * k0, 14)) : 0.0;
            f.v[lane][j >> 1] |= cx_bf(v) << (16 * (j & 1));
          }
      } else {
        for (int j = 0; j < 4; ++j) {
          const long n = cx_pi(g, 4 * (sub & 1) + j) * k0;
          f.v[lane][j] = cx_f32(sub < 4 ? cx_cos(n, 448) : (ri ? -cx_sin(n, 448) : cx_sin(n, 448)));
        }
      }
    }
  }
  return f;
}
template <int I> struct FragC { static constexpr Frag f = make_frag(I); };
#define FR(i) FragC<(i)>::f
#define FR6(i) FR(i), FR(i + 1), FR(i + 2), FR(i + 3), FR(i + 4), FR(i + 5)
__device__ const __attribute__((aligned(1024))) Frag TF[TF_N] = {FR6(0), FR(6), FR(7), FR(8), FR6(9), FR6(15), FR6(21), FR6(27)};

struct KTab { u16 v[2080]; };
constexpr KTab make_ktab() {
  KTab t{};
  for (int i = 0; i < 2080; ++i) {
    const int l = 2048 - i;
    int idx = 768;
    if (l >= 0) {
      const int sh = l > 256 ? (l >= 1024 ? 3 : (l >= 512 ? 2 : 1)) : 0;
      const int v = sh ? (((l + (1 << (sh - 1)) - 1 + ((l >> sh) & 1)) >> sh) << sh) : l;
      if (v < 256) idx = ((v & 7) + 8 * (v >> 7)) * 16 + ((v >> 3) & 15);
      else if (v < 512) idx = 256 + ((((v - 256) >> 1) & 3) + 4 * ((v - 256) >> 7)) * 16 + (((v - 256) >> 3) & 15);
      else if (v < 1024) idx = 256 + (8 + (((v - 512) >> 2) & 1) + 2 * ((v - 512) >> 7)) * 16 + (((v - 512) >> 3) & 15);
      else if (v < 2048) idx = 512 + ((v - 1024) >> 7) * 16 + (((v - 1024) >> 3) & 15);
      else idx = 640;
    }
    t.v[i] = (u16)(2 * idx);
  }
  return t;
}
__device__ const __attribute__((aligned(16))) KTab KTAB = make_ktab();

template <class T, class F> DI T bc(F v) { return __builtin_bit_cast(T, v); }
DI f32x4 mfma32(u32x4 a, u32x4 b, f32x4 c) { return __builtin_amdgcn_mfma_f32_16x16x32_bf16(bc<bf16x8>(a), bc<bf16x8>(b), c, 0, 0, 0); }
DI f32x4 mfma16(u32x2 a, u32x2 b, f32x4 c) { return __builtin_amdgcn_mfma_f32_16x16x16bf16_1k(bc<s16x4>(a), bc<s16x4>(b), c, 0, 0, 0); }
DI u32 pk(float a, float b) { f32x2 v = {a, b}; return bc<u32>(__builtin_convertvector(v, bf16x2)); }
DI u32 pk2(f32x2 v) { return bc<u32>(__builtin_convertvector(v, bf16x2)); }
DI float bfr(float a) { return bc<float>(pk(0.f, a) & 0xffff0000u); }
DI float lo16(u32 v) { return bc<float>(v << 16); }
DI float hi16(u32 v) { return bc<float>(v & 0xffff0000u); }
DI f32x2 up2(u32 v) { return f32x2{lo16(v), hi16(v)}; }
DI float b2f(u16 v) { return bc<float>((u32)v << 16); }
DI u32x2 trread(u32 a) { return bc<u32x2>(__builtin_amdgcn_ds_read_tr16_b64_v4i16((LAS s16x4*)(size_t)a)); }
DI u32x4 cat(u32x2 a, u32x2 b) { u32x4 r = {a.x, a.y, b.x, b.y}; return r; }
DI float sinr(float x) { return __builtin_amdgcn_sinf(x); }
DI float cosr(float x) { return __builtin_amdgcn_cosf(x); }
DI float fx1(float v) { return __builtin_amdgcn_update_dpp(v, v, 0xB1, 0xf, 0xf, true); }
DI float fr8(float v) { return __builtin_amdgcn_update_dpp(v, v, 0x128, 0xf, 0xf, true); }
DI u32 ux1(u32 v) { return (u32)__builtin_amdgcn_update_dpp((int)v, (int)v, 0xB1, 0xf, 0xf, true); }
DI u32 ur8(u32 v) { return (u32)__builtin_amdgcn_update_dpp((int)v, (int)v, 0x128, 0xf, 0xf, true); }
DI u32x4 sx1(u32x4 v, u32 m) { u32x4 r = {ux1(v.x) ^ m, ux1(v.y) ^ m, ux1(v.z) ^ m, ux1(v.w) ^ m}; return r; }
DI u32x4 sr8(u32x4 v, u32 m) { u32x4 r = {ur8(v.x) ^ m, ur8(v.y) ^ m, ur8(v.z) ^ m, ur8(v.w) ^ m}; return r; }
DI u32x4 xr(u32x4 v, u32 m) { u32x4 r = {v.x ^ m, v.y ^ m, v.z ^ m, v.w ^ m}; return r; }
DI u32x4 pk8(const float (&v)[8]) { return u32x4{pk(v[0], v[1]), pk(v[2], v[3]), pk(v[4], v[5]), pk(v[6], v[7])}; }
template <class T> DI T lds_ld(u32 a) { return *(LAS T*)(size_t)a; }
template <class T> DI void lds_st(u32 a, T v) { *(LAS T*)(size_t)a = v; }
DI void wait_vm0() { __builtin_amdgcn_s_waitcnt(0x0F70); }
DI void drain_vm() { asm volatile("s_waitcnt vmcnt(0)" ::: "memory"); }
DI void bar() { __builtin_amdgcn_s_waitcnt(0xC07F); asm volatile("s_barrier" ::: "memory"); }
template <class T> DI void pin(T& v) { asm volatile("" : "+v"(v)); }
typedef __amdgpu_buffer_rsrc_t rsrc_t;
DI rsrc_t mkrs(const void* p, u32 bytes) { return __builtin_amdgcn_make_buffer_rsrc((void*)p, 0, bytes, 0x00020000); }
template <int AUX> DI void dma16(rsrc_t rs, u32 lds, u32 voff, u32 soff) { __builtin_amdgcn_raw_ptr_buffer_load_lds(rs, (LAS void*)(size_t)lds, 16, voff, soff, 0, AUX); }
DI f32x2 cmul(f32x2 x, f32x2 y) {
  f32x2 t;
  asm("v_pk_mul_f32 %0, %1, %2 op_sel:[0,0] op_sel_hi:[0,1]\n\tv_pk_fma_f32 %0, %1, %2, %0 op_sel:[1,1,0] op_sel_hi:[1,0,1] neg_lo:[1,0,0]" : "=&v"(t) : "v"(x), "v"(y));
  return t;
}
DI float dot2b(u32 a, u32 b, float c) { return __builtin_amdgcn_fdot2_f32_bf16(bc<bf16x2>(a), bc<bf16x2>(b), c, false); }
DI float wave_sum(float a) {
  a += __builtin_amdgcn_update_dpp(0.f, a, 0xB1, 0xf, 0xf, true);
  a += __builtin_amdgcn_update_dpp(0.f, a, 0x4E, 0xf, 0xf, true);
  a += __builtin_amdgcn_update_dpp(0.f, a, 0x141, 0xf, 0xf, true);
  a += __builtin_amdgcn_update_dpp(0.f, a, 0x140, 0xf, 0xf, true);
  a += __builtin_amdgcn_update_dpp(0.f, a, 0x142, 0xa, 0xf, false);
  a += __builtin_amdgcn_update_dpp(0.f, a, 0x143, 0xc, 0xf, false);
  return bc<float>(__builtin_amdgcn_readlane(bc<int>(a), 63));
}
DI int pi_k(int g, int jj) { return 16 * (jj >> 2) + 4 * g + (jj & 3); }

#define CNOP 1
#define SH2PRIO 2
#define SLP 2
#define RLX_AGENT __ATOMIC_RELAXED, __HIP_MEMORY_SCOPE_AGENT
template <bool P> DI void st16(rsrc_t rs, u32 voff, u32 soff, u32x4 v) {
  __builtin_amdgcn_raw_buffer_store_b128(v, rs, voff, soff, P ? 16 : 0);
  asm volatile("s_nop 1" :: "v"(v) : "memory");
}
template <bool P> DI u32x4 ld16(rsrc_t rs, u32 voff, u32 soff) { return __builtin_amdgcn_raw_buffer_load_b128(rs, voff, soff, P ? 16 : 0); }
DI void publish(u32* set, int idx, u32 epoch) {
  if (threadIdx.x < 8) __hip_atomic_store((GAS u32*)set + threadIdx.x * FR_ + idx * FL, epoch, RLX_AGENT);
}
DI void poll_flags(const u32* first, int n, u32 epoch) {
  const int lane = threadIdx.x & 63;
  const GAS u32* f = (const GAS u32*)first + (lane < n ? lane : 0) * FL;
  for (int spin = 0; spin < (1 << 18); ++spin) {
    if (__all(__hip_atomic_load(f, RLX_AGENT) == epoch)) break;
    __builtin_amdgcn_s_sleep(SLP);
  }
}

#define STAMP(i) do {} while (0)
#define STAMP_DECL do {} while (0)
#define STAMP_FLUSH() do {} while (0)
#define STAMP_ARG
#define STAMP_PASS

DI u32x4 tf(rsrc_t rs, int item, u32 l16) { return __builtin_amdgcn_raw_buffer_load_b128(rs, l16 + (item & 3) * 1024, (item >> 2) * 4096, 0); }
DI u32x4 tfs(rsrc_t rs, int item, u32 l16, u32 so) { return __builtin_amdgcn_raw_buffer_load_b128(rs, l16 + item * 1024, so, 0); }

template <int PH>
DI void fft_role(const Params& p, const u32 lb, const int b, u32* ctl, u32& gen STAMP_ARG) {
  constexpr bool P = PH == 0;
  constexpr int AUX = P ? 16 : 0;
  const int tid = threadIdx.x, lane = tid & 63, w = __builtin_amdgcn_readfirstlane(tid >> 6), i16 = lane & 15, g = lane >> 4;
  const u32 l16 = lane * 16;
  const int s2 = b & 63, q = b >> 6;
  const int xx = b & 7, mm = (b >> 3) & 3, kq = b >> 5;
  const int k1w = (((mm & 1) ? (xx ? 32 - xx : 8) : xx) + 16 * (mm >> 1)) & 31;
  const f32x4 z4 = {0, 0, 0, 0};
  const rsrc_t rws = mkrs(p.ws, 16u << 20), rtf = mkrs(TF, TF_N * 1024);
  const u32 tq = (TF_Q0 + 6 * q) * 1024;
  if (P) __builtin_amdgcn_s_setprio(3);

  if (PH == 0 || PH == 1) {
    const rsrc_t rx = mkrs(p.x, S_ * D_ * 2);
    if (lane < 56) {
#pragma unroll
      for (int r = 0; r < 8; ++r) dma16<0>(rx, lb + L_XT + (8 * w + r) * XRS, l16, (64 * (8 * w + r) + s2) * (D_ * 2));
    }
  }
  if (P) gen = __hip_atomic_load((GAS u32*)ctl + CT_GEN, RLX_AGENT);
  const u32 tpo = (u32)((tid >> 3) * TP_S2 + (tid & 7) * 16), tps = (u32)WS_TP + k1w * 1024 + kq * 128;
  u32x4 fc[2], fs[2];
  if (PH == 0 || PH == 1) {
    dma16<0>(rtf, lb + L_C1 + w * 2048, l16, w == 0 ? 0u : tq + (2 * w - 2) * 1024);
    dma16<0>(rtf, lb + L_C1 + w * 2048 + 1024, l16, w == 0 ? 1024u : tq + (2 * w - 1) * 1024);
  } else {
    fc[0] = tf(rtf, TF_FC0, l16); fs[0] = tf(rtf, TF_FS0, l16);
    fc[1] = xr(fc[0], 0x80000000u); fs[1] = xr(fs[0], 0x80000000u);
  }

  u32x4 co[2], so[2], cot[2], sot[2];
  auto load_p2 = [&]() {
    const bool mir = kq >= 4;
    const int col0 = mir ? 448 - (56 * kq + 49) : 56 * kq;
    const rsrc_t rr = mkrs(p.wr, S_ * DF * 2), ri_ = mkrs(p.wi, S_ * DF * 2);
#pragma unroll
    for (int i = 0; i < 3; ++i) {
      const int gi = w + 4 * i;
      if (gi < 9) {
        const int gam = 64 * gi + lane, k2 = (gam * 7282) >> 16, chunk = gam - 9 * k2;
        const int ks = k1w + 32 * k2, row = mir ? ((2048 - ks) & 2047) : ks;
        const u32 vo = (u32)(((row * DF + col0) * 2) & ~15) + chunk * 16;
        dma16<0>(rr, lb + L_W + gi * 1024, vo, 0);
        dma16<0>(ri_, lb + L_W + 9216 + gi * 1024, vo, 0);
      }
    }
    dma16<0>(rtf, lb + L_C2 + w * 1024, l16, (TF_CO0 + w) * 1024);
  };

  if (PH == 0 || PH == 1) {
    u32x4 C1, S1, nS1;
    {
      const float k1 = (float)(16 * (w >> 1) + i16) * (1.0f / 2048.0f), P0 = k1 * (float)(64 * g + s2), Pl = k1 * 256.f;
      float c[8], s[8];
#pragma unroll
      for (int j = 0; j < 8; ++j) { const float ph = __builtin_fmaf(Pl, (float)j, P0); c[j] = cosr(ph); s[j] = sinr(ph); }
      C1 = pk8(c); S1 = pk8(s); nS1 = xr(S1, 0x80008000u);
      pin(C1); pin(S1); pin(nS1);
    }
    int d0a = 8 * (g >> 1) + (i16 >> 2), d0b = d0a + 4;
    d0b = d0b < 14 ? d0b : 0;
    u32 xa = lb + L_XT + (8 * w + (g & 1)) * XRS + 8 * (i16 & 3);
    u32 xb = xa + d0b * 64; xa += d0a * 64;
    u32 xw = lb + L_XCH + w * 256 + lane * 4;
    u32 xr_ = lb + L_XCH + (w & 1) * 4096 + lane * 4;
    u32 toff = (u32)((16 * (w >> 1) + i16) * T_K1 + 4 * (w & 1) * 32 + g * 16);
    u32 imm = (i16 & 1) ? 0x80008000u : 0u;
    pin(xa); pin(xb); pin(xw); pin(xr_); pin(toff); pin(imm);
    const u32 tso = (u32)WS_T + s2 * 1024 + q * 256;
    STAMP(1);
    wait_vm0();
    bar();
    const u32 cb = lb + L_C1 + l16;
    fc[0] = lds_ld<u32x4>(cb); fs[0] = lds_ld<u32x4>(cb + 1024);
    const u32x4 Ba1 = lds_ld<u32x4>(cb + 2048), Ba2 = lds_ld<u32x4>(cb + 3072);
    const f32x4 twc0 = lds_ld<f32x4>(cb + 4096), twc1 = lds_ld<f32x4>(cb + 5120), tws0 = lds_ld<f32x4>(cb + 6144), tws1 = lds_ld<f32x4>(cb + 7168);
    fc[1] = xr(fc[0], 0x80000000u); fs[1] = xr(fs[0], 0x80000000u);
    STAMP(2);
    f32x4 D2[2][2];
#pragma unroll
    for (int sg = 0; sg < 2; ++sg) {
      f32x4 H[2];
#pragma unroll
      for (int td = 0; td < 2; ++td) {
        const int o = td * 32 + sg * 4 * XRS;
        u32x4 A0 = cat(trread(xa + o), trread(xb + o));
        u32x4 A1 = cat(trread(xa + o + 2 * XRS), trread(xb + o + 2 * XRS));
        H[td] = mfma32(A1, Ba2, mfma32(A0, Ba1, z4));
      }
      float h[8];
#pragma unroll
      for (int j = 0; j < 4; ++j) {
        h[j] = __builtin_fmaf(fx1(H[0][j]), tws0[j], H[0][j] * twc0[j]);
        h[4 + j] = __builtin_fmaf(fx1(H[1][j]), tws1[j], H[1][j] * twc1[j]);
      }
      u32x4 A = pk8(h), A2 = sx1(A, imm);
#pragma unroll
      for (int tk = 0; tk < 2; ++tk) D2[sg][tk] = mfma32(A2, fs[tk], mfma32(A, fc[tk], z4));
    }
#pragma unroll
    for (int tk = 0; tk < 2; ++tk)
#pragma unroll
      for (int r = 0; r < 4; ++r) lds_st<u32>(xw + (tk * 4 + r) * 1024, pk(D2[0][tk][r], D2[1][tk][r]));
    bar();
    STAMP(3);
    {
      u32 pc[4][2];
#pragma unroll
      for (int kk = 0; kk < 2; ++kk) {
        const u32 br = xr_ + kk * 2048;
        u32x4 Xr = {lds_ld<u32>(br), lds_ld<u32>(br + 256), lds_ld<u32>(br + 512), lds_ld<u32>(br + 768)};
        u32x4 Xi = {lds_ld<u32>(br + 1024), lds_ld<u32>(br + 1280), lds_ld<u32>(br + 1536), lds_ld<u32>(br + 1792)};
        f32x4 Tr = mfma32(Xi, S1, mfma32(Xr, C1, z4));
        f32x4 Ti = mfma32(Xr, nS1, mfma32(Xi, C1, z4));
        pc[kk][0] = pk(Tr[0], Tr[1]); pc[kk][1] = pk(Tr[2], Tr[3]);
        pc[2 + kk][0] = pk(Ti[0], Ti[1]); pc[2 + kk][1] = pk(Ti[2], Ti[3]);
      }
      u32x4 lo = {pc[0][0], pc[0][1], pc[1][0], pc[1][1]}, hi = {pc[2][0], pc[2][1], pc[3][0], pc[3][1]};
#pragma unroll
      for (int e = 0; e < 4; ++e) {
        auto r1 = __builtin_amdgcn_permlane16_swap(lo[e], hi[e], false, false);
        auto r2 = __builtin_amdgcn_permlane32_swap(r1[0], r1[1], false, false);
        lo[e] = r2[0]; hi[e] = r2[1];
      }
      st16<P>(rws, toff, tso, lo);
      st16<P>(rws, toff + 64, tso, hi);
    }
    STAMP(4);
    if (P) {
      __builtin_amdgcn_sched_barrier(0);
      load_p2();
      __builtin_amdgcn_sched_barrier(0);
      asm volatile("s_waitcnt vmcnt(5)" ::: "memory");
      bar();
      publish(ctl + CT_F1, b, gen + 1u);
      __builtin_amdgcn_s_setprio(2);
    }
    STAMP(5);
  }
  const u32 epoch = __builtin_amdgcn_readfirstlane(gen) + 1u;

  if (PH == 2) load_p2();
  if (PH == 0 || PH == 2) {
    const int ri = i16 >> 3;
    u32 tsv = (u32)((lane >> 1) * 1024 + w * 256 + (lane & 1) * 16);
    u32 tiw = lb + L_TI + w * 2048 + ((lane >> 1) & 1) * 1024 + (lane >> 2) * 32 + (lane & 1) * 16;
    u32 ta = lb + L_TI + w * 2048 + (4 * g + (i16 >> 2)) * 32 + (i16 & 3) * 8;
    u32 tow = lb + L_TO + 2 * i16 * 144 + w * 32 + g * 8;
    u32 tor = lb + L_TO + (tid >> 3) * 144 + (tid & 7) * 16;
    u32 mf = ri ? 0x80008000u : 0u, mi = ri ? 0u : 0x80008000u;
    float wr_[16], wi_[16];
    {
      const bool mir = kq >= 4;
      const int c = i16 & 7, kd = 2 * w + (c >> 2) + 14 * (4 * kq + (c & 3)), col0 = mir ? 448 - (56 * kq + 49) : 56 * kq;
      const int row0 = mir ? ((2048 - k1w) & 2047) : k1w;
      u32 wa = lb + L_W + 4 * g * 144 + (((row0 * DF + col0) * 2) & 15) + ((mir ? 448 - kd : kd) - col0) * 2;
      const u32 sg = (mir != (ri != 0)) ? 0u : 0x80000000u;
      pin(tsv); pin(tiw); pin(ta); pin(tow); pin(tor); pin(mf); pin(mi); pin(wa);
      STAMP(7);
      if (P) poll_flags(ctl + CT_F1 + (b & 7) * FR_ + w * 64 * FL, 64, epoch);
      wait_vm0();
      bar();
      const u32 so_ = (u32)WS_T + k1w * T_K1 + kq * 32;
      const u32x4 t0 = ld16<P>(rws, tsv, so_), t1 = ld16<P>(rws, tsv, so_ + 32 * 1024);
      __builtin_amdgcn_sched_barrier(0);
      co[0] = lds_ld<u32x4>(lb + L_C2 + l16); so[0] = lds_ld<u32x4>(lb + L_C2 + 1024 + l16);
      cot[0] = lds_ld<u32x4>(lb + L_C2 + 2048 + l16); sot[0] = lds_ld<u32x4>(lb + L_C2 + 3072 + l16);
      co[1] = xr(so[0], 0x00008000u);
      so[1] = xr(co[0], 0x80000000u);
      cot[1] = xr(cot[0], 0x80000000u); sot[1] = xr(sot[0], 0x80000000u);
      pin(co[0]); pin(co[1]); pin(so[0]); pin(so[1]); pin(cot[0]); pin(cot[1]); pin(sot[0]); pin(sot[1]);
#pragma unroll
      for (int j = 0; j < 16; ++j) {
        const int o = (32 * (j >> 3) + 16 * ((j >> 2) & 1) + (j & 3)) * 144;
        wr_[j] = bc<float>((u32)lds_ld<u16>(wa + o) << 16);
        wi_[j] = bc<float>(((u32)lds_ld<u16>(wa + 9216 + o) << 16) + sg);
      }
#pragma unroll
      for (int j = 0; j < 16; ++j) { pin(wr_[j]); pin(wi_[j]); }
      __builtin_amdgcn_sched_barrier(0);
      if (P) __builtin_amdgcn_s_setprio(3);
      STAMP(8);
      lds_st<u32x4>(tiw, t0);
      lds_st<u32x4>(tiw + 512, t1);
    }
    STAMP(9);
    f32x4 E[2], O[2];
    {
      u32x4 B0 = cat(trread(ta), trread(ta + 512)), B1 = cat(trread(ta + 1024), trread(ta + 1536));
      u32x4 B0s = sr8(B0, mf), B1s = sr8(B1, mf);
#pragma unroll
      for (int t = 0; t < 2; ++t) {
        E[t] = mfma32(fs[t], B0s, mfma32(fc[t], B0, z4));
        O[t] = mfma32(so[t], B1s, mfma32(co[t], B1, z4));
      }
    }
    float Pp[8], Mm[8];
#pragma unroll
    for (int j = 0; j < 8; ++j) {
      const float e = E[j >> 2][j & 3], o = O[j >> 2][j & 3];
      const float ylo = e + o, yhi = e - o;
      const float vlo = __builtin_fmaf(fr8(ylo), wi_[j], ylo * wr_[j]);
      const float vhi = __builtin_fmaf(fr8(yhi), wi_[8 + j], yhi * wr_[8 + j]);
      Pp[j] = vlo + vhi; Mm[j] = vlo - vhi;
    }
    {
      u32x4 AP = pk8(Pp), AM = pk8(Mm);
      u32x4 APs = sr8(AP, mi), AMs = sr8(AM, mi);
#pragma unroll
      for (int t = 0; t < 2; ++t) {
        f32x4 ze = mfma32(APs, fs[t], mfma32(AP, fc[t], z4));
        f32x4 zo = mfma32(AMs, sot[t], mfma32(AM, cot[t], z4));
        lds_st<u32x2>(tow + t * 32 * 144, u32x2{pk(ze[0], ze[1]), pk(ze[2], ze[3])});
        lds_st<u32x2>(tow + t * 32 * 144 + 144, u32x2{pk(zo[0], zo[1]), pk(zo[2], zo[3])});
      }
    }
    bar();
    STAMP(10);
    {
      st16<P>(rws, tpo, tps, lds_ld<u32x4>(tor));
      st16<P>(rws, tpo, tps + 32 * TP_S2, lds_ld<u32x4>(tor + 32 * 144));
    }
    STAMP(11);
    if (P) { drain_vm(); bar(); publish(ctl + CT_F2, b, epoch); __builtin_amdgcn_s_setprio(SH2PRIO); }
    STAMP(12);
  }

  if (PH == 0 || PH == 3) {
    u32x4 gwv = {0, 0, 0, 0};
    if (lane < 56) gwv = *(const GAS u32x4*)((const char*)p.gw + l16);
#pragma unroll
    for (int r = 0; r < 2; ++r) {
      float acc = 0.f;
      if (lane < 56) {
        u32x4 xv;
        if (PH == 0) xv = lds_ld<u32x4>(lb + L_XT + (8 * q + 2 * w + r) * XRS + l16);
        else xv = *(const GAS u32x4*)((const char*)(p.x + (size_t)(64 * (8 * q + 2 * w + r) + s2) * D_) + l16);
#pragma unroll
        for (int e = 0; e < 4; ++e) acc = dot2b(pk2(up2(xv[e]) * up2(gwv[e])), 0x3f803f80u, acc);
      }
      const float gg = bfr(__builtin_amdgcn_rcpf(1.0f + __expf(-bfr(wave_sum(acc)))));
      if (lane == 0) lds_st<f32x2>(lb + L_G + (2 * w + r) * 8, f32x2{gg, bfr(1.0f - gg)});
    }
  }

  if (PH == 0 || PH == 3) {
    const f32x4 tc = bc<f32x4>(tf(rtf, TF_TC, l16)), ts = bc<f32x4>(tf(rtf, TF_TS, l16));
    const u32x4 m4 = tf(rtf, TF_M, l16);
    u32x4 b1, b2;
    {
      const int ro = i16 >> 3;
      const float a = (float)(64 * (8 * q + (i16 & 7)) + s2) * (1.0f / 2048.0f), P0 = a * (float)(4 * g);
      float u[8], v[8];
#pragma unroll
      for (int jj = 0; jj < 8; ++jj) {
        const float ph = __builtin_fmaf(a, (float)(16 * (jj >> 2) + (jj & 3)), P0);
        u[jj] = sinr(ph + (ro ? 0.f : 0.25f));
        v[jj] = sinr(ph + (ro ? 0.25f : 0.5f));
      }
      b1 = pk8(u); b2 = pk8(v);
      pin(b1); pin(b2);
    }
    u32 tpa = lb + L_TP + (4 * g + (i16 >> 2)) * TPRS + (i16 & 3) * 128 + w * 32;
    u32 zw[2], zr;
#pragma unroll
    for (int kk = 0; kk < 2; ++kk) {
      const int row = 2 * (2 * w + kk) + (i16 >> 3);
      zw[kk] = lb + L_Z + ((i16 & 7) * 16 + row) * 64 + ((g ^ (row >> 2)) & 3) * 16;
      pin(zw[kk]);
    }
    zr = lb + L_Z + (2 * w * 16 + i16) * 64 + ((g ^ (i16 >> 2)) & 3) * 16;
    u32 boff = (u32)(tid * B_D);
    u32 biw = lb + L_BI + tid * 2, bir = lb + L_BI + 2 * w * BRS + (32 * i16 + 4 * g) * 2;
    u32 ooff = (u32)((64 * (8 * q + 2 * w) + s2) * (D_ * 2) + (32 * i16 + 4 * g) * 2);
    u32 mi = (i16 & 1) ? 0u : 0x80008000u;
    pin(tpa); pin(zr); pin(boff); pin(biw); pin(bir); pin(ooff); pin(mi);
    STAMP(13);
    if (P) { poll_flags(ctl + CT_F2 + (b & 7) * FR_ + w * 64 * FL, 64, epoch); bar(); __builtin_amdgcn_s_setprio(3); }
    STAMP(14);
#pragma unroll
    for (int r = 0; r < 8; ++r) dma16<AUX>(rws, lb + L_TP + (8 * w + r) * TPRS, l16, (u32)WS_TP + s2 * TP_S2 + (8 * w + r) * 1024);
    wait_vm0();
    bar();
    STAMP(15);
#pragma unroll
    for (int kk = 0; kk < 2; ++kk)
#pragma unroll
      for (int t = 0; t < 2; ++t) {
        const int o = t * 512 + kk * 8;
        u32x4 Tr = cat(trread(tpa + o), trread(tpa + o + 16 * TPRS));
        u32x4 Ti = cat(trread(tpa + o + 16), trread(tpa + o + 16 + 16 * TPRS));
        f32x4 z = mfma32(Ti, b2, mfma32(Tr, b1, z4));
        lds_st<u32x2>(zw[kk] + t * 8, u32x2{pk(z[0], z[1]), pk(z[2], z[3])});
      }
    bar();
    STAMP(16);
    f32x2 ga[2][2][2];
#pragma unroll
    for (int rr = 0; rr < 2; ++rr) {
      u32x4 A = lds_ld<u32x4>(zr + rr * 1024), A2 = sx1(A, mi);
      const float gg = lds_ld<float>(lb + L_G + (2 * w + rr) * 8);
#pragma unroll
      for (int td = 0; td < 2; ++td) {
        f32x4 G = mfma32(A2, fs[td], mfma32(A, fc[td], z4));
        const float c0 = tc[2 * td], s0 = ts[2 * td], c1 = tc[2 * td + 1], s1 = ts[2 * td + 1];
        const float g0r = G[0] * c0 - G[1] * s0, g0i = G[1] * c0 + G[0] * s0;
        const float g1r = G[2] * c1 - G[3] * s1, g1i = G[3] * c1 + G[2] * s1;
        f32x4 y = mfma16(u32x2{pk(g0r, g0i), pk(g1r, g1i)}, u32x2{m4.x, m4.y}, z4);
        ga[rr][td][0] = up2(pk2(up2(pk(y[0], y[1])) * gg));
        ga[rr][td][1] = up2(pk2(up2(pk(y[2], y[3])) * gg));
        pin(ga[rr][td][0]); pin(ga[rr][td][1]);
      }
    }
    STAMP(17);
    if (P) { poll_flags(ctl + CT_FB + (b & 7) * FR_ + w * 56 * FL, 56, epoch); bar(); }
    STAMP(18);
    {
      const u32 so_ = (u32)WS_B + (((s2 & 7) * 8 + (s2 >> 3)) * 32 + 8 * q) * 2;
      const u32x4 bv0 = ld16<P>(rws, boff, so_);
      u32x4 bv1 = {0, 0, 0, 0};
      if (tid < D_ - 256) bv1 = ld16<P>(rws, boff, so_ + 256 * B_D);
#pragma unroll
      for (int e = 0; e < 4; ++e) {
        lds_st<u16>(biw + (2 * e) * BRS, (u16)bv0[e]);
        lds_st<u16>(biw + (2 * e + 1) * BRS, (u16)(bv0[e] >> 16));
      }
      if (tid < D_ - 256) {
#pragma unroll
        for (int e = 0; e < 4; ++e) {
          lds_st<u16>(biw + 512 + (2 * e) * BRS, (u16)bv1[e]);
          lds_st<u16>(biw + 512 + (2 * e + 1) * BRS, (u16)(bv1[e] >> 16));
        }
      }
    }
    bar();
    const rsrc_t ro_ = mkrs(p.out, S_ * D_ * 2);
    u32x2 bq[2][2];
    float og[2];
#pragma unroll
    for (int rr = 0; rr < 2; ++rr) {
      og[rr] = lds_ld<float>(lb + L_G + (2 * w + rr) * 8 + 4);
#pragma unroll
      for (int td = 0; td < 2; ++td) bq[rr][td] = lds_ld<u32x2>(bir + rr * BRS + td * 32);
    }
#pragma unroll
    for (int rr = 0; rr < 2; ++rr)
#pragma unroll
      for (int td = 0; td < 2; ++td) {
        const f32x2 o01 = ga[rr][td][0] + up2(pk2(up2(bq[rr][td].x) * og[rr]));
        const f32x2 o23 = ga[rr][td][1] + up2(pk2(up2(bq[rr][td].y) * og[rr]));
        if (i16 < 14) __builtin_amdgcn_raw_buffer_store_b64(u32x2{pk2(o01), pk2(o23)}, ro_, ooff, rr * 64 * (D_ * 2) + td * 32, 0);
      }
    STAMP(19);
    if (P && b == 0 && tid == 0) __hip_atomic_store((GAS u32*)ctl + CT_GEN, epoch, RLX_AGENT);
  }
}

DI f32x2 cexp_rev(float x2, float ph) {
  const float e = __builtin_amdgcn_exp2f(x2);
  const float f = __builtin_amdgcn_fractf(ph);
  f32x2 cs = {cosr(f), sinr(f)};
  return cs * e;
}
struct KW { u32 a[8]; };
template <int ROW> DI void wrow(const KW& k, f32x2 v) { lds_st<u32>(k.a[ROW & 7] + ROW * 256, pk2(v)); }
template <int ROW> DI void wrow2(const KW& k, f32x2 lo, f32x2 hi) {
  const u32 a = pk2(lo), b = pk2(hi);
  asm volatile("ds_write2st64_b32 %0, %1, %2 offset0:%c3 offset1:%c4" :: "v"(k.a[ROW & 7]), "v"(a), "v"(b), "i"(ROW), "i"(ROW + 8) : "memory");
}
DI void kfrag(u32 fa, u32 fb, u32x4 (&A)[4]) {
  A[0] = lds_ld<u32x4>(fa); A[1] = lds_ld<u32x4>(fb); A[2] = lds_ld<u32x4>(fa + 128); A[3] = lds_ld<u32x4>(fb + 128);
}
DI f32x4 ktile(const u32x4 (&A)[4], const u32x4 (&V)[4]) {
  f32x4 acc = {0, 0, 0, 0};
#pragma unroll
  for (int ks = 0; ks < 4; ++ks) acc = mfma32(A[ks], V[ks], acc);
  return acc;
}
template <class F0, class F1>
DI void kgen(const u32 (&in)[6], float dt, const KW& k, u32 fa, u32 fb, u32 pb, F0&& hook0, F1&& hook1) {
  const float ar = dt * bc<float>(in[0] << 16), ai = dt * bc<float>(in[1] << 16);
  const f32x2 dB = {dt * bc<float>(in[2] << 16), dt * bc<float>(in[3] << 16)};
  const f32x2 Cc = {bc<float>(in[4] << 16), bc<float>(in[5] << 16)};
  const f32x2 CB = cmul(Cc, dB);
  const float x1 = ar * 1.44269504088896341f, p1 = ai * 0.15915494309189535f;
  const f32x2 E1 = cexp_rev(x1, p1), E8c = cexp_rev(8.f * x1, -8.f * p1), E128 = cexp_rev(128.f * x1, 128.f * p1);
  u32x4 V[4], A0[4], A1[4];
  {
    const f32x2 v2 = cmul(E8c, E8c), v3 = cmul(v2, E8c), v4 = cmul(v2, v2), v8 = cmul(v4, v4);
    const f32x2 v5 = cmul(v4, E8c), v6 = cmul(v4, v2), v7 = cmul(v4, v3);
    wrow2<0>(k, f32x2{1.f, 0.f}, v8); wrow2<1>(k, E8c, cmul(v8, E8c)); wrow2<2>(k, v2, cmul(v8, v2)); wrow2<3>(k, v3, cmul(v8, v3));
    wrow2<4>(k, v4, cmul(v8, v4)); wrow2<5>(k, v5, cmul(v8, v5)); wrow2<6>(k, v6, cmul(v8, v6)); wrow2<7>(k, v7, cmul(v8, v7));
    kfrag(fa, fb, V);
  }
  hook0();
  const f32x2 E2 = cmul(E1, E1), E3 = cmul(E2, E1), E4 = cmul(E2, E2), E5 = cmul(E4, E1), E6 = cmul(E4, E2), E7 = cmul(E4, E3);
  const f32x2 G1 = cmul(CB, E128);
  wrow2<0>(k, CB, G1); wrow2<1>(k, cmul(CB, E1), cmul(G1, E1)); wrow2<2>(k, cmul(CB, E2), cmul(G1, E2)); wrow2<3>(k, cmul(CB, E3), cmul(G1, E3));
  wrow2<4>(k, cmul(CB, E4), cmul(G1, E4)); wrow2<5>(k, cmul(CB, E5), cmul(G1, E5)); wrow2<6>(k, cmul(CB, E6), cmul(G1, E6)); wrow2<7>(k, cmul(CB, E7), cmul(G1, E7));
  kfrag(fa, fb, A0);
  const f32x2 G2 = cmul(G1, E128), G3 = cmul(G2, E128), G4 = cmul(G3, E128), G5 = cmul(G4, E128), G6 = cmul(G5, E128), G7 = cmul(G6, E128);
  wrow2<0>(k, G2, G4); wrow2<1>(k, cmul(G2, E2), cmul(G4, E4)); wrow2<2>(k, cmul(G2, E4), G5); wrow2<3>(k, cmul(G2, E6), cmul(G5, E4));
  wrow2<4>(k, G3, G6); wrow2<5>(k, cmul(G3, E2), cmul(G6, E4)); wrow2<6>(k, cmul(G3, E4), G7); wrow2<7>(k, cmul(G3, E6), cmul(G7, E4));
  kfrag(fa, fb, A1);
  hook1();
  __builtin_amdgcn_sched_barrier(0);
  const f32x4 a0 = ktile(A0, V);
  const f32x2 G8 = cmul(G7, E128), G9 = cmul(G8, E128), G10 = cmul(G9, E128), G11 = cmul(G10, E128);
  const f32x2 G12 = cmul(G11, E128), G13 = cmul(G12, E128), G14 = cmul(G13, E128), G15 = cmul(G14, E128);
  wrow2<0>(k, G8, cmul(G15, E128)); wrow<1>(k, G9); wrow<2>(k, G10); wrow<3>(k, G11);
  wrow<4>(k, G12); wrow<5>(k, G13); wrow<6>(k, G14); wrow<7>(k, G15);
  kfrag(fa, fb, A0);
  __builtin_amdgcn_sched_barrier(0);
  const f32x4 a1 = ktile(A1, V), a2 = ktile(A0, V);
  lds_st<f32x4>(pb, a0);
  lds_st<f32x4>(pb + 1024, a1);
  lds_st<f32x4>(pb + 2048, a2);
}

template <int PH>
DI void ssm_role(const Params& p, const u32 lb, const int j, u32* ctl, u32& gen STAMP_ARG) {
  constexpr bool P = PH == 0;
  const int tid = threadIdx.x, lane = tid & 63, w = __builtin_amdgcn_readfirstlane(tid >> 6), i16 = lane & 15, g = lane >> 4;
  const int d = 2 * j, ch = w >> 2, n = tid & 255;
  const f32x4 z4 = {0, 0, 0, 0};
  const rsrc_t rws = mkrs(p.ws, 16u << 20);
  if (P) __builtin_amdgcn_s_setprio(3);
  const int sb_ = j / 7, db_ = j - sb_ * 7;
  u32x4 xt_v = {0, 0, 0, 0};
  if (PH == 0 || PH == 1)
    xt_v = __builtin_amdgcn_raw_buffer_load_b128(mkrs(p.x, S_ * D_ * 2), (u32)((tid >> 3) * (D_ * 2) + (tid & 7) * 16), (u32)(64 * sb_ * (D_ * 2) + db_ * 128), 0);
  u32 in[6];
  u32x4 kt0 = {0, 0, 0, 0}; u32x2 kt1 = {0, 0};
  u32 ldt = 0;
  if (PH != 1) {
    const u32 vo = n * 2, so_ = (d + ch) * (NS * 2), nb = D_ * NS * 2;
    const u16* arr[6] = {p.are, p.aim, p.bre, p.bim, p.cre, p.cim};
#pragma unroll
    for (int a = 0; a < 6; ++a) in[a] = __builtin_amdgcn_raw_buffer_load_b16(mkrs(arr[a], nb), vo, so_, 0);
    ldt = *(const GAS u16*)(p.logdt + d + ch);
    kt0 = *(const GAS u32x4*)(KTAB.v + 8 * n);
    kt1 = *(const GAS u32x2*)(KTAB.v + 8 * n + 8);
  }
  if (P) {
    gen = __hip_atomic_load((GAS u32*)ctl + CT_GEN, RLX_AGENT);
  }
  auto transpose = [&]() {
    lds_st<u32x4>(lb + L_XP + (tid >> 3) * 144 + (tid & 7) * 16, xt_v);
    bar();
    const int chi = g + 4 * (w >> 2);
    const u32 a = lb + L_XP + (8 * chi + (i16 >> 2)) * 144 + (16 * (w & 3) + 4 * (i16 & 3)) * 2;
    st16<P>(rws, (u32)((16 * (w & 3) + i16) * XT_D + chi * 16), (u32)WS_XT + 64 * db_ * XT_D + sb_ * 128, cat(trread(a), trread(a + 4 * 144)));
  };
  if (PH == 1) { transpose(); return; }
  if (P) {
    if (tid == 0) lds_st<u32>(lb + L_CNT, 0u);
    __builtin_amdgcn_sched_barrier(0);
    transpose();
    __builtin_amdgcn_s_setprio(1);
    __builtin_amdgcn_sched_barrier(0);
  }
  KW kw;
  const u32 sb = lb + w * KSCR;
#pragma unroll
  for (int h = 0; h < 8; ++h) { kw.a[h] = sb + (((lane >> 2) ^ h) << 4) + (lane & 3) * 4; pin(kw.a[h]); }
  u32 fa = sb + i16 * 256 + ((g ^ (i16 & 3)) << 4) + ((i16 >> 2) & 1) * 64, fb = fa ^ 64;
  pin(fa); pin(fb);
  STAMP(1);
  kgen(in, bfr(expf(bc<float>(ldt << 16))), kw, fa, fb, sb + lane * 16,
       [&]() {
         if (P) {
           drain_vm();
           STAMP(5);
           if (lane == 0 && __hip_atomic_fetch_add((LAS u32*)(size_t)(lb + L_CNT), 1u, __ATOMIC_RELAXED, __HIP_MEMORY_SCOPE_WORKGROUP) == 7u)
             __hip_atomic_store((GAS u32*)ctl + CT_FX + (db_ * 32 + sb_) * FL, gen + 1u, RLX_AGENT);
         }
       },
       []() {});
  const u32 epoch = __builtin_amdgcn_readfirstlane(gen) + 1u;
  STAMP(2);
  if (P && w == 0) poll_flags(ctl + CT_FX + (d >> 6) * 32 * FL, 32, epoch);
  STAMP(6);
  bar();
  const u32x4 uv_ = ld16<P>(rws, n * 16, (u32)WS_XT + (d + ch) * XT_D);
  STAMP(3);
  if ((w & 3) < 3) {
    const u32 pb = lb + ch * 4 * KSCR + (w & 3) * 1024 + lane * 16;
    const f32x4 s = (lds_ld<f32x4>(pb) + lds_ld<f32x4>(pb + KSCR)) + (lds_ld<f32x4>(pb + 2 * KSCR) + lds_ld<f32x4>(pb + 3 * KSCR));
    const u32 tb = lb + L_KT + ch * 1600 + ((w & 3) * 256 + 64 * g + i16) * 2, a = pk(s[0], s[1]), cc = pk(s[2], s[3]);
    lds_st<u16>(tb, (u16)a); lds_st<u16>(tb + 32, (u16)(a >> 16));
    lds_st<u16>(tb + 64, (u16)cc); lds_st<u16>(tb + 96, (u16)(cc >> 16));
  } else {
    if (lane == 0) lds_st<u16>(lb + L_KT + ch * 1600 + 1536, (u16)0);
    if (lane < 32) lds_st<u32x4>(lb + L_U + ch * 4608 + lane * 16, u32x4{0, 0, 0, 0});
  }
  bar();
  STAMP(4);
  {
    const u32 ktb = lb + L_KT + ch * 1600;
    u32 e[11];
#pragma unroll
    for (int k = 0; k < 4; ++k) { e[2 * k] = lds_ld<u16>(ktb + (kt0[k] & 0xffffu)); e[2 * k + 1] = lds_ld<u16>(ktb + (kt0[k] >> 16)); }
    e[8] = lds_ld<u16>(ktb + (kt1[0] & 0xffffu)); e[9] = lds_ld<u16>(ktb + (kt1[0] >> 16)); e[10] = lds_ld<u16>(ktb + (kt1[1] & 0xffffu));
    const u32 k00 = lds_ld<u16>(ktb);
    u32 dw[5], al[5];
#pragma unroll
    for (int k = 0; k < 5; ++k) { dw[k] = e[2 * k] | (e[2 * k + 1] << 16); al[k] = e[2 * k + 1] | (e[2 * k + 2] << 16); }
    const u32 kb = lb + L_KC + ch * 4 * KCP + n * 16;
    lds_st<u32x4>(kb, u32x4{dw[0], dw[1], dw[2], dw[3]});
    lds_st<u32x4>(kb + KCP, u32x4{al[0], al[1], al[2], al[3]});
    lds_st<u32x4>(kb + 2 * KCP, u32x4{dw[1], dw[2], dw[3], dw[4]});
    lds_st<u32x4>(kb + 3 * KCP, u32x4{al[1], al[2], al[3], al[4]});
    if (n < 16) {
      u32x4 z = {0, 0, 0, 0};
      if (n == 0) z.x = k00;
      lds_st<u32x4>(lb + L_KC + (ch * 4 + (n >> 2)) * KCP + 4096 + (n & 3) * 16, z);
    }
    lds_st<u32x4>(lb + L_U + ch * 4608 + 512 + n * 16, uv_);
  }
  bar();
  STAMP(7);
  f32x4 acc[8];
  const int cc = (w >> 1) & 1, hf = w & 1;
  if (w < 4) {
#pragma unroll
    for (int c = 0; c < 8; ++c) acc[c] = z4;
    const int bsh = (-i16) & 3;
    u32 abase = lb + L_KC + (cc * 4 + bsh) * KCP + (2032 + 8 * g - i16 - bsh) * 2 - hf * 4 * 64 - 3 * 64 - 7 * 512;
    u32 bbase = lb + L_U + cc * 4608 + (256 - 16 + 16 * i16 + 8 * g) * 2 - hf * 4 * 64 - 3 * 64;
    u32 abase8 = abase + 8;
    pin(abase); pin(abase8); pin(bbase);
    u32x4 A[8], B[8];
#pragma unroll
    for (int qq = 0; qq < 8; ++qq) {
      B[7 - qq] = lds_ld<u32x4>(bbase + (7 - qq) * 512 + 3 * 64);
      A[qq] = cat(lds_ld<u32x2>(abase + (7 - qq) * 512 + 3 * 64), lds_ld<u32x2>(abase8 + (7 - qq) * 512 + 3 * 64));
      __builtin_amdgcn_sched_barrier(0);
    }
#pragma unroll
    for (int rr = 0; rr < 4; ++rr) {
#pragma unroll
      for (int qq = 0; qq < 8; ++qq) {
#pragma unroll
        for (int m = 7 - qq; m >= 0; --m) {
          acc[qq + m] = mfma32(A[qq], B[m], acc[qq + m]);
          asm volatile("s_nop %c0" :: "i"(CNOP));
        }
        if (rr < 3) {
          A[qq] = cat(lds_ld<u32x2>(abase + (7 - qq) * 512 + (2 - rr) * 64), lds_ld<u32x2>(abase8 + (7 - qq) * 512 + (2 - rr) * 64));
          B[7 - qq] = lds_ld<u32x4>(bbase + (7 - qq) * 512 + (2 - rr) * 64);
        }
        __builtin_amdgcn_sched_barrier(0);
      }
    }
    if (hf) {
#pragma unroll
      for (int c = 0; c < 4; ++c) lds_st<f32x4>(lb + L_RED + (w * 4 + c) * 1024 + lane * 16, acc[c]);
#pragma unroll
      for (int c = 0; c < 4; ++c) acc[c] = acc[4 + c];
    } else {
#pragma unroll
      for (int c = 0; c < 4; ++c) lds_st<f32x4>(lb + L_RED + (w * 4 + c) * 1024 + lane * 16, acc[4 + c]);
    }
  }
  bar();
  STAMP(8);
  if (w < 4) {
    const float dk = b2f(p.dskip[d + cc]);
    const u32 tb = (1024 * hf + 16 * i16 + 4 * g) * 2;
    f32x4 o[4];
    u32x2 uv[4];
#pragma unroll
    for (int c = 0; c < 4; ++c) {
      o[c] = lds_ld<f32x4>(lb + L_RED + ((w ^ 1) * 4 + c) * 1024 + lane * 16);
      uv[c] = lds_ld<u32x2>(lb + L_U + cc * 4608 + 512 + tb + c * 512);
    }
#pragma unroll
    for (int c = 0; c < 4; ++c) {
      const f32x4 y = acc[c] + o[c];
      const f32x2 b01 = up2(pk(y[0], y[1])) + up2(pk2(up2(uv[c].x) * dk));
      const f32x2 b23 = up2(pk(y[2], y[3])) + up2(pk2(up2(uv[c].y) * dk));
      lds_st<u32x2>(lb + L_BO + cc * 4096 + tb + c * 512, u32x2{pk2(b01), pk2(b23)});
    }
  }
  bar();
  STAMP(9);
  {
    const int st = w & 3, s2 = 16 * st + i16;
    const u32 a = lb + L_BO + ch * 4096 + (8 * g + (i16 >> 2)) * 128 + (16 * st + 4 * (i16 & 3)) * 2;
    st16<P>(rws, (u32)((((s2 & 7) * 8 + (s2 >> 3)) * 32 + 8 * g) * 2), (u32)WS_B + (d + ch) * B_D, cat(trread(a), trread(a + 4 * 128)));
  }
  STAMP(10);
  if (P) { drain_vm(); bar(); publish(ctl + CT_FB, j, epoch); }
  STAMP(11);
}

template <int PH>
__global__ __launch_bounds__(NT, 4) void gfs4_kernel(Params p) {
  extern __shared__ __attribute__((aligned(16))) char smem[];
  const u32 lb = (u32)(size_t)(LAS char*)smem;
  const int b = blockIdx.x;
  u32* ctl = (u32*)(p.ws + WS_CTL);
  STAMP_DECL;
  if (PH == 0 || PH == 1) STAMP(0); else if (PH == 2) STAMP(20); else STAMP(21);
  u32 gen = 0;
  if (b < N_FFT) {
    if (threadIdx.x >= 256) return;
    fft_role<PH>(p, lb, b, ctl, gen STAMP_PASS);
  }
  else ssm_role<PH>(p, lb, b - N_FFT, ctl, gen STAMP_PASS);
  STAMP_FLUSH();
}

extern "C" void kernel_launch(void* const* d_in, const int* in_sizes, int n_in,
                              void* d_out, int out_size, void* d_ws, size_t ws_size,
                              hipStream_t stream) {
  (void)in_sizes; (void)n_in; (void)out_size; (void)ws_size;
  Params p;
  p.x = (const u16*)d_in[0]; p.wr = (const u16*)d_in[1]; p.wi = (const u16*)d_in[2];
  p.are = (const u16*)d_in[4]; p.aim = (const u16*)d_in[5]; p.bre = (const u16*)d_in[6]; p.bim = (const u16*)d_in[7];
  p.cre = (const u16*)d_in[8]; p.cim = (const u16*)d_in[9]; p.logdt = (const u16*)d_in[10]; p.dskip = (const u16*)d_in[11];
  p.gw = (const u16*)d_in[12];
  p.out = (u16*)d_out; p.ws = (char*)d_ws;
  (void)hipFuncSetAttribute((const void*)gfs4_kernel<0>, hipFuncAttributeMaxDynamicSharedMemorySize, LDS_BYTES);
  gfs4_kernel<0><<<N_WG, NT, LDS_BYTES, stream>>>(p);
}
```

```cpp
#include <hip/hip_runtime.h>
#include <hip/hip_bf16.h>
#include <stdint.h>

typedef unsigned int u32;
typedef unsigned short u16;
typedef unsigned long long u64;
typedef float f32x4 __attribute__((ext_vector_type(4)));
typedef float f32x2 __attribute__((ext_vector_type(2)));
typedef u32 u32x4 __attribute__((ext_vector_type(4)));
typedef u32 u32x2 __attribute__((ext_vector_type(2)));
typedef __bf16 bf16x8 __attribute__((ext_vector_type(8)));
typedef __bf16 bf16x2 __attribute__((ext_vector_type(2)));
typedef short s16x4 __attribute__((ext_vector_type(4)));
#define LAS __attribute__((address_space(3)))
#define GAS __attribute__((address_space(1)))
#define DI __device__ __forceinline__

#define DIAG 5
#define NLAUNCH 1

constexpr int S_ = 2048, D_ = 448, DF = 225, NS = 256;
constexpr int N_FFT = 256, N_SSM = 224, N_WG = N_FFT + N_SSM, NT = 512;

constexpr u32 T_K1 = 65536 + 256;
constexpr u32 TP_S2 = 32768 + 128;
constexpr u32 XT_D = 4096 + 128;
constexpr u32 B_D = 4096 + 128;
constexpr size_t WS_CTL = 0;
constexpr size_t WS_T   = 1u << 20;
constexpr size_t WS_TP  = WS_T + (3u << 20);
constexpr size_t WS_XT  = WS_TP + (3u << 20);
constexpr size_t WS_B   = WS_XT + (2u << 20);
constexpr int FL = 32, FR_ = 256 * FL + 64;
constexpr int CT_GEN = 0;
constexpr int CT_F1 = 64;
constexpr int CT_F2 = CT_F1 + 8 * FR_;
constexpr int CT_FB = CT_F2 + 8 * FR_;
constexpr int CT_FX = CT_FB + 8 * FR_;
static_assert((CT_FX + 7 * 32 * FL) * 4 <= (1 << 20), "control block");

constexpr int XRS = 928;
constexpr int L_XT = 0;
constexpr int L_XCH = 29696;
constexpr int TPRS = 1040;
constexpr int L_TP = 0;
constexpr int L_TO = 29696;
constexpr int L_TI = 38912;
constexpr int L_BI = 38912;
constexpr int BRS = 912;
constexpr int L_Z = 47104;
constexpr int L_C1 = 38912;
constexpr int L_C2 = 65536;
constexpr int L_W = 47104;
constexpr int L_G = 70208;
constexpr int KCP = 4160;
constexpr int L_KC = 0;
constexpr int KSCR = 4096;
constexpr int L_RED = 33280;
constexpr int L_BO = 49664;
constexpr int L_U = 57856;
constexpr int L_XP = 57856;
constexpr int L_KT = 67072;
constexpr int L_CNT = 70272;
constexpr int LDS_BYTES = 70288;

struct Params {
  const u16* x; const u16* wr; const u16* wi;
  const u16* are; const u16* aim; const u16* bre; const u16* bim; const u16* cre; const u16* cim;
  const u16* logdt; const u16* dskip; const u16* gw;
  u16* out; char* ws;
};

constexpr double cx_sin_oct(double x) { double x2 = x * x, t = x, s = x; for (int i = 1; i < 9; ++i) { t *= -x2 / ((2 * i) * (2 * i + 1)); s += t; } return s; }
constexpr double cx_cos_oct(double x) { double x2 = x * x, t = 1, s = 1; for (int i = 1; i < 9; ++i) { t *= -x2 / ((2 * i - 1) * (2 * i)); s += t; } return s; }
constexpr double cx_sin(long n, long d) {
  n %= d; if (n < 0) n += d;
  const long o = (8 * n) / d;
  const double r = 6.283185307179586476925 * (double)(8 * n - o * d) / (8.0 * (double)d), q = 0.78539816339744830962;
  switch (o) {
    case 0: return cx_sin_oct(r);       case 1: return cx_cos_oct(q - r);
    case 2: return cx_cos_oct(r);       case 3: return cx_sin_oct(q - r);
    case 4: return -cx_sin_oct(r);      case 5: return -cx_cos_oct(q - r);
    case 6: return -cx_cos_oct(r);      default: return -cx_sin_oct(q - r);
  }
}
constexpr double cx_cos(long n, long d) { return cx_sin(4 * n + d, 4 * d); }
constexpr u32 cx_f32(double v) { return __builtin_bit_cast(u32, (float)v); }
constexpr u32 cx_bf(double v) { const u32 u = cx_f32(v); return (u + 0x7fffu + ((u >> 16) & 1u)) >> 16; }
constexpr int cx_pi(int g, int jj) { return 16 * (jj >> 2) + 4 * g + (jj & 3); }
struct Frag { u32 v[64][4]; };
enum { TF_FC0, TF_FS0, TF_CO0, TF_SO0, TF_COT0, TF_SOT0, TF_TC, TF_TS, TF_M, TF_Q0, TF_N = TF_Q0 + 24 };
constexpr Frag make_frag(int item) {
  Frag f{};
  for (int lane = 0; lane < 64; ++lane) {
    const int i16 = lane & 15, g = lane >> 4;
    if (item < TF_TC) {
      const int sn = item & 1;
      for (int jj = 0; jj < 8; ++jj) {
        const int m = cx_pi(g, jj), a = i16;
        long n = 0, d = 32;
        if (item < TF_CO0) { n = a * m; d = 32; }
        else if (item < TF_COT0) { n = a * (2 * m + 1); d = 64; }
        else { n = m * (2 * a + 1); d = 64; }
        f.v[lane][jj >> 1] |= cx_bf(sn ? cx_sin(n, d) : cx_cos(n, d)) << (16 * (jj & 1));
      }
    } else if (item == TF_TC || item == TF_TS) {
      for (int j = 0; j < 4; ++j) {
        const long n = (2 * g + (j & 1)) * (16 * (j >> 1) + i16);
        f.v[lane][j] = cx_f32(item == TF_TC ? cx_cos(n, 448) : cx_sin(n, 448));
      }
    } else if (item == TF_M) {
      for (int j = 0; j < 4; ++j) {
        const int k0 = 2 * g + (j >> 1);
        const double cf = ((k0 == 0 || k0 == 7) ? 1.0 : 2.0) / (2048.0 * 448.0);
        const double v = i16 < 14 ? cf * ((j & 1) ? -cx_sin(k0 * i16, 14) : cx_cos(k0 * i16, 14)) : 0.0;
        f.v[lane][j >> 1] |= cx_bf(v) << (16 * (j & 1));
      }
    } else {
      const int q = (item - TF_Q0) / 6, sub = (item - TF_Q0) % 6, k0 = 2 * q + ((i16 >> 1) & 1), ri = i16 & 1;
      if (sub < 2) {
        if ((i16 >> 2) == 2 * sub + (g & 1))
          for (int j = 0; j < 8; ++j) {
            const int d0 = 8 * (g >> 1) + j;
            const double v = d0 < 14 ? (ri ? -cx_sin(d0 * k0, 14) : cx_cos(d0 * k0, 14)) : 0.0;
            f.v[lane][j >> 1] |= cx_bf(v) << (16 * (j & 1));
          }
      } else {
        for (int j = 0; j < 4; ++j) {
          const long n = cx_pi(g, 4 * (sub & 1) + j) * k0;
          f.v[lane][j] = cx_f32(sub < 4 ? cx_cos(n, 448) : (ri ? -cx_sin(n, 448) : cx_sin(n, 448)));
        }
      }
    }
  }
  return f;
}
template <int I> struct FragC { static constexpr Frag f = make_frag(I); };
#define FR(i) FragC<(i)>::f
#define FR6(i) FR(i), FR(i + 1), FR(i + 2), FR(i + 3), FR(i + 4), FR(i + 5)
__device__ const __attribute__((aligned(1024))) Frag TF[TF_N] = {FR6(0), FR(6), FR(7), FR(8), FR6(9), FR6(15), FR6(21), FR6(27)};

struct KTab { u16 v[2080]; };
constexpr KTab make_ktab() {
  KTab t{};
  for (int i = 0; i < 2080; ++i) {
    const int l = 2048 - i;
    int idx = 768;
    if (l >= 0) {
      const int sh = l > 256 ? (l >= 1024 ? 3 : (l >= 512 ? 2 : 1)) : 0;
      const int v = sh ? (((l + (1 << (sh - 1)) - 1 + ((l >> sh) & 1)) >> sh) << sh) : l;
      if (v < 256) idx = ((v & 7) + 8 * (v >> 7)) * 16 + ((v >> 3) & 15);
      else if (v < 512) idx = 256 + ((((v - 256) >> 1) & 3) + 4 * ((v - 256) >> 7)) * 16 + (((v - 256) >> 3) & 15);
      else if (v < 1024) idx = 256 + (8 + (((v - 512) >> 2) & 1) + 2 * ((v - 512) >> 7)) * 16 + (((v - 512) >> 3) & 15);
      else if (v < 2048) idx = 512 + ((v - 1024) >> 7) * 16 + (((v - 1024) >> 3) & 15);
      else idx = 640;
    }
    t.v[i] = (u16)(2 * idx);
  }
  return t;
}
__device__ const __attribute__((aligned(16))) KTab KTAB = make_ktab();

template <class T, class F> DI T bc(F v) { return __builtin_bit_cast(T, v); }
DI f32x4 mfma32(u32x4 a, u32x4 b, f32x4 c) { return __builtin_amdgcn_mfma_f32_16x16x32_bf16(bc<bf16x8>(a), bc<bf16x8>(b), c, 0, 0, 0); }
DI f32x4 mfma16(u32x2 a, u32x2 b, f32x4 c) { return __builtin_amdgcn_mfma_f32_16x16x16bf16_1k(bc<s16x4>(a), bc<s16x4>(b), c, 0, 0, 0); }
DI u32 pk(float a, float b) { f32x2 v = {a, b}; return bc<u32>(__builtin_convertvector(v, bf16x2)); }
DI u32 pk2(f32x2 v) { return bc<u32>(__builtin_convertvector(v, bf16x2)); }
DI float bfr(float a) { return bc<float>(pk(0.f, a) & 0xffff0000u); }
DI float lo16(u32 v) { return bc<float>(v << 16); }
DI float hi16(u32 v) { return bc<float>(v & 0xffff0000u); }
DI f32x2 up2(u32 v) { return f32x2{lo16(v), hi16(v)}; }
DI float b2f(u16 v) { return bc<float>((u32)v << 16); }
DI u32x2 trread(u32 a) { return bc<u32x2>(__builtin_amdgcn_ds_read_tr16_b64_v4i16((LAS s16x4*)(size_t)a)); }
DI u32x4 cat(u32x2 a, u32x2 b) { u32x4 r = {a.x, a.y, b.x, b.y}; return r; }
DI float sinr(float x) { return __builtin_amdgcn_sinf(x); }
DI float cosr(float x) { return __builtin_amdgcn_cosf(x); }
DI float fx1(float v) { return __builtin_amdgcn_update_dpp(v, v, 0xB1, 0xf, 0xf, true); }
DI float fr8(float v) { return __builtin_amdgcn_update_dpp(v, v, 0x128, 0xf, 0xf, true); }
DI u32 ux1(u32 v) { return (u32)__builtin_amdgcn_update_dpp((int)v, (int)v, 0xB1, 0xf, 0xf, true); }
DI u32 ur8(u32 v) { return (u32)__builtin_amdgcn_update_dpp((int)v, (int)v, 0x128, 0xf, 0xf, true); }
DI u32x4 sx1(u32x4 v, u32 m) { u32x4 r = {ux1(v.x) ^ m, ux1(v.y) ^ m, ux1(v.z) ^ m, ux1(v.w) ^ m}; return r; }
DI u32x4 sr8(u32x4 v, u32 m) { u32x4 r = {ur8(v.x) ^ m, ur8(v.y) ^ m, ur8(v.z) ^ m, ur8(v.w) ^ m}; return r; }
DI u32x4 xr(u32x4 v, u32 m) { u32x4 r = {v.x ^ m, v.y ^ m, v.z ^ m, v.w ^ m}; return r; }
DI u32x4 pk8(const float (&v)[8]) { return u32x4{pk(v[0], v[1]), pk(v[2], v[3]), pk(v[4], v[5]), pk(v[6], v[7])}; }
template <class T> DI T lds_ld(u32 a) { return *(LAS T*)(size_t)a; }
template <class T> DI void lds_st(u32 a, T v) { *(LAS T*)(size_t)a = v; }
DI void wait_vm0() { __builtin_amdgcn_s_waitcnt(0x0F70); }
DI void drain_vm() { asm volatile("s_waitcnt vmcnt(0)" ::: "memory"); }
DI void bar() { __builtin_amdgcn_s_waitcnt(0xC07F); asm volatile("s_barrier" ::: "memory"); }
template <class T> DI void pin(T& v) { asm volatile("" : "+v"(v)); }
typedef __amdgpu_buffer_rsrc_t rsrc_t;
DI rsrc_t mkrs(const void* p, u32 bytes) { return __builtin_amdgcn_make_buffer_rsrc((void*)p, 0, bytes, 0x00020000); }
template <int AUX> DI void dma16(rsrc_t rs, u32 lds, u32 voff, u32 soff) { __builtin_amdgcn_raw_ptr_buffer_load_lds(rs, (LAS void*)(size_t)lds, 16, voff, soff, 0, AUX); }
DI f32x2 cmul(f32x2 x, f32x2 y) {
  f32x2 t;
  asm("v_pk_mul_f32 %0, %1, %2 op_sel:[0,0] op_sel_hi:[0,1]\n\tv_pk_fma_f32 %0, %1, %2, %0 op_sel:[1,1,0] op_sel_hi:[1,0,1] neg_lo:[1,0,0]" : "=&v"(t) : "v"(x), "v"(y));
  return t;
}
DI float dot2b(u32 a, u32 b, float c) { return __builtin_amdgcn_fdot2_f32_bf16(bc<bf16x2>(a), bc<bf16x2>(b), c, false); }
DI float wave_sum(float a) {
  a += __builtin_amdgcn_update_dpp(0.f, a, 0xB1, 0xf, 0xf, true);
  a += __builtin_amdgcn_update_dpp(0.f, a, 0x4E, 0xf, 0xf, true);
  a += __builtin_amdgcn_update_dpp(0.f, a, 0x141, 0xf, 0xf, true);
  a += __builtin_amdgcn_update_dpp(0.f, a, 0x140, 0xf, 0xf, true);
  a += __builtin_amdgcn_update_dpp(0.f, a, 0x142, 0xa, 0xf, false);
  a += __builtin_amdgcn_update_dpp(0.f, a, 0x143, 0xc, 0xf, false);
  return bc<float>(__builtin_amdgcn_readlane(bc<int>(a), 63));
}
DI int pi_k(int g, int jj) { return 16 * (jj >> 2) + 4 * g + (jj & 3); }

#define CNOP 1
#define SH2PRIO 2
#define SLP 2
#define RLX_AGENT __ATOMIC_RELAXED, __HIP_MEMORY_SCOPE_AGENT
template <bool P> DI void st16(rsrc_t rs, u32 voff, u32 soff, u32x4 v) {
  __builtin_amdgcn_raw_buffer_store_b128(v, rs, voff, soff, P ? 16 : 0);
  asm volatile("s_nop 1" :: "v"(v) : "memory");
}
template <bool P> DI u32x4 ld16(rsrc_t rs, u32 voff, u32 soff) { return __builtin_amdgcn_raw_buffer_load_b128(rs, voff, soff, P ? 16 : 0); }
DI void publish(u32* set, int idx, u32 epoch) {
  if (threadIdx.x < 8) __hip_atomic_store((GAS u32*)set + threadIdx.x * FR_ + idx * FL, epoch, RLX_AGENT);
}
DI void poll_flags(const u32* first, int n, u32 epoch) {
  const int lane = threadIdx.x & 63;
  const GAS u32* f = (const GAS u32*)first + (lane < n ? lane : 0) * FL;
  for (int spin = 0; spin < (1 << 18); ++spin) {
    if (__all(__hip_atomic_load(f, RLX_AGENT) == epoch)) break;
    __builtin_amdgcn_s_sleep(SLP);
  }
}

#define STAMP(i) do {} while (0)
#define STAMP_DECL do {} while (0)
#define STAMP_FLUSH() do {} while (0)
#define STAMP_ARG
#define STAMP_PASS

DI u32x4 tf(rsrc_t rs, int item, u32 l16) { return __builtin_amdgcn_raw_buffer_load_b128(rs, l16 + (item & 3) * 1024, (item >> 2) * 4096, 0); }
DI u32x4 tfs(rsrc_t rs, int item, u32 l16, u32 so) { return __builtin_amdgcn_raw_buffer_load_b128(rs, l16 + item * 1024, so, 0); }

template <int PH>
DI void fft_role(const Params& p, const u32 lb, const int b, u32* ctl, u32& gen STAMP_ARG) {
  constexpr bool P = PH == 0;
  constexpr int AUX = P ? 16 : 0;
  const int tid = threadIdx.x, lane = tid & 63, w = __builtin_amdgcn_readfirstlane(tid >> 6), i16 = lane & 15, g = lane >> 4;
  const u32 l16 = lane * 16;
  const int s2 = b & 63, q = b >> 6;
  const int xx = b & 7, mm = (b >> 3) & 3, kq = b >> 5;
  const int k1w = (((mm & 1) ? (xx ? 32 - xx : 8) : xx) + 16 * (mm >> 1)) & 31;
  const f32x4 z4 = {0, 0, 0, 0};
  const rsrc_t rws = mkrs(p.ws, 16u << 20), rtf = mkrs(TF, TF_N * 1024);
  const u32 tq = (TF_Q0 + 6 * q) * 1024;
  if (P) __builtin_amdgcn_s_setprio(3);

  if (PH == 0 || PH == 1) {
    const rsrc_t rx = mkrs(p.x, S_ * D_ * 2);
    if (lane < 56) {
#pragma unroll
      for (int r = 0; r < 8; ++r) dma16<0>(rx, lb + L_XT + (8 * w + r) * XRS, l16, (64 * (8 * w + r) + s2) * (D_ * 2));
    }
  }
  if (P) gen = __hip_atomic_load((GAS u32*)ctl + CT_GEN, RLX_AGENT);
  const u32 tpo = (u32)((tid >> 3) * TP_S2 + (tid & 7) * 16), tps = (u32)WS_TP + k1w * 1024 + kq * 128;
  u32x4 fc[2], fs[2];
  if (PH == 0 || PH == 1) {
    dma16<0>(rtf, lb + L_C1 + w * 2048, l16, w == 0 ? 0u : tq + (2 * w - 2) * 1024);
    dma16<0>(rtf, lb + L_C1 + w * 2048 + 1024, l16, w == 0 ? 1024u : tq + (2 * w - 1) * 1024);
  } else {
    fc[0] = tf(rtf, TF_FC0, l16); fs[0] = tf(rtf, TF_FS0, l16);
    fc[1] = xr(fc[0], 0x80000000u); fs[1] = xr(fs[0], 0x80000000u);
  }

  u32x4 co[2], so[2], cot[2], sot[2];
  auto load_p2 = [&]() {
    const bool mir = kq >= 4;
    const int col0 = mir ? 448 - (56 * kq + 49) : 56 * kq;
    const rsrc_t rr = mkrs(p.wr, S_ * DF * 2), ri_ = mkrs(p.wi, S_ * DF * 2);
#pragma unroll
    for (int i = 0; i < 3; ++i) {
      const int gi = w + 4 * i;
      if (gi < 9) {
        const int gam = 64 * gi + lane, k2 = (gam * 7282) >> 16, chunk = gam - 9 * k2;
        const int ks = k1w + 32 * k2, row = mir ? ((2048 - ks) & 2047) : ks;
        const u32 vo = (u32)(((row * DF + col0) * 2) & ~15) + chunk * 16;
        dma16<0>(rr, lb + L_W + gi * 1024, vo, 0);
        dma16<0>(ri_, lb + L_W + 9216 + gi * 1024, vo, 0);
      }
    }
    dma16<0>(rtf, lb + L_C2 + w * 1024, l16, (TF_CO0 + w) * 1024);
  };

  if (PH == 0 || PH == 1) {
    u32x4 C1, S1, nS1;
    {
      const float k1 = (float)(16 * (w >> 1) + i16) * (1.0f / 2048.0f), P0 = k1 * (float)(64 * g + s2), Pl = k1 * 256.f;
      float c[8], s[8];
#pragma unroll
      for (int j = 0; j < 8; ++j) { const float ph = __builtin_fmaf(Pl, (float)j, P0); c[j] = cosr(ph); s[j] = sinr(ph); }
      C1 = pk8(c); S1 = pk8(s); nS1 = xr(S1, 0x80008000u);
      pin(C1); pin(S1); pin(nS1);
    }
    int d0a = 8 * (g >> 1) + (i16 >> 2), d0b = d0a + 4;
    d0b = d0b < 14 ? d0b : 0;
    u32 xa = lb + L_XT + (8 * w + (g & 1)) * XRS + 8 * (i16 & 3);
    u32 xb = xa + d0b * 64; xa += d0a * 64;
    u32 xw = lb + L_XCH + w * 256 + lane * 4;
    u32 xr_ = lb + L_XCH + (w & 1) * 4096 + lane * 4;
    u32 toff = (u32)((16 * (w >> 1) + i16) * T_K1 + 4 * (w & 1) * 32 + g * 16);
    u32 imm = (i16 & 1) ? 0x80008000u : 0u;
    pin(xa); pin(xb); pin(xw); pin(xr_); pin(toff); pin(imm);
    const u32 tso = (u32)WS_T + s2 * 1024 + q * 256;
    STAMP(1);
    wait_vm0();
    bar();
    const u32 cb = lb + L_C1 + l16;
    fc[0] = lds_ld<u32x4>(cb); fs[0] = lds_ld<u32x4>(cb + 1024);
    const u32x4 Ba1 = lds_ld<u32x4>(cb + 2048), Ba2 = lds_ld<u32x4>(cb + 3072);
    const f32x4 twc0 = lds_ld<f32x4>(cb + 4096), twc1 = lds_ld<f32x4>(cb + 5120), tws0 = lds_ld<f32x4>(cb + 6144), tws1 = lds_ld<f32x4>(cb + 7168);
    fc[1] = xr(fc[0], 0x80000000u); fs[1] = xr(fs[0], 0x80000000u);
    STAMP(2);
    f32x4 D2[2][2];
#pragma unroll
    for (int sg = 0; sg < 2; ++sg) {
      f32x4 H[2];
#pragma unroll
      for (int td = 0; td < 2; ++td) {
        const int o = td * 32 + sg * 4 * XRS;
        u32x4 A0 = cat(trread(xa + o), trread(xb + o));
        u32x4 A1 = cat(trread(xa + o + 2 * XRS), trread(xb + o + 2 * XRS));
        H[td] = mfma32(A1, Ba2, mfma32(A0, Ba1, z4));
      }
      float h[8];
#pragma unroll
      for (int j = 0; j < 4; ++j) {
        h[j] = __builtin_fmaf(fx1(H[0][j]), tws0[j], H[0][j] * twc0[j]);
        h[4 + j] = __builtin_fmaf(fx1(H[1][j]), tws1[j], H[1][j] * twc1[j]);
      }
      u32x4 A = pk8(h), A2 = sx1(A, imm);
#pragma unroll
      for (int tk = 0; tk < 2; ++tk) D2[sg][tk] = mfma32(A2, fs[tk], mfma32(A, fc[tk], z4));
    }
#pragma unroll
    for (int tk = 0; tk < 2; ++tk)
#pragma unroll
      for (int r = 0; r < 4; ++r) lds_st<u32>(xw + (tk * 4 + r) * 1024, pk(D2[0][tk][r], D2[1][tk][r]));
    bar();
    STAMP(3);
    {
      u32 pc[4][2];
#pragma unroll
      for (int kk = 0; kk < 2; ++kk) {
        const u32 br = xr_ + kk * 2048;
        u32x4 Xr = {lds_ld<u32>(br), lds_ld<u32>(br + 256), lds_ld<u32>(br + 512), lds_ld<u32>(br + 768)};
        u32x4 Xi = {lds_ld<u32>(br + 1024), lds_ld<u32>(br + 1280), lds_ld<u32>(br + 1536), lds_ld<u32>(br + 1792)};
        f32x4 Tr = mfma32(Xi, S1, mfma32(Xr, C1, z4));
        f32x4 Ti = mfma32(Xr, nS1, mfma32(Xi, C1, z4));
        pc[kk][0] = pk(Tr[0], Tr[1]); pc[kk][1] = pk(Tr[2], Tr[3]);
        pc[2 + kk][0] = pk(Ti[0], Ti[1]); pc[2 + kk][1] = pk(Ti[2], Ti[3]);
      }
      u32x4 lo = {pc[0][0], pc[0][1], pc[1][0], pc[1][1]}, hi = {pc[2][0], pc[2][1], pc[3][0], pc[3][1]};
#pragma unroll
      for (int e = 0; e < 4; ++e) {
        auto r1 = __builtin_amdgcn_permlane16_swap(lo[e], hi[e], false, false);
        auto r2 = __builtin_amdgcn_permlane32_swap(r1[0], r1[1], false, false);
        lo[e] = r2[0]; hi[e] = r2[1];
      }
      st16<P>(rws, toff, tso, lo);
      st16<P>(rws, toff + 64, tso, hi);
    }
    STAMP(4);
    if (P) {
      __builtin_amdgcn_sched_barrier(0);
      load_p2();
      __builtin_amdgcn_sched_barrier(0);
      asm volatile("s_waitcnt vmcnt(5)" ::: "memory");
      bar();
      publish(ctl + CT_F1, b, gen + 1u);
      __builtin_amdgcn_s_setprio(2);
    }
    STAMP(5);
  }
  const u32 epoch = __builtin_amdgcn_readfirstlane(gen) + 1u;

  if (PH == 2) load_p2();
  if (PH == 0 || PH == 2) {
    const int ri = i16 >> 3;
    u32 tsv = (u32)((lane >> 1) * 1024 + w * 256 + (lane & 1) * 16);
    u32 tiw = lb + L_TI + w * 2048 + ((lane >> 1) & 1) * 1024 + (lane >> 2) * 32 + (lane & 1) * 16;
    u32 ta = lb + L_TI + w * 2048 + (4 * g + (i16 >> 2)) * 32 + (i16 & 3) * 8;
    u32 tow = lb + L_TO + 2 * i16 * 144 + w * 32 + g * 8;
    u32 tor = lb + L_TO + (tid >> 3) * 144 + (tid & 7) * 16;
    u32 mf = ri ? 0x80008000u : 0u, mi = ri ? 0u : 0x80008000u;
    float wr_[16], wi_[16];
    {
      const bool mir = kq >= 4;
      const int c = i16 & 7, kd = 2 * w + (c >> 2) + 14 * (4 * kq + (c & 3)), col0 = mir ? 448 - (56 * kq + 49) : 56 * kq;
      const int row0 = mir ? ((2048 - k1w) & 2047) : k1w;
      u32 wa = lb + L_W + 4 * g * 144 + (((row0 * DF + col0) * 2) & 15) + ((mir ? 448 - kd : kd) - col0) * 2;
      const u32 sg = (mir != (ri != 0)) ? 0u : 0x80000000u;
      pin(tsv); pin(tiw); pin(ta); pin(tow); pin(tor); pin(mf); pin(mi); pin(wa);
      STAMP(7);
      if (P) poll_flags(ctl + CT_F1 + (b & 7) * FR_ + w * 64 * FL, 64, epoch);
      wait_vm0();
      bar();
      const u32 so_ = (u32)WS_T + k1w * T_K1 + kq * 32;
      const u32x4 t0 = ld16<P>(rws, tsv, so_), t1 = ld16<P>(rws, tsv, so_ + 32 * 1024);
      __builtin_amdgcn_sched_barrier(0);
      co[0] = lds_ld<u32x4>(lb + L_C2 + l16); so[0] = lds_ld<u32x4>(lb + L_C2 + 1024 + l16);
      cot[0] = lds_ld<u32x4>(lb + L_C2 + 2048 + l16); sot[0] = lds_ld<u32x4>(lb + L_C2 + 3072 + l16);
      co[1] = xr(so[0], 0x00008000u);
      so[1] = xr(co[0], 0x80000000u);
      cot[1] = xr(cot[0], 0x80000000u); sot[1] = xr(sot[0], 0x80000000u);
      pin(co[0]); pin(co[1]); pin(so[0]); pin(so[1]); pin(cot[0]); pin(cot[1]); pin(sot[0]); pin(sot[1]);
#pragma unroll
      for (int j = 0; j < 16; ++j) {
        const int o = (32 * (j >> 3) + 16 * ((j >> 2) & 1) + (j & 3)) * 144;
        wr_[j] = bc<float>((u32)lds_ld<u16>(wa + o) << 16);
        wi_[j] = bc<float>(((u32)lds_ld<u16>(wa + 9216 + o) << 16) + sg);
      }
#pragma unroll
      for (int j = 0; j < 16; ++j) { pin(wr_[j]); pin(wi_[j]); }
      __builtin_amdgcn_sched_barrier(0);
      if (P) __builtin_amdgcn_s_setprio(3);
      STAMP(8);
      lds_st<u32x4>(tiw, t0);
      lds_st<u32x4>(tiw + 512, t1);
    }
    STAMP(9);
    f32x4 E[2], O[2];
    {
      u32x4 B0 = cat(trread(ta), trread(ta + 512)), B1 = cat(trread(ta + 1024), trread(ta + 1536));
      u32x4 B0s = sr8(B0, mf), B1s = sr8(B1, mf);
#pragma unroll
      for (int t = 0; t < 2; ++t) {
        E[t] = mfma32(fs[t], B0s, mfma32(fc[t], B0, z4));
        O[t] = mfma32(so[t], B1s, mfma32(co[t], B1, z4));
      }
    }
    float Pp[8], Mm[8];
#pragma unroll
    for (int j = 0; j < 8; ++j) {
      const float e = E[j >> 2][j & 3], o = O[j >> 2][j & 3];
      const float ylo = e + o, yhi = e - o;
      const float vlo = __builtin_fmaf(fr8(ylo), wi_[j], ylo * wr_[j]);
      const float vhi = __builtin_fmaf(fr8(yhi), wi_[8 + j], yhi * wr_[8 + j]);
      Pp[j] = vlo + vhi; Mm[j] = vlo - vhi;
    }
    {
      u32x4 AP = pk8(Pp), AM = pk8(Mm);
      u32x4 APs = sr8(AP, mi), AMs = sr8(AM, mi);
#pragma unroll
      for (int t = 0; t < 2; ++t) {
        f32x4 ze = mfma32(APs, fs[t], mfma32(AP, fc[t], z4));
        f32x4 zo = mfma32(AMs, sot[t], mfma32(AM, cot[t], z4));
        lds_st<u32x2>(tow + t * 32 * 144, u32x2{pk(ze[0], ze[1]), pk(ze[2], ze[3])});
        lds_st<u32x2>(tow + t * 32 * 144 + 144, u32x2{pk(zo[0], zo[1]), pk(zo[2], zo[3])});
      }
    }
    bar();
    STAMP(10);
    {
      st16<P>(rws, tpo, tps, lds_ld<u32x4>(tor));
      st16<P>(rws, tpo, tps + 32 * TP_S2, lds_ld<u32x4>(tor + 32 * 144));
    }
    STAMP(11);
    if (P) { drain_vm(); bar(); publish(ctl + CT_F2, b, epoch); __builtin_amdgcn_s_setprio(SH2PRIO); }
    STAMP(12);
  }

  if (PH == 0 || PH == 3) {
    u32x4 gwv = {0, 0, 0, 0};
    if (lane < 56) gwv = *(const GAS u32x4*)((const char*)p.gw + l16);
#pragma unroll
    for (int r = 0; r < 2; ++r) {
      float acc = 0.f;
      if (lane < 56) {
        u32x4 xv;
        if (PH == 0) xv = lds_ld<u32x4>(lb + L_XT + (8 * q + 2 * w + r) * XRS + l16);
        else xv = *(const GAS u32x4*)((const char*)(p.x + (size_t)(64 * (8 * q + 2 * w + r) + s2) * D_) + l16);
#pragma unroll
        for (int e = 0; e < 4; ++e) acc = dot2b(pk2(up2(xv[e]) * up2(gwv[e])), 0x3f803f80u, acc);
      }
      const float gg = bfr(__builtin_amdgcn_rcpf(1.0f + __expf(-bfr(wave_sum(acc)))));
      if (lane == 0) lds_st<f32x2>(lb + L_G + (2 * w + r) * 8, f32x2{gg, bfr(1.0f - gg)});
    }
  }

  if (PH == 0 || PH == 3) {
    const f32x4 tc = bc<f32x4>(tf(rtf, TF_TC, l16)), ts = bc<f32x4>(tf(rtf, TF_TS, l16));
    const u32x4 m4 = tf(rtf, TF_M, l16);
    u32x4 b1, b2;
    {
      const int ro = i16 >> 3;
      const float a = (float)(64 * (8 * q + (i16 & 7)) + s2) * (1.0f / 2048.0f), P0 = a * (float)(4 * g);
      float u[8], v[8];
#pragma unroll
      for (int jj = 0; jj < 8; ++jj) {
        const float ph = __builtin_fmaf(a, (float)(16 * (jj >> 2) + (jj & 3)), P0);
        u[jj] = sinr(ph + (ro ? 0.f : 0.25f));
        v[jj] = sinr(ph + (ro ? 0.25f : 0.5f));
      }
      b1 = pk8(u); b2 = pk8(v);
      pin(b1); pin(b2);
    }
    u32 tpa = lb + L_TP + (4 * g + (i16 >> 2)) * TPRS + (i16 & 3) * 128 + w * 32;
    u32 zw[2], zr;
#pragma unroll
    for (int kk = 0; kk < 2; ++kk) {
      const int row = 2 * (2 * w + kk) + (i16 >> 3);
      zw[kk] = lb + L_Z + ((i16 & 7) * 16 + row) * 64 + ((g ^ (row >> 2)) & 3) * 16;
      pin(zw[kk]);
    }
    zr = lb + L_Z + (2 * w * 16 + i16) * 64 + ((g ^ (i16 >> 2)) & 3) * 16;
    u32 boff = (u32)(tid * B_D);
    u32 biw = lb + L_BI + tid * 2, bir = lb + L_BI + 2 * w * BRS + (32 * i16 + 4 * g) * 2;
    u32 ooff = (u32)((64 * (8 * q + 2 * w) + s2) * (D_ * 2) + (32 * i16 + 4 * g) * 2);
    u32 mi = (i16 & 1) ? 0u : 0x80008000u;
    pin(tpa); pin(zr); pin(boff); pin(biw); pin(bir); pin(ooff); pin(mi);
    STAMP(13);
    if (P) { poll_flags(ctl + CT_F2 + (b & 7) * FR_ + w * 64 * FL, 64, epoch); bar(); __builtin_amdgcn_s_setprio(3); }
    STAMP(14);
#pragma unroll
    for (int r = 0; r < 8; ++r) dma16<AUX>(rws, lb + L_TP + (8 * w + r) * TPRS, l16, (u32)WS_TP + s2 * TP_S2 + (8 * w + r) * 1024);
    wait_vm0();
    bar();
    STAMP(15);
#pragma unroll
    for (int kk = 0; kk < 2; ++kk)
#pragma unroll
      for (int t = 0; t < 2; ++t) {
        const int o = t * 512 + kk * 8;
        u32x4 Tr = cat(trread(tpa + o), trread(tpa + o + 16 * TPRS));
        u32x4 Ti = cat(trread(tpa + o + 16), trread(tpa + o + 16 + 16 * TPRS));
        f32x4 z = mfma32(Ti, b2, mfma32(Tr, b1, z4));
        lds_st<u32x2>(zw[kk] + t * 8, u32x2{pk(z[0], z[1]), pk(z[2], z[3])});
      }
    bar();
    STAMP(16);
    f32x2 ga[2][2][2];
#pragma unroll
    for (int rr = 0; rr < 2; ++rr) {
      u32x4 A = lds_ld<u32x4>(zr + rr * 1024), A2 = sx1(A, mi);
      const float gg = lds_ld<float>(lb + L_G + (2 * w + rr) * 8);
#pragma unroll
      for (int td = 0; td < 2; ++td) {
        f32x4 G = mfma32(A2, fs[td], mfma32(A, fc[td], z4));
        const float c0 = tc[2 * td], s0 = ts[2 * td], c1 = tc[2 * td + 1], s1 = ts[2 * td + 1];
        const float g0r = G[0] * c0 - G[1] * s0, g0i = G[1] * c0 + G[0] * s0;
        const float g1r = G[2] * c1 - G[3] * s1, g1i = G[3] * c1 + G[2] * s1;
        f32x4 y = mfma16(u32x2{pk(g0r, g0i), pk(g1r, g1i)}, u32x2{m4.x, m4.y}, z4);
        ga[rr][td][0] = up2(pk2(up2(pk(y[0], y[1])) * gg));
        ga[rr][td][1] = up2(pk2(up2(pk(y[2], y[3])) * gg));
        pin(ga[rr][td][0]); pin(ga[rr][td][1]);
      }
    }
    STAMP(17);
    if (P) { poll_flags(ctl + CT_FB + (b & 7) * FR_ + w * 56 * FL, 56, epoch); bar(); }
    STAMP(18);
    {
      const u32 so_ = (u32)WS_B + (((s2 & 7) * 8 + (s2 >> 3)) * 32 + 8 * q) * 2;
      const u32x4 bv0 = ld16<P>(rws, boff, so_);
      u32x4 bv1 = {0, 0, 0, 0};
      if (tid < D_ - 256) bv1 = ld16<P>(rws, boff, so_ + 256 * B_D);
#pragma unroll
      for (int e = 0; e < 4; ++e) {
        lds_st<u16>(biw + (2 * e) * BRS, (u16)bv0[e]);
        lds_st<u16>(biw + (2 * e + 1) * BRS, (u16)(bv0[e] >> 16));
      }
      if (tid < D_ - 256) {
#pragma unroll
        for (int e = 0; e < 4; ++e) {
          lds_st<u16>(biw + 512 + (2 * e) * BRS, (u16)bv1[e]);
          lds_st<u16>(biw + 512 + (2 * e + 1) * BRS, (u16)(bv1[e] >> 16));
        }
      }
    }
    bar();
    const rsrc_t ro_ = mkrs(p.out, S_ * D_ * 2);
    u32x2 bq[2][2];
    float og[2];
#pragma unroll
    for (int rr = 0; rr < 2; ++rr) {
      og[rr] = lds_ld<float>(lb + L_G + (2 * w + rr) * 8 + 4);
#pragma unroll
      for (int td = 0; td < 2; ++td) bq[rr][td] = lds_ld<u32x2>(bir + rr * BRS + td * 32);
    }
#pragma unroll
    for (int rr = 0; rr < 2; ++rr)
#pragma unroll
      for (int td = 0; td < 2; ++td) {
        const f32x2 o01 = ga[rr][td][0] + up2(pk2(up2(bq[rr][td].x) * og[rr]));
        const f32x2 o23 = ga[rr][td][1] + up2(pk2(up2(bq[rr][td].y) * og[rr]));
        if (i16 < 14) __builtin_amdgcn_raw_buffer_store_b64(u32x2{pk2(o01), pk2(o23)}, ro_, ooff, rr * 64 * (D_ * 2) + td * 32, 0);
      }
    STAMP(19);
    if (P && b == 0 && tid == 0) __hip_atomic_store((GAS u32*)ctl + CT_GEN, epoch, RLX_AGENT);
  }
}

DI f32x2 cexp_rev(float x2, float ph) {
  const float e = __builtin_amdgcn_exp2f(x2);
  const float f = __builtin_amdgcn_fractf(ph);
  f32x2 cs = {cosr(f), sinr(f)};
  return cs * e;
}
struct KW { u32 a[8]; };
template <int ROW> DI void wrow(const KW& k, f32x2 v) { lds_st<u32>(k.a[ROW & 7] + ROW * 256, pk2(v)); }
template <int ROW> DI void wrow2(const KW& k, f32x2 lo, f32x2 hi) {
  const u32 a = pk2(lo), b = pk2(hi);
  asm volatile("ds_write2st64_b32 %0, %1, %2 offset0:%c3 offset1:%c4" :: "v"(k.a[ROW & 7]), "v"(a), "v"(b), "i"(ROW), "i"(ROW + 8) : "memory");
}
DI void kfrag(u32 fa, u32 fb, u32x4 (&A)[4]) {
  A[0] = lds_ld<u32x4>(fa); A[1] = lds_ld<u32x4>(fb); A[2] = lds_ld<u32x4>(fa + 128); A[3] = lds_ld<u32x4>(fb + 128);
}
DI f32x4 ktile(const u32x4 (&A)[4], const u32x4 (&V)[4]) {
  f32x4 acc = {0, 0, 0, 0};
#pragma unroll
  for (int ks = 0; ks < 4; ++ks) acc = mfma32(A[ks], V[ks], acc);
  return acc;
}
template <class F0, class F1>
DI void kgen(const u32 (&in)[6], float dt, const KW& k, u32 fa, u32 fb, u32 pb, F0&& hook0, F1&& hook1) {
  const float ar = dt * bc<float>(in[0] << 16), ai = dt * bc<float>(in[1] << 16);
  const f32x2 dB = {dt * bc<float>(in[2] << 16), dt * bc<float>(in[3] << 16)};
  const f32x2 Cc = {bc<float>(in[4] << 16), bc<float>(in[5] << 16)};
  const f32x2 CB = cmul(Cc, dB);
  const float x1 = ar * 1.44269504088896341f, p1 = ai * 0.15915494309189535f;
  const f32x2 E1 = cexp_rev(x1, p1), E8c = cexp_rev(8.f * x1, -8.f * p1), E128 = cexp_rev(128.f * x1, 128.f * p1);
  u32x4 V[4], A0[4], A1[4];
  {
    const f32x2 v2 = cmul(E8c, E8c), v3 = cmul(v2, E8c), v4 = cmul(v2, v2), v8 = cmul(v4, v4);
    const f32x2 v5 = cmul(v4, E8c), v6 = cmul(v4, v2), v7 = cmul(v4, v3);
    wrow2<0>(k, f32x2{1.f, 0.f}, v8); wrow2<1>(k, E8c, cmul(v8, E8c)); wrow2<2>(k, v2, cmul(v8, v2)); wrow2<3>(k, v3, cmul(v8, v3));
    wrow2<4>(k, v4, cmul(v8, v4)); wrow2<5>(k, v5, cmul(v8, v5)); wrow2<6>(k, v6, cmul(v8, v6)); wrow2<7>(k, v7, cmul(v8, v7));
    kfrag(fa, fb, V);
  }
  hook0();
  const f32x2 E2 = cmul(E1, E1), E3 = cmul(E2, E1), E4 = cmul(E2, E2), E5 = cmul(E4, E1), E6 = cmul(E4, E2), E7 = cmul(E4, E3);
  const f32x2 G1 = cmul(CB, E128);
  wrow2<0>(k, CB, G1); wrow2<1>(k, cmul(CB, E1), cmul(G1, E1)); wrow2<2>(k, cmul(CB, E2), cmul(G1, E2)); wrow2<3>(k, cmul(CB, E3), cmul(G1, E3));
  wrow2<4>(k, cmul(CB, E4), cmul(G1, E4)); wrow2<5>(k, cmul(CB, E5), cmul(G1, E5)); wrow2<6>(k, cmul(CB, E6), cmul(G1, E6)); wrow2<7>(k, cmul(CB, E7), cmul(G1, E7));
  kfrag(fa, fb, A0);
  const f32x2 G2 = cmul(G1, E128), G3 = cmul(G2, E128), G4 = cmul(G3, E128), G5 = cmul(G4, E128), G6 = cmul(G5, E128), G7 = cmul(G6, E128);
  wrow2<0>(k, G2, G4); wrow2<1>(k, cmul(G2, E2), cmul(G4, E4)); wrow2<2>(k, cmul(G2, E4), G5); wrow2<3>(k, cmul(G2, E6), cmul(G5, E4));
  wrow2<4>(k, G3, G6); wrow2<5>(k, cmul(G3, E2), cmul(G6, E4)); wrow2<6>(k, cmul(G3, E4), G7); wrow2<7>(k, cmul(G3, E6), cmul(G7, E4));
  kfrag(fa, fb, A1);
  hook1();
  __builtin_amdgcn_sched_barrier(0);
  const f32x4 a0 = ktile(A0, V);
  const f32x2 G8 = cmul(G7, E128), G9 = cmul(G8, E128), G10 = cmul(G9, E128), G11 = cmul(G10, E128);
  const f32x2 G12 = cmul(G11, E128), G13 = cmul(G12, E128), G14 = cmul(G13, E128), G15 = cmul(G14, E128);
  wrow2<0>(k, G8, cmul(G15, E128)); wrow<1>(k, G9); wrow<2>(k, G10); wrow<3>(k, G11);
  wrow<4>(k, G12); wrow<5>(k, G13); wrow<6>(k, G14); wrow<7>(k, G15);
  kfrag(fa, fb, A0);
  __builtin_amdgcn_sched_barrier(0);
  const f32x4 a1 = ktile(A1, V), a2 = ktile(A0, V);
  lds_st<f32x4>(pb, a0);
  lds_st<f32x4>(pb + 1024, a1);
  lds_st<f32x4>(pb + 2048, a2);
}

template <int PH>
DI void ssm_role(const Params& p, const u32 lb, const int j, u32* ctl, u32& gen STAMP_ARG) {
  constexpr bool P = PH == 0;
  const int tid = threadIdx.x, lane = tid & 63, w = __builtin_amdgcn_readfirstlane(tid >> 6), i16 = lane & 15, g = lane >> 4;
  const int d = 2 * j, ch = w >> 2, n = tid & 255;
  const f32x4 z4 = {0, 0, 0, 0};
  const rsrc_t rws = mkrs(p.ws, 16u << 20);
  if (P) __builtin_amdgcn_s_setprio(3);
  const int sb_ = j / 7, db_ = j - sb_ * 7;
  u32x4 xt_v = {0, 0, 0, 0};
  if (PH == 0 || PH == 1)
    xt_v = __builtin_amdgcn_raw_buffer_load_b128(mkrs(p.x, S_ * D_ * 2), (u32)((tid >> 3) * (D_ * 2) + (tid & 7) * 16), (u32)(64 * sb_ * (D_ * 2) + db_ * 128), 0);
  u32 in[6];
  u32x4 kt0 = {0, 0, 0, 0}; u32x2 kt1 = {0, 0};
  u32 ldt = 0;
  if (PH != 1) {
    const u32 vo = n * 2, so_ = (d + ch) * (NS * 2), nb = D_ * NS * 2;
    const u16* arr[6] = {p.are, p.aim, p.bre, p.bim, p.cre, p.cim};
#pragma unroll
    for (int a = 0; a < 6; ++a) in[a] = __builtin_amdgcn_raw_buffer_load_b16(mkrs(arr[a], nb), vo, so_, 0);
    ldt = *(const GAS u16*)(p.logdt + d + ch);
    kt0 = *(const GAS u32x4*)(KTAB.v + 8 * n);
    kt1 = *(const GAS u32x2*)(KTAB.v + 8 * n + 8);
  }
  if (P) {
    gen = __hip_atomic_load((GAS u32*)ctl + CT_GEN, RLX_AGENT);
  }
  auto transpose = [&]() {
    lds_st<u32x4>(lb + L_XP + (tid >> 3) * 144 + (tid & 7) * 16, xt_v);
    bar();
    const int chi = g + 4 * (w >> 2);
    const u32 a = lb + L_XP + (8 * chi + (i16 >> 2)) * 144 + (16 * (w & 3) + 4 * (i16 & 3)) * 2;
    st16<P>(rws, (u32)((16 * (w & 3) + i16) * XT_D + chi * 16), (u32)WS_XT + 64 * db_ * XT_D + sb_ * 128, cat(trread(a), trread(a + 4 * 144)));
  };
  if (PH == 1) { transpose(); return; }
  if (P) {
    if (tid == 0) lds_st<u32>(lb + L_CNT, 0u);
    __builtin_amdgcn_sched_barrier(0);
    transpose();
    __builtin_amdgcn_s_setprio(1);
    __builtin_amdgcn_sched_barrier(0);
  }
  KW kw;
  const u32 sb = lb + w * KSCR;
#pragma unroll
  for (int h = 0; h < 8; ++h) { kw.a[h] = sb + (((lane >> 2) ^ h) << 4) + (lane & 3) * 4; pin(kw.a[h]); }
  u32 fa = sb + i16 * 256 + ((g ^ (i16 & 3)) << 4) + ((i16 >> 2) & 1) * 64, fb = fa ^ 64;
  pin(fa); pin(fb);
  STAMP(1);
  kgen(in, bfr(expf(bc<float>(ldt << 16))), kw, fa, fb, sb + lane * 16,
       [&]() {
         if (P) {
           drain_vm();
           STAMP(5);
           if (lane == 0 && __hip_atomic_fetch_add((LAS u32*)(size_t)(lb + L_CNT), 1u, __ATOMIC_RELAXED, __HIP_MEMORY_SCOPE_WORKGROUP) == 7u)
             __hip_atomic_store((GAS u32*)ctl + CT_FX + (db_ * 32 + sb_) * FL, gen + 1u, RLX_AGENT);
         }
       },
       []() {});
  const u32 epoch = __builtin_amdgcn_readfirstlane(gen) + 1u;
  STAMP(2);
  if (P && w == 0) poll_flags(ctl + CT_FX + (d >> 6) * 32 * FL, 32, epoch);
  STAMP(6);
  bar();
  const u32x4 uv_ = ld16<P>(rws, n * 16, (u32)WS_XT + (d + ch) * XT_D);
  STAMP(3);
  if ((w & 3) < 3) {
    const u32 pb = lb + ch * 4 * KSCR + (w & 3) * 1024 + lane * 16;
    const f32x4 s = (lds_ld<f32x4>(pb) + lds_ld<f32x4>(pb + KSCR)) + (lds_ld<f32x4>(pb + 2 * KSCR) + lds_ld<f32x4>(pb + 3 * KSCR));
    const u32 tb = lb + L_KT + ch * 1600 + ((w & 3) * 256 + 64 * g + i16) * 2, a = pk(s[0], s[1]), cc = pk(s[2], s[3]);
    lds_st<u16>(tb, (u16)a); lds_st<u16>(tb + 32, (u16)(a >> 16));
    lds_st<u16>(tb + 64, (u16)cc); lds_st<u16>(tb + 96, (u16)(cc >> 16));
  } else {
    if (lane == 0) lds_st<u16>(lb + L_KT + ch * 1600 + 1536, (u16)0);
    if (lane < 32) lds_st<u32x4>(lb + L_U + ch * 4608 + lane * 16, u32x4{0, 0, 0, 0});
  }
  bar();
  STAMP(4);
  {
    const u32 ktb = lb + L_KT + ch * 1600;
    u32 e[11];
#pragma unroll
    for (int k = 0; k < 4; ++k) { e[2 * k] = lds_ld<u16>(ktb + (kt0[k] & 0xffffu)); e[2 * k + 1] = lds_ld<u16>(ktb + (kt0[k] >> 16)); }
    e[8] = lds_ld<u16>(ktb + (kt1[0] & 0xffffu)); e[9] = lds_ld<u16>(ktb + (kt1[0] >> 16)); e[10] = lds_ld<u16>(ktb + (kt1[1] & 0xffffu));
    const u32 k00 = lds_ld<u16>(ktb);
    u32 dw[5], al[5];
#pragma unroll
    for (int k = 0; k < 5; ++k) { dw[k] = e[2 * k] | (e[2 * k + 1] << 16); al[k] = e[2 * k + 1] | (e[2 * k + 2] << 16); }
    const u32 kb = lb + L_KC + ch * 4 * KCP + n * 16;
    lds_st<u32x4>(kb, u32x4{dw[0], dw[1], dw[2], dw[3]});
    lds_st<u32x4>(kb + KCP, u32x4{al[0], al[1], al[2], al[3]});
    lds_st<u32x4>(kb + 2 * KCP, u32x4{dw[1], dw[2], dw[3], dw[4]});
    lds_st<u32x4>(kb + 3 * KCP, u32x4{al[1], al[2], al[3], al[4]});
    if (n < 16) {
      u32x4 z = {0, 0, 0, 0};
      if (n == 0) z.x = k00;
      lds_st<u32x4>(lb + L_KC + (ch * 4 + (n >> 2)) * KCP + 4096 + (n & 3) * 16, z);
    }
    lds_st<u32x4>(lb + L_U + ch * 4608 + 512 + n * 16, uv_);
  }
  bar();
  STAMP(7);
  f32x4 acc[8];
  const int cc = (w >> 1) & 1, hf = w & 1;
  if (w < 4) {
#pragma unroll
    for (int c = 0; c < 8; ++c) acc[c] = z4;
    const int bsh = (-i16) & 3;
    u32 abase = lb + L_KC + (cc * 4 + bsh) * KCP + (2032 + 8 * g - i16 - bsh) * 2 - hf * 4 * 64 - 3 * 64 - 7 * 512;
    u32 bbase = lb + L_U + cc * 4608 + (256 - 16 + 16 * i16 + 8 * g) * 2 - hf * 4 * 64 - 3 * 64;
    u32 abase8 = abase + 8;
    pin(abase); pin(abase8); pin(bbase);
    u32x4 A[8], B[8];
#pragma unroll
    for (int qq = 0; qq < 8; ++qq) {
      B[7 - qq] = lds_ld<u32x4>(bbase + (7 - qq) * 512 + 3 * 64);
      A[qq] = cat(lds_ld<u32x2>(abase + (7 - qq) * 512 + 3 * 64), lds_ld<u32x2>(abase8 + (7 - qq) * 512 + 3 * 64));
      __builtin_amdgcn_sched_barrier(0);
    }
#pragma unroll
    for (int rr = 0; rr < 4; ++rr) {
#pragma unroll
      for (int qq = 0; qq < 8; ++qq) {
#pragma unroll
        for (int m = 7 - qq; m >= 0; --m) {
          acc[qq + m] = mfma32(A[qq], B[m], acc[qq + m]);
          asm volatile("s_nop %c0" :: "i"(CNOP));
        }
        if (rr < 3) {
          A[qq] = cat(lds_ld<u32x2>(abase + (7 - qq) * 512 + (2 - rr) * 64), lds_ld<u32x2>(abase8 + (7 - qq) * 512 + (2 - rr) * 64));
          B[7 - qq] = lds_ld<u32x4>(bbase + (7 - qq) * 512 + (2 - rr) * 64);
        }
        __builtin_amdgcn_sched_barrier(0);
      }
    }
    if (hf) {
#pragma unroll
      for (int c = 0; c < 4; ++c) lds_st<f32x4>(lb + L_RED + (w * 4 + c) * 1024 + lane * 16, acc[c]);
#pragma unroll
      for (int c = 0; c < 4; ++c) acc[c] = acc[4 + c];
    } else {
#pragma unroll
      for (int c = 0; c < 4; ++c) lds_st<f32x4>(lb + L_RED + (w * 4 + c) * 1024 + lane * 16, acc[4 + c]);
    }
  }
  bar();
  STAMP(8);
  if (w < 4) {
    const float dk = b2f(p.dskip[d + cc]);
    const u32 tb = (1024 * hf + 16 * i16 + 4 * g) * 2;
    f32x4 o[4];
    u32x2 uv[4];
#pragma unroll
    for (int c = 0; c < 4; ++c) {
      o[c] = lds_ld<f32x4>(lb + L_RED + ((w ^ 1) * 4 + c) * 1024 + lane * 16);
      uv[c] = lds_ld<u32x2>(lb + L_U + cc * 4608 + 512 + tb + c * 512);
    }
#pragma unroll
    for (int c = 0; c < 4; ++c) {
      const f32x4 y = acc[c] + o[c];
      const f32x2 b01 = up2(pk(y[0], y[1])) + up2(pk2(up2(uv[c].x) * dk));
      const f32x2 b23 = up2(pk(y[2], y[3])) + up2(pk2(up2(uv[c].y) * dk));
      lds_st<u32x2>(lb + L_BO + cc * 4096 + tb + c * 512, u32x2{pk2(b01), pk2(b23)});
    }
  }
  bar();
  STAMP(9);
  {
    const int st = w & 3, s2 = 16 * st + i16;
    const u32 a = lb + L_BO + ch * 4096 + (8 * g + (i16 >> 2)) * 128 + (16 * st + 4 * (i16 & 3)) * 2;
    st16<P>(rws, (u32)((((s2 & 7) * 8 + (s2 >> 3)) * 32 + 8 * g) * 2), (u32)WS_B + (d + ch) * B_D, cat(trread(a), trread(a + 4 * 128)));
  }
  STAMP(10);
  if (P) { drain_vm(); bar(); publish(ctl + CT_FB, j, epoch); }
  STAMP(11);
}

template <int PH>
__global__ __launch_bounds__(NT, 4) void gfs4_kernel(Params p) {
  extern __shared__ __attribute__((aligned(16))) char smem[];
  const u32 lb = (u32)(size_t)(LAS char*)smem;
  const int b = blockIdx.x;
  u32* ctl = (u32*)(p.ws + WS_CTL);
  STAMP_DECL;
  if (PH == 0 || PH == 1) STAMP(0); else if (PH == 2) STAMP(20); else STAMP(21);
  u32 gen = 0;
  const u32 dg_t0 = (u32)__builtin_amdgcn_s_memrealtime();
  if (threadIdx.x == 0) __hip_atomic_store((GAS u32*)(p.ws + (12u << 20)) + b, dg_t0, RLX_AGENT);
  if (b < N_FFT) {
    if (threadIdx.x >= 256) return;
    fft_role<PH>(p, lb, b, ctl, gen STAMP_PASS);
  }
  else ssm_role<PH>(p, lb, b - N_FFT, ctl, gen STAMP_PASS);
  STAMP_FLUSH();
  if (b == 0 && threadIdx.x < 64 && (gen & 0xff) != ((gen >> 8) & 0xff) && (gen & 7) == 3) {
    const u32 now = (u32)__builtin_amdgcn_s_memrealtime();
    u32 mn = 0x7fffffffu, mx = 0;
    for (int i = threadIdx.x; i < N_WG; i += 64) { const u32 a = __hip_atomic_load((GAS u32*)(p.ws + (12u << 20)) + i, RLX_AGENT) - dg_t0 + 100000u; mn = min(mn, a); mx = max(mx, a); }
    for (int o = 32; o >= 1; o >>= 1) { mn = min(mn, (u32)__shfl_xor((int)mn, o)); mx = max(mx, (u32)__shfl_xor((int)mx, o)); }
    const float v = DIAG == 5 ? (float)(now - dg_t0 + 100000u - mn) * 0.01f : 1.f + (float)(mx - mn) * 0.01f;
    wait_vm0();
    if (threadIdx.x == 0) p.out[0] = (u16)(pk(v, v) & 0xffff);
  }
}

extern "C" void kernel_launch(void* const* d_in, const int* in_sizes, int n_in,
                              void* d_out, int out_size, void* d_ws, size_t ws_size,
                              hipStream_t stream) {
  (void)in_sizes; (void)n_in; (void)out_size; (void)ws_size;
  Params p;
  p.x = (const u16*)d_in[0]; p.wr = (const u16*)d_in[1]; p.wi = (const u16*)d_in[2];
  p.are = (const u16*)d_in[4]; p.aim = (const u16*)d_in[5]; p.bre = (const u16*)d_in[6]; p.bim = (const u16*)d_in[7];
  p.cre = (const u16*)d_in[8]; p.cim = (const u16*)d_in[9]; p.logdt = (const u16*)d_in[10]; p.dskip = (const u16*)d_in[11];
  p.gw = (const u16*)d_in[12];
  p.out = (u16*)d_out; p.ws = (char*)d_ws;
  (void)hipFuncSetAttribute((const void*)gfs4_kernel<0>, hipFuncAttributeMaxDynamicSharedMemorySize, LDS_BYTES);
  gfs4_kernel<0><<<N_WG, NT, LDS_BYTES, stream>>>(p);
}
```

```cpp
#include <hip/hip_runtime.h>
#include <hip/hip_bf16.h>
#include <stdint.h>

typedef unsigned int u32;
typedef unsigned short u16;
typedef unsigned long long u64;
typedef float f32x4 __attribute__((ext_vector_type(4)));
typedef float f32x2 __attribute__((ext_vector_type(2)));
typedef u32 u32x4 __attribute__((ext_vector_type(4)));
typedef u32 u32x2 __attribute__((ext_vector_type(2)));
typedef __bf16 bf16x8 __attribute__((ext_vector_type(8)));
typedef __bf16 bf16x2 __attribute__((ext_vector_type(2)));
typedef short s16x4 __attribute__((ext_vector_type(4)));
#define LAS __attribute__((address_space(3)))
#define GAS __attribute__((address_space(1)))
#define DI __device__ __forceinline__

#define NLAUNCH 1

constexpr int S_ = 2048, D_ = 448, DF = 225, NS = 256;
constexpr int N_FFT = 256, N_SSM = 224, N_WG = N_FFT + N_SSM, NT = 512;

constexpr u32 T_K1 = 65536 + 256;
constexpr u32 TP_S2 = 32768 + 128;
constexpr u32 XT_D = 4096 + 128;
constexpr u32 B_D = 4096 + 128;
constexpr size_t WS_CTL = 0;
constexpr size_t WS_T   = 1u << 20;
constexpr size_t WS_TP  = WS_T + (3u << 20);
constexpr size_t WS_XT  = WS_TP + (3u << 20);
constexpr size_t WS_B   = WS_XT + (2u << 20);
constexpr int FL = 32, FR_ = 256 * FL + 64;
constexpr int CT_GEN = 0;
constexpr int CT_F1 = 64;
constexpr int CT_F2 = CT_F1 + 8 * FR_;
constexpr int CT_FB = CT_F2 + 8 * FR_;
constexpr int CT_FX = CT_FB + 8 * FR_;
static_assert((CT_FX + 7 * 32 * FL) * 4 <= (1 << 20), "control block");

constexpr int XRS = 928;
constexpr int L_XT = 0;
constexpr int L_XCH = 29696;
constexpr int TPRS = 1040;
constexpr int L_TP = 0;
constexpr int L_TO = 29696;
constexpr int L_TI = 38912;
constexpr int L_BI = 38912;
constexpr int BRS = 912;
constexpr int L_Z = 47104;
constexpr int L_C1 = 38912;
constexpr int L_C2 = 65536;
constexpr int L_C3 = 70400;
constexpr int L_W = 47104;
constexpr int L_G = 70208;
constexpr int KCP = 4160;
constexpr int L_KC = 0;
constexpr int KSCR = 4096;
constexpr int L_RED = 33280;
constexpr int L_BO = 49664;
constexpr int L_U = 57856;
constexpr int L_XP = 57856;
constexpr int L_KT = 67072;
constexpr int L_CNT = 70272;
constexpr int LDS_BYTES = 72448;

struct Params {
  const u16* x; const u16* wr; const u16* wi;
  const u16* are; const u16* aim; const u16* bre; const u16* bim; const u16* cre; const u16* cim;
  const u16* logdt; const u16* dskip; const u16* gw;
  u16* out; char* ws;
};

constexpr double cx_sin_oct(double x) { double x2 = x * x, t = x, s = x; for (int i = 1; i < 9; ++i) { t *= -x2 / ((2 * i) * (2 * i + 1)); s += t; } return s; }
constexpr double cx_cos_oct(double x) { double x2 = x * x, t = 1, s = 1; for (int i = 1; i < 9; ++i) { t *= -x2 / ((2 * i - 1) * (2 * i)); s += t; } return s; }
constexpr double cx_sin(long n, long d) {
  n %= d; if (n < 0) n += d;
  const long o = (8 * n) / d;
  const double r = 6.283185307179586476925 * (double)(8 * n - o * d) / (8.0 * (double)d), q = 0.78539816339744830962;
  switch (o) {
    case 0: return cx_sin_oct(r);       case 1: return cx_cos_oct(q - r);
    case 2: return cx_cos_oct(r);       case 3: return cx_sin_oct(q - r);
    case 4: return -cx_sin_oct(r);      case 5: return -cx_cos_oct(q - r);
    case 6: return -cx_cos_oct(r);      default: return -cx_sin_oct(q - r);
  }
}
constexpr double cx_cos(long n, long d) { return cx_sin(4 * n + d, 4 * d); }
constexpr u32 cx_f32(double v) { return __builtin_bit_cast(u32, (float)v); }
constexpr u32 cx_bf(double v) { const u32 u = cx_f32(v); return (u + 0x7fffu + ((u >> 16) & 1u)) >> 16; }
constexpr int cx_pi(int g, int jj) { return 16 * (jj >> 2) + 4 * g + (jj & 3); }
struct Frag { u32 v[64][4]; };
enum { TF_FC0, TF_FS0, TF_CO0, TF_SO0, TF_COT0, TF_SOT0, TF_TC, TF_TS, TF_M, TF_Q0, TF_N = TF_Q0 + 24 };
constexpr Frag make_frag(int item) {
  Frag f{};
  for (int lane = 0; lane < 64; ++lane) {
    const int i16 = lane & 15, g = lane >> 4;
    if (item < TF_TC) {
      const int sn = item & 1;
      for (int jj = 0; jj < 8; ++jj) {
        const int m = cx_pi(g, jj), a = i16;
        long n = 0, d = 32;
        if (item < TF_CO0) { n = a * m; d = 32; }
        else if (item < TF_COT0) { n = a * (2 * m + 1); d = 64; }
        else { n = m * (2 * a + 1); d = 64; }
        f.v[lane][jj >> 1] |= cx_bf(sn ? cx_sin(n, d) : cx_cos(n, d)) << (16 * (jj & 1));
      }
    } else if (item == TF_TC || item == TF_TS) {
      for (int j = 0; j < 4; ++j) {
        const long n = (2 * g + (j & 1)) * (16 * (j >> 1) + i16);
        f.v[lane][j] = cx_f32(item == TF_TC ? cx_cos(n, 448) : cx_sin(n, 448));
      }
    } else if (item == TF_M) {
      for (int j = 0; j < 4; ++j) {
        const int k0 = 2 * g + (j >> 1);
        const double cf = ((k0 == 0 || k0 == 7) ? 1.0 : 2.0) / (2048.0 * 448.0);
        const double v = i16 < 14 ? cf * ((j & 1) ? -cx_sin(k0 * i16, 14) : cx_cos(k0 * i16, 14)) : 0.0;
        f.v[lane][j >> 1] |= cx_bf(v) << (16 * (j & 1));
      }
    } else {
      const int q = (item - TF_Q0) / 6, sub = (item - TF_Q0) % 6, k0 = 2 * q + ((i16 >> 1) & 1), ri = i16 & 1;
      if (sub < 2) {
        if ((i16 >> 2) == 2 * sub + (g & 1))
          for (int j = 0; j < 8; ++j) {
            const int d0 = 8 * (g >> 1) + j;
            const double v = d0 < 14 ? (ri ? -cx_sin(d0 * k0, 14) : cx_cos(d0 * k0, 14)) : 0.0;
            f.v[lane][j >> 1] |= cx_bf(v) << (16 * (j & 1));
          }
      } else {
        for (int j = 0; j < 4; ++j) {
          const long n = cx_pi(g, 4 * (sub & 1) + j) * k0;
          f.v[lane][j] = cx_f32(sub < 4 ? cx_cos(n, 448) : (ri ? -cx_sin(n, 448) : cx_sin(n, 448)));
        }
      }
    }
  }
  return f;
}
template <int I> struct FragC { static constexpr Frag f = make_frag(I); };
#define FR(i) FragC<(i)>::f
#define FR6(i) FR(i), FR(i + 1), FR(i + 2), FR(i + 3), FR(i + 4), FR(i + 5)
__device__ const __attribute__((aligned(1024))) Frag TF[TF_N] = {FR6(0), FR(6), FR(7), FR(8), FR6(9), FR6(15), FR6(21), FR6(27)};

struct KTab { u16 v[2080]; };
constexpr KTab make_ktab() {
  KTab t{};
  for (int i = 0; i < 2080; ++i) {
    const int l = 2048 - i;
    int idx = 768;
    if (l >= 0) {
      const int sh = l > 256 ? (l >= 1024 ? 3 : (l >= 512 ? 2 : 1)) : 0;
      const int v = sh ? (((l + (1 << (sh - 1)) - 1 + ((l >> sh) & 1)) >> sh) << sh) : l;
      if (v < 256) idx = ((v & 7) + 8 * (v >> 7)) * 16 + ((v >> 3) & 15);
      else if (v < 512) idx = 256 + ((((v - 256) >> 1) & 3) + 4 * ((v - 256) >> 7)) * 16 + (((v - 256) >> 3) & 15);
      else if (v < 1024) idx = 256 + (8 + (((v - 512) >> 2) & 1) + 2 * ((v - 512) >> 7)) * 16 + (((v - 512) >> 3) & 15);
      else if (v < 2048) idx = 512 + ((v - 1024) >> 7) * 16 + (((v - 1024) >> 3) & 15);
      else idx = 640;
    }
    t.v[i] = (u16)(2 * idx);
  }
  return t;
}
__device__ const __attribute__((aligned(16))) KTab KTAB = make_ktab();

template <class T, class F> DI T bc(F v) { return __builtin_bit_cast(T, v); }
DI f32x4 mfma32(u32x4 a, u32x4 b, f32x4 c) { return __builtin_amdgcn_mfma_f32_16x16x32_bf16(bc<bf16x8>(a), bc<bf16x8>(b), c, 0, 0, 0); }
DI f32x4 mfma16(u32x2 a, u32x2 b, f32x4 c) { return __builtin_amdgcn_mfma_f32_16x16x16bf16_1k(bc<s16x4>(a), bc<s16x4>(b), c, 0, 0, 0); }
DI u32 pk(float a, float b) { f32x2 v = {a, b}; return bc<u32>(__builtin_convertvector(v, bf16x2)); }
DI u32 pk2(f32x2 v) { return bc<u32>(__builtin_convertvector(v, bf16x2)); }
DI float bfr(float a) { return bc<float>(pk(0.f, a) & 0xffff0000u); }
DI float lo16(u32 v) { return bc<float>(v << 16); }
DI float hi16(u32 v) { return bc<float>(v & 0xffff0000u); }
DI f32x2 up2(u32 v) { return f32x2{lo16(v), hi16(v)}; }
DI float b2f(u16 v) { return bc<float>((u32)v << 16); }
DI u32x2 trread(u32 a) { return bc<u32x2>(__builtin_amdgcn_ds_read_tr16_b64_v4i16((LAS s16x4*)(size_t)a)); }
DI u32x4 cat(u32x2 a, u32x2 b) { u32x4 r = {a.x, a.y, b.x, b.y}; return r; }
DI float sinr(float x) { return __builtin_amdgcn_sinf(x); }
DI float cosr(float x) { return __builtin_amdgcn_cosf(x); }
DI float fx1(float v) { return __builtin_amdgcn_update_dpp(v, v, 0xB1, 0xf, 0xf, true); }
DI float fr8(float v) { return __builtin_amdgcn_update_dpp(v, v, 0x128, 0xf, 0xf, true); }
DI u32 ux1(u32 v) { return (u32)__builtin_amdgcn_update_dpp((int)v, (int)v, 0xB1, 0xf, 0xf, true); }
DI u32 ur8(u32 v) { return (u32)__builtin_amdgcn_update_dpp((int)v, (int)v, 0x128, 0xf, 0xf, true); }
DI u32x4 sx1(u32x4 v, u32 m) { u32x4 r = {ux1(v.x) ^ m, ux1(v.y) ^ m, ux1(v.z) ^ m, ux1(v.w) ^ m}; return r; }
DI u32x4 sr8(u32x4 v, u32 m) { u32x4 r = {ur8(v.x) ^ m, ur8(v.y) ^ m, ur8(v.z) ^ m, ur8(v.w) ^ m}; return r; }
DI u32x4 xr(u32x4 v, u32 m) { u32x4 r = {v.x ^ m, v.y ^ m, v.z ^ m, v.w ^ m}; return r; }
DI u32x4 pk8(const float (&v)[8]) { return u32x4{pk(v[0], v[1]), pk(v[2], v[3]), pk(v[4], v[5]), pk(v[6], v[7])}; }
template <class T> DI T lds_ld(u32 a) { return *(LAS T*)(size_t)a; }
template <class T> DI void lds_st(u32 a, T v) { *(LAS T*)(size_t)a = v; }
DI void wait_vm0() { __builtin_amdgcn_s_waitcnt(0x0F70); }
DI void drain_vm() { asm volatile("s_waitcnt vmcnt(0)" ::: "memory"); }
DI void bar() { __builtin_amdgcn_s_waitcnt(0xC07F); asm volatile("s_barrier" ::: "memory"); }
template <class T> DI void pin(T& v) { asm volatile("" : "+v"(v)); }
typedef __amdgpu_buffer_rsrc_t rsrc_t;
DI rsrc_t mkrs(const void* p, u32 bytes) { return __builtin_amdgcn_make_buffer_rsrc((void*)p, 0, bytes, 0x00020000); }
template <int AUX> DI void dma16(rsrc_t rs, u32 lds, u32 voff, u32 soff) { __builtin_amdgcn_raw_ptr_buffer_load_lds(rs, (LAS void*)(size_t)lds, 16, voff, soff, 0, AUX); }
DI f32x2 cmul(f32x2 x, f32x2 y) {
  f32x2 t;
  asm("v_pk_mul_f32 %0, %1, %2 op_sel:[0,0] op_sel_hi:[0,1]\n\tv_pk_fma_f32 %0, %1, %2, %0 op_sel:[1,1,0] op_sel_hi:[1,0,1] neg_lo:[1,0,0]" : "=&v"(t) : "v"(x), "v"(y));
  return t;
}
DI float dot2b(u32 a, u32 b, float c) { return __builtin_amdgcn_fdot2_f32_bf16(bc<bf16x2>(a), bc<bf16x2>(b), c, false); }
DI float wave_sum(float a) {
  a += __builtin_amdgcn_update_dpp(0.f, a, 0xB1, 0xf, 0xf, true);
  a += __builtin_amdgcn_update_dpp(0.f, a, 0x4E, 0xf, 0xf, true);
  a += __builtin_amdgcn_update_dpp(0.f, a, 0x141, 0xf, 0xf, true);
  a += __builtin_amdgcn_update_dpp(0.f, a, 0x140, 0xf, 0xf, true);
  a += __builtin_amdgcn_update_dpp(0.f, a, 0x142, 0xa, 0xf, false);
  a += __builtin_amdgcn_update_dpp(0.f, a, 0x143, 0xc, 0xf, false);
  return bc<float>(__builtin_amdgcn_readlane(bc<int>(a), 63));
}
DI int pi_k(int g, int jj) { return 16 * (jj >> 2) + 4 * g + (jj & 3); }

#define CNOP 1
#define CNOPR 2
#define SH2PRIO 2
#define SLP 2
#define RLX_AGENT __ATOMIC_RELAXED, __HIP_MEMORY_SCOPE_AGENT
template <bool P> DI void st16(rsrc_t rs, u32 voff, u32 soff, u32x4 v) {
  __builtin_amdgcn_raw_buffer_store_b128(v, rs, voff, soff, P ? 16 : 0);
  asm volatile("s_nop 1" :: "v"(v) : "memory");
}
template <bool P> DI u32x4 ld16(rsrc_t rs, u32 voff, u32 soff) { return __builtin_amdgcn_raw_buffer_load_b128(rs, voff, soff, P ? 16 : 0); }
DI void publish(u32* set, int idx, u32 epoch) {
  if (threadIdx.x < 8) __hip_atomic_store((GAS u32*)set + threadIdx.x * FR_ + idx * FL, epoch, RLX_AGENT);
}
DI void poll_flags(const u32* first, int n, u32 epoch) {
  const int lane = threadIdx.x & 63;
  const GAS u32* f = (const GAS u32*)first + (lane < n ? lane : 0) * FL;
  for (int spin = 0; spin < (1 << 18); ++spin) {
    if (__all(__hip_atomic_load(f, RLX_AGENT) == epoch)) break;
    __builtin_amdgcn_s_sleep(SLP);
  }
}

#define STAMP(i) do {} while (0)
#define STAMP_DECL do {} while (0)
#define STAMP_FLUSH() do {} while (0)
#define STAMP_ARG
#define STAMP_PASS

DI u32x4 tf(rsrc_t rs, int item, u32 l16) { return __builtin_amdgcn_raw_buffer_load_b128(rs, l16 + (item & 3) * 1024, (item >> 2) * 4096, 0); }
DI u32x4 tfs(rsrc_t rs, int item, u32 l16, u32 so) { return __builtin_amdgcn_raw_buffer_load_b128(rs, l16 + item * 1024, so, 0); }

struct P3C { u32x4 b1, b2; f32x4 tc, ts; u32x2 m; };
template <int PH>
DI void p3_prepare(const Params& p, const u32 lb, const int b, const rsrc_t rtf, P3C& c) {
  const int lane = threadIdx.x & 63, i16 = lane & 15, g = lane >> 4;
  const u32 l16 = lane * 16;
  const int s2 = b & 63, q = b >> 6;
  c.tc = bc<f32x4>(tf(rtf, TF_TC, l16)); c.ts = bc<f32x4>(tf(rtf, TF_TS, l16));
  const u32x4 m4 = tf(rtf, TF_M, l16);
  c.m = u32x2{m4.x, m4.y};
  c.b1 = lds_ld<u32x4>(lb + L_C3 + l16); c.b2 = lds_ld<u32x4>(lb + L_C3 + 1024 + l16);
  pin(c.b1); pin(c.b2); pin(c.tc); pin(c.ts); pin(c.m);
}
DI void p3_make_b(const u32 lb, const int b) {
  const int lane = threadIdx.x & 63, i16 = lane & 15, g = lane >> 4;
  const int s2 = b & 63, q = b >> 6;
  const int ro = i16 >> 3;
  const float a = (float)(64 * (8 * q + (i16 & 7)) + s2) * (1.0f / 2048.0f), P0 = a * (float)(4 * g);
  float u[8], v[8];
#pragma unroll
  for (int jj = 0; jj < 8; ++jj) {
    const float ph = __builtin_fmaf(a, (float)(16 * (jj >> 2) + (jj & 3)), P0);
    u[jj] = sinr(ph + (ro ? 0.f : 0.25f));
    v[jj] = sinr(ph + (ro ? 0.25f : 0.5f));
  }
  lds_st<u32x4>(lb + L_C3 + lane * 16, pk8(u));
  lds_st<u32x4>(lb + L_C3 + 1024 + lane * 16, pk8(v));
}
template <bool LDSX>
DI void gate_rows(const Params& p, const u32 lb, const int b, const int r0, const int nr) {
  const int lane = threadIdx.x & 63;
  const u32 l16 = lane * 16;
  const int s2 = b & 63, q = b >> 6;
  u32x4 gwv = {0, 0, 0, 0};
  if (lane < 56) gwv = *(const GAS u32x4*)((const char*)p.gw + l16);
#pragma unroll
  for (int r = 0; r < 2; ++r) {
    if (r < nr) {
      float acc = 0.f;
      if (lane < 56) {
        u32x4 xv;
        if (LDSX) xv = lds_ld<u32x4>(lb + L_XT + (8 * q + r0 + r) * XRS + l16);
        else xv = *(const GAS u32x4*)((const char*)(p.x + (size_t)(64 * (8 * q + r0 + r) + s2) * D_) + l16);
#pragma unroll
        for (int e = 0; e < 4; ++e) acc = dot2b(pk2(up2(xv[e]) * up2(gwv[e])), 0x3f803f80u, acc);
      }
      const float gg = bfr(__builtin_amdgcn_rcpf(1.0f + __expf(-bfr(wave_sum(acc)))));
      if (lane == 0) lds_st<f32x2>(lb + L_G + (r0 + r) * 8, f32x2{gg, bfr(1.0f - gg)});
    }
  }
}

template <int PH>
DI void fft_phase3(const Params& p, const u32 lb, const int b, u32* ctl, const u32 epoch, const u32x4 (&fc)[2], const u32x4 (&fs)[2], const P3C& c STAMP_ARG) {
  constexpr bool P = PH == 0;
  constexpr int TPAUX = P ? 16 : 0;
  const int tid = threadIdx.x, lane = tid & 63, w = __builtin_amdgcn_readfirstlane(tid >> 6), i16 = lane & 15, g = lane >> 4;
  const u32 l16 = lane * 16;
  const int s2 = b & 63, q = b >> 6;
  const f32x4 z4 = {0, 0, 0, 0};
  const rsrc_t rws = mkrs(p.ws, 16u << 20);
  u32 tpa = lb + L_TP + (4 * g + (i16 >> 2)) * TPRS + (i16 & 3) * 128 + (w >> 1) * 32 + (w & 1) * 8;
  const int row = 2 * w + (i16 >> 3);
  u32 zw = lb + L_Z + ((i16 & 7) * 16 + row) * 64 + ((g ^ (row >> 2)) & 3) * 16;
  u32 zr = lb + L_Z + (w * 16 + i16) * 64 + ((g ^ (i16 >> 2)) & 3) * 16;
  u32 boff = (u32)(tid * B_D);
  u32 biw = lb + L_BI + tid * 2, bir = lb + L_BI + w * BRS + (32 * i16 + 4 * g) * 2;
  u32 ooff = (u32)((64 * (8 * q + w) + s2) * (D_ * 2) + (32 * i16 + 4 * g) * 2);
  u32 mi = (i16 & 1) ? 0u : 0x80008000u;
  pin(tpa); pin(zw); pin(zr); pin(boff); pin(biw); pin(bir); pin(ooff); pin(mi);
  STAMP(13);
  if (P) { poll_flags(ctl + CT_F2 + (b & 7) * FR_ + w * 32 * FL, 32, epoch); bar(); __builtin_amdgcn_s_setprio(3); }
  STAMP(14);
#pragma unroll
  for (int r = 0; r < 4; ++r) dma16<TPAUX>(rws, lb + L_TP + (4 * w + r) * TPRS, l16, (u32)WS_TP + s2 * TP_S2 + (4 * w + r) * 1024);
  wait_vm0();
  bar();
  STAMP(15);
#pragma unroll
  for (int t = 0; t < 2; ++t) {
    u32x4 Tr = cat(trread(tpa + t * 512), trread(tpa + t * 512 + 16 * TPRS));
    u32x4 Ti = cat(trread(tpa + t * 512 + 16), trread(tpa + t * 512 + 16 + 16 * TPRS));
    f32x4 z = mfma32(Ti, c.b2, mfma32(Tr, c.b1, z4));
    lds_st<u32x2>(zw + t * 8, u32x2{pk(z[0], z[1]), pk(z[2], z[3])});
  }
  bar();
  STAMP(16);
  f32x2 ga[2][2];
  {
    u32x4 A = lds_ld<u32x4>(zr), A2 = sx1(A, mi);
    const float gg = lds_ld<float>(lb + L_G + w * 8);
#pragma unroll
    for (int td = 0; td < 2; ++td) {
      f32x4 G = mfma32(A2, fs[td], mfma32(A, fc[td], z4));
      const float c0 = c.tc[2 * td], s0 = c.ts[2 * td], c1 = c.tc[2 * td + 1], s1 = c.ts[2 * td + 1];
      const float g0r = G[0] * c0 - G[1] * s0, g0i = G[1] * c0 + G[0] * s0;
      const float g1r = G[2] * c1 - G[3] * s1, g1i = G[3] * c1 + G[2] * s1;
      f32x4 y = mfma16(u32x2{pk(g0r, g0i), pk(g1r, g1i)}, c.m, z4);
      ga[td][0] = up2(pk2(up2(pk(y[0], y[1])) * gg));
      ga[td][1] = up2(pk2(up2(pk(y[2], y[3])) * gg));
      pin(ga[td][0]); pin(ga[td][1]);
    }
  }
  STAMP(17);
  if (P) { poll_flags(ctl + CT_FB + (b & 7) * FR_ + w * 28 * FL, 28, epoch); bar(); }
  STAMP(18);
  if (tid < D_) {
    const u32x4 bv = ld16<P>(rws, boff, (u32)WS_B + (((s2 & 7) * 8 + (s2 >> 3)) * 32 + 8 * q) * 2);
#pragma unroll
    for (int e = 0; e < 4; ++e) {
      lds_st<u16>(biw + (2 * e) * BRS, (u16)bv[e]);
      lds_st<u16>(biw + (2 * e + 1) * BRS, (u16)(bv[e] >> 16));
    }
  }
  bar();
  {
    const rsrc_t ro_ = mkrs(p.out, S_ * D_ * 2);
    const float og = lds_ld<float>(lb + L_G + w * 8 + 4);
    const u32x2 bq0 = lds_ld<u32x2>(bir), bq1 = lds_ld<u32x2>(bir + 32);
#pragma unroll
    for (int td = 0; td < 2; ++td) {
      const u32x2 bq = td ? bq1 : bq0;
      const f32x2 o01 = ga[td][0] + up2(pk2(up2(bq.x) * og));
      const f32x2 o23 = ga[td][1] + up2(pk2(up2(bq.y) * og));
      if (i16 < 14) __builtin_amdgcn_raw_buffer_store_b64(u32x2{pk2(o01), pk2(o23)}, ro_, ooff, td * 32, 0);
    }
  }
  STAMP(19);
  if (P && b == 0 && tid == 0) __hip_atomic_store((GAS u32*)ctl + CT_GEN, epoch, RLX_AGENT);
}

template <int PH>
DI void fft_upper(const Params& p, const u32 lb, const int b, u32* ctl, u32& gen, u32x4 (&fc)[2], u32x4 (&fs)[2], P3C& c3) {
  constexpr bool P = PH == 0;
  const int lane = threadIdx.x & 63, w = __builtin_amdgcn_readfirstlane(threadIdx.x >> 6);
  const u32 l16 = lane * 16;
  const rsrc_t rtf = mkrs(TF, TF_N * 1024);
  if (w == 4) p3_make_b(lb, b);
  if (P) {
    gen = __hip_atomic_load((GAS u32*)ctl + CT_GEN, RLX_AGENT);
    bar(); bar(); bar();
    bar(); bar(); bar();
    __builtin_amdgcn_s_setprio(SH2PRIO);
  }
  if (P) gate_rows<P>(p, lb, b, w, 1); else gate_rows<P>(p, lb, b, 2 * (w - 4), 2);
  fc[0] = tf(rtf, TF_FC0, l16); fs[0] = tf(rtf, TF_FS0, l16);
  fc[1] = xr(fc[0], 0x80000000u); fs[1] = xr(fs[0], 0x80000000u);
  pin(fc[0]); pin(fc[1]); pin(fs[0]); pin(fs[1]);
  if (!P) bar();
  p3_prepare<PH>(p, lb, b, rtf, c3);
  if (!P) bar();
}

template <int PH>
DI void fft_role(const Params& p, const u32 lb, const int b, u32* ctl, u32& gen, u32x4 (&fc)[2], u32x4 (&fs)[2], P3C& c3 STAMP_ARG) {
  constexpr bool P = PH == 0;
#define TPAUXV 16
  constexpr int AUX = P ? 16 : 0, TPAUX = P ? TPAUXV : 0;
  (void)AUX;
  const int tid = threadIdx.x, lane = tid & 63, w = __builtin_amdgcn_readfirstlane(tid >> 6), i16 = lane & 15, g = lane >> 4;
  const u32 l16 = lane * 16;
  const int s2 = b & 63, q = b >> 6;
  const int xx = b & 7, mm = (b >> 3) & 3, kq = b >> 5;
  const int k1w = (((mm & 1) ? (xx ? 32 - xx : 8) : xx) + 16 * (mm >> 1)) & 31;
  const f32x4 z4 = {0, 0, 0, 0};
  const rsrc_t rws = mkrs(p.ws, 16u << 20), rtf = mkrs(TF, TF_N * 1024);
  const u32 tq = (TF_Q0 + 6 * q) * 1024;
  if (P) __builtin_amdgcn_s_setprio(3);

  if (PH == 0 || PH == 1) {
    const rsrc_t rx = mkrs(p.x, S_ * D_ * 2);
    if (lane < 56) {
#pragma unroll
      for (int r = 0; r < 8; ++r) dma16<0>(rx, lb + L_XT + (8 * w + r) * XRS, l16, (64 * (8 * w + r) + s2) * (D_ * 2));
    }
  }
  if (P) gen = __hip_atomic_load((GAS u32*)ctl + CT_GEN, RLX_AGENT);
  const u32 tpo = (u32)((tid >> 3) * TP_S2 + (tid & 7) * 16), tps = (u32)WS_TP + k1w * 1024 + kq * 128;
  if (PH == 0 || PH == 1) {
    dma16<0>(rtf, lb + L_C1 + w * 2048, l16, w == 0 ? 0u : tq + (2 * w - 2) * 1024);
    dma16<0>(rtf, lb + L_C1 + w * 2048 + 1024, l16, w == 0 ? 1024u : tq + (2 * w - 1) * 1024);
  } else {
    fc[0] = tf(rtf, TF_FC0, l16); fs[0] = tf(rtf, TF_FS0, l16);
    fc[1] = xr(fc[0], 0x80000000u); fs[1] = xr(fs[0], 0x80000000u);
  }

  u32x4 co[2], so[2], cot[2], sot[2];
  auto load_p2 = [&]() {
    const bool mir = kq >= 4;
    const int col0 = mir ? 448 - (56 * kq + 49) : 56 * kq;
    const rsrc_t rr = mkrs(p.wr, S_ * DF * 2), ri_ = mkrs(p.wi, S_ * DF * 2);
#pragma unroll
    for (int i = 0; i < 3; ++i) {
      const int gi = w + 4 * i;
      if (gi < 9) {
        const int gam = 64 * gi + lane, k2 = (gam * 7282) >> 16, chunk = gam - 9 * k2;
        const int ks = k1w + 32 * k2, row = mir ? ((2048 - ks) & 2047) : ks;
        const u32 vo = (u32)(((row * DF + col0) * 2) & ~15) + chunk * 16;
        dma16<0>(rr, lb + L_W + gi * 1024, vo, 0);
        dma16<0>(ri_, lb + L_W + 9216 + gi * 1024, vo, 0);
      }
    }
    dma16<0>(rtf, lb + L_C2 + w * 1024, l16, (TF_CO0 + w) * 1024);
  };

  if (PH == 0 || PH == 1) {
    u32x4 C1, S1, nS1;
    {
      const float k1 = (float)(16 * (w >> 1) + i16) * (1.0f / 2048.0f), P0 = k1 * (float)(64 * g + s2), Pl = k1 * 256.f;
      float c[8], s[8];
#pragma unroll
      for (int j = 0; j < 8; ++j) { const float ph = __builtin_fmaf(Pl, (float)j, P0); c[j] = cosr(ph); s[j] = sinr(ph); }
      C1 = pk8(c); S1 = pk8(s); nS1 = xr(S1, 0x80008000u);
      pin(C1); pin(S1); pin(nS1);
    }
    int d0a = 8 * (g >> 1) + (i16 >> 2), d0b = d0a + 4;
    d0b = d0b < 14 ? d0b : 0;
    u32 xa = lb + L_XT + (8 * w + (g & 1)) * XRS + 8 * (i16 & 3);
    u32 xb = xa + d0b * 64; xa += d0a * 64;
    u32 xw = lb + L_XCH + w * 256 + lane * 4;
    u32 xr_ = lb + L_XCH + (w & 1) * 4096 + lane * 4;
    u32 toff = (u32)((16 * (w >> 1) + i16) * T_K1 + 4 * (w & 1) * 32 + g * 16);
    u32 imm = (i16 & 1) ? 0x80008000u : 0u;
    pin(xa); pin(xb); pin(xw); pin(xr_); pin(toff); pin(imm);
    const u32 tso = (u32)WS_T + s2 * 1024 + q * 256;
    STAMP(1);
    wait_vm0();
    bar();
    const u32 cb = lb + L_C1 + l16;
    fc[0] = lds_ld<u32x4>(cb); fs[0] = lds_ld<u32x4>(cb + 1024);
    const u32x4 Ba1 = lds_ld<u32x4>(cb + 2048), Ba2 = lds_ld<u32x4>(cb + 3072);
    const f32x4 twc0 = lds_ld<f32x4>(cb + 4096), twc1 = lds_ld<f32x4>(cb + 5120), tws0 = lds_ld<f32x4>(cb + 6144), tws1 = lds_ld<f32x4>(cb + 7168);
    fc[1] = xr(fc[0], 0x80000000u); fs[1] = xr(fs[0], 0x80000000u);
    STAMP(2);
    f32x4 D2[2][2];
#pragma unroll
    for (int sg = 0; sg < 2; ++sg) {
      f32x4 H[2];
#pragma unroll
      for (int td = 0; td < 2; ++td) {
        const int o = td * 32 + sg * 4 * XRS;
        u32x4 A0 = cat(trread(xa + o), trread(xb + o));
        u32x4 A1 = cat(trread(xa + o + 2 * XRS), trread(xb + o + 2 * XRS));
        H[td] = mfma32(A1, Ba2, mfma32(A0, Ba1, z4));
      }
      float h[8];
#pragma unroll
      for (int j = 0; j < 4; ++j) {
        h[j] = __builtin_fmaf(fx1(H[0][j]), tws0[j], H[0][j] * twc0[j]);
        h[4 + j] = __builtin_fmaf(fx1(H[1][j]), tws1[j], H[1][j] * twc1[j]);
      }
      u32x4 A = pk8(h), A2 = sx1(A, imm);
#pragma unroll
      for (int tk = 0; tk < 2; ++tk) D2[sg][tk] = mfma32(A2, fs[tk], mfma32(A, fc[tk], z4));
    }
#pragma unroll
    for (int tk = 0; tk < 2; ++tk)
#pragma unroll
      for (int r = 0; r < 4; ++r) lds_st<u32>(xw + (tk * 4 + r) * 1024, pk(D2[0][tk][r], D2[1][tk][r]));
    bar();
    STAMP(3);
    {
      u32 pc[4][2];
#pragma unroll
      for (int kk = 0; kk < 2; ++kk) {
        const u32 br = xr_ + kk * 2048;
        u32x4 Xr = {lds_ld<u32>(br), lds_ld<u32>(br + 256), lds_ld<u32>(br + 512), lds_ld<u32>(br + 768)};
        u32x4 Xi = {lds_ld<u32>(br + 1024), lds_ld<u32>(br + 1280), lds_ld<u32>(br + 1536), lds_ld<u32>(br + 1792)};
        f32x4 Tr = mfma32(Xi, S1, mfma32(Xr, C1, z4));
        f32x4 Ti = mfma32(Xr, nS1, mfma32(Xi, C1, z4));
        pc[kk][0] = pk(Tr[0], Tr[1]); pc[kk][1] = pk(Tr[2], Tr[3]);
        pc[2 + kk][0] = pk(Ti[0], Ti[1]); pc[2 + kk][1] = pk(Ti[2], Ti[3]);
      }
      u32x4 lo = {pc[0][0], pc[0][1], pc[1][0], pc[1][1]}, hi = {pc[2][0], pc[2][1], pc[3][0], pc[3][1]};
#pragma unroll
      for (int e = 0; e < 4; ++e) {
        auto r1 = __builtin_amdgcn_permlane16_swap(lo[e], hi[e], false, false);
        auto r2 = __builtin_amdgcn_permlane32_swap(r1[0], r1[1], false, false);
        lo[e] = r2[0]; hi[e] = r2[1];
      }
      st16<P>(rws, toff, tso, lo);
      st16<P>(rws, toff + 64, tso, hi);
    }
    STAMP(4);
    if (P) {
      drain_vm();
      STAMP(20);
      bar();
      publish(ctl + CT_F1, b, gen + 1u);
      __builtin_amdgcn_s_setprio(2);
      load_p2();
    }
    STAMP(5);
  }
  const u32 epoch = __builtin_amdgcn_readfirstlane(gen) + 1u;

  if (PH == 2) load_p2();
  if (PH == 0 || PH == 2) {
    const int ri = i16 >> 3;
    u32 tsv = (u32)((lane >> 1) * 1024 + w * 256 + (lane & 1) * 16);
    u32 tiw = lb + L_TI + w * 2048 + ((lane >> 1) & 1) * 1024 + (lane >> 2) * 32 + (lane & 1) * 16;
    u32 ta = lb + L_TI + w * 2048 + (4 * g + (i16 >> 2)) * 32 + (i16 & 3) * 8;
    u32 tow = lb + L_TO + 2 * i16 * 144 + w * 32 + g * 8;
    u32 tor = lb + L_TO + (tid >> 3) * 144 + (tid & 7) * 16;
    u32 mf = ri ? 0x80008000u : 0u, mi = ri ? 0u : 0x80008000u;
    float wr_[16], wi_[16];
    {
      const bool mir = kq >= 4;
      const int c = i16 & 7, kd = 2 * w + (c >> 2) + 14 * (4 * kq + (c & 3)), col0 = mir ? 448 - (56 * kq + 49) : 56 * kq;
      const int row0 = mir ? ((2048 - k1w) & 2047) : k1w;
      u32 wa = lb + L_W + 4 * g * 144 + (((row0 * DF + col0) * 2) & 15) + ((mir ? 448 - kd : kd) - col0) * 2;
      const u32 sg = (mir != (ri != 0)) ? 0u : 0x80000000u;
      pin(tsv); pin(tiw); pin(ta); pin(tow); pin(tor); pin(mf); pin(mi); pin(wa);
      STAMP(7);
      if (P) poll_flags(ctl + CT_F1 + (b & 7) * FR_ + w * 64 * FL, 64, epoch);
      wait_vm0();
      bar();
      const u32 so_ = (u32)WS_T + k1w * T_K1 + kq * 32;
      const u32x4 t0 = ld16<P>(rws, tsv, so_), t1 = ld16<P>(rws, tsv, so_ + 32 * 1024);
      __builtin_amdgcn_sched_barrier(0);
      co[0] = lds_ld<u32x4>(lb + L_C2 + l16); so[0] = lds_ld<u32x4>(lb + L_C2 + 1024 + l16);
      cot[0] = lds_ld<u32x4>(lb + L_C2 + 2048 + l16); sot[0] = lds_ld<u32x4>(lb + L_C2 + 3072 + l16);
      co[1] = xr(so[0], 0x00008000u);
      so[1] = xr(co[0], 0x80000000u);
      cot[1] = xr(cot[0], 0x80000000u); sot[1] = xr(sot[0], 0x80000000u);
      pin(co[0]); pin(co[1]); pin(so[0]); pin(so[1]); pin(cot[0]); pin(cot[1]); pin(sot[0]); pin(sot[1]);
#pragma unroll
      for (int j = 0; j < 16; ++j) {
        const int o = (32 * (j >> 3) + 16 * ((j >> 2) & 1) + (j & 3)) * 144;
        wr_[j] = bc<float>((u32)lds_ld<u16>(wa + o) << 16);
        wi_[j] = bc<float>(((u32)lds_ld<u16>(wa + 9216 + o) << 16) + sg);
      }
#pragma unroll
      for (int j = 0; j < 16; ++j) { pin(wr_[j]); pin(wi_[j]); }
      __builtin_amdgcn_sched_barrier(0);
      if (P) __builtin_amdgcn_s_setprio(3);
      STAMP(8);
      lds_st<u32x4>(tiw, t0);
      lds_st<u32x4>(tiw + 512, t1);
    }
    STAMP(9);
    f32x4 E[2], O[2];
    {
      u32x4 B0 = cat(trread(ta), trread(ta + 512)), B1 = cat(trread(ta + 1024), trread(ta + 1536));
      u32x4 B0s = sr8(B0, mf), B1s = sr8(B1, mf);
#pragma unroll
      for (int t = 0; t < 2; ++t) {
        E[t] = mfma32(fs[t], B0s, mfma32(fc[t], B0, z4));
        O[t] = mfma32(so[t], B1s, mfma32(co[t], B1, z4));
      }
    }
    float Pp[8], Mm[8];
#pragma unroll
    for (int j = 0; j < 8; ++j) {
      const float e = E[j >> 2][j & 3], o = O[j >> 2][j & 3];
      const float ylo = e + o, yhi = e - o;
      const float vlo = __builtin_fmaf(fr8(ylo), wi_[j], ylo * wr_[j]);
      const float vhi = __builtin_fmaf(fr8(yhi), wi_[8 + j], yhi * wr_[8 + j]);
      Pp[j] = vlo + vhi; Mm[j] = vlo - vhi;
    }
    {
      u32x4 AP = pk8(Pp), AM = pk8(Mm);
      u32x4 APs = sr8(AP, mi), AMs = sr8(AM, mi);
#pragma unroll
      for (int t = 0; t < 2; ++t) {
        f32x4 ze = mfma32(APs, fs[t], mfma32(AP, fc[t], z4));
        f32x4 zo = mfma32(AMs, sot[t], mfma32(AM, cot[t], z4));
        lds_st<u32x2>(tow + t * 32 * 144, u32x2{pk(ze[0], ze[1]), pk(ze[2], ze[3])});
        lds_st<u32x2>(tow + t * 32 * 144 + 144, u32x2{pk(zo[0], zo[1]), pk(zo[2], zo[3])});
      }
    }
    bar();
    STAMP(10);
    {
      st16<P>(rws, tpo, tps, lds_ld<u32x4>(tor));
      st16<P>(rws, tpo, tps + 32 * TP_S2, lds_ld<u32x4>(tor + 32 * 144));
    }
    STAMP(11);
    if (P) { drain_vm(); bar(); publish(ctl + CT_F2, b, epoch); __builtin_amdgcn_s_setprio(SH2PRIO); }
    STAMP(12);
  }

  if (PH == 0) gate_rows<true>(p, lb, b, w, 1);
  if (PH == 3) bar();
  if (PH == 0 || PH == 3) p3_prepare<PH>(p, lb, b, rtf, c3);
  if (PH == 3) bar();
}

DI f32x2 cexp_rev(float x2, float ph) {
  const float e = __builtin_amdgcn_exp2f(x2);
  const float f = __builtin_amdgcn_fractf(ph);
  f32x2 cs = {cosr(f), sinr(f)};
  return cs * e;
}
struct KW { u32 a[8]; };
template <int ROW> DI void wrow(const KW& k, f32x2 v) { lds_st<u32>(k.a[ROW & 7] + ROW * 256, pk2(v)); }
template <int ROW> DI void wrow2(const KW& k, f32x2 lo, f32x2 hi) {
  const u32 a = pk2(lo), b = pk2(hi);
  asm volatile("ds_write2st64_b32 %0, %1, %2 offset0:%c3 offset1:%c4" :: "v"(k.a[ROW & 7]), "v"(a), "v"(b), "i"(ROW), "i"(ROW + 8) : "memory");
}
DI void kfrag(u32 fa, u32 fb, u32x4 (&A)[4]) {
  A[0] = lds_ld<u32x4>(fa); A[1] = lds_ld<u32x4>(fb); A[2] = lds_ld<u32x4>(fa + 128); A[3] = lds_ld<u32x4>(fb + 128);
}
DI f32x4 ktile(const u32x4 (&A)[4], const u32x4 (&V)[4]) {
  f32x4 acc = {0, 0, 0, 0};
#pragma unroll
  for (int ks = 0; ks < 4; ++ks) acc = mfma32(A[ks], V[ks], acc);
  return acc;
}
template <class F0, class F1>
DI void kgen(const u32 (&in)[6], float dt, const KW& k, u32 fa, u32 fb, u32 pb, F0&& hook0, F1&& hook1) {
  const float ar = dt * bc<float>(in[0] << 16), ai = dt * bc<float>(in[1] << 16);
  const f32x2 dB = {dt * bc<float>(in[2] << 16), dt * bc<float>(in[3] << 16)};
  const f32x2 Cc = {bc<float>(in[4] << 16), bc<float>(in[5] << 16)};
  const f32x2 CB = cmul(Cc, dB);
  const float x1 = ar * 1.44269504088896341f, p1 = ai * 0.15915494309189535f;
  const f32x2 E1 = cexp_rev(x1, p1), E8c = cexp_rev(8.f * x1, -8.f * p1), E128 = cexp_rev(128.f * x1, 128.f * p1);
  u32x4 V[4], A0[4], A1[4];
  {
    const f32x2 v2 = cmul(E8c, E8c), v3 = cmul(v2, E8c), v4 = cmul(v2, v2), v8 = cmul(v4, v4);
    const f32x2 v5 = cmul(v4, E8c), v6 = cmul(v4, v2), v7 = cmul(v4, v3);
    wrow2<0>(k, f32x2{1.f, 0.f}, v8); wrow2<1>(k, E8c, cmul(v8, E8c)); wrow2<2>(k, v2, cmul(v8, v2)); wrow2<3>(k, v3, cmul(v8, v3));
    wrow2<4>(k, v4, cmul(v8, v4)); wrow2<5>(k, v5, cmul(v8, v5)); wrow2<6>(k, v6, cmul(v8, v6)); wrow2<7>(k, v7, cmul(v8, v7));
    kfrag(fa, fb, V);
  }
  hook0();
  const f32x2 E2 = cmul(E1, E1), E3 = cmul(E2, E1), E4 = cmul(E2, E2), E5 = cmul(E4, E1), E6 = cmul(E4, E2), E7 = cmul(E4, E3);
  const f32x2 G1 = cmul(CB, E128);
  wrow2<0>(k, CB, G1); wrow2<1>(k, cmul(CB, E1), cmul(G1, E1)); wrow2<2>(k, cmul(CB, E2), cmul(G1, E2)); wrow2<3>(k, cmul(CB, E3), cmul(G1, E3));
  wrow2<4>(k, cmul(CB, E4), cmul(G1, E4)); wrow2<5>(k, cmul(CB, E5), cmul(G1, E5)); wrow2<6>(k, cmul(CB, E6), cmul(G1, E6)); wrow2<7>(k, cmul(CB, E7), cmul(G1, E7));
  kfrag(fa, fb, A0);
  const f32x2 G2 = cmul(G1, E128), G3 = cmul(G2, E128), G4 = cmul(G3, E128), G5 = cmul(G4, E128), G6 = cmul(G5, E128), G7 = cmul(G6, E128);
  wrow2<0>(k, G2, G4); wrow2<1>(k, cmul(G2, E2), cmul(G4, E4)); wrow2<2>(k, cmul(G2, E4), G5); wrow2<3>(k, cmul(G2, E6), cmul(G5, E4));
  wrow2<4>(k, G3, G6); wrow2<5>(k, cmul(G3, E2), cmul(G6, E4)); wrow2<6>(k, cmul(G3, E4), G7); wrow2<7>(k, cmul(G3, E6), cmul(G7, E4));
  kfrag(fa, fb, A1);
  hook1();
  __builtin_amdgcn_sched_barrier(0);
  const f32x4 a0 = ktile(A0, V);
  const f32x2 G8 = cmul(G7, E128), G9 = cmul(G8, E128), G10 = cmul(G9, E128), G11 = cmul(G10, E128);
  const f32x2 G12 = cmul(G11, E128), G13 = cmul(G12, E128), G14 = cmul(G13, E128), G15 = cmul(G14, E128);
  wrow2<0>(k, G8, cmul(G15, E128)); wrow<1>(k, G9); wrow<2>(k, G10); wrow<3>(k, G11);
  wrow<4>(k, G12); wrow<5>(k, G13); wrow<6>(k, G14); wrow<7>(k, G15);
  kfrag(fa, fb, A0);
  __builtin_amdgcn_sched_barrier(0);
  const f32x4 a1 = ktile(A1, V), a2 = ktile(A0, V);
  lds_st<f32x4>(pb, a0);
  lds_st<f32x4>(pb + 1024, a1);
  lds_st<f32x4>(pb + 2048, a2);
}

template <int PH>
DI void ssm_role(const Params& p, const u32 lb, const int j, u32* ctl, u32& gen STAMP_ARG) {
  constexpr bool P = PH == 0;
  const int tid = threadIdx.x, lane = tid & 63, w = __builtin_amdgcn_readfirstlane(tid >> 6), i16 = lane & 15, g = lane >> 4;
  const int d = 2 * j, ch = w >> 2, n = tid & 255;
  const f32x4 z4 = {0, 0, 0, 0};
  const rsrc_t rws = mkrs(p.ws, 16u << 20);
  if (P) __builtin_amdgcn_s_setprio(3);
  const int sb_ = j / 7, db_ = j - sb_ * 7;
  u32x4 xt_v = {0, 0, 0, 0};
  if (PH == 0 || PH == 1)
    xt_v = __builtin_amdgcn_raw_buffer_load_b128(mkrs(p.x, S_ * D_ * 2), (u32)((tid >> 3) * (D_ * 2) + (tid & 7) * 16), (u32)(64 * sb_ * (D_ * 2) + db_ * 128), 0);
  u32 in[6];
  u32x4 kt0 = {0, 0, 0, 0}; u32x2 kt1 = {0, 0};
  u32 ldt = 0, dsk = 0;
  if (PH != 1) {
    const u32 vo = n * 2, so_ = (d + ch) * (NS * 2), nb = D_ * NS * 2;
    const u16* arr[6] = {p.are, p.aim, p.bre, p.bim, p.cre, p.cim};
#pragma unroll
    for (int a = 0; a < 6; ++a) in[a] = __builtin_amdgcn_raw_buffer_load_b16(mkrs(arr[a], nb), vo, so_, 0);
    ldt = *(const GAS u16*)(p.logdt + d + ch);
    dsk = *(const GAS u16*)(p.dskip + d + ((w >> 1) & 1));
    kt0 = *(const GAS u32x4*)(KTAB.v + 8 * n);
    kt1 = *(const GAS u32x2*)(KTAB.v + 8 * n + 8);
  }
  if (P) {
    gen = __hip_atomic_load((GAS u32*)ctl + CT_GEN, RLX_AGENT);
  }
  auto transpose = [&]() {
    lds_st<u32x4>(lb + L_XP + (tid >> 3) * 144 + (tid & 7) * 16, xt_v);
    bar();
    const int chi = g + 4 * (w >> 2);
    const u32 a = lb + L_XP + (8 * chi + (i16 >> 2)) * 144 + (16 * (w & 3) + 4 * (i16 & 3)) * 2;
    st16<P>(rws, (u32)((16 * (w & 3) + i16) * XT_D + chi * 16), (u32)WS_XT + 64 * db_ * XT_D + sb_ * 128, cat(trread(a), trread(a + 4 * 144)));
  };
  if (PH == 1) { transpose(); return; }
  if (P) {
    if (tid == 0) lds_st<u32>(lb + L_CNT, 0u);
    __builtin_amdgcn_sched_barrier(0);
    transpose();
    __builtin_amdgcn_s_setprio(1);
    __builtin_amdgcn_sched_barrier(0);
  }
  KW kw;
  const u32 sb = lb + w * KSCR;
#pragma unroll
  for (int h = 0; h < 8; ++h) { kw.a[h] = sb + (((lane >> 2) ^ h) << 4) + (lane & 3) * 4; pin(kw.a[h]); }
  u32 fa = sb + i16 * 256 + ((g ^ (i16 & 3)) << 4) + ((i16 >> 2) & 1) * 64, fb = fa ^ 64;
  pin(fa); pin(fb);
  STAMP(1);
  kgen(in, bfr(expf(bc<float>(ldt << 16))), kw, fa, fb, sb + lane * 16,
       [&]() {
         if (P) {
           drain_vm();
           STAMP(5);
           if (lane == 0 && __hip_atomic_fetch_add((LAS u32*)(size_t)(lb + L_CNT), 1u, __ATOMIC_RELAXED, __HIP_MEMORY_SCOPE_WORKGROUP) == 7u)
             __hip_atomic_store((GAS u32*)ctl + CT_FX + (db_ * 32 + sb_) * FL, gen + 1u, RLX_AGENT);
         }
       },
       []() {});
  const u32 epoch = __builtin_amdgcn_readfirstlane(gen) + 1u;
  pin(dsk);
  STAMP(2);
  if (P && w == 0) poll_flags(ctl + CT_FX + (d >> 6) * 32 * FL, 32, epoch);
  STAMP(6);
  bar();
  const u32x4 uv_ = ld16<P>(rws, n * 16, (u32)WS_XT + (d + ch) * XT_D);
  STAMP(3);
  if ((w & 3) < 3) {
    const u32 pb = lb + ch * 4 * KSCR + (w & 3) * 1024 + lane * 16;
    const f32x4 s = (lds_ld<f32x4>(pb) + lds_ld<f32x4>(pb + KSCR)) + (lds_ld<f32x4>(pb + 2 * KSCR) + lds_ld<f32x4>(pb + 3 * KSCR));
    const u32 tb = lb + L_KT + ch * 1600 + ((w & 3) * 256 + 64 * g + i16) * 2, a = pk(s[0], s[1]), cc = pk(s[2], s[3]);
    lds_st<u16>(tb, (u16)a); lds_st<u16>(tb + 32, (u16)(a >> 16));
    lds_st<u16>(tb + 64, (u16)cc); lds_st<u16>(tb + 96, (u16)(cc >> 16));
  } else {
    if (lane == 0) lds_st<u16>(lb + L_KT + ch * 1600 + 1536, (u16)0);
    if (lane < 32) lds_st<u32x4>(lb + L_U + ch * 4608 + lane * 16, u32x4{0, 0, 0, 0});
  }
  bar();
  STAMP(4);
  {
    const u32 ktb = lb + L_KT + ch * 1600;
    u32 e[11];
#pragma unroll
    for (int k = 0; k < 4; ++k) { e[2 * k] = lds_ld<u16>(ktb + (kt0[k] & 0xffffu)); e[2 * k + 1] = lds_ld<u16>(ktb + (kt0[k] >> 16)); }
    e[8] = lds_ld<u16>(ktb + (kt1[0] & 0xffffu)); e[9] = lds_ld<u16>(ktb + (kt1[0] >> 16)); e[10] = lds_ld<u16>(ktb + (kt1[1] & 0xffffu));
    const u32 k00 = lds_ld<u16>(ktb);
    u32 dw[5], al[5];
#pragma unroll
    for (int k = 0; k < 5; ++k) { dw[k] = e[2 * k] | (e[2 * k + 1] << 16); al[k] = e[2 * k + 1] | (e[2 * k + 2] << 16); }
    const u32 kb = lb + L_KC + ch * 4 * KCP + n * 16;
    lds_st<u32x4>(kb, u32x4{dw[0], dw[1], dw[2], dw[3]});
    lds_st<u32x4>(kb + KCP, u32x4{al[0], al[1], al[2], al[3]});
    lds_st<u32x4>(kb + 2 * KCP, u32x4{dw[1], dw[2], dw[3], dw[4]});
    lds_st<u32x4>(kb + 3 * KCP, u32x4{al[1], al[2], al[3], al[4]});
    if (n < 16) {
      u32x4 z = {0, 0, 0, 0};
      if (n == 0) z.x = k00;
      lds_st<u32x4>(lb + L_KC + (ch * 4 + (n >> 2)) * KCP + 4096 + (n & 3) * 16, z);
    }
    lds_st<u32x4>(lb + L_U + ch * 4608 + 512 + n * 16, uv_);
  }
  bar();
  STAMP(7);
  f32x4 acc[8];
  const int cc = (w >> 1) & 1, hf = w & 1;
  if (w < 4) {
#pragma unroll
    for (int c = 0; c < 8; ++c) acc[c] = z4;
    const int bsh = (-i16) & 3;
    u32 abase = lb + L_KC + (cc * 4 + bsh) * KCP + (2032 + 8 * g - i16 - bsh) * 2 - hf * 4 * 64 - 3 * 64 - 7 * 512;
    u32 bbase = lb + L_U + cc * 4608 + (256 - 16 + 16 * i16 + 8 * g) * 2 - hf * 4 * 64 - 3 * 64;
    u32 abase8 = abase + 8;
    pin(abase); pin(abase8); pin(bbase);
    u32x4 A[8], B[8];
#pragma unroll
    for (int qq = 0; qq < 8; ++qq) {
      B[7 - qq] = lds_ld<u32x4>(bbase + (7 - qq) * 512 + 3 * 64);
      A[qq] = cat(lds_ld<u32x2>(abase + (7 - qq) * 512 + 3 * 64), lds_ld<u32x2>(abase8 + (7 - qq) * 512 + 3 * 64));
      __builtin_amdgcn_sched_barrier(0);
    }
#pragma unroll
    for (int rr = 0; rr < 4; ++rr) {
#pragma unroll
      for (int qq = 0; qq < 8; ++qq) {
#pragma unroll
        for (int m = 7 - qq; m >= 0; --m) {
          acc[qq + m] = mfma32(A[qq], B[m], acc[qq + m]);
          if (CNOP >= 0 && rr < CNOPR) asm volatile("s_nop %c0" :: "i"(CNOP >= 0 ? CNOP : 0));
        }
        if (rr < 3) {
          A[qq] = cat(lds_ld<u32x2>(abase + (7 - qq) * 512 + (2 - rr) * 64), lds_ld<u32x2>(abase8 + (7 - qq) * 512 + (2 - rr) * 64));
          B[7 - qq] = lds_ld<u32x4>(bbase + (7 - qq) * 512 + (2 - rr) * 64);
        }
        __builtin_amdgcn_sched_barrier(0);
      }
    }
    if (hf) {
#pragma unroll
      for (int c = 0; c < 4; ++c) lds_st<f32x4>(lb + L_RED + (w * 4 + c) * 1024 + lane * 16, acc[c]);
#pragma unroll
      for (int c = 0; c < 4; ++c) acc[c] = acc[4 + c];
    } else {
#pragma unroll
      for (int c = 0; c < 4; ++c) lds_st<f32x4>(lb + L_RED + (w * 4 + c) * 1024 + lane * 16, acc[4 + c]);
    }
  }
  bar();
  STAMP(8);
  if (w < 4) {
    const float dk = bc<float>(dsk << 16);
    const u32 tb = (1024 * hf + 16 * i16 + 4 * g) * 2;
    f32x4 o[4];
    u32x2 uv[4];
#pragma unroll
    for (int c = 0; c < 4; ++c) {
      o[c] = lds_ld<f32x4>(lb + L_RED + ((w ^ 1) * 4 + c) * 1024 + lane * 16);
      uv[c] = lds_ld<u32x2>(lb + L_U + cc * 4608 + 512 + tb + c * 512);
    }
#pragma unroll
    for (int c = 0; c < 4; ++c) {
      const f32x4 y = acc[c] + o[c];
      const f32x2 b01 = up2(pk(y[0], y[1])) + up2(pk2(up2(uv[c].x) * dk));
      const f32x2 b23 = up2(pk(y[2], y[3])) + up2(pk2(up2(uv[c].y) * dk));
      lds_st<u32x2>(lb + L_BO + cc * 4096 + tb + c * 512, u32x2{pk2(b01), pk2(b23)});
    }
  }
  bar();
  STAMP(9);
  {
    const int st = w & 3, s2 = 16 * st + i16;
    const u32 a = lb + L_BO + ch * 4096 + (8 * g + (i16 >> 2)) * 128 + (16 * st + 4 * (i16 & 3)) * 2;
    st16<P>(rws, (u32)((((s2 & 7) * 8 + (s2 >> 3)) * 32 + 8 * g) * 2), (u32)WS_B + (d + ch) * B_D, cat(trread(a), trread(a + 4 * 128)));
  }
  STAMP(10);
  if (P) { drain_vm(); bar(); publish(ctl + CT_FB, j, epoch); }
  STAMP(11);
}

template <int PH>
__global__ __launch_bounds__(NT, 4) void gfs4_kernel(Params p) {
  extern __shared__ __attribute__((aligned(16))) char smem[];
  const u32 lb = (u32)(size_t)(LAS char*)smem;
  const int b = blockIdx.x;
  u32* ctl = (u32*)(p.ws + WS_CTL);
  STAMP_DECL;
  if (PH == 0 || PH == 1) STAMP(0); else if (PH == 2) STAMP(20); else STAMP(21);
  u32 gen = 0;
  if (b < N_FFT) {
    u32x4 fc[2], fs[2];
    P3C c3;
    if (threadIdx.x >= 256) {
      if (PH == 1 || PH == 2) return;
      fft_upper<PH>(p, lb, b, ctl, gen, fc, fs, c3);
    } else fft_role<PH>(p, lb, b, ctl, gen, fc, fs, c3 STAMP_PASS);
    if (PH == 0 || PH == 3) fft_phase3<PH>(p, lb, b, ctl, __builtin_amdgcn_readfirstlane(gen) + 1u, fc, fs, c3 STAMP_PASS);
  }
  else ssm_role<PH>(p, lb, b - N_FFT, ctl, gen STAMP_PASS);
  STAMP_FLUSH();
}

extern "C" void kernel_launch(void* const* d_in, const int* in_sizes, int n_in,
                              void* d_out, int out_size, void* d_ws, size_t ws_size,
                              hipStream_t stream) {
  (void)in_sizes; (void)n_in; (void)out_size; (void)ws_size;
  Params p;
  p.x = (const u16*)d_in[0]; p.wr = (const u16*)d_in[1]; p.wi = (const u16*)d_in[2];
  p.are = (const u16*)d_in[4]; p.aim = (const u16*)d_in[5]; p.bre = (const u16*)d_in[6]; p.bim = (const u16*)d_in[7];
  p.cre = (const u16*)d_in[8]; p.cim = (const u16*)d_in[9]; p.logdt = (const u16*)d_in[10]; p.dskip = (const u16*)d_in[11];
  p.gw = (const u16*)d_in[12];
  p.out = (u16*)d_out; p.ws = (char*)d_ws;
  (void)hipFuncSetAttribute((const void*)gfs4_kernel<0>, hipFuncAttributeMaxDynamicSharedMemorySize, LDS_BYTES);
  gfs4_kernel<0><<<N_WG, NT, LDS_BYTES, stream>>>(p);
}
```

```cpp
#include <hip/hip_runtime.h>
#include <hip/hip_bf16.h>
#include <stdint.h>

typedef unsigned int u32;
typedef unsigned short u16;
typedef unsigned long long u64;
typedef float f32x4 __attribute__((ext_vector_type(4)));
typedef float f32x2 __attribute__((ext_vector_type(2)));
typedef u32 u32x4 __attribute__((ext_vector_type(4)));
typedef u32 u32x2 __attribute__((ext_vector_type(2)));
typedef __bf16 bf16x8 __attribute__((ext_vector_type(8)));
typedef __bf16 bf16x2 __attribute__((ext_vector_type(2)));
typedef short s16x4 __attribute__((ext_vector_type(4)));
#define LAS __attribute__((address_space(3)))
#define GAS __attribute__((address_space(1)))
#define DI __device__ __forceinline__

#define DIAG 5
#define NLAUNCH 1

constexpr int S_ = 2048, D_ = 448, DF = 225, NS = 256;
constexpr int N_FFT = 256, N_SSM = 224, N_WG = N_FFT + N_SSM, NT = 512;

constexpr u32 T_K1 = 65536 + 256;
constexpr u32 TP_S2 = 32768 + 128;
constexpr u32 XT_D = 4096 + 128;
constexpr u32 B_D = 4096 + 128;
constexpr size_t WS_CTL = 0;
constexpr size_t WS_T   = 1u << 20;
constexpr size_t WS_TP  = WS_T + (3u << 20);
constexpr size_t WS_XT  = WS_TP + (3u << 20);
constexpr size_t WS_B   = WS_XT + (2u << 20);
constexpr int FL = 32, FR_ = 256 * FL + 64;
constexpr int CT_GEN = 0;
constexpr int CT_F1 = 64;
constexpr int CT_F2 = CT_F1 + 8 * FR_;
constexpr int CT_FB = CT_F2 + 8 * FR_;
constexpr int CT_FX = CT_FB + 8 * FR_;
static_assert((CT_FX + 7 * 32 * FL) * 4 <= (1 << 20), "control block");

constexpr int XRS = 928;
constexpr int L_XT = 0;
constexpr int L_XCH = 29696;
constexpr int TPRS = 1040;
constexpr int L_TP = 0;
constexpr int L_TO = 29696;
constexpr int L_TI = 38912;
constexpr int L_BI = 38912;
constexpr int BRS = 912;
constexpr int L_Z = 47104;
constexpr int L_C1 = 38912;
constexpr int L_C2 = 65536;
constexpr int L_C3 = 70400;
constexpr int L_W = 47104;
constexpr int L_G = 70208;
constexpr int KCP = 4160;
constexpr int L_KC = 0;
constexpr int KSCR = 4096;
constexpr int L_RED = 33280;
constexpr int L_BO = 49664;
constexpr int L_U = 57856;
constexpr int L_XP = 57856;
constexpr int L_KT = 67072;
constexpr int L_CNT = 70272;
constexpr int LDS_BYTES = 72448;

struct Params {
  const u16* x; const u16* wr; const u16* wi;
  const u16* are; const u16* aim; const u16* bre; const u16* bim; const u16* cre; const u16* cim;
  const u16* logdt; const u16* dskip; const u16* gw;
  u16* out; char* ws;
};

constexpr double cx_sin_oct(double x) { double x2 = x * x, t = x, s = x; for (int i = 1; i < 9; ++i) { t *= -x2 / ((2 * i) * (2 * i + 1)); s += t; } return s; }
constexpr double cx_cos_oct(double x) { double x2 = x * x, t = 1, s = 1; for (int i = 1; i < 9; ++i) { t *= -x2 / ((2 * i - 1) * (2 * i)); s += t; } return s; }
constexpr double cx_sin(long n, long d) {
  n %= d; if (n < 0) n += d;
  const long o = (8 * n) / d;
  const double r = 6.283185307179586476925 * (double)(8 * n - o * d) / (8.0 * (double)d), q = 0.78539816339744830962;
  switch (o) {
    case 0: return cx_sin_oct(r);       case 1: return cx_cos_oct(q - r);
    case 2: return cx_cos_oct(r);       case 3: return cx_sin_oct(q - r);
    case 4: return -cx_sin_oct(r);      case 5: return -cx_cos_oct(q - r);
    case 6: return -cx_cos_oct(r);      default: return -cx_sin_oct(q - r);
  }
}
constexpr double cx_cos(long n, long d) { return cx_sin(4 * n + d, 4 * d); }
constexpr u32 cx_f32(double v) { return __builtin_bit_cast(u32, (float)v); }
constexpr u32 cx_bf(double v) { const u32 u = cx_f32(v); return (u + 0x7fffu + ((u >> 16) & 1u)) >> 16; }
constexpr int cx_pi(int g, int jj) { return 16 * (jj >> 2) + 4 * g + (jj & 3); }
struct Frag { u32 v[64][4]; };
enum { TF_FC0, TF_FS0, TF_CO0, TF_SO0, TF_COT0, TF_SOT0, TF_TC, TF_TS, TF_M, TF_Q0, TF_N = TF_Q0 + 24 };
constexpr Frag make_frag(int item) {
  Frag f{};
  for (int lane = 0; lane < 64; ++lane) {
    const int i16 = lane & 15, g = lane >> 4;
    if (item < TF_TC) {
      const int sn = item & 1;
      for (int jj = 0; jj < 8; ++jj) {
        const int m = cx_pi(g, jj), a = i16;
        long n = 0, d = 32;
        if (item < TF_CO0) { n = a * m; d = 32; }
        else if (item < TF_COT0) { n = a * (2 * m + 1); d = 64; }
        else { n = m * (2 * a + 1); d = 64; }
        f.v[lane][jj >> 1] |= cx_bf(sn ? cx_sin(n, d) : cx_cos(n, d)) << (16 * (jj & 1));
      }
    } else if (item == TF_TC || item == TF_TS) {
      for (int j = 0; j < 4; ++j) {
        const long n = (2 * g + (j & 1)) * (16 * (j >> 1) + i16);
        f.v[lane][j] = cx_f32(item == TF_TC ? cx_cos(n, 448) : cx_sin(n, 448));
      }
    } else if (item == TF_M) {
      for (int j = 0; j < 4; ++j) {
        const int k0 = 2 * g + (j >> 1);
        const double cf = ((k0 == 0 || k0 == 7) ? 1.0 : 2.0) / (2048.0 * 448.0);
        const double v = i16 < 14 ? cf * ((j & 1) ? -cx_sin(k0 * i16, 14) : cx_cos(k0 * i16, 14)) : 0.0;
        f.v[lane][j >> 1] |= cx_bf(v) << (16 * (j & 1));
      }
    } else {
      const int q = (item - TF_Q0) / 6, sub = (item - TF_Q0) % 6, k0 = 2 * q + ((i16 >> 1) & 1), ri = i16 & 1;
      if (sub < 2) {
        if ((i16 >> 2) == 2 * sub + (g & 1))
          for (int j = 0; j < 8; ++j) {
            const int d0 = 8 * (g >> 1) + j;
            const double v = d0 < 14 ? (ri ? -cx_sin(d0 * k0, 14) : cx_cos(d0 * k0, 14)) : 0.0;
            f.v[lane][j >> 1] |= cx_bf(v) << (16 * (j & 1));
          }
      } else {
        for (int j = 0; j < 4; ++j) {
          const long n = cx_pi(g, 4 * (sub & 1) + j) * k0;
          f.v[lane][j] = cx_f32(sub < 4 ? cx_cos(n, 448) : (ri ? -cx_sin(n, 448) : cx_sin(n, 448)));
        }
      }
    }
  }
  return f;
}
template <int I> struct FragC { static constexpr Frag f = make_frag(I); };
#define FR(i) FragC<(i)>::f
#define FR6(i) FR(i), FR(i + 1), FR(i + 2), FR(i + 3), FR(i + 4), FR(i + 5)
__device__ const __attribute__((aligned(1024))) Frag TF[TF_N] = {FR6(0), FR(6), FR(7), FR(8), FR6(9), FR6(15), FR6(21), FR6(27)};

struct KTab { u16 v[2080]; };
constexpr KTab make_ktab() {
  KTab t{};
  for (int i = 0; i < 2080; ++i) {
    const int l = 2048 - i;
    int idx = 768;
    if (l >= 0) {
      const int sh = l > 256 ? (l >= 1024 ? 3 : (l >= 512 ? 2 : 1)) : 0;
      const int v = sh ? (((l + (1 << (sh - 1)) - 1 + ((l >> sh) & 1)) >> sh) << sh) : l;
      if (v < 256) idx = ((v & 7) + 8 * (v >> 7)) * 16 + ((v >> 3) & 15);
      else if (v < 512) idx = 256 + ((((v - 256) >> 1) & 3) + 4 * ((v - 256) >> 7)) * 16 + (((v - 256) >> 3) & 15);
      else if (v < 1024) idx = 256 + (8 + (((v - 512) >> 2) & 1) + 2 * ((v - 512) >> 7)) * 16 + (((v - 512) >> 3) & 15);
      else if (v < 2048) idx = 512 + ((v - 1024) >> 7) * 16 + (((v - 1024) >> 3) & 15);
      else idx = 640;
    }
    t.v[i] = (u16)(2 * idx);
  }
  return t;
}
__device__ const __attribute__((aligned(16))) KTab KTAB = make_ktab();

template <class T, class F> DI T bc(F v) { return __builtin_bit_cast(T, v); }
DI f32x4 mfma32(u32x4 a, u32x4 b, f32x4 c) { return __builtin_amdgcn_mfma_f32_16x16x32_bf16(bc<bf16x8>(a), bc<bf16x8>(b), c, 0, 0, 0); }
DI f32x4 mfma16(u32x2 a, u32x2 b, f32x4 c) { return __builtin_amdgcn_mfma_f32_16x16x16bf16_1k(bc<s16x4>(a), bc<s16x4>(b), c, 0, 0, 0); }
DI u32 pk(float a, float b) { f32x2 v = {a, b}; return bc<u32>(__builtin_convertvector(v, bf16x2)); }
DI u32 pk2(f32x2 v) { return bc<u32>(__builtin_convertvector(v, bf16x2)); }
DI float bfr(float a) { return bc<float>(pk(0.f, a) & 0xffff0000u); }
DI float lo16(u32 v) { return bc<float>(v << 16); }
DI float hi16(u32 v) { return bc<float>(v & 0xffff0000u); }
DI f32x2 up2(u32 v) { return f32x2{lo16(v), hi16(v)}; }
DI float b2f(u16 v) { return bc<float>((u32)v << 16); }
DI u32x2 trread(u32 a) { return bc<u32x2>(__builtin_amdgcn_ds_read_tr16_b64_v4i16((LAS s16x4*)(size_t)a)); }
DI u32x4 cat(u32x2 a, u32x2 b) { u32x4 r = {a.x, a.y, b.x, b.y}; return r; }
DI float sinr(float x) { return __builtin_amdgcn_sinf(x); }
DI float cosr(float x) { return __builtin_amdgcn_cosf(x); }
DI float fx1(float v) { return __builtin_amdgcn_update_dpp(v, v, 0xB1, 0xf, 0xf, true); }
DI float fr8(float v) { return __builtin_amdgcn_update_dpp(v, v, 0x128, 0xf, 0xf, true); }
DI u32 ux1(u32 v) { return (u32)__builtin_amdgcn_update_dpp((int)v, (int)v, 0xB1, 0xf, 0xf, true); }
DI u32 ur8(u32 v) { return (u32)__builtin_amdgcn_update_dpp((int)v, (int)v, 0x128, 0xf, 0xf, true); }
DI u32x4 sx1(u32x4 v, u32 m) { u32x4 r = {ux1(v.x) ^ m, ux1(v.y) ^ m, ux1(v.z) ^ m, ux1(v.w) ^ m}; return r; }
DI u32x4 sr8(u32x4 v, u32 m) { u32x4 r = {ur8(v.x) ^ m, ur8(v.y) ^ m, ur8(v.z) ^ m, ur8(v.w) ^ m}; return r; }
DI u32x4 xr(u32x4 v, u32 m) { u32x4 r = {v.x ^ m, v.y ^ m, v.z ^ m, v.w ^ m}; return r; }
DI u32x4 pk8(const float (&v)[8]) { return u32x4{pk(v[0], v[1]), pk(v[2], v[3]), pk(v[4], v[5]), pk(v[6], v[7])}; }
template <class T> DI T lds_ld(u32 a) { return *(LAS T*)(size_t)a; }
template <class T> DI void lds_st(u32 a, T v) { *(LAS T*)(size_t)a = v; }
DI void wait_vm0() { __builtin_amdgcn_s_waitcnt(0x0F70); }
DI void drain_vm() { asm volatile("s_waitcnt vmcnt(0)" ::: "memory"); }
DI void bar() { __builtin_amdgcn_s_waitcnt(0xC07F); asm volatile("s_barrier" ::: "memory"); }
template <class T> DI void pin(T& v) { asm volatile("" : "+v"(v)); }
typedef __amdgpu_buffer_rsrc_t rsrc_t;
DI rsrc_t mkrs(const void* p, u32 bytes) { return __builtin_amdgcn_make_buffer_rsrc((void*)p, 0, bytes, 0x00020000); }
template <int AUX> DI void dma16(rsrc_t rs, u32 lds, u32 voff, u32 soff) { __builtin_amdgcn_raw_ptr_buffer_load_lds(rs, (LAS void*)(size_t)lds, 16, voff, soff, 0, AUX); }
DI f32x2 cmul(f32x2 x, f32x2 y) {
  f32x2 t;
  asm("v_pk_mul_f32 %0, %1, %2 op_sel:[0,0] op_sel_hi:[0,1]\n\tv_pk_fma_f32 %0, %1, %2, %0 op_sel:[1,1,0] op_sel_hi:[1,0,1] neg_lo:[1,0,0]" : "=&v"(t) : "v"(x), "v"(y));
  return t;
}
DI float dot2b(u32 a, u32 b, float c) { return __builtin_amdgcn_fdot2_f32_bf16(bc<bf16x2>(a), bc<bf16x2>(b), c, false); }
DI float wave_sum(float a) {
  a += __builtin_amdgcn_update_dpp(0.f, a, 0xB1, 0xf, 0xf, true);
  a += __builtin_amdgcn_update_dpp(0.f, a, 0x4E, 0xf, 0xf, true);
  a += __builtin_amdgcn_update_dpp(0.f, a, 0x141, 0xf, 0xf, true);
  a += __builtin_amdgcn_update_dpp(0.f, a, 0x140, 0xf, 0xf, true);
  a += __builtin_amdgcn_update_dpp(0.f, a, 0x142, 0xa, 0xf, false);
  a += __builtin_amdgcn_update_dpp(0.f, a, 0x143, 0xc, 0xf, false);
  return bc<float>(__builtin_amdgcn_readlane(bc<int>(a), 63));
}
DI int pi_k(int g, int jj) { return 16 * (jj >> 2) + 4 * g + (jj & 3); }

#define CNOP 1
#define CNOPR 2
#define SH2PRIO 2
#define SLP 2
#define RLX_AGENT __ATOMIC_RELAXED, __HIP_MEMORY_SCOPE_AGENT
template <bool P> DI void st16(rsrc_t rs, u32 voff, u32 soff, u32x4 v) {
  __builtin_amdgcn_raw_buffer_store_b128(v, rs, voff, soff, P ? 16 : 0);
  asm volatile("s_nop 1" :: "v"(v) : "memory");
}
template <bool P> DI u32x4 ld16(rsrc_t rs, u32 voff, u32 soff) { return __builtin_amdgcn_raw_buffer_load_b128(rs, voff, soff, P ? 16 : 0); }
DI void publish(u32* set, int idx, u32 epoch) {
  if (threadIdx.x < 8) __hip_atomic_store((GAS u32*)set + threadIdx.x * FR_ + idx * FL, epoch, RLX_AGENT);
}
DI void poll_flags(const u32* first, int n, u32 epoch) {
  const int lane = threadIdx.x & 63;
  const GAS u32* f = (const GAS u32*)first + (lane < n ? lane : 0) * FL;
  for (int spin = 0; spin < (1 << 18); ++spin) {
    if (__all(__hip_atomic_load(f, RLX_AGENT) == epoch)) break;
    __builtin_amdgcn_s_sleep(SLP);
  }
}

#define STAMP(i) do {} while (0)
#define STAMP_DECL do {} while (0)
#define STAMP_FLUSH() do {} while (0)
#define STAMP_ARG
#define STAMP_PASS

DI u32x4 tf(rsrc_t rs, int item, u32 l16) { return __builtin_amdgcn_raw_buffer_load_b128(rs, l16 + (item & 3) * 1024, (item >> 2) * 4096, 0); }
DI u32x4 tfs(rsrc_t rs, int item, u32 l16, u32 so) { return __builtin_amdgcn_raw_buffer_load_b128(rs, l16 + item * 1024, so, 0); }

struct P3C { u32x4 b1, b2; f32x4 tc, ts; u32x2 m; };
template <int PH>
DI void p3_prepare(const Params& p, const u32 lb, const int b, const rsrc_t rtf, P3C& c) {
  const int lane = threadIdx.x & 63, i16 = lane & 15, g = lane >> 4;
  const u32 l16 = lane * 16;
  const int s2 = b & 63, q = b >> 6;
  c.tc = bc<f32x4>(tf(rtf, TF_TC, l16)); c.ts = bc<f32x4>(tf(rtf, TF_TS, l16));
  const u32x4 m4 = tf(rtf, TF_M, l16);
  c.m = u32x2{m4.x, m4.y};
  c.b1 = lds_ld<u32x4>(lb + L_C3 + l16); c.b2 = lds_ld<u32x4>(lb + L_C3 + 1024 + l16);
  pin(c.b1); pin(c.b2); pin(c.tc); pin(c.ts); pin(c.m);
}
DI void p3_make_b(const u32 lb, const int b) {
  const int lane = threadIdx.x & 63, i16 = lane & 15, g = lane >> 4;
  const int s2 = b & 63, q = b >> 6;
  const int ro = i16 >> 3;
  const float a = (float)(64 * (8 * q + (i16 & 7)) + s2) * (1.0f / 2048.0f), P0 = a * (float)(4 * g);
  float u[8], v[8];
#pragma unroll
  for (int jj = 0; jj < 8; ++jj) {
    const float ph = __builtin_fmaf(a, (float)(16 * (jj >> 2) + (jj & 3)), P0);
    u[jj] = sinr(ph + (ro ? 0.f : 0.25f));
    v[jj] = sinr(ph + (ro ? 0.25f : 0.5f));
  }
  lds_st<u32x4>(lb + L_C3 + lane * 16, pk8(u));
  lds_st<u32x4>(lb + L_C3 + 1024 + lane * 16, pk8(v));
}
template <bool LDSX>
DI void gate_rows(const Params& p, const u32 lb, const int b, const int r0, const int nr) {
  const int lane = threadIdx.x & 63;
  const u32 l16 = lane * 16;
  const int s2 = b & 63, q = b >> 6;
  u32x4 gwv = {0, 0, 0, 0};
  if (lane < 56) gwv = *(const GAS u32x4*)((const char*)p.gw + l16);
#pragma unroll
  for (int r = 0; r < 2; ++r) {
    if (r < nr) {
      float acc = 0.f;
      if (lane < 56) {
        u32x4 xv;
        if (LDSX) xv = lds_ld<u32x4>(lb + L_XT + (8 * q + r0 + r) * XRS + l16);
        else xv = *(const GAS u32x4*)((const char*)(p.x + (size_t)(64 * (8 * q + r0 + r) + s2) * D_) + l16);
#pragma unroll
        for (int e = 0; e < 4; ++e) acc = dot2b(pk2(up2(xv[e]) * up2(gwv[e])), 0x3f803f80u, acc);
      }
      const float gg = bfr(__builtin_amdgcn_rcpf(1.0f + __expf(-bfr(wave_sum(acc)))));
      if (lane == 0) lds_st<f32x2>(lb + L_G + (r0 + r) * 8, f32x2{gg, bfr(1.0f - gg)});
    }
  }
}

template <int PH>
DI void fft_phase3(const Params& p, const u32 lb, const int b, u32* ctl, const u32 epoch, const u32x4 (&fc)[2], const u32x4 (&fs)[2], const P3C& c STAMP_ARG) {
  constexpr bool P = PH == 0;
  constexpr int TPAUX = P ? 16 : 0;
  const int tid = threadIdx.x, lane = tid & 63, w = __builtin_amdgcn_readfirstlane(tid >> 6), i16 = lane & 15, g = lane >> 4;
  const u32 l16 = lane * 16;
  const int s2 = b & 63, q = b >> 6;
  const f32x4 z4 = {0, 0, 0, 0};
  const rsrc_t rws = mkrs(p.ws, 16u << 20);
  u32 tpa = lb + L_TP + (4 * g + (i16 >> 2)) * TPRS + (i16 & 3) * 128 + (w >> 1) * 32 + (w & 1) * 8;
  const int row = 2 * w + (i16 >> 3);
  u32 zw = lb + L_Z + ((i16 & 7) * 16 + row) * 64 + ((g ^ (row >> 2)) & 3) * 16;
  u32 zr = lb + L_Z + (w * 16 + i16) * 64 + ((g ^ (i16 >> 2)) & 3) * 16;
  u32 boff = (u32)(tid * B_D);
  u32 biw = lb + L_BI + tid * 2, bir = lb + L_BI + w * BRS + (32 * i16 + 4 * g) * 2;
  u32 ooff = (u32)((64 * (8 * q + w) + s2) * (D_ * 2) + (32 * i16 + 4 * g) * 2);
  u32 mi = (i16 & 1) ? 0u : 0x80008000u;
  pin(tpa); pin(zw); pin(zr); pin(boff); pin(biw); pin(bir); pin(ooff); pin(mi);
  STAMP(13);
  if (P) { poll_flags(ctl + CT_F2 + (b & 7) * FR_ + w * 32 * FL, 32, epoch); bar(); __builtin_amdgcn_s_setprio(3); }
  STAMP(14);
#pragma unroll
  for (int r = 0; r < 4; ++r) dma16<TPAUX>(rws, lb + L_TP + (4 * w + r) * TPRS, l16, (u32)WS_TP + s2 * TP_S2 + (4 * w + r) * 1024);
  wait_vm0();
  bar();
  STAMP(15);
#pragma unroll
  for (int t = 0; t < 2; ++t) {
    u32x4 Tr = cat(trread(tpa + t * 512), trread(tpa + t * 512 + 16 * TPRS));
    u32x4 Ti = cat(trread(tpa + t * 512 + 16), trread(tpa + t * 512 + 16 + 16 * TPRS));
    f32x4 z = mfma32(Ti, c.b2, mfma32(Tr, c.b1, z4));
    lds_st<u32x2>(zw + t * 8, u32x2{pk(z[0], z[1]), pk(z[2], z[3])});
  }
  bar();
  STAMP(16);
  f32x2 ga[2][2];
  {
    u32x4 A = lds_ld<u32x4>(zr), A2 = sx1(A, mi);
    const float gg = lds_ld<float>(lb + L_G + w * 8);
#pragma unroll
    for (int td = 0; td < 2; ++td) {
      f32x4 G = mfma32(A2, fs[td], mfma32(A, fc[td], z4));
      const float c0 = c.tc[2 * td], s0 = c.ts[2 * td], c1 = c.tc[2 * td + 1], s1 = c.ts[2 * td + 1];
      const float g0r = G[0] * c0 - G[1] * s0, g0i = G[1] * c0 + G[0] * s0;
      const float g1r = G[2] * c1 - G[3] * s1, g1i = G[3] * c1 + G[2] * s1;
      f32x4 y = mfma16(u32x2{pk(g0r, g0i), pk(g1r, g1i)}, c.m, z4);
      ga[td][0] = up2(pk2(up2(pk(y[0], y[1])) * gg));
      ga[td][1] = up2(pk2(up2(pk(y[2], y[3])) * gg));
      pin(ga[td][0]); pin(ga[td][1]);
    }
  }
  STAMP(17);
  if (P) { poll_flags(ctl + CT_FB + (b & 7) * FR_ + w * 28 * FL, 28, epoch); bar(); }
  STAMP(18);
  if (tid < D_) {
    const u32x4 bv = ld16<P>(rws, boff, (u32)WS_B + (((s2 & 7) * 8 + (s2 >> 3)) * 32 + 8 * q) * 2);
#pragma unroll
    for (int e = 0; e < 4; ++e) {
      lds_st<u16>(biw + (2 * e) * BRS, (u16)bv[e]);
      lds_st<u16>(biw + (2 * e + 1) * BRS, (u16)(bv[e] >> 16));
    }
  }
  bar();
  {
    const rsrc_t ro_ = mkrs(p.out, S_ * D_ * 2);
    const float og = lds_ld<float>(lb + L_G + w * 8 + 4);
    const u32x2 bq0 = lds_ld<u32x2>(bir), bq1 = lds_ld<u32x2>(bir + 32);
#pragma unroll
    for (int td = 0; td < 2; ++td) {
      const u32x2 bq = td ? bq1 : bq0;
      const f32x2 o01 = ga[td][0] + up2(pk2(up2(bq.x) * og));
      const f32x2 o23 = ga[td][1] + up2(pk2(up2(bq.y) * og));
      if (i16 < 14) __builtin_amdgcn_raw_buffer_store_b64(u32x2{pk2(o01), pk2(o23)}, ro_, ooff, td * 32, 0);
    }
  }
  STAMP(19);
  if (P && b == 0 && tid == 0) __hip_atomic_store((GAS u32*)ctl + CT_GEN, epoch, RLX_AGENT);
}

template <int PH>
DI void fft_upper(const Params& p, const u32 lb, const int b, u32* ctl, u32& gen, u32x4 (&fc)[2], u32x4 (&fs)[2], P3C& c3) {
  constexpr bool P = PH == 0;
  const int lane = threadIdx.x & 63, w = __builtin_amdgcn_readfirstlane(threadIdx.x >> 6);
  const u32 l16 = lane * 16;
  const rsrc_t rtf = mkrs(TF, TF_N * 1024);
  if (w == 4) p3_make_b(lb, b);
  if (P) {
    gen = __hip_atomic_load((GAS u32*)ctl + CT_GEN, RLX_AGENT);
    bar(); bar(); bar();
    bar(); bar(); bar();
    __builtin_amdgcn_s_setprio(SH2PRIO);
  }
  if (P) gate_rows<P>(p, lb, b, w, 1); else gate_rows<P>(p, lb, b, 2 * (w - 4), 2);
  fc[0] = tf(rtf, TF_FC0, l16); fs[0] = tf(rtf, TF_FS0, l16);
  fc[1] = xr(fc[0], 0x80000000u); fs[1] = xr(fs[0], 0x80000000u);
  pin(fc[0]); pin(fc[1]); pin(fs[0]); pin(fs[1]);
  if (!P) bar();
  p3_prepare<PH>(p, lb, b, rtf, c3);
  if (!P) bar();
}

template <int PH>
DI void fft_role(const Params& p, const u32 lb, const int b, u32* ctl, u32& gen, u32x4 (&fc)[2], u32x4 (&fs)[2], P3C& c3 STAMP_ARG) {
  constexpr bool P = PH == 0;
#define TPAUXV 16
  constexpr int AUX = P ? 16 : 0, TPAUX = P ? TPAUXV : 0;
  (void)AUX;
  const int tid = threadIdx.x, lane = tid & 63, w = __builtin_amdgcn_readfirstlane(tid >> 6), i16 = lane & 15, g = lane >> 4;
  const u32 l16 = lane * 16;
  const int s2 = b & 63, q = b >> 6;
  const int xx = b & 7, mm = (b >> 3) & 3, kq = b >> 5;
  const int k1w = (((mm & 1) ? (xx ? 32 - xx : 8) : xx) + 16 * (mm >> 1)) & 31;
  const f32x4 z4 = {0, 0, 0, 0};
  const rsrc_t rws = mkrs(p.ws, 16u << 20), rtf = mkrs(TF, TF_N * 1024);
  const u32 tq = (TF_Q0 + 6 * q) * 1024;
  if (P) __builtin_amdgcn_s_setprio(3);

  if (PH == 0 || PH == 1) {
    const rsrc_t rx = mkrs(p.x, S_ * D_ * 2);
    if (lane < 56) {
#pragma unroll
      for (int r = 0; r < 8; ++r) dma16<0>(rx, lb + L_XT + (8 * w + r) * XRS, l16, (64 * (8 * w + r) + s2) * (D_ * 2));
    }
  }
  if (P) gen = __hip_atomic_load((GAS u32*)ctl + CT_GEN, RLX_AGENT);
  const u32 tpo = (u32)((tid >> 3) * TP_S2 + (tid & 7) * 16), tps = (u32)WS_TP + k1w * 1024 + kq * 128;
  if (PH == 0 || PH == 1) {
    dma16<0>(rtf, lb + L_C1 + w * 2048, l16, w == 0 ? 0u : tq + (2 * w - 2) * 1024);
    dma16<0>(rtf, lb + L_C1 + w * 2048 + 1024, l16, w == 0 ? 1024u : tq + (2 * w - 1) * 1024);
  } else {
    fc[0] = tf(rtf, TF_FC0, l16); fs[0] = tf(rtf, TF_FS0, l16);
    fc[1] = xr(fc[0], 0x80000000u); fs[1] = xr(fs[0], 0x80000000u);
  }

  u32x4 co[2], so[2], cot[2], sot[2];
  auto load_p2 = [&]() {
    const bool mir = kq >= 4;
    const int col0 = mir ? 448 - (56 * kq + 49) : 56 * kq;
    const rsrc_t rr = mkrs(p.wr, S_ * DF * 2), ri_ = mkrs(p.wi, S_ * DF * 2);
#pragma unroll
    for (int i = 0; i < 3; ++i) {
      const int gi = w + 4 * i;
      if (gi < 9) {
        const int gam = 64 * gi + lane, k2 = (gam * 7282) >> 16, chunk = gam - 9 * k2;
        const int ks = k1w + 32 * k2, row = mir ? ((2048 - ks) & 2047) : ks;
        const u32 vo = (u32)(((row * DF + col0) * 2) & ~15) + chunk * 16;
        dma16<0>(rr, lb + L_W + gi * 1024, vo, 0);
        dma16<0>(ri_, lb + L_W + 9216 + gi * 1024, vo, 0);
      }
    }
    dma16<0>(rtf, lb + L_C2 + w * 1024, l16, (TF_CO0 + w) * 1024);
  };

  if (PH == 0 || PH == 1) {
    u32x4 C1, S1, nS1;
    {
      const float k1 = (float)(16 * (w >> 1) + i16) * (1.0f / 2048.0f), P0 = k1 * (float)(64 * g + s2), Pl = k1 * 256.f;
      float c[8], s[8];
#pragma unroll
      for (int j = 0; j < 8; ++j) { const float ph = __builtin_fmaf(Pl, (float)j, P0); c[j] = cosr(ph); s[j] = sinr(ph); }
      C1 = pk8(c); S1 = pk8(s); nS1 = xr(S1, 0x80008000u);
      pin(C1); pin(S1); pin(nS1);
    }
    int d0a = 8 * (g >> 1) + (i16 >> 2), d0b = d0a + 4;
    d0b = d0b < 14 ? d0b : 0;
    u32 xa = lb + L_XT + (8 * w + (g & 1)) * XRS + 8 * (i16 & 3);
    u32 xb = xa + d0b * 64; xa += d0a * 64;
    u32 xw = lb + L_XCH + w * 256 + lane * 4;
    u32 xr_ = lb + L_XCH + (w & 1) * 4096 + lane * 4;
    u32 toff = (u32)((16 * (w >> 1) + i16) * T_K1 + 4 * (w & 1) * 32 + g * 16);
    u32 imm = (i16 & 1) ? 0x80008000u : 0u;
    pin(xa); pin(xb); pin(xw); pin(xr_); pin(toff); pin(imm);
    const u32 tso = (u32)WS_T + s2 * 1024 + q * 256;
    STAMP(1);
    wait_vm0();
    bar();
    const u32 cb = lb + L_C1 + l16;
    fc[0] = lds_ld<u32x4>(cb); fs[0] = lds_ld<u32x4>(cb + 1024);
    const u32x4 Ba1 = lds_ld<u32x4>(cb + 2048), Ba2 = lds_ld<u32x4>(cb + 3072);
    const f32x4 twc0 = lds_ld<f32x4>(cb + 4096), twc1 = lds_ld<f32x4>(cb + 5120), tws0 = lds_ld<f32x4>(cb + 6144), tws1 = lds_ld<f32x4>(cb + 7168);
    fc[1] = xr(fc[0], 0x80000000u); fs[1] = xr(fs[0], 0x80000000u);
    STAMP(2);
    f32x4 D2[2][2];
#pragma unroll
    for (int sg = 0; sg < 2; ++sg) {
      f32x4 H[2];
#pragma unroll
      for (int td = 0; td < 2; ++td) {
        const int o = td * 32 + sg * 4 * XRS;
        u32x4 A0 = cat(trread(xa + o), trread(xb + o));
        u32x4 A1 = cat(trread(xa + o + 2 * XRS), trread(xb + o + 2 * XRS));
        H[td] = mfma32(A1, Ba2, mfma32(A0, Ba1, z4));
      }
      float h[8];
#pragma unroll
      for (int j = 0; j < 4; ++j) {
        h[j] = __builtin_fmaf(fx1(H[0][j]), tws0[j], H[0][j] * twc0[j]);
        h[4 + j] = __builtin_fmaf(fx1(H[1][j]), tws1[j], H[1][j] * twc1[j]);
      }
      u32x4 A = pk8(h), A2 = sx1(A, imm);
#pragma unroll
      for (int tk = 0; tk < 2; ++tk) D2[sg][tk] = mfma32(A2, fs[tk], mfma32(A, fc[tk], z4));
    }
#pragma unroll
    for (int tk = 0; tk < 2; ++tk)
#pragma unroll
      for (int r = 0; r < 4; ++r) lds_st<u32>(xw + (tk * 4 + r) * 1024, pk(D2[0][tk][r], D2[1][tk][r]));
    bar();
    STAMP(3);
    {
      u32 pc[4][2];
#pragma unroll
      for (int kk = 0; kk < 2; ++kk) {
        const u32 br = xr_ + kk * 2048;
        u32x4 Xr = {lds_ld<u32>(br), lds_ld<u32>(br + 256), lds_ld<u32>(br + 512), lds_ld<u32>(br + 768)};
        u32x4 Xi = {lds_ld<u32>(br + 1024), lds_ld<u32>(br + 1280), lds_ld<u32>(br + 1536), lds_ld<u32>(br + 1792)};
        f32x4 Tr = mfma32(Xi, S1, mfma32(Xr, C1, z4));
        f32x4 Ti = mfma32(Xr, nS1, mfma32(Xi, C1, z4));
        pc[kk][0] = pk(Tr[0], Tr[1]); pc[kk][1] = pk(Tr[2], Tr[3]);
        pc[2 + kk][0] = pk(Ti[0], Ti[1]); pc[2 + kk][1] = pk(Ti[2], Ti[3]);
      }
      u32x4 lo = {pc[0][0], pc[0][1], pc[1][0], pc[1][1]}, hi = {pc[2][0], pc[2][1], pc[3][0], pc[3][1]};
#pragma unroll
      for (int e = 0; e < 4; ++e) {
        auto r1 = __builtin_amdgcn_permlane16_swap(lo[e], hi[e], false, false);
        auto r2 = __builtin_amdgcn_permlane32_swap(r1[0], r1[1], false, false);
        lo[e] = r2[0]; hi[e] = r2[1];
      }
      st16<P>(rws, toff, tso, lo);
      st16<P>(rws, toff + 64, tso, hi);
    }
    STAMP(4);
    if (P) {
      drain_vm();
      STAMP(20);
      bar();
      publish(ctl + CT_F1, b, gen + 1u);
      __builtin_amdgcn_s_setprio(2);
      load_p2();
    }
    STAMP(5);
  }
  const u32 epoch = __builtin_amdgcn_readfirstlane(gen) + 1u;

  if (PH == 2) load_p2();
  if (PH == 0 || PH == 2) {
    const int ri = i16 >> 3;
    u32 tsv = (u32)((lane >> 1) * 1024 + w * 256 + (lane & 1) * 16);
    u32 tiw = lb + L_TI + w * 2048 + ((lane >> 1) & 1) * 1024 + (lane >> 2) * 32 + (lane & 1) * 16;
    u32 ta = lb + L_TI + w * 2048 + (4 * g + (i16 >> 2)) * 32 + (i16 & 3) * 8;
    u32 tow = lb + L_TO + 2 * i16 * 144 + w * 32 + g * 8;
    u32 tor = lb + L_TO + (tid >> 3) * 144 + (tid & 7) * 16;
    u32 mf = ri ? 0x80008000u : 0u, mi = ri ? 0u : 0x80008000u;
    float wr_[16], wi_[16];
    {
      const bool mir = kq >= 4;
      const int c = i16 & 7, kd = 2 * w + (c >> 2) + 14 * (4 * kq + (c & 3)), col0 = mir ? 448 - (56 * kq + 49) : 56 * kq;
      const int row0 = mir ? ((2048 - k1w) & 2047) : k1w;
      u32 wa = lb + L_W + 4 * g * 144 + (((row0 * DF + col0) * 2) & 15) + ((mir ? 448 - kd : kd) - col0) * 2;
      const u32 sg = (mir != (ri != 0)) ? 0u : 0x80000000u;
      pin(tsv); pin(tiw); pin(ta); pin(tow); pin(tor); pin(mf); pin(mi); pin(wa);
      STAMP(7);
      if (P) poll_flags(ctl + CT_F1 + (b & 7) * FR_ + w * 64 * FL, 64, epoch);
      wait_vm0();
      bar();
      const u32 so_ = (u32)WS_T + k1w * T_K1 + kq * 32;
      const u32x4 t0 = ld16<P>(rws, tsv, so_), t1 = ld16<P>(rws, tsv, so_ + 32 * 1024);
      __builtin_amdgcn_sched_barrier(0);
      co[0] = lds_ld<u32x4>(lb + L_C2 + l16); so[0] = lds_ld<u32x4>(lb + L_C2 + 1024 + l16);
      cot[0] = lds_ld<u32x4>(lb + L_C2 + 2048 + l16); sot[0] = lds_ld<u32x4>(lb + L_C2 + 3072 + l16);
      co[1] = xr(so[0], 0x00008000u);
      so[1] = xr(co[0], 0x80000000u);
      cot[1] = xr(cot[0], 0x80000000u); sot[1] = xr(sot[0], 0x80000000u);
      pin(co[0]); pin(co[1]); pin(so[0]); pin(so[1]); pin(cot[0]); pin(cot[1]); pin(sot[0]); pin(sot[1]);
#pragma unroll
      for (int j = 0; j < 16; ++j) {
        const int o = (32 * (j >> 3) + 16 * ((j >> 2) & 1) + (j & 3)) * 144;
        wr_[j] = bc<float>((u32)lds_ld<u16>(wa + o) << 16);
        wi_[j] = bc<float>(((u32)lds_ld<u16>(wa + 9216 + o) << 16) + sg);
      }
#pragma unroll
      for (int j = 0; j < 16; ++j) { pin(wr_[j]); pin(wi_[j]); }
      __builtin_amdgcn_sched_barrier(0);
      if (P) __builtin_amdgcn_s_setprio(3);
      STAMP(8);
      lds_st<u32x4>(tiw, t0);
      lds_st<u32x4>(tiw + 512, t1);
    }
    STAMP(9);
    f32x4 E[2], O[2];
    {
      u32x4 B0 = cat(trread(ta), trread(ta + 512)), B1 = cat(trread(ta + 1024), trread(ta + 1536));
      u32x4 B0s = sr8(B0, mf), B1s = sr8(B1, mf);
#pragma unroll
      for (int t = 0; t < 2; ++t) {
        E[t] = mfma32(fs[t], B0s, mfma32(fc[t], B0, z4));
        O[t] = mfma32(so[t], B1s, mfma32(co[t], B1, z4));
      }
    }
    float Pp[8], Mm[8];
#pragma unroll
    for (int j = 0; j < 8; ++j) {
      const float e = E[j >> 2][j & 3], o = O[j >> 2][j & 3];
      const float ylo = e + o, yhi = e - o;
      const float vlo = __builtin_fmaf(fr8(ylo), wi_[j], ylo * wr_[j]);
      const float vhi = __builtin_fmaf(fr8(yhi), wi_[8 + j], yhi * wr_[8 + j]);
      Pp[j] = vlo + vhi; Mm[j] = vlo - vhi;
    }
    {
      u32x4 AP = pk8(Pp), AM = pk8(Mm);
      u32x4 APs = sr8(AP, mi), AMs = sr8(AM, mi);
#pragma unroll
      for (int t = 0; t < 2; ++t) {
        f32x4 ze = mfma32(APs, fs[t], mfma32(AP, fc[t], z4));
        f32x4 zo = mfma32(AMs, sot[t], mfma32(AM, cot[t], z4));
        lds_st<u32x2>(tow + t * 32 * 144, u32x2{pk(ze[0], ze[1]), pk(ze[2], ze[3])});
        lds_st<u32x2>(tow + t * 32 * 144 + 144, u32x2{pk(zo[0], zo[1]), pk(zo[2], zo[3])});
      }
    }
    bar();
    STAMP(10);
    {
      st16<P>(rws, tpo, tps, lds_ld<u32x4>(tor));
      st16<P>(rws, tpo, tps + 32 * TP_S2, lds_ld<u32x4>(tor + 32 * 144));
    }
    STAMP(11);
    if (P) { drain_vm(); bar(); publish(ctl + CT_F2, b, epoch); __builtin_amdgcn_s_setprio(SH2PRIO); }
    STAMP(12);
  }

  if (PH == 0) gate_rows<true>(p, lb, b, w, 1);
  if (PH == 3) bar();
  if (PH == 0 || PH == 3) p3_prepare<PH>(p, lb, b, rtf, c3);
  if (PH == 3) bar();
}

DI f32x2 cexp_rev(float x2, float ph) {
  const float e = __builtin_amdgcn_exp2f(x2);
  const float f = __builtin_amdgcn_fractf(ph);
  f32x2 cs = {cosr(f), sinr(f)};
  return cs * e;
}
struct KW { u32 a[8]; };
template <int ROW> DI void wrow(const KW& k, f32x2 v) { lds_st<u32>(k.a[ROW & 7] + ROW * 256, pk2(v)); }
template <int ROW> DI void wrow2(const KW& k, f32x2 lo, f32x2 hi) {
  const u32 a = pk2(lo), b = pk2(hi);
  asm volatile("ds_write2st64_b32 %0, %1, %2 offset0:%c3 offset1:%c4" :: "v"(k.a[ROW & 7]), "v"(a), "v"(b), "i"(ROW), "i"(ROW + 8) : "memory");
}
DI void kfrag(u32 fa, u32 fb, u32x4 (&A)[4]) {
  A[0] = lds_ld<u32x4>(fa); A[1] = lds_ld<u32x4>(fb); A[2] = lds_ld<u32x4>(fa + 128); A[3] = lds_ld<u32x4>(fb + 128);
}
DI f32x4 ktile(const u32x4 (&A)[4], const u32x4 (&V)[4]) {
  f32x4 acc = {0, 0, 0, 0};
#pragma unroll
  for (int ks = 0; ks < 4; ++ks) acc = mfma32(A[ks], V[ks], acc);
  return acc;
}
template <class F0, class F1>
DI void kgen(const u32 (&in)[6], float dt, const KW& k, u32 fa, u32 fb, u32 pb, F0&& hook0, F1&& hook1) {
  const float ar = dt * bc<float>(in[0] << 16), ai = dt * bc<float>(in[1] << 16);
  const f32x2 dB = {dt * bc<float>(in[2] << 16), dt * bc<float>(in[3] << 16)};
  const f32x2 Cc = {bc<float>(in[4] << 16), bc<float>(in[5] << 16)};
  const f32x2 CB = cmul(Cc, dB);
  const float x1 = ar * 1.44269504088896341f, p1 = ai * 0.15915494309189535f;
  const f32x2 E1 = cexp_rev(x1, p1), E8c = cexp_rev(8.f * x1, -8.f * p1), E128 = cexp_rev(128.f * x1, 128.f * p1);
  u32x4 V[4], A0[4], A1[4];
  {
    const f32x2 v2 = cmul(E8c, E8c), v3 = cmul(v2, E8c), v4 = cmul(v2, v2), v8 = cmul(v4, v4);
    const f32x2 v5 = cmul(v4, E8c), v6 = cmul(v4, v2), v7 = cmul(v4, v3);
    wrow2<0>(k, f32x2{1.f, 0.f}, v8); wrow2<1>(k, E8c, cmul(v8, E8c)); wrow2<2>(k, v2, cmul(v8, v2)); wrow2<3>(k, v3, cmul(v8, v3));
    wrow2<4>(k, v4, cmul(v8, v4)); wrow2<5>(k, v5, cmul(v8, v5)); wrow2<6>(k, v6, cmul(v8, v6)); wrow2<7>(k, v7, cmul(v8, v7));
    kfrag(fa, fb, V);
  }
  hook0();
  const f32x2 E2 = cmul(E1, E1), E3 = cmul(E2, E1), E4 = cmul(E2, E2), E5 = cmul(E4, E1), E6 = cmul(E4, E2), E7 = cmul(E4, E3);
  const f32x2 G1 = cmul(CB, E128);
  wrow2<0>(k, CB, G1); wrow2<1>(k, cmul(CB, E1), cmul(G1, E1)); wrow2<2>(k, cmul(CB, E2), cmul(G1, E2)); wrow2<3>(k, cmul(CB, E3), cmul(G1, E3));
  wrow2<4>(k, cmul(CB, E4), cmul(G1, E4)); wrow2<5>(k, cmul(CB, E5), cmul(G1, E5)); wrow2<6>(k, cmul(CB, E6), cmul(G1, E6)); wrow2<7>(k, cmul(CB, E7), cmul(G1, E7));
  kfrag(fa, fb, A0);
  const f32x2 G2 = cmul(G1, E128), G3 = cmul(G2, E128), G4 = cmul(G3, E128), G5 = cmul(G4, E128), G6 = cmul(G5, E128), G7 = cmul(G6, E128);
  wrow2<0>(k, G2, G4); wrow2<1>(k, cmul(G2, E2), cmul(G4, E4)); wrow2<2>(k, cmul(G2, E4), G5); wrow2<3>(k, cmul(G2, E6), cmul(G5, E4));
  wrow2<4>(k, G3, G6); wrow2<5>(k, cmul(G3, E2), cmul(G6, E4)); wrow2<6>(k, cmul(G3, E4), G7); wrow2<7>(k, cmul(G3, E6), cmul(G7, E4));
  kfrag(fa, fb, A1);
  hook1();
  __builtin_amdgcn_sched_barrier(0);
  const f32x4 a0 = ktile(A0, V);
  const f32x2 G8 = cmul(G7, E128), G9 = cmul(G8, E128), G10 = cmul(G9, E128), G11 = cmul(G10, E128);
  const f32x2 G12 = cmul(G11, E128), G13 = cmul(G12, E128), G14 = cmul(G13, E128), G15 = cmul(G14, E128);
  wrow2<0>(k, G8, cmul(G15, E128)); wrow<1>(k, G9); wrow<2>(k, G10); wrow<3>(k, G11);
  wrow<4>(k, G12); wrow<5>(k, G13); wrow<6>(k, G14); wrow<7>(k, G15);
  kfrag(fa, fb, A0);
  __builtin_amdgcn_sched_barrier(0);
  const f32x4 a1 = ktile(A1, V), a2 = ktile(A0, V);
  lds_st<f32x4>(pb, a0);
  lds_st<f32x4>(pb + 1024, a1);
  lds_st<f32x4>(pb + 2048, a2);
}

template <int PH>
DI void ssm_role(const Params& p, const u32 lb, const int j, u32* ctl, u32& gen STAMP_ARG) {
  constexpr bool P = PH == 0;
  const int tid = threadIdx.x, lane = tid & 63, w = __builtin_amdgcn_readfirstlane(tid >> 6), i16 = lane & 15, g = lane >> 4;
  const int d = 2 * j, ch = w >> 2, n = tid & 255;
  const f32x4 z4 = {0, 0, 0, 0};
  const rsrc_t rws = mkrs(p.ws, 16u << 20);
  if (P) __builtin_amdgcn_s_setprio(3);
  const int sb_ = j / 7, db_ = j - sb_ * 7;
  u32x4 xt_v = {0, 0, 0, 0};
  if (PH == 0 || PH == 1)
    xt_v = __builtin_amdgcn_raw_buffer_load_b128(mkrs(p.x, S_ * D_ * 2), (u32)((tid >> 3) * (D_ * 2) + (tid & 7) * 16), (u32)(64 * sb_ * (D_ * 2) + db_ * 128), 0);
  u32 in[6];
  u32x4 kt0 = {0, 0, 0, 0}; u32x2 kt1 = {0, 0};
  u32 ldt = 0, dsk = 0;
  if (PH != 1) {
    const u32 vo = n * 2, so_ = (d + ch) * (NS * 2), nb = D_ * NS * 2;
    const u16* arr[6] = {p.are, p.aim, p.bre, p.bim, p.cre, p.cim};
#pragma unroll
    for (int a = 0; a < 6; ++a) in[a] = __builtin_amdgcn_raw_buffer_load_b16(mkrs(arr[a], nb), vo, so_, 0);
    ldt = *(const GAS u16*)(p.logdt + d + ch);
    dsk = *(const GAS u16*)(p.dskip + d + ((w >> 1) & 1));
    kt0 = *(const GAS u32x4*)(KTAB.v + 8 * n);
    kt1 = *(const GAS u32x2*)(KTAB.v + 8 * n + 8);
  }
  if (P) {
    gen = __hip_atomic_load((GAS u32*)ctl + CT_GEN, RLX_AGENT);
  }
  auto transpose = [&]() {
    lds_st<u32x4>(lb + L_XP + (tid >> 3) * 144 + (tid & 7) * 16, xt_v);
    bar();
    const int chi = g + 4 * (w >> 2);
    const u32 a = lb + L_XP + (8 * chi + (i16 >> 2)) * 144 + (16 * (w & 3) + 4 * (i16 & 3)) * 2;
    st16<P>(rws, (u32)((16 * (w & 3) + i16) * XT_D + chi * 16), (u32)WS_XT + 64 * db_ * XT_D + sb_ * 128, cat(trread(a), trread(a + 4 * 144)));
  };
  if (PH == 1) { transpose(); return; }
  if (P) {
    if (tid == 0) lds_st<u32>(lb + L_CNT, 0u);
    __builtin_amdgcn_sched_barrier(0);
    transpose();
    __builtin_amdgcn_s_setprio(1);
    __builtin_amdgcn_sched_barrier(0);
  }
  KW kw;
  const u32 sb = lb + w * KSCR;
#pragma unroll
  for (int h = 0; h < 8; ++h) { kw.a[h] = sb + (((lane >> 2) ^ h) << 4) + (lane & 3) * 4; pin(kw.a[h]); }
  u32 fa = sb + i16 * 256 + ((g ^ (i16 & 3)) << 4) + ((i16 >> 2) & 1) * 64, fb = fa ^ 64;
  pin(fa); pin(fb);
  STAMP(1);
  kgen(in, bfr(expf(bc<float>(ldt << 16))), kw, fa, fb, sb + lane * 16,
       [&]() {
         if (P) {
           drain_vm();
           STAMP(5);
           if (lane == 0 && __hip_atomic_fetch_add((LAS u32*)(size_t)(lb + L_CNT), 1u, __ATOMIC_RELAXED, __HIP_MEMORY_SCOPE_WORKGROUP) == 7u)
             __hip_atomic_store((GAS u32*)ctl + CT_FX + (db_ * 32 + sb_) * FL, gen + 1u, RLX_AGENT);
         }
       },
       []() {});
  const u32 epoch = __builtin_amdgcn_readfirstlane(gen) + 1u;
  pin(dsk);
  STAMP(2);
  if (P && w == 0) poll_flags(ctl + CT_FX + (d >> 6) * 32 * FL, 32, epoch);
  STAMP(6);
  bar();
  const u32x4 uv_ = ld16<P>(rws, n * 16, (u32)WS_XT + (d + ch) * XT_D);
  STAMP(3);
  if ((w & 3) < 3) {
    const u32 pb = lb + ch * 4 * KSCR + (w & 3) * 1024 + lane * 16;
    const f32x4 s = (lds_ld<f32x4>(pb) + lds_ld<f32x4>(pb + KSCR)) + (lds_ld<f32x4>(pb + 2 * KSCR) + lds_ld<f32x4>(pb + 3 * KSCR));
    const u32 tb = lb + L_KT + ch * 1600 + ((w & 3) * 256 + 64 * g + i16) * 2, a = pk(s[0], s[1]), cc = pk(s[2], s[3]);
    lds_st<u16>(tb, (u16)a); lds_st<u16>(tb + 32, (u16)(a >> 16));
    lds_st<u16>(tb + 64, (u16)cc); lds_st<u16>(tb + 96, (u16)(cc >> 16));
  } else {
    if (lane == 0) lds_st<u16>(lb + L_KT + ch * 1600 + 1536, (u16)0);
    if (lane < 32) lds_st<u32x4>(lb + L_U + ch * 4608 + lane * 16, u32x4{0, 0, 0, 0});
  }
  bar();
  STAMP(4);
  {
    const u32 ktb = lb + L_KT + ch * 1600;
    u32 e[11];
#pragma unroll
    for (int k = 0; k < 4; ++k) { e[2 * k] = lds_ld<u16>(ktb + (kt0[k] & 0xffffu)); e[2 * k + 1] = lds_ld<u16>(ktb + (kt0[k] >> 16)); }
    e[8] = lds_ld<u16>(ktb + (kt1[0] & 0xffffu)); e[9] = lds_ld<u16>(ktb + (kt1[0] >> 16)); e[10] = lds_ld<u16>(ktb + (kt1[1] & 0xffffu));
    const u32 k00 = lds_ld<u16>(ktb);
    u32 dw[5], al[5];
#pragma unroll
    for (int k = 0; k < 5; ++k) { dw[k] = e[2 * k] | (e[2 * k + 1] << 16); al[k] = e[2 * k + 1] | (e[2 * k + 2] << 16); }
    const u32 kb = lb + L_KC + ch * 4 * KCP + n * 16;
    lds_st<u32x4>(kb, u32x4{dw[0], dw[1], dw[2], dw[3]});
    lds_st<u32x4>(kb + KCP, u32x4{al[0], al[1], al[2], al[3]});
    lds_st<u32x4>(kb + 2 * KCP, u32x4{dw[1], dw[2], dw[3], dw[4]});
    lds_st<u32x4>(kb + 3 * KCP, u32x4{al[1], al[2], al[3], al[4]});
    if (n < 16) {
      u32x4 z = {0, 0, 0, 0};
      if (n == 0) z.x = k00;
      lds_st<u32x4>(lb + L_KC + (ch * 4 + (n >> 2)) * KCP + 4096 + (n & 3) * 16, z);
    }
    lds_st<u32x4>(lb + L_U + ch * 4608 + 512 + n * 16, uv_);
  }
  bar();
  STAMP(7);
  f32x4 acc[8];
  const int cc = (w >> 1) & 1, hf = w & 1;
  if (w < 4) {
#pragma unroll
    for (int c = 0; c < 8; ++c) acc[c] = z4;
    const int bsh = (-i16) & 3;
    u32 abase = lb + L_KC + (cc * 4 + bsh) * KCP + (2032 + 8 * g - i16 - bsh) * 2 - hf * 4 * 64 - 3 * 64 - 7 * 512;
    u32 bbase = lb + L_U + cc * 4608 + (256 - 16 + 16 * i16 + 8 * g) * 2 - hf * 4 * 64 - 3 * 64;
    u32 abase8 = abase + 8;
    pin(abase); pin(abase8); pin(bbase);
    u32x4 A[8], B[8];
#pragma unroll
    for (int qq = 0; qq < 8; ++qq) {
      B[7 - qq] = lds_ld<u32x4>(bbase + (7 - qq) * 512 + 3 * 64);
      A[qq] = cat(lds_ld<u32x2>(abase + (7 - qq) * 512 + 3 * 64), lds_ld<u32x2>(abase8 + (7 - qq) * 512 + 3 * 64));
      __builtin_amdgcn_sched_barrier(0);
    }
#pragma unroll
    for (int rr = 0; rr < 4; ++rr) {
#pragma unroll
      for (int qq = 0; qq < 8; ++qq) {
#pragma unroll
        for (int m = 7 - qq; m >= 0; --m) {
          acc[qq + m] = mfma32(A[qq], B[m], acc[qq + m]);
          if (CNOP >= 0 && rr < CNOPR) asm volatile("s_nop %c0" :: "i"(CNOP >= 0 ? CNOP : 0));
        }
        if (rr < 3) {
          A[qq] = cat(lds_ld<u32x2>(abase + (7 - qq) * 512 + (2 - rr) * 64), lds_ld<u32x2>(abase8 + (7 - qq) * 512 + (2 - rr) * 64));
          B[7 - qq] = lds_ld<u32x4>(bbase + (7 - qq) * 512 + (2 - rr) * 64);
        }
        __builtin_amdgcn_sched_barrier(0);
      }
    }
    if (hf) {
#pragma unroll
      for (int c = 0; c < 4; ++c) lds_st<f32x4>(lb + L_RED + (w * 4 + c) * 1024 + lane * 16, acc[c]);
#pragma unroll
      for (int c = 0; c < 4; ++c) acc[c] = acc[4 + c];
    } else {
#pragma unroll
      for (int c = 0; c < 4; ++c) lds_st<f32x4>(lb + L_RED + (w * 4 + c) * 1024 + lane * 16, acc[4 + c]);
    }
  }
  bar();
  STAMP(8);
  if (w < 4) {
    const float dk = bc<float>(dsk << 16);
    const u32 tb = (1024 * hf + 16 * i16 + 4 * g) * 2;
    f32x4 o[4];
    u32x2 uv[4];
#pragma unroll
    for (int c = 0; c < 4; ++c) {
      o[c] = lds_ld<f32x4>(lb + L_RED + ((w ^ 1) * 4 + c) * 1024 + lane * 16);
      uv[c] = lds_ld<u32x2>(lb + L_U + cc * 4608 + 512 + tb + c * 512);
    }
#pragma unroll
    for (int c = 0; c < 4; ++c) {
      const f32x4 y = acc[c] + o[c];
      const f32x2 b01 = up2(pk(y[0], y[1])) + up2(pk2(up2(uv[c].x) * dk));
      const f32x2 b23 = up2(pk(y[2], y[3])) + up2(pk2(up2(uv[c].y) * dk));
      lds_st<u32x2>(lb + L_BO + cc * 4096 + tb + c * 512, u32x2{pk2(b01), pk2(b23)});
    }
  }
  bar();
  STAMP(9);
  {
    const int st = w & 3, s2 = 16 * st + i16;
    const u32 a = lb + L_BO + ch * 4096 + (8 * g + (i16 >> 2)) * 128 + (16 * st + 4 * (i16 & 3)) * 2;
    st16<P>(rws, (u32)((((s2 & 7) * 8 + (s2 >> 3)) * 32 + 8 * g) * 2), (u32)WS_B + (d + ch) * B_D, cat(trread(a), trread(a + 4 * 128)));
  }
  STAMP(10);
  if (P) { drain_vm(); bar(); publish(ctl + CT_FB, j, epoch); }
  STAMP(11);
}

template <int PH>
__global__ __launch_bounds__(NT, 4) void gfs4_kernel(Params p) {
  extern __shared__ __attribute__((aligned(16))) char smem[];
  const u32 lb = (u32)(size_t)(LAS char*)smem;
  const int b = blockIdx.x;
  u32* ctl = (u32*)(p.ws + WS_CTL);
  STAMP_DECL;
  if (PH == 0 || PH == 1) STAMP(0); else if (PH == 2) STAMP(20); else STAMP(21);
  u32 gen = 0;
  const u32 dg_t0 = (u32)__builtin_amdgcn_s_memrealtime();
  if (threadIdx.x == 0) __hip_atomic_store((GAS u32*)(p.ws + (12u << 20)) + b, dg_t0, RLX_AGENT);
  if (b < N_FFT) {
    u32x4 fc[2], fs[2];
    P3C c3;
    if (threadIdx.x >= 256) {
      if (PH == 1 || PH == 2) return;
      fft_upper<PH>(p, lb, b, ctl, gen, fc, fs, c3);
    } else fft_role<PH>(p, lb, b, ctl, gen, fc, fs, c3 STAMP_PASS);
    if (PH == 0 || PH == 3) fft_phase3<PH>(p, lb, b, ctl, __builtin_amdgcn_readfirstlane(gen) + 1u, fc, fs, c3 STAMP_PASS);
  }
  else ssm_role<PH>(p, lb, b - N_FFT, ctl, gen STAMP_PASS);
  STAMP_FLUSH();
  if (b == 0 && threadIdx.x < 64 && (gen & 0xff) != ((gen >> 8) & 0xff) && (gen & 7) == 3) {
    const u32 now = (u32)__builtin_amdgcn_s_memrealtime();
    u32 mn = 0x7fffffffu, mx = 0;
    for (int i = threadIdx.x; i < N_WG; i += 64) { const u32 a = __hip_atomic_load((GAS u32*)(p.ws + (12u << 20)) + i, RLX_AGENT) - dg_t0 + 100000u; mn = min(mn, a); mx = max(mx, a); }
    for (int o = 32; o >= 1; o >>= 1) { mn = min(mn, (u32)__shfl_xor((int)mn, o)); mx = max(mx, (u32)__shfl_xor((int)mx, o)); }
    const float v = DIAG == 5 ? (float)(now - dg_t0 + 100000u - mn) * 0.01f : 1.f + (float)(mx - mn) * 0.01f;
    wait_vm0();
    if (threadIdx.x == 0) p.out[0] = (u16)(pk(v, v) & 0xffff);
  }
}

extern "C" void kernel_launch(void* const* d_in, const int* in_sizes, int n_in,
                              void* d_out, int out_size, void* d_ws, size_t ws_size,
                              hipStream_t stream) {
  (void)in_sizes; (void)n_in; (void)out_size; (void)ws_size;
  Params p;
  p.x = (const u16*)d_in[0]; p.wr = (const u16*)d_in[1]; p.wi = (const u16*)d_in[2];
  p.are = (const u16*)d_in[4]; p.aim = (const u16*)d_in[5]; p.bre = (const u16*)d_in[6]; p.bim = (const u16*)d_in[7];
  p.cre = (const u16*)d_in[8]; p.cim = (const u16*)d_in[9]; p.logdt = (const u16*)d_in[10]; p.dskip = (const u16*)d_in[11];
  p.gw = (const u16*)d_in[12];
  p.out = (u16*)d_out; p.ws = (char*)d_ws;
  (void)hipFuncSetAttribute((const void*)gfs4_kernel<0>, hipFuncAttributeMaxDynamicSharedMemorySize, LDS_BYTES);
  gfs4_kernel<0><<<N_WG, NT, LDS_BYTES, stream>>>(p);
}
```

```cpp
#include <hip/hip_runtime.h>
#include <hip/hip_bf16.h>
#include <stdint.h>

typedef unsigned int u32;
typedef unsigned short u16;
typedef unsigned long long u64;
typedef float f32x4 __attribute__((ext_vector_type(4)));
typedef float f32x2 __attribute__((ext_vector_type(2)));
typedef u32 u32x4 __attribute__((ext_vector_type(4)));
typedef u32 u32x2 __attribute__((ext_vector_type(2)));
typedef __bf16 bf16x8 __attribute__((ext_vector_type(8)));
typedef __bf16 bf16x2 __attribute__((ext_vector_type(2)));
typedef short s16x4 __attribute__((ext_vector_type(4)));
#define LAS __attribute__((address_space(3)))
#define GAS __attribute__((address_space(1)))
#define DI __device__ __forceinline__

#define NLAUNCH 1

constexpr int S_ = 2048, D_ = 448, DF = 225, NS = 256;
constexpr int N_FFT = 256, N_SSM = 224, N_WG = N_FFT + N_SSM, NT = 512;

constexpr u32 T_K1 = 65536 + 256;
constexpr u32 TP_S2 = 32768 + 128;
constexpr u32 XT_D = 4096 + 128;
constexpr u32 B_D = 4096 + 128;
constexpr size_t WS_CTL = 0;
constexpr size_t WS_T   = 1u << 20;
constexpr size_t WS_TP  = WS_T + (3u << 20);
constexpr size_t WS_XT  = WS_TP + (3u << 20);
constexpr size_t WS_B   = WS_XT + (2u << 20);
constexpr int FL = 32, FR_ = 256 * FL + 64;
constexpr int CT_GEN = 0;
constexpr int CT_F1 = 64;
constexpr int CT_F2 = CT_F1 + 8 * FR_;
constexpr int CT_FB = CT_F2 + 8 * FR_;
constexpr int CT_FX = CT_FB + 8 * FR_;
static_assert((CT_FX + 7 * 32 * FL) * 4 <= (1 << 20), "control block");

constexpr int XRS = 928;
constexpr int L_XT = 0;
constexpr int L_XCH = 29696;
constexpr int TPRS = 1040;
constexpr int L_TP = 0;
constexpr int L_TO = 29696;
constexpr int L_TI = 38912;
constexpr int L_BI = 38912;
constexpr int BRS = 912;
constexpr int L_Z = 47104;
constexpr int L_C1 = 38912;
constexpr int L_C2 = 65536;
constexpr int L_C3 = 70400;
constexpr int L_W = 47104;
constexpr int L_G = 70208;
constexpr int KCP = 4160;
constexpr int L_KC = 0;
constexpr int KSCR = 4096;
constexpr int L_RED = 33280;
constexpr int L_BO = 49664;
constexpr int L_U = 57856;
constexpr int L_XP = 57856;
constexpr int L_KT = 67072;
constexpr int L_CNT = 70272;
constexpr int LDS_BYTES = 72448;

constexpr int CODE_LINES = 150;

struct Params {
  const u16* x; const u16* wr; const u16* wi;
  const u16* are; const u16* aim; const u16* bre; const u16* bim; const u16* cre; const u16* cim;
  const u16* logdt; const u16* dskip; const u16* gw;
  u16* out; char* ws;
};

constexpr double cx_sin_oct(double x) { double x2 = x * x, t = x, s = x; for (int i = 1; i < 9; ++i) { t *= -x2 / ((2 * i) * (2 * i + 1)); s += t; } return s; }
constexpr double cx_cos_oct(double x) { double x2 = x * x, t = 1, s = 1; for (int i = 1; i < 9; ++i) { t *= -x2 / ((2 * i - 1) * (2 * i)); s += t; } return s; }
constexpr double cx_sin(long n, long d) {
  n %= d; if (n < 0) n += d;
  const long o = (8 * n) / d;
  const double r = 6.283185307179586476925 * (double)(8 * n - o * d) / (8.0 * (double)d), q = 0.78539816339744830962;
  switch (o) {
    case 0: return cx_sin_oct(r);       case 1: return cx_cos_oct(q - r);
    case 2: return cx_cos_oct(r);       case 3: return cx_sin_oct(q - r);
    case 4: return -cx_sin_oct(r);      case 5: return -cx_cos_oct(q - r);
    case 6: return -cx_cos_oct(r);      default: return -cx_sin_oct(q - r);
  }
}
constexpr double cx_cos(long n, long d) { return cx_sin(4 * n + d, 4 * d); }
constexpr u32 cx_f32(double v) { return __builtin_bit_cast(u32, (float)v); }
constexpr u32 cx_bf(double v) { const u32 u = cx_f32(v); return (u + 0x7fffu + ((u >> 16) & 1u)) >> 16; }
constexpr int cx_pi(int g, int jj) { return 16 * (jj >> 2) + 4 * g + (jj & 3); }
struct Frag { u32 v[64][4]; };
enum { TF_FC0, TF_FS0, TF_CO0, TF_SO0, TF_COT0, TF_SOT0, TF_TC, TF_TS, TF_M, TF_Q0, TF_N = TF_Q0 + 24 };
constexpr Frag make_frag(int item) {
  Frag f{};
  for (int lane = 0; lane < 64; ++lane) {
    const int i16 = lane & 15, g = lane >> 4;
    if (item < TF_TC) {
      const int sn = item & 1;
      for (int jj = 0; jj < 8; ++jj) {
        const int m = cx_pi(g, jj), a = i16;
        long n = 0, d = 32;
        if (item < TF_CO0) { n = a * m; d = 32; }
        else if (item < TF_COT0) { n = a * (2 * m + 1); d = 64; }
        else { n = m * (2 * a + 1); d = 64; }
        f.v[lane][jj >> 1] |= cx_bf(sn ? cx_sin(n, d) : cx_cos(n, d)) << (16 * (jj & 1));
      }
    } else if (item == TF_TC || item == TF_TS) {
      for (int j = 0; j < 4; ++j) {
        const long n = (2 * g + (j & 1)) * (16 * (j >> 1) + i16);
        f.v[lane][j] = cx_f32(item == TF_TC ? cx_cos(n, 448) : cx_sin(n, 448));
      }
    } else if (item == TF_M) {
      for (int j = 0; j < 4; ++j) {
        const int k0 = 2 * g + (j >> 1);
        const double cf = ((k0 == 0 || k0 == 7) ? 1.0 : 2.0) / (2048.0 * 448.0);
        const double v = i16 < 14 ? cf * ((j & 1) ? -cx_sin(k0 * i16, 14) : cx_cos(k0 * i16, 14)) : 0.0;
        f.v[lane][j >> 1] |= cx_bf(v) << (16 * (j & 1));
      }
    } else {
      const int q = (item - TF_Q0) / 6, sub = (item - TF_Q0) % 6, k0 = 2 * q + ((i16 >> 1) & 1), ri = i16 & 1;
      if (sub < 2) {
        if ((i16 >> 2) == 2 * sub + (g & 1))
          for (int j = 0; j < 8; ++j) {
            const int d0 = 8 * (g >> 1) + j;
            const double v = d0 < 14 ? (ri ? -cx_sin(d0 * k0, 14) : cx_cos(d0 * k0, 14)) : 0.0;
            f.v[lane][j >> 1] |= cx_bf(v) << (16 * (j & 1));
          }
      } else {
        for (int j = 0; j < 4; ++j) {
          const long n = cx_pi(g, 4 * (sub & 1) + j) * k0;
          f.v[lane][j] = cx_f32(sub < 4 ? cx_cos(n, 448) : (ri ? -cx_sin(n, 448) : cx_sin(n, 448)));
        }
      }
    }
  }
  return f;
}
template <int I> struct FragC { static constexpr Frag f = make_frag(I); };
#define FR(i) FragC<(i)>::f
#define FR6(i) FR(i), FR(i + 1), FR(i + 2), FR(i + 3), FR(i + 4), FR(i + 5)
__device__ const __attribute__((aligned(1024))) Frag TF[TF_N] = {FR6(0), FR(6), FR(7), FR(8), FR6(9), FR6(15), FR6(21), FR6(27)};

struct KTab { u16 v[2080]; };
constexpr KTab make_ktab() {
  KTab t{};
  for (int i = 0; i < 2080; ++i) {
    const int l = 2048 - i;
    int idx = 768;
    if (l >= 0) {
      const int sh = l > 256 ? (l >= 1024 ? 3 : (l >= 512 ? 2 : 1)) : 0;
      const int v = sh ? (((l + (1 << (sh - 1)) - 1 + ((l >> sh) & 1)) >> sh) << sh) : l;
      if (v < 256) idx = ((v & 7) + 8 * (v >> 7)) * 16 + ((v >> 3) & 15);
      else if (v < 512) idx = 256 + ((((v - 256) >> 1) & 3) + 4 * ((v - 256) >> 7)) * 16 + (((v - 256) >> 3) & 15);
      else if (v < 1024) idx = 256 + (8 + (((v - 512) >> 2) & 1) + 2 * ((v - 512) >> 7)) * 16 + (((v - 512) >> 3) & 15);
      else if (v < 2048) idx = 512 + ((v - 1024) >> 7) * 16 + (((v - 1024) >> 3) & 15);
      else idx = 640;
    }
    t.v[i] = (u16)(2 * idx);
  }
  return t;
}
__device__ const __attribute__((aligned(16))) KTab KTAB = make_ktab();

template <class T, class F> DI T bc(F v) { return __builtin_bit_cast(T, v); }
DI f32x4 mfma32(u32x4 a, u32x4 b, f32x4 c) { return __builtin_amdgcn_mfma_f32_16x16x32_bf16(bc<bf16x8>(a), bc<bf16x8>(b), c, 0, 0, 0); }
DI f32x4 mfma16(u32x2 a, u32x2 b, f32x4 c) { return __builtin_amdgcn_mfma_f32_16x16x16bf16_1k(bc<s16x4>(a), bc<s16x4>(b), c, 0, 0, 0); }
DI u32 pk(float a, float b) { f32x2 v = {a, b}; return bc<u32>(__builtin_convertvector(v, bf16x2)); }
DI u32 pk2(f32x2 v) { return bc<u32>(__builtin_convertvector(v, bf16x2)); }
DI float bfr(float a) { return bc<float>(pk(0.f, a) & 0xffff0000u); }
DI float lo16(u32 v) { return bc<float>(v << 16); }
DI float hi16(u32 v) { return bc<float>(v & 0xffff0000u); }
DI f32x2 up2(u32 v) { return f32x2{lo16(v), hi16(v)}; }
DI float b2f(u16 v) { return bc<float>((u32)v << 16); }
DI u32x2 trread(u32 a) { return bc<u32x2>(__builtin_amdgcn_ds_read_tr16_b64_v4i16((LAS s16x4*)(size_t)a)); }
DI u32x4 cat(u32x2 a, u32x2 b) { u32x4 r = {a.x, a.y, b.x, b.y}; return r; }
DI float sinr(float x) { return __builtin_amdgcn_sinf(x); }
DI float cosr(float x) { return __builtin_amdgcn_cosf(x); }
DI float fx1(float v) { return __builtin_amdgcn_update_dpp(v, v, 0xB1, 0xf, 0xf, true); }
DI float fr8(float v) { return __builtin_amdgcn_update_dpp(v, v, 0x128, 0xf, 0xf, true); }
DI u32 ux1(u32 v) { return (u32)__builtin_amdgcn_update_dpp((int)v, (int)v, 0xB1, 0xf, 0xf, true); }
DI u32 ur8(u32 v) { return (u32)__builtin_amdgcn_update_dpp((int)v, (int)v, 0x128, 0xf, 0xf, true); }
DI u32x4 sx1(u32x4 v, u32 m) { u32x4 r = {ux1(v.x) ^ m, ux1(v.y) ^ m, ux1(v.z) ^ m, ux1(v.w) ^ m}; return r; }
DI u32x4 sr8(u32x4 v, u32 m) { u32x4 r = {ur8(v.x) ^ m, ur8(v.y) ^ m, ur8(v.z) ^ m, ur8(v.w) ^ m}; return r; }
DI u32x4 xr(u32x4 v, u32 m) { u32x4 r = {v.x ^ m, v.y ^ m, v.z ^ m, v.w ^ m}; return r; }
DI u32x4 pk8(const float (&v)[8]) { return u32x4{pk(v[0], v[1]), pk(v[2], v[3]), pk(v[4], v[5]), pk(v[6], v[7])}; }
template <class T> DI T lds_ld(u32 a) { return *(LAS T*)(size_t)a; }
template <class T> DI void lds_st(u32 a, T v) { *(LAS T*)(size_t)a = v; }
DI void wait_vm0() { __builtin_amdgcn_s_waitcnt(0x0F70); }
DI void drain_vm() { asm volatile("s_waitcnt vmcnt(0)" ::: "memory"); }
DI void bar() { __builtin_amdgcn_s_waitcnt(0xC07F); asm volatile("s_barrier" ::: "memory"); }
template <class T> DI void pin(T& v) { asm volatile("" : "+v"(v)); }
typedef __amdgpu_buffer_rsrc_t rsrc_t;
DI rsrc_t mkrs(const void* p, u32 bytes) { return __builtin_amdgcn_make_buffer_rsrc((void*)p, 0, bytes, 0x00020000); }
template <int AUX> DI void dma16(rsrc_t rs, u32 lds, u32 voff, u32 soff) { __builtin_amdgcn_raw_ptr_buffer_load_lds(rs, (LAS void*)(size_t)lds, 16, voff, soff, 0, AUX); }
DI f32x2 cmul(f32x2 x, f32x2 y) {
  f32x2 t;
  asm("v_pk_mul_f32 %0, %1, %2 op_sel:[0,0] op_sel_hi:[0,1]\n\tv_pk_fma_f32 %0, %1, %2, %0 op_sel:[1,1,0] op_sel_hi:[1,0,1] neg_lo:[1,0,0]" : "=&v"(t) : "v"(x), "v"(y));
  return t;
}
DI float dot2b(u32 a, u32 b, float c) { return __builtin_amdgcn_fdot2_f32_bf16(bc<bf16x2>(a), bc<bf16x2>(b), c, false); }
DI float wave_sum(float a) {
  a += __builtin_amdgcn_update_dpp(0.f, a, 0xB1, 0xf, 0xf, true);
  a += __builtin_amdgcn_update_dpp(0.f, a, 0x4E, 0xf, 0xf, true);
  a += __builtin_amdgcn_update_dpp(0.f, a, 0x141, 0xf, 0xf, true);
  a += __builtin_amdgcn_update_dpp(0.f, a, 0x140, 0xf, 0xf, true);
  a += __builtin_amdgcn_update_dpp(0.f, a, 0x142, 0xa, 0xf, false);
  a += __builtin_amdgcn_update_dpp(0.f, a, 0x143, 0xc, 0xf, false);
  return bc<float>(__builtin_amdgcn_readlane(bc<int>(a), 63));
}
DI int pi_k(int g, int jj) { return 16 * (jj >> 2) + 4 * g + (jj & 3); }

#define CNOP 1
#define CNOPR 2
#define SH2PRIO 2
#define SLP 2
#define RLX_AGENT __ATOMIC_RELAXED, __HIP_MEMORY_SCOPE_AGENT
template <bool P> DI void st16(rsrc_t rs, u32 voff, u32 soff, u32x4 v) {
  __builtin_amdgcn_raw_buffer_store_b128(v, rs, voff, soff, P ? 16 : 0);
  asm volatile("s_nop 1" :: "v"(v) : "memory");
}
template <bool P> DI u32x4 ld16(rsrc_t rs, u32 voff, u32 soff) { return __builtin_amdgcn_raw_buffer_load_b128(rs, voff, soff, P ? 16 : 0); }
DI void publish(u32* set, int idx, u32 epoch) {
  if (threadIdx.x < 8) __hip_atomic_store((GAS u32*)set + threadIdx.x * FR_ + idx * FL, epoch, RLX_AGENT);
}
DI void poll_flags(const u32* first, int n, u32 epoch) {
  const int lane = threadIdx.x & 63;
  const GAS u32* f = (const GAS u32*)first + (lane < n ? lane : 0) * FL;
  for (int spin = 0; spin < (1 << 18); ++spin) {
    if (__all(__hip_atomic_load(f, RLX_AGENT) == epoch)) break;
    __builtin_amdgcn_s_sleep(SLP);
  }
}

#define STAMP(i) do {} while (0)
#define STAMP_DECL do {} while (0)
#define STAMP_FLUSH() do {} while (0)
#define STAMP_ARG
#define STAMP_PASS

DI u32x4 tf(rsrc_t rs, int item, u32 l16) { return __builtin_amdgcn_raw_buffer_load_b128(rs, l16 + (item & 3) * 1024, (item >> 2) * 4096, 0); }
DI u32x4 tfs(rsrc_t rs, int item, u32 l16, u32 so) { return __builtin_amdgcn_raw_buffer_load_b128(rs, l16 + item * 1024, so, 0); }

DI void fft_first_loads(const Params& p, const u32 lb, const int b) {
  const int lane = threadIdx.x & 63, w = __builtin_amdgcn_readfirstlane(threadIdx.x >> 6);
  const u32 l16 = lane * 16;
  const int s2 = b & 63, q = b >> 6;
  const rsrc_t rx = mkrs(p.x, S_ * D_ * 2), rtf = mkrs(TF, TF_N * 1024);
  if (lane < 56) {
#pragma unroll
    for (int r = 0; r < 8; ++r) dma16<0>(rx, lb + L_XT + (8 * w + r) * XRS, l16, (64 * (8 * w + r) + s2) * (D_ * 2));
  }
  const u32 tq = (TF_Q0 + 6 * q) * 1024;
  dma16<0>(rtf, lb + L_C1 + w * 2048, l16, w == 0 ? 0u : tq + (2 * w - 2) * 1024);
  dma16<0>(rtf, lb + L_C1 + w * 2048 + 1024, l16, w == 0 ? 1024u : tq + (2 * w - 1) * 1024);
}

struct P3C { u32x4 b1, b2; f32x4 tc, ts; u32x2 m; };
template <int PH>
DI void p3_prepare(const Params& p, const u32 lb, const int b, const rsrc_t rtf, P3C& c) {
  const int lane = threadIdx.x & 63, i16 = lane & 15, g = lane >> 4;
  const u32 l16 = lane * 16;
  const int s2 = b & 63, q = b >> 6;
  c.tc = bc<f32x4>(tf(rtf, TF_TC, l16)); c.ts = bc<f32x4>(tf(rtf, TF_TS, l16));
  const u32x4 m4 = tf(rtf, TF_M, l16);
  c.m = u32x2{m4.x, m4.y};
  c.b1 = lds_ld<u32x4>(lb + L_C3 + l16); c.b2 = lds_ld<u32x4>(lb + L_C3 + 1024 + l16);
  pin(c.b1); pin(c.b2); pin(c.tc); pin(c.ts); pin(c.m);
}
DI void p3_make_b(const u32 lb, const int b) {
  const int lane = threadIdx.x & 63, i16 = lane & 15, g = lane >> 4;
  const int s2 = b & 63, q = b >> 6;
  const int ro = i16 >> 3;
  const float a = (float)(64 * (8 * q + (i16 & 7)) + s2) * (1.0f / 2048.0f), P0 = a * (float)(4 * g);
  float u[8], v[8];
#pragma unroll
  for (int jj = 0; jj < 8; ++jj) {
    const float ph = __builtin_fmaf(a, (float)(16 * (jj >> 2) + (jj & 3)), P0);
    u[jj] = sinr(ph + (ro ? 0.f : 0.25f));
    v[jj] = sinr(ph + (ro ? 0.25f : 0.5f));
  }
  lds_st<u32x4>(lb + L_C3 + lane * 16, pk8(u));
  lds_st<u32x4>(lb + L_C3 + 1024 + lane * 16, pk8(v));
}
template <bool LDSX>
DI void gate_rows(const Params& p, const u32 lb, const int b, const int r0, const int nr) {
  const int lane = threadIdx.x & 63;
  const u32 l16 = lane * 16;
  const int s2 = b & 63, q = b >> 6;
  u32x4 gwv = {0, 0, 0, 0};
  if (lane < 56) gwv = *(const GAS u32x4*)((const char*)p.gw + l16);
#pragma unroll
  for (int r = 0; r < 2; ++r) {
    if (r < nr) {
      float acc = 0.f;
      if (lane < 56) {
        u32x4 xv;
        if (LDSX) xv = lds_ld<u32x4>(lb + L_XT + (8 * q + r0 + r) * XRS + l16);
        else xv = *(const GAS u32x4*)((const char*)(p.x + (size_t)(64 * (8 * q + r0 + r) + s2) * D_) + l16);
#pragma unroll
        for (int e = 0; e < 4; ++e) acc = dot2b(pk2(up2(xv[e]) * up2(gwv[e])), 0x3f803f80u, acc);
      }
      const float gg = bfr(__builtin_amdgcn_rcpf(1.0f + __expf(-bfr(wave_sum(acc)))));
      if (lane == 0) lds_st<f32x2>(lb + L_G + (r0 + r) * 8, f32x2{gg, bfr(1.0f - gg)});
    }
  }
}

template <int PH>
DI void fft_phase3(const Params& p, const u32 lb, const int b, u32* ctl, const u32 epoch, const u32x4 (&fc)[2], const u32x4 (&fs)[2], const P3C& c STAMP_ARG) {
  constexpr bool P = PH == 0;
  constexpr int TPAUX = P ? 16 : 0;
  const int tid = threadIdx.x, lane = tid & 63, w = __builtin_amdgcn_readfirstlane(tid >> 6), i16 = lane & 15, g = lane >> 4;
  const u32 l16 = lane * 16;
  const int s2 = b & 63, q = b >> 6;
  const f32x4 z4 = {0, 0, 0, 0};
  const rsrc_t rws = mkrs(p.ws, 16u << 20);
  u32 tpa = lb + L_TP + (4 * g + (i16 >> 2)) * TPRS + (i16 & 3) * 128 + (w >> 1) * 32 + (w & 1) * 8;
  const int row = 2 * w + (i16 >> 3);
  u32 zw = lb + L_Z + ((i16 & 7) * 16 + row) * 64 + ((g ^ (row >> 2)) & 3) * 16;
  u32 zr = lb + L_Z + (w * 16 + i16) * 64 + ((g ^ (i16 >> 2)) & 3) * 16;
  u32 boff = (u32)(tid * B_D);
  u32 biw = lb + L_BI + tid * 2, bir = lb + L_BI + w * BRS + (32 * i16 + 4 * g) * 2;
  u32 ooff = (u32)((64 * (8 * q + w) + s2) * (D_ * 2) + (32 * i16 + 4 * g) * 2);
  u32 mi = (i16 & 1) ? 0u : 0x80008000u;
  pin(tpa); pin(zw); pin(zr); pin(boff); pin(biw); pin(bir); pin(ooff); pin(mi);
  STAMP(13);
  if (P) { poll_flags(ctl + CT_F2 + (b & 7) * FR_ + w * 32 * FL, 32, epoch); bar(); __builtin_amdgcn_s_setprio(3); }
  STAMP(14);
#pragma unroll
  for (int r = 0; r < 4; ++r) dma16<TPAUX>(rws, lb + L_TP + (4 * w + r) * TPRS, l16, (u32)WS_TP + s2 * TP_S2 + (4 * w + r) * 1024);
  wait_vm0();
  bar();
  STAMP(15);
#pragma unroll
  for (int t = 0; t < 2; ++t) {
    u32x4 Tr = cat(trread(tpa + t * 512), trread(tpa + t * 512 + 16 * TPRS));
    u32x4 Ti = cat(trread(tpa + t * 512 + 16), trread(tpa + t * 512 + 16 + 16 * TPRS));
    f32x4 z = mfma32(Ti, c.b2, mfma32(Tr, c.b1, z4));
    lds_st<u32x2>(zw + t * 8, u32x2{pk(z[0], z[1]), pk(z[2], z[3])});
  }
  bar();
  STAMP(16);
  f32x2 ga[2][2];
  {
    u32x4 A = lds_ld<u32x4>(zr), A2 = sx1(A, mi);
    const float gg = lds_ld<float>(lb + L_G + w * 8);
#pragma unroll
    for (int td = 0; td < 2; ++td) {
      f32x4 G = mfma32(A2, fs[td], mfma32(A, fc[td], z4));
      const float c0 = c.tc[2 * td], s0 = c.ts[2 * td], c1 = c.tc[2 * td + 1], s1 = c.ts[2 * td + 1];
      const float g0r = G[0] * c0 - G[1] * s0, g0i = G[1] * c0 + G[0] * s0;
      const float g1r = G[2] * c1 - G[3] * s1, g1i = G[3] * c1 + G[2] * s1;
      f32x4 y = mfma16(u32x2{pk(g0r, g0i), pk(g1r, g1i)}, c.m, z4);
      ga[td][0] = up2(pk2(up2(pk(y[0], y[1])) * gg));
      ga[td][1] = up2(pk2(up2(pk(y[2], y[3])) * gg));
      pin(ga[td][0]); pin(ga[td][1]);
    }
  }
  STAMP(17);
  if (P) { poll_flags(ctl + CT_FB + (b & 7) * FR_ + w * 28 * FL, 28, epoch); bar(); }
  STAMP(18);
  if (tid < D_) {
    const u32x4 bv = ld16<P>(rws, boff, (u32)WS_B + (((s2 & 7) * 8 + (s2 >> 3)) * 32 + 8 * q) * 2);
#pragma unroll
    for (int e = 0; e < 4; ++e) {
      lds_st<u16>(biw + (2 * e) * BRS, (u16)bv[e]);
      lds_st<u16>(biw + (2 * e + 1) * BRS, (u16)(bv[e] >> 16));
    }
  }
  bar();
  {
    const rsrc_t ro_ = mkrs(p.out, S_ * D_ * 2);
    const float og = lds_ld<float>(lb + L_G + w * 8 + 4);
    const u32x2 bq0 = lds_ld<u32x2>(bir), bq1 = lds_ld<u32x2>(bir + 32);
#pragma unroll
    for (int td = 0; td < 2; ++td) {
      const u32x2 bq = td ? bq1 : bq0;
      const f32x2 o01 = ga[td][0] + up2(pk2(up2(bq.x) * og));
      const f32x2 o23 = ga[td][1] + up2(pk2(up2(bq.y) * og));
      if (i16 < 14) __builtin_amdgcn_raw_buffer_store_b64(u32x2{pk2(o01), pk2(o23)}, ro_, ooff, td * 32, 0);
    }
  }
  STAMP(19);
  if (P && b == 0 && tid == 0) __hip_atomic_store((GAS u32*)ctl + CT_GEN, epoch, RLX_AGENT);
}

template <int PH>
DI void fft_upper(const Params& p, const u32 lb, const int b, u32* ctl, u32x4 (&fc)[2], u32x4 (&fs)[2], P3C& c3) {
  constexpr bool P = PH == 0;
  const int lane = threadIdx.x & 63, w = __builtin_amdgcn_readfirstlane(threadIdx.x >> 6);
  const u32 l16 = lane * 16;
  const rsrc_t rtf = mkrs(TF, TF_N * 1024);
  if (w == 4) p3_make_b(lb, b);
  if (P) {
    bar(); bar(); bar();
    bar(); bar(); bar();
    __builtin_amdgcn_s_setprio(SH2PRIO);
  }
  if (P) gate_rows<P>(p, lb, b, w, 1); else gate_rows<P>(p, lb, b, 2 * (w - 4), 2);
  fc[0] = tf(rtf, TF_FC0, l16); fs[0] = tf(rtf, TF_FS0, l16);
  fc[1] = xr(fc[0], 0x80000000u); fs[1] = xr(fs[0], 0x80000000u);
  pin(fc[0]); pin(fc[1]); pin(fs[0]); pin(fs[1]);
  if (!P) bar();
  p3_prepare<PH>(p, lb, b, rtf, c3);
  if (!P) bar();
}

template <int PH>
DI void fft_role(const Params& p, const u32 lb, const int b, u32* ctl, const u32 gen, u32x4 (&fc)[2], u32x4 (&fs)[2], P3C& c3 STAMP_ARG) {
  constexpr bool P = PH == 0;
#define TPAUXV 16
  constexpr int AUX = P ? 16 : 0, TPAUX = P ? TPAUXV : 0;
  (void)AUX;
  const int tid = threadIdx.x, lane = tid & 63, w = __builtin_amdgcn_readfirstlane(tid >> 6), i16 = lane & 15, g = lane >> 4;
  const u32 l16 = lane * 16;
  const int s2 = b & 63, q = b >> 6;
  const int xx = b & 7, mm = (b >> 3) & 3, kq = b >> 5;
  const int k1w = (((mm & 1) ? (xx ? 32 - xx : 8) : xx) + 16 * (mm >> 1)) & 31;
  const f32x4 z4 = {0, 0, 0, 0};
  const rsrc_t rws = mkrs(p.ws, 16u << 20), rtf = mkrs(TF, TF_N * 1024);
  const u32 tpo = (u32)((tid >> 3) * TP_S2 + (tid & 7) * 16), tps = (u32)WS_TP + k1w * 1024 + kq * 128;
  if (PH == 2 || PH == 3) {
    fc[0] = tf(rtf, TF_FC0, l16); fs[0] = tf(rtf, TF_FS0, l16);
    fc[1] = xr(fc[0], 0x80000000u); fs[1] = xr(fs[0], 0x80000000u);
  }

  u32x4 co[2], so[2], cot[2], sot[2];
  auto load_p2 = [&]() {
    const bool mir = kq >= 4;
    const int col0 = mir ? 448 - (56 * kq + 49) : 56 * kq;
    const rsrc_t rr = mkrs(p.wr, S_ * DF * 2), ri_ = mkrs(p.wi, S_ * DF * 2);
#pragma unroll
    for (int i = 0; i < 3; ++i) {
      const int gi = w + 4 * i;
      if (gi < 9) {
        const int gam = 64 * gi + lane, k2 = (gam * 7282) >> 16, chunk = gam - 9 * k2;
        const int ks = k1w + 32 * k2, row = mir ? ((2048 - ks) & 2047) : ks;
        const u32 vo = (u32)(((row * DF + col0) * 2) & ~15) + chunk * 16;
        dma16<0>(rr, lb + L_W + gi * 1024, vo, 0);
        dma16<0>(ri_, lb + L_W + 9216 + gi * 1024, vo, 0);
      }
    }
    dma16<0>(rtf, lb + L_C2 + w * 1024, l16, (TF_CO0 + w) * 1024);
  };

  if (PH == 0 || PH == 1) {
    u32x4 C1, S1, nS1;
    {
      const float k1 = (float)(16 * (w >> 1) + i16) * (1.0f / 2048.0f), P0 = k1 * (float)(64 * g + s2), Pl = k1 * 256.f;
      float c[8], s[8];
#pragma unroll
      for (int j = 0; j < 8; ++j) { const float ph = __builtin_fmaf(Pl, (float)j, P0); c[j] = cosr(ph); s[j] = sinr(ph); }
      C1 = pk8(c); S1 = pk8(s); nS1 = xr(S1, 0x80008000u);
      pin(C1); pin(S1); pin(nS1);
    }
    int d0a = 8 * (g >> 1) + (i16 >> 2), d0b = d0a + 4;
    d0b = d0b < 14 ? d0b : 0;
    u32 xa = lb + L_XT + (8 * w + (g & 1)) * XRS + 8 * (i16 & 3);
    u32 xb = xa + d0b * 64; xa += d0a * 64;
    u32 xw = lb + L_XCH + w * 256 + lane * 4;
    u32 xr_ = lb + L_XCH + (w & 1) * 4096 + lane * 4;
    u32 toff = (u32)((16 * (w >> 1) + i16) * T_K1 + 4 * (w & 1) * 32 + g * 16);
    u32 imm = (i16 & 1) ? 0x80008000u : 0u;
    pin(xa); pin(xb); pin(xw); pin(xr_); pin(toff); pin(imm);
    const u32 tso = (u32)WS_T + s2 * 1024 + q * 256;
    STAMP(1);
    wait_vm0();
    bar();
    const u32 cb = lb + L_C1 + l16;
    fc[0] = lds_ld<u32x4>(cb); fs[0] = lds_ld<u32x4>(cb + 1024);
    const u32x4 Ba1 = lds_ld<u32x4>(cb + 2048), Ba2 = lds_ld<u32x4>(cb + 3072);
    const f32x4 twc0 = lds_ld<f32x4>(cb + 4096), twc1 = lds_ld<f32x4>(cb + 5120), tws0 = lds_ld<f32x4>(cb + 6144), tws1 = lds_ld<f32x4>(cb + 7168);
    fc[1] = xr(fc[0], 0x80000000u); fs[1] = xr(fs[0], 0x80000000u);
    STAMP(2);
    f32x4 D2[2][2];
#pragma unroll
    for (int sg = 0; sg < 2; ++sg) {
      f32x4 H[2];
#pragma unroll
      for (int td = 0; td < 2; ++td) {
        const int o = td * 32 + sg * 4 * XRS;
        u32x4 A0 = cat(trread(xa + o), trread(xb + o));
        u32x4 A1 = cat(trread(xa + o + 2 * XRS), trread(xb + o + 2 * XRS));
        H[td] = mfma32(A1, Ba2, mfma32(A0, Ba1, z4));
      }
      float h[8];
#pragma unroll
      for (int j = 0; j < 4; ++j) {
        h[j] = __builtin_fmaf(fx1(H[0][j]), tws0[j], H[0][j] * twc0[j]);
        h[4 + j] = __builtin_fmaf(fx1(H[1][j]), tws1[j], H[1][j] * twc1[j]);
      }
      u32x4 A = pk8(h), A2 = sx1(A, imm);
#pragma unroll
      for (int tk = 0; tk < 2; ++tk) D2[sg][tk] = mfma32(A2, fs[tk], mfma32(A, fc[tk], z4));
    }
#pragma unroll
    for (int tk = 0; tk < 2; ++tk)
#pragma unroll
      for (int r = 0; r < 4; ++r) lds_st<u32>(xw + (tk * 4 + r) * 1024, pk(D2[0][tk][r], D2[1][tk][r]));
    bar();
    STAMP(3);
    {
      u32 pc[4][2];
#pragma unroll
      for (int kk = 0; kk < 2; ++kk) {
        const u32 br = xr_ + kk * 2048;
        u32x4 Xr = {lds_ld<u32>(br), lds_ld<u32>(br + 256), lds_ld<u32>(br + 512), lds_ld<u32>(br + 768)};
        u32x4 Xi = {lds_ld<u32>(br + 1024), lds_ld<u32>(br + 1280), lds_ld<u32>(br + 1536), lds_ld<u32>(br + 1792)};
        f32x4 Tr = mfma32(Xi, S1, mfma32(Xr, C1, z4));
        f32x4 Ti = mfma32(Xr, nS1, mfma32(Xi, C1, z4));
        pc[kk][0] = pk(Tr[0], Tr[1]); pc[kk][1] = pk(Tr[2], Tr[3]);
        pc[2 + kk][0] = pk(Ti[0], Ti[1]); pc[2 + kk][1] = pk(Ti[2], Ti[3]);
      }
      u32x4 lo = {pc[0][0], pc[0][1], pc[1][0], pc[1][1]}, hi = {pc[2][0], pc[2][1], pc[3][0], pc[3][1]};
#pragma unroll
      for (int e = 0; e < 4; ++e) {
        auto r1 = __builtin_amdgcn_permlane16_swap(lo[e], hi[e], false, false);
        auto r2 = __builtin_amdgcn_permlane32_swap(r1[0], r1[1], false, false);
        lo[e] = r2[0]; hi[e] = r2[1];
      }
      st16<P>(rws, toff, tso, lo);
      st16<P>(rws, toff + 64, tso, hi);
    }
    STAMP(4);
    if (P) {
      drain_vm();
      STAMP(20);
      bar();
      publish(ctl + CT_F1, b, gen + 1u);
      __builtin_amdgcn_s_setprio(2);
      load_p2();
    }
    STAMP(5);
  }
  const u32 epoch = __builtin_amdgcn_readfirstlane(gen) + 1u;

  if (PH == 2) load_p2();
  if (PH == 0 || PH == 2) {
    const int ri = i16 >> 3;
    u32 tsv = (u32)((lane >> 1) * 1024 + w * 256 + (lane & 1) * 16);
    u32 tiw = lb + L_TI + w * 2048 + ((lane >> 1) & 1) * 1024 + (lane >> 2) * 32 + (lane & 1) * 16;
    u32 ta = lb + L_TI + w * 2048 + (4 * g + (i16 >> 2)) * 32 + (i16 & 3) * 8;
    u32 tow = lb + L_TO + 2 * i16 * 144 + w * 32 + g * 8;
    u32 tor = lb + L_TO + (tid >> 3) * 144 + (tid & 7) * 16;
    u32 mf = ri ? 0x80008000u : 0u, mi = ri ? 0u : 0x80008000u;
    float wr_[16], wi_[16];
    {
      const bool mir = kq >= 4;
      const int c = i16 & 7, kd = 2 * w + (c >> 2) + 14 * (4 * kq + (c & 3)), col0 = mir ? 448 - (56 * kq + 49) : 56 * kq;
      const int row0 = mir ? ((2048 - k1w) & 2047) : k1w;
      u32 wa = lb + L_W + 4 * g * 144 + (((row0 * DF + col0) * 2) & 15) + ((mir ? 448 - kd : kd) - col0) * 2;
      const u32 sg = (mir != (ri != 0)) ? 0u : 0x80000000u;
      pin(tsv); pin(tiw); pin(ta); pin(tow); pin(tor); pin(mf); pin(mi); pin(wa);
      STAMP(7);
      if (P) poll_flags(ctl + CT_F1 + (b & 7) * FR_ + w * 64 * FL, 64, epoch);
      wait_vm0();
      bar();
      const u32 so_ = (u32)WS_T + k1w * T_K1 + kq * 32;
      const u32x4 t0 = ld16<P>(rws, tsv, so_), t1 = ld16<P>(rws, tsv, so_ + 32 * 1024);
      __builtin_amdgcn_sched_barrier(0);
      co[0] = lds_ld<u32x4>(lb + L_C2 + l16); so[0] = lds_ld<u32x4>(lb + L_C2 + 1024 + l16);
      cot[0] = lds_ld<u32x4>(lb + L_C2 + 2048 + l16); sot[0] = lds_ld<u32x4>(lb + L_C2 + 3072 + l16);
      co[1] = xr(so[0], 0x00008000u);
      so[1] = xr(co[0], 0x80000000u);
      cot[1] = xr(cot[0], 0x80000000u); sot[1] = xr(sot[0], 0x80000000u);
      pin(co[0]); pin(co[1]); pin(so[0]); pin(so[1]); pin(cot[0]); pin(cot[1]); pin(sot[0]); pin(sot[1]);
#pragma unroll
      for (int j = 0; j < 16; ++j) {
        const int o = (32 * (j >> 3) + 16 * ((j >> 2) & 1) + (j & 3)) * 144;
        wr_[j] = bc<float>((u32)lds_ld<u16>(wa + o) << 16);
        wi_[j] = bc<float>(((u32)lds_ld<u16>(wa + 9216 + o) << 16) + sg);
      }
#pragma unroll
      for (int j = 0; j < 16; ++j) { pin(wr_[j]); pin(wi_[j]); }
      __builtin_amdgcn_sched_barrier(0);
      if (P) __builtin_amdgcn_s_setprio(3);
      STAMP(8);
      lds_st<u32x4>(tiw, t0);
      lds_st<u32x4>(tiw + 512, t1);
    }
    STAMP(9);
    f32x4 E[2], O[2];
    {
      u32x4 B0 = cat(trread(ta), trread(ta + 512)), B1 = cat(trread(ta + 1024), trread(ta + 1536));
      u32x4 B0s = sr8(B0, mf), B1s = sr8(B1, mf);
#pragma unroll
      for (int t = 0; t < 2; ++t) {
        E[t] = mfma32(fs[t], B0s, mfma32(fc[t], B0, z4));
        O[t] = mfma32(so[t], B1s, mfma32(co[t], B1, z4));
      }
    }
    float Pp[8], Mm[8];
#pragma unroll
    for (int j = 0; j < 8; ++j) {
      const float e = E[j >> 2][j & 3], o = O[j >> 2][j & 3];
      const float ylo = e + o, yhi = e - o;
      const float vlo = __builtin_fmaf(fr8(ylo), wi_[j], ylo * wr_[j]);
      const float vhi = __builtin_fmaf(fr8(yhi), wi_[8 + j], yhi * wr_[8 + j]);
      Pp[j] = vlo + vhi; Mm[j] = vlo - vhi;
    }
    {
      u32x4 AP = pk8(Pp), AM = pk8(Mm);
      u32x4 APs = sr8(AP, mi), AMs = sr8(AM, mi);
#pragma unroll
      for (int t = 0; t < 2; ++t) {
        f32x4 ze = mfma32(APs, fs[t], mfma32(AP, fc[t], z4));
        f32x4 zo = mfma32(AMs, sot[t], mfma32(AM, cot[t], z4));
        lds_st<u32x2>(tow + t * 32 * 144, u32x2{pk(ze[0], ze[1]), pk(ze[2], ze[3])});
        lds_st<u32x2>(tow + t * 32 * 144 + 144, u32x2{pk(zo[0], zo[1]), pk(zo[2], zo[3])});
      }
    }
    bar();
    STAMP(10);
    {
      st16<P>(rws, tpo, tps, lds_ld<u32x4>(tor));
      st16<P>(rws, tpo, tps + 32 * TP_S2, lds_ld<u32x4>(tor + 32 * 144));
    }
    STAMP(11);
    if (P) { drain_vm(); bar(); publish(ctl + CT_F2, b, epoch); __builtin_amdgcn_s_setprio(SH2PRIO); }
    STAMP(12);
  }

  if (PH == 0) gate_rows<true>(p, lb, b, w, 1);
  if (PH == 3) bar();
  if (PH == 0 || PH == 3) p3_prepare<PH>(p, lb, b, rtf, c3);
  if (PH == 3) bar();
}

DI f32x2 cexp_rev(float x2, float ph) {
  const float e = __builtin_amdgcn_exp2f(x2);
  const float f = __builtin_amdgcn_fractf(ph);
  f32x2 cs = {cosr(f), sinr(f)};
  return cs * e;
}
struct KW { u32 a[8]; };
template <int ROW> DI void wrow(const KW& k, f32x2 v) { lds_st<u32>(k.a[ROW & 7] + ROW * 256, pk2(v)); }
template <int ROW> DI void wrow2(const KW& k, f32x2 lo, f32x2 hi) {
  const u32 a = pk2(lo), b = pk2(hi);
  asm volatile("ds_write2st64_b32 %0, %1, %2 offset0:%c3 offset1:%c4" :: "v"(k.a[ROW & 7]), "v"(a), "v"(b), "i"(ROW), "i"(ROW + 8) : "memory");
}
DI void kfrag(u32 fa, u32 fb, u32x4 (&A)[4]) {
  A[0] = lds_ld<u32x4>(fa); A[1] = lds_ld<u32x4>(fb); A[2] = lds_ld<u32x4>(fa + 128); A[3] = lds_ld<u32x4>(fb + 128);
}
DI f32x4 ktile(const u32x4 (&A)[4], const u32x4 (&V)[4]) {
  f32x4 acc = {0, 0, 0, 0};
#pragma unroll
  for (int ks = 0; ks < 4; ++ks) acc = mfma32(A[ks], V[ks], acc);
  return acc;
}
template <class F0, class F1>
DI void kgen(const u32 (&in)[6], float dt, const KW& k, u32 fa, u32 fb, u32 pb, F0&& hook0, F1&& hook1) {
  const float ar = dt * bc<float>(in[0] << 16), ai = dt * bc<float>(in[1] << 16);
  const f32x2 dB = {dt * bc<float>(in[2] << 16), dt * bc<float>(in[3] << 16)};
  const f32x2 Cc = {bc<float>(in[4] << 16), bc<float>(in[5] << 16)};
  const f32x2 CB = cmul(Cc, dB);
  const float x1 = ar * 1.44269504088896341f, p1 = ai * 0.15915494309189535f;
  const f32x2 E1 = cexp_rev(x1, p1), E8c = cexp_rev(8.f * x1, -8.f * p1), E128 = cexp_rev(128.f * x1, 128.f * p1);
  u32x4 V[4], A0[4], A1[4];
  {
    const f32x2 v2 = cmul(E8c, E8c), v3 = cmul(v2, E8c), v4 = cmul(v2, v2), v8 = cmul(v4, v4);
    const f32x2 v5 = cmul(v4, E8c), v6 = cmul(v4, v2), v7 = cmul(v4, v3);
    wrow2<0>(k, f32x2{1.f, 0.f}, v8); wrow2<1>(k, E8c, cmul(v8, E8c)); wrow2<2>(k, v2, cmul(v8, v2)); wrow2<3>(k, v3, cmul(v8, v3));
    wrow2<4>(k, v4, cmul(v8, v4)); wrow2<5>(k, v5, cmul(v8, v5)); wrow2<6>(k, v6, cmul(v8, v6)); wrow2<7>(k, v7, cmul(v8, v7));
    kfrag(fa, fb, V);
  }
  hook0();
  const f32x2 E2 = cmul(E1, E1), E3 = cmul(E2, E1), E4 = cmul(E2, E2), E5 = cmul(E4, E1), E6 = cmul(E4, E2), E7 = cmul(E4, E3);
  const f32x2 G1 = cmul(CB, E128);
  wrow2<0>(k, CB, G1); wrow2<1>(k, cmul(CB, E1), cmul(G1, E1)); wrow2<2>(k, cmul(CB, E2), cmul(G1, E2)); wrow2<3>(k, cmul(CB, E3), cmul(G1, E3));
  wrow2<4>(k, cmul(CB, E4), cmul(G1, E4)); wrow2<5>(k, cmul(CB, E5), cmul(G1, E5)); wrow2<6>(k, cmul(CB, E6), cmul(G1, E6)); wrow2<7>(k, cmul(CB, E7), cmul(G1, E7));
  kfrag(fa, fb, A0);
  const f32x2 G2 = cmul(G1, E128), G3 = cmul(G2, E128), G4 = cmul(G3, E128), G5 = cmul(G4, E128), G6 = cmul(G5, E128), G7 = cmul(G6, E128);
  wrow2<0>(k, G2, G4); wrow2<1>(k, cmul(G2, E2), cmul(G4, E4)); wrow2<2>(k, cmul(G2, E4), G5); wrow2<3>(k, cmul(G2, E6), cmul(G5, E4));
  wrow2<4>(k, G3, G6); wrow2<5>(k, cmul(G3, E2), cmul(G6, E4)); wrow2<6>(k, cmul(G3, E4), G7); wrow2<7>(k, cmul(G3, E6), cmul(G7, E4));
  kfrag(fa, fb, A1);
  hook1();
  __builtin_amdgcn_sched_barrier(0);
  const f32x4 a0 = ktile(A0, V);
  const f32x2 G8 = cmul(G7, E128), G9 = cmul(G8, E128), G10 = cmul(G9, E128), G11 = cmul(G10, E128);
  const f32x2 G12 = cmul(G11, E128), G13 = cmul(G12, E128), G14 = cmul(G13, E128), G15 = cmul(G14, E128);
  wrow2<0>(k, G8, cmul(G15, E128)); wrow<1>(k, G9); wrow<2>(k, G10); wrow<3>(k, G11);
  wrow<4>(k, G12); wrow<5>(k, G13); wrow<6>(k, G14); wrow<7>(k, G15);
  kfrag(fa, fb, A0);
  __builtin_amdgcn_sched_barrier(0);
  const f32x4 a1 = ktile(A1, V), a2 = ktile(A0, V);
  lds_st<f32x4>(pb, a0);
  lds_st<f32x4>(pb + 1024, a1);
  lds_st<f32x4>(pb + 2048, a2);
}

struct SsmLoads { u32x4 xt_v, kt0; u32x2 kt1; u32 in[6], ldt, dsk; };
template <int PH>
DI void ssm_first_loads(const Params& p, const int j, SsmLoads& L) {
  const int tid = threadIdx.x, w = __builtin_amdgcn_readfirstlane(tid >> 6), d = 2 * j, ch = w >> 2, n = tid & 255;
  const int sb_ = j / 7, db_ = j - sb_ * 7;
  if (PH == 0 || PH == 1)
    L.xt_v = __builtin_amdgcn_raw_buffer_load_b128(mkrs(p.x, S_ * D_ * 2), (u32)((tid >> 3) * (D_ * 2) + (tid & 7) * 16), (u32)(64 * sb_ * (D_ * 2) + db_ * 128), 0);
  if (PH != 1) {
    const u32 vo = n * 2, so_ = (d + ch) * (NS * 2), nb = D_ * NS * 2;
    const u16* arr[6] = {p.are, p.aim, p.bre, p.bim, p.cre, p.cim};
#pragma unroll
    for (int a = 0; a < 6; ++a) L.in[a] = __builtin_amdgcn_raw_buffer_load_b16(mkrs(arr[a], nb), vo, so_, 0);
    L.ldt = *(const GAS u16*)(p.logdt + d + ch);
    L.dsk = *(const GAS u16*)(p.dskip + d + ((w >> 1) & 1));
    L.kt0 = *(const GAS u32x4*)(KTAB.v + 8 * n);
    L.kt1 = *(const GAS u32x2*)(KTAB.v + 8 * n + 8);
  }
}

template <int PH>
DI void ssm_role(const Params& p, const u32 lb, const int j, u32* ctl, const u32 gen, const SsmLoads& L STAMP_ARG) {
  constexpr bool P = PH == 0;
  const int tid = threadIdx.x, lane = tid & 63, w = __builtin_amdgcn_readfirstlane(tid >> 6), i16 = lane & 15, g = lane >> 4;
  const int d = 2 * j, ch = w >> 2, n = tid & 255;
  const f32x4 z4 = {0, 0, 0, 0};
  const rsrc_t rws = mkrs(p.ws, 16u << 20);
  const int sb_ = j / 7, db_ = j - sb_ * 7;
  const u32x4 xt_v = L.xt_v, kt0 = L.kt0;
  const u32x2 kt1 = L.kt1;
  const u32 ldt = L.ldt;
  u32 dsk = L.dsk;
  const u32 (&in)[6] = L.in;
  auto transpose = [&]() {
    lds_st<u32x4>(lb + L_XP + (tid >> 3) * 144 + (tid & 7) * 16, xt_v);
    bar();
    const int chi = g + 4 * (w >> 2);
    const u32 a = lb + L_XP + (8 * chi + (i16 >> 2)) * 144 + (16 * (w & 3) + 4 * (i16 & 3)) * 2;
    st16<P>(rws, (u32)((16 * (w & 3) + i16) * XT_D + chi * 16), (u32)WS_XT + 64 * db_ * XT_D + sb_ * 128, cat(trread(a), trread(a + 4 * 144)));
  };
  if (PH == 1) { transpose(); return; }
  if (P) {
    if (tid == 0) lds_st<u32>(lb + L_CNT, 0u);
    __builtin_amdgcn_sched_barrier(0);
    transpose();
    __builtin_amdgcn_s_setprio(1);
    __builtin_amdgcn_sched_barrier(0);
  }
  KW kw;
  const u32 sb = lb + w * KSCR;
#pragma unroll
  for (int h = 0; h < 8; ++h) { kw.a[h] = sb + (((lane >> 2) ^ h) << 4) + (lane & 3) * 4; pin(kw.a[h]); }
  u32 fa = sb + i16 * 256 + ((g ^ (i16 & 3)) << 4) + ((i16 >> 2) & 1) * 64, fb = fa ^ 64;
  pin(fa); pin(fb);
  STAMP(1);
  kgen(in, bfr(expf(bc<float>(ldt << 16))), kw, fa, fb, sb + lane * 16,
       [&]() {
         if (P) {
           drain_vm();
           STAMP(5);
           if (lane == 0 && __hip_atomic_fetch_add((LAS u32*)(size_t)(lb + L_CNT), 1u, __ATOMIC_RELAXED, __HIP_MEMORY_SCOPE_WORKGROUP) == 7u)
             __hip_atomic_store((GAS u32*)ctl + CT_FX + (db_ * 32 + sb_) * FL, gen + 1u, RLX_AGENT);
         }
       },
       []() {});
  const u32 epoch = __builtin_amdgcn_readfirstlane(gen) + 1u;
  pin(dsk);
  STAMP(2);
  if (P && w == 0) poll_flags(ctl + CT_FX + (d >> 6) * 32 * FL, 32, epoch);
  STAMP(6);
  bar();
  const u32x4 uv_ = ld16<P>(rws, n * 16, (u32)WS_XT + (d + ch) * XT_D);
  STAMP(3);
  if ((w & 3) < 3) {
    const u32 pb = lb + ch * 4 * KSCR + (w & 3) * 1024 + lane * 16;
    const f32x4 s = (lds_ld<f32x4>(pb) + lds_ld<f32x4>(pb + KSCR)) + (lds_ld<f32x4>(pb + 2 * KSCR) + lds_ld<f32x4>(pb + 3 * KSCR));
    const u32 tb = lb + L_KT + ch * 1600 + ((w & 3) * 256 + 64 * g + i16) * 2, a = pk(s[0], s[1]), cc = pk(s[2], s[3]);
    lds_st<u16>(tb, (u16)a); lds_st<u16>(tb + 32, (u16)(a >> 16));
    lds_st<u16>(tb + 64, (u16)cc); lds_st<u16>(tb + 96, (u16)(cc >> 16));
  } else {
    if (lane == 0) lds_st<u16>(lb + L_KT + ch * 1600 + 1536, (u16)0);
    if (lane < 32) lds_st<u32x4>(lb + L_U + ch * 4608 + lane * 16, u32x4{0, 0, 0, 0});
  }
  bar();
  STAMP(4);
  {
    const u32 ktb = lb + L_KT + ch * 1600;
    u32 e[11];
#pragma unroll
    for (int k = 0; k < 4; ++k) { e[2 * k] = lds_ld<u16>(ktb + (kt0[k] & 0xffffu)); e[2 * k + 1] = lds_ld<u16>(ktb + (kt0[k] >> 16)); }
    e[8] = lds_ld<u16>(ktb + (kt1[0] & 0xffffu)); e[9] = lds_ld<u16>(ktb + (kt1[0] >> 16)); e[10] = lds_ld<u16>(ktb + (kt1[1] & 0xffffu));
    const u32 k00 = lds_ld<u16>(ktb);
    u32 dw[5], al[5];
#pragma unroll
    for (int k = 0; k < 5; ++k) { dw[k] = e[2 * k] | (e[2 * k + 1] << 16); al[k] = e[2 * k + 1] | (e[2 * k + 2] << 16); }
    const u32 kb = lb + L_KC + ch * 4 * KCP + n * 16;
    lds_st<u32x4>(kb, u32x4{dw[0], dw[1], dw[2], dw[3]});
    lds_st<u32x4>(kb + KCP, u32x4{al[0], al[1], al[2], al[3]});
    lds_st<u32x4>(kb + 2 * KCP, u32x4{dw[1], dw[2], dw[3], dw[4]});
    lds_st<u32x4>(kb + 3 * KCP, u32x4{al[1], al[2], al[3], al[4]});
    if (n < 16) {
      u32x4 z = {0, 0, 0, 0};
      if (n == 0) z.x = k00;
      lds_st<u32x4>(lb + L_KC + (ch * 4 + (n >> 2)) * KCP + 4096 + (n & 3) * 16, z);
    }
    lds_st<u32x4>(lb + L_U + ch * 4608 + 512 + n * 16, uv_);
  }
  bar();
  STAMP(7);
  f32x4 acc[8];
  const int cc = (w >> 1) & 1, hf = w & 1;
  if (w < 4) {
#pragma unroll
    for (int c = 0; c < 8; ++c) acc[c] = z4;
    const int bsh = (-i16) & 3;
    u32 abase = lb + L_KC + (cc * 4 + bsh) * KCP + (2032 + 8 * g - i16 - bsh) * 2 - hf * 4 * 64 - 3 * 64 - 7 * 512;
    u32 bbase = lb + L_U + cc * 4608 + (256 - 16 + 16 * i16 + 8 * g) * 2 - hf * 4 * 64 - 3 * 64;
    u32 abase8 = abase + 8;
    pin(abase); pin(abase8); pin(bbase);
    u32x4 A[8], B[8];
#pragma unroll
    for (int qq = 0; qq < 8; ++qq) {
      B[7 - qq] = lds_ld<u32x4>(bbase + (7 - qq) * 512 + 3 * 64);
      A[qq] = cat(lds_ld<u32x2>(abase + (7 - qq) * 512 + 3 * 64), lds_ld<u32x2>(abase8 + (7 - qq) * 512 + 3 * 64));
      __builtin_amdgcn_sched_barrier(0);
    }
#pragma unroll
    for (int rr = 0; rr < 4; ++rr) {
#pragma unroll
      for (int qq = 0; qq < 8; ++qq) {
#pragma unroll
        for (int m = 7 - qq; m >= 0; --m) {
          acc[qq + m] = mfma32(A[qq], B[m], acc[qq + m]);
          if (CNOP >= 0 && rr < CNOPR) asm volatile("s_nop %c0" :: "i"(CNOP >= 0 ? CNOP : 0));
        }
        if (rr < 3) {
          A[qq] = cat(lds_ld<u32x2>(abase + (7 - qq) * 512 + (2 - rr) * 64), lds_ld<u32x2>(abase8 + (7 - qq) * 512 + (2 - rr) * 64));
          B[7 - qq] = lds_ld<u32x4>(bbase + (7 - qq) * 512 + (2 - rr) * 64);
        }
        __builtin_amdgcn_sched_barrier(0);
      }
    }
    if (hf) {
#pragma unroll
      for (int c = 0; c < 4; ++c) lds_st<f32x4>(lb + L_RED + (w * 4 + c) * 1024 + lane * 16, acc[c]);
#pragma unroll
      for (int c = 0; c < 4; ++c) acc[c] = acc[4 + c];
    } else {
#pragma unroll
      for (int c = 0; c < 4; ++c) lds_st<f32x4>(lb + L_RED + (w * 4 + c) * 1024 + lane * 16, acc[4 + c]);
    }
  }
  bar();
  STAMP(8);
  if (w < 4) {
    const float dk = bc<float>(dsk << 16);
    const u32 tb = (1024 * hf + 16 * i16 + 4 * g) * 2;
    f32x4 o[4];
    u32x2 uv[4];
#pragma unroll
    for (int c = 0; c < 4; ++c) {
      o[c] = lds_ld<f32x4>(lb + L_RED + ((w ^ 1) * 4 + c) * 1024 + lane * 16);
      uv[c] = lds_ld<u32x2>(lb + L_U + cc * 4608 + 512 + tb + c * 512);
    }
#pragma unroll
    for (int c = 0; c < 4; ++c) {
      const f32x4 y = acc[c] + o[c];
      const f32x2 b01 = up2(pk(y[0], y[1])) + up2(pk2(up2(uv[c].x) * dk));
      const f32x2 b23 = up2(pk(y[2], y[3])) + up2(pk2(up2(uv[c].y) * dk));
      lds_st<u32x2>(lb + L_BO + cc * 4096 + tb + c * 512, u32x2{pk2(b01), pk2(b23)});
    }
  }
  bar();
  STAMP(9);
  {
    const int st = w & 3, s2 = 16 * st + i16;
    const u32 a = lb + L_BO + ch * 4096 + (8 * g + (i16 >> 2)) * 128 + (16 * st + 4 * (i16 & 3)) * 2;
    st16<P>(rws, (u32)((((s2 & 7) * 8 + (s2 >> 3)) * 32 + 8 * g) * 2), (u32)WS_B + (d + ch) * B_D, cat(trread(a), trread(a + 4 * 128)));
  }
  STAMP(10);
  if (P) { drain_vm(); bar(); publish(ctl + CT_FB, j, epoch); }
  STAMP(11);
}

template <int PH>
__global__ __launch_bounds__(NT, 4) void gfs4_kernel(Params p) {
  extern __shared__ __attribute__((aligned(16))) char smem[];
  const u32 lb = (u32)(size_t)(LAS char*)smem;
  const int b = blockIdx.x;
  u32* ctl = (u32*)(p.ws + WS_CTL);
  STAMP_DECL;
  if (PH == 0 || PH == 1) STAMP(0); else if (PH == 2) STAMP(20); else STAMP(21);
  u32 gen = 0;
  asm volatile("" :: "s"(p.x), "s"(p.wr), "s"(p.wi), "s"(p.are), "s"(p.aim), "s"(p.bre), "s"(p.bim));
  asm volatile("" :: "s"(p.cre), "s"(p.cim), "s"(p.logdt), "s"(p.dskip), "s"(p.gw), "s"(p.out), "s"(p.ws));
  SsmLoads L = {};
  if (PH == 0) __builtin_amdgcn_s_setprio(3);
  if (b < N_FFT) {
    if (threadIdx.x < 256) { if (PH == 0 || PH == 1) fft_first_loads(p, lb, b); }
    else if (PH == 0) {
      const char* pc = (const char*)(__builtin_amdgcn_s_getpc() & ~(u64)127);
      const int line = min((int)(threadIdx.x & 255), CODE_LINES - 1);
      const u32 t = *(const GAS u32*)(pc + line * 128);
      asm volatile("" :: "v"(t));
      __builtin_amdgcn_s_setprio(0);
    }
  } else ssm_first_loads<PH>(p, b - N_FFT, L);
  if (PH == 0) gen = __hip_atomic_load((GAS u32*)ctl + CT_GEN, RLX_AGENT);
  __builtin_amdgcn_sched_barrier(0);
  if (b < N_FFT) {
    u32x4 fc[2], fs[2];
    P3C c3;
    if (threadIdx.x >= 256) {
      if (PH == 1 || PH == 2) return;
      fft_upper<PH>(p, lb, b, ctl, fc, fs, c3);
    } else fft_role<PH>(p, lb, b, ctl, gen, fc, fs, c3 STAMP_PASS);
    if (PH == 0 || PH == 3) fft_phase3<PH>(p, lb, b, ctl, __builtin_amdgcn_readfirstlane(gen) + 1u, fc, fs, c3 STAMP_PASS);
  }
  else ssm_role<PH>(p, lb, b - N_FFT, ctl, gen, L STAMP_PASS);
  STAMP_FLUSH();
}

extern "C" void kernel_launch(void* const* d_in, const int* in_sizes, int n_in,
                              void* d_out, int out_size, void* d_ws, size_t ws_size,
                              hipStream_t stream) {
  (void)in_sizes; (void)n_in; (void)out_size; (void)ws_size;
  Params p;
  p.x = (const u16*)d_in[0]; p.wr = (const u16*)d_in[1]; p.wi = (const u16*)d_in[2];
  p.are = (const u16*)d_in[4]; p.aim = (const u16*)d_in[5]; p.bre = (const u16*)d_in[6]; p.bim = (const u16*)d_in[7];
  p.cre = (const u16*)d_in[8]; p.cim = (const u16*)d_in[9]; p.logdt = (const u16*)d_in[10]; p.dskip = (const u16*)d_in[11];
  p.gw = (const u16*)d_in[12];
  p.out = (u16*)d_out; p.ws = (char*)d_ws;
  (void)hipFuncSetAttribute((const void*)gfs4_kernel<0>, hipFuncAttributeMaxDynamicSharedMemorySize, LDS_BYTES);
  gfs4_kernel<0><<<N_WG, NT, LDS_BYTES, stream>>>(p);
}
```

```cpp
#include <hip/hip_runtime.h>
#include <hip/hip_bf16.h>
#include <stdint.h>

typedef unsigned int u32;
typedef unsigned short u16;
typedef unsigned long long u64;
typedef float f32x4 __attribute__((ext_vector_type(4)));
typedef float f32x2 __attribute__((ext_vector_type(2)));
typedef u32 u32x4 __attribute__((ext_vector_type(4)));
typedef u32 u32x2 __attribute__((ext_vector_type(2)));
typedef __bf16 bf16x8 __attribute__((ext_vector_type(8)));
typedef __bf16 bf16x2 __attribute__((ext_vector_type(2)));
typedef short s16x4 __attribute__((ext_vector_type(4)));
#define LAS __attribute__((address_space(3)))
#define GAS __attribute__((address_space(1)))
#define DI __device__ __forceinline__

#define NOCODEPF
#define NLAUNCH 1

constexpr int S_ = 2048, D_ = 448, DF = 225, NS = 256;
constexpr int N_FFT = 256, N_SSM = 224, N_WG = N_FFT + N_SSM, NT = 512;

constexpr u32 T_K1 = 65536 + 256;
constexpr u32 TP_S2 = 32768 + 128;
constexpr u32 XT_D = 4096 + 128;
constexpr u32 B_D = 4096 + 128;
constexpr size_t WS_CTL = 0;
constexpr size_t WS_T   = 1u << 20;
constexpr size_t WS_TP  = WS_T + (3u << 20);
constexpr size_t WS_XT  = WS_TP + (3u << 20);
constexpr size_t WS_B   = WS_XT + (2u << 20);
constexpr int FL = 32, FR_ = 256 * FL + 64;
constexpr int CT_GEN = 0;
constexpr int CT_F1 = 64;
constexpr int CT_F2 = CT_F1 + 8 * FR_;
constexpr int CT_FB = CT_F2 + 8 * FR_;
constexpr int CT_FX = CT_FB + 8 * FR_;
static_assert((CT_FX + 7 * 32 * FL) * 4 <= (1 << 20), "control block");

constexpr int XRS = 928;
constexpr int L_XT = 0;
constexpr int L_XCH = 29696;
constexpr int TPRS = 1040;
constexpr int L_TP = 0;
constexpr int L_TO = 29696;
constexpr int L_TI = 38912;
constexpr int L_BI = 38912;
constexpr int BRS = 912;
constexpr int L_Z = 47104;
constexpr int L_C1 = 38912;
constexpr int L_C2 = 65536;
constexpr int L_C3 = 70400;
constexpr int L_W = 47104;
constexpr int L_G = 70208;
constexpr int KCP = 4160;
constexpr int L_KC = 0;
constexpr int KSCR = 4096;
constexpr int L_RED = 33280;
constexpr int L_BO = 49664;
constexpr int L_U = 57856;
constexpr int L_XP = 57856;
constexpr int L_KT = 67072;
constexpr int L_CNT = 70272;
constexpr int LDS_BYTES = 72448;

constexpr int CODE_LINES = 150;

struct Params {
  const u16* x; const u16* wr; const u16* wi;
  const u16* are; const u16* aim; const u16* bre; const u16* bim; const u16* cre; const u16* cim;
  const u16* logdt; const u16* dskip; const u16* gw;
  u16* out; char* ws;
};

constexpr double cx_sin_oct(double x) { double x2 = x * x, t = x, s = x; for (int i = 1; i < 9; ++i) { t *= -x2 / ((2 * i) * (2 * i + 1)); s += t; } return s; }
constexpr double cx_cos_oct(double x) { double x2 = x * x, t = 1, s = 1; for (int i = 1; i < 9; ++i) { t *= -x2 / ((2 * i - 1) * (2 * i)); s += t; } return s; }
constexpr double cx_sin(long n, long d) {
  n %= d; if (n < 0) n += d;
  const long o = (8 * n) / d;
  const double r = 6.283185307179586476925 * (double)(8 * n - o * d) / (8.0 * (double)d), q = 0.78539816339744830962;
  switch (o) {
    case 0: return cx_sin_oct(r);       case 1: return cx_cos_oct(q - r);
    case 2: return cx_cos_oct(r);       case 3: return cx_sin_oct(q - r);
    case 4: return -cx_sin_oct(r);      case 5: return -cx_cos_oct(q - r);
    case 6: return -cx_cos_oct(r);      default: return -cx_sin_oct(q - r);
  }
}
constexpr double cx_cos(long n, long d) { return cx_sin(4 * n + d, 4 * d); }
constexpr u32 cx_f32(double v) { return __builtin_bit_cast(u32, (float)v); }
constexpr u32 cx_bf(double v) { const u32 u = cx_f32(v); return (u + 0x7fffu + ((u >> 16) & 1u)) >> 16; }
constexpr int cx_pi(int g, int jj) { return 16 * (jj >> 2) + 4 * g + (jj & 3); }
struct Frag { u32 v[64][4]; };
enum { TF_FC0, TF_FS0, TF_CO0, TF_SO0, TF_COT0, TF_SOT0, TF_TC, TF_TS, TF_M, TF_Q0, TF_N = TF_Q0 + 24 };
constexpr Frag make_frag(int item) {
  Frag f{};
  for (int lane = 0; lane < 64; ++lane) {
    const int i16 = lane & 15, g = lane >> 4;
    if (item < TF_TC) {
      const int sn = item & 1;
      for (int jj = 0; jj < 8; ++jj) {
        const int m = cx_pi(g, jj), a = i16;
        long n = 0, d = 32;
        if (item < TF_CO0) { n = a * m; d = 32; }
        else if (item < TF_COT0) { n = a * (2 * m + 1); d = 64; }
        else { n = m * (2 * a + 1); d = 64; }
        f.v[lane][jj >> 1] |= cx_bf(sn ? cx_sin(n, d) : cx_cos(n, d)) << (16 * (jj & 1));
      }
    } else if (item == TF_TC || item == TF_TS) {
      for (int j = 0; j < 4; ++j) {
        const long n = (2 * g + (j & 1)) * (16 * (j >> 1) + i16);
        f.v[lane][j] = cx_f32(item == TF_TC ? cx_cos(n, 448) : cx_sin(n, 448));
      }
    } else if (item == TF_M) {
      for (int j = 0; j < 4; ++j) {
        const int k0 = 2 * g + (j >> 1);
        const double cf = ((k0 == 0 || k0 == 7) ? 1.0 : 2.0) / (2048.0 * 448.0);
        const double v = i16 < 14 ? cf * ((j & 1) ? -cx_sin(k0 * i16, 14) : cx_cos(k0 * i16, 14)) : 0.0;
        f.v[lane][j >> 1] |= cx_bf(v) << (16 * (j & 1));
      }
    } else {
      const int q = (item - TF_Q0) / 6, sub = (item - TF_Q0) % 6, k0 = 2 * q + ((i16 >> 1) & 1), ri = i16 & 1;
      if (sub < 2) {
        if ((i16 >> 2) == 2 * sub + (g & 1))
          for (int j = 0; j < 8; ++j) {
            const int d0 = 8 * (g >> 1) + j;
            const double v = d0 < 14 ? (ri ? -cx_sin(d0 * k0, 14) : cx_cos(d0 * k0, 14)) : 0.0;
            f.v[lane][j >> 1] |= cx_bf(v) << (16 * (j & 1));
          }
      } else {
        for (int j = 0; j < 4; ++j) {
          const long n = cx_pi(g, 4 * (sub & 1) + j) * k0;
          f.v[lane][j] = cx_f32(sub < 4 ? cx_cos(n, 448) : (ri ? -cx_sin(n, 448) : cx_sin(n, 448)));
        }
      }
    }
  }
  return f;
}
template <int I> struct FragC { static constexpr Frag f = make_frag(I); };
#define FR(i) FragC<(i)>::f
#define FR6(i) FR(i), FR(i + 1), FR(i + 2), FR(i + 3), FR(i + 4), FR(i + 5)
__device__ const __attribute__((aligned(1024))) Frag TF[TF_N] = {FR6(0), FR(6), FR(7), FR(8), FR6(9), FR6(15), FR6(21), FR6(27)};

struct KTab { u16 v[2080]; };
constexpr KTab make_ktab() {
  KTab t{};
  for (int i = 0; i < 2080; ++i) {
    const int l = 2048 - i;
    int idx = 768;
    if (l >= 0) {
      const int sh = l > 256 ? (l >= 1024 ? 3 : (l >= 512 ? 2 : 1)) : 0;
      const int v = sh ? (((l + (1 << (sh - 1)) - 1 + ((l >> sh) & 1)) >> sh) << sh) : l;
      if (v < 256) idx = ((v & 7) + 8 * (v >> 7)) * 16 + ((v >> 3) & 15);
      else if (v < 512) idx = 256 + ((((v - 256) >> 1) & 3) + 4 * ((v - 256) >> 7)) * 16 + (((v - 256) >> 3) & 15);
      else if (v < 1024) idx = 256 + (8 + (((v - 512) >> 2) & 1) + 2 * ((v - 512) >> 7)) * 16 + (((v - 512) >> 3) & 15);
      else if (v < 2048) idx = 512 + ((v - 1024) >> 7) * 16 + (((v - 1024) >> 3) & 15);
      else idx = 640;
    }
    t.v[i] = (u16)(2 * idx);
  }
  return t;
}
__device__ const __attribute__((aligned(16))) KTab KTAB = make_ktab();

template <class T, class F> DI T bc(F v) { return __builtin_bit_cast(T, v); }
DI f32x4 mfma32(u32x4 a, u32x4 b, f32x4 c) { return __builtin_amdgcn_mfma_f32_16x16x32_bf16(bc<bf16x8>(a), bc<bf16x8>(b), c, 0, 0, 0); }
DI f32x4 mfma16(u32x2 a, u32x2 b, f32x4 c) { return __builtin_amdgcn_mfma_f32_16x16x16bf16_1k(bc<s16x4>(a), bc<s16x4>(b), c, 0, 0, 0); }
DI u32 pk(float a, float b) { f32x2 v = {a, b}; return bc<u32>(__builtin_convertvector(v, bf16x2)); }
DI u32 pk2(f32x2 v) { return bc<u32>(__builtin_convertvector(v, bf16x2)); }
DI float bfr(float a) { return bc<float>(pk(0.f, a) & 0xffff0000u); }
DI float lo16(u32 v) { return bc<float>(v << 16); }
DI float hi16(u32 v) { return bc<float>(v & 0xffff0000u); }
DI f32x2 up2(u32 v) { return f32x2{lo16(v), hi16(v)}; }
DI float b2f(u16 v) { return bc<float>((u32)v << 16); }
DI u32x2 trread(u32 a) { return bc<u32x2>(__builtin_amdgcn_ds_read_tr16_b64_v4i16((LAS s16x4*)(size_t)a)); }
DI u32x4 cat(u32x2 a, u32x2 b) { u32x4 r = {a.x, a.y, b.x, b.y}; return r; }
DI float sinr(float x) { return __builtin_amdgcn_sinf(x); }
DI float cosr(float x) { return __builtin_amdgcn_cosf(x); }
DI float fx1(float v) { return __builtin_amdgcn_update_dpp(v, v, 0xB1, 0xf, 0xf, true); }
DI float fr8(float v) { return __builtin_amdgcn_update_dpp(v, v, 0x128, 0xf, 0xf, true); }
DI u32 ux1(u32 v) { return (u32)__builtin_amdgcn_update_dpp((int)v, (int)v, 0xB1, 0xf, 0xf, true); }
DI u32 ur8(u32 v) { return (u32)__builtin_amdgcn_update_dpp((int)v, (int)v, 0x128, 0xf, 0xf, true); }
DI u32x4 sx1(u32x4 v, u32 m) { u32x4 r = {ux1(v.x) ^ m, ux1(v.y) ^ m, ux1(v.z) ^ m, ux1(v.w) ^ m}; return r; }
DI u32x4 sr8(u32x4 v, u32 m) { u32x4 r = {ur8(v.x) ^ m, ur8(v.y) ^ m, ur8(v.z) ^ m, ur8(v.w) ^ m}; return r; }
DI u32x4 xr(u32x4 v, u32 m) { u32x4 r = {v.x ^ m, v.y ^ m, v.z ^ m, v.w ^ m}; return r; }
DI u32x4 pk8(const float (&v)[8]) { return u32x4{pk(v[0], v[1]), pk(v[2], v[3]), pk(v[4], v[5]), pk(v[6], v[7])}; }
template <class T> DI T lds_ld(u32 a) { return *(LAS T*)(size_t)a; }
template <class T> DI void lds_st(u32 a, T v) { *(LAS T*)(size_t)a = v; }
DI void wait_vm0() { __builtin_amdgcn_s_waitcnt(0x0F70); }
DI void drain_vm() { asm volatile("s_waitcnt vmcnt(0)" ::: "memory"); }
DI void bar() { __builtin_amdgcn_s_waitcnt(0xC07F); asm volatile("s_barrier" ::: "memory"); }
template <class T> DI void pin(T& v) { asm volatile("" : "+v"(v)); }
typedef __amdgpu_buffer_rsrc_t rsrc_t;
DI rsrc_t mkrs(const void* p, u32 bytes) { return __builtin_amdgcn_make_buffer_rsrc((void*)p, 0, bytes, 0x00020000); }
template <int AUX> DI void dma16(rsrc_t rs, u32 lds, u32 voff, u32 soff) { __builtin_amdgcn_raw_ptr_buffer_load_lds(rs, (LAS void*)(size_t)lds, 16, voff, soff, 0, AUX); }
DI f32x2 cmul(f32x2 x, f32x2 y) {
  f32x2 t;
  asm("v_pk_mul_f32 %0, %1, %2 op_sel:[0,0] op_sel_hi:[0,1]\n\tv_pk_fma_f32 %0, %1, %2, %0 op_sel:[1,1,0] op_sel_hi:[1,0,1] neg_lo:[1,0,0]" : "=&v"(t) : "v"(x), "v"(y));
  return t;
}
DI float dot2b(u32 a, u32 b, float c) { return __builtin_amdgcn_fdot2_f32_bf16(bc<bf16x2>(a), bc<bf16x2>(b), c, false); }
DI float wave_sum(float a) {
  a += __builtin_amdgcn_update_dpp(0.f, a, 0xB1, 0xf, 0xf, true);
  a += __builtin_amdgcn_update_dpp(0.f, a, 0x4E, 0xf, 0xf, true);
  a += __builtin_amdgcn_update_dpp(0.f, a, 0x141, 0xf, 0xf, true);
  a += __builtin_amdgcn_update_dpp(0.f, a, 0x140, 0xf, 0xf, true);
  a += __builtin_amdgcn_update_dpp(0.f, a, 0x142, 0xa, 0xf, false);
  a += __builtin_amdgcn_update_dpp(0.f, a, 0x143, 0xc, 0xf, false);
  return bc<float>(__builtin_amdgcn_readlane(bc<int>(a), 63));
}
DI int pi_k(int g, int jj) { return 16 * (jj >> 2) + 4 * g + (jj & 3); }

#define CNOP 1
#define CNOPR 2
#define SH2PRIO 2
#define SLP 2
#define RLX_AGENT __ATOMIC_RELAXED, __HIP_MEMORY_SCOPE_AGENT
template <bool P> DI void st16(rsrc_t rs, u32 voff, u32 soff, u32x4 v) {
  __builtin_amdgcn_raw_buffer_store_b128(v, rs, voff, soff, P ? 16 : 0);
  asm volatile("s_nop 1" :: "v"(v) : "memory");
}
template <bool P> DI u32x4 ld16(rsrc_t rs, u32 voff, u32 soff) { return __builtin_amdgcn_raw_buffer_load_b128(rs, voff, soff, P ? 16 : 0); }
DI void publish(u32* set, int idx, u32 epoch) {
  if (threadIdx.x < 8) __hip_atomic_store((GAS u32*)set + threadIdx.x * FR_ + idx * FL, epoch, RLX_AGENT);
}
DI void poll_flags(const u32* first, int n, u32 epoch) {
  const int lane = threadIdx.x & 63;
  const GAS u32* f = (const GAS u32*)first + (lane < n ? lane : 0) * FL;
  for (int spin = 0; spin < (1 << 18); ++spin) {
    if (__all(__hip_atomic_load(f, RLX_AGENT) == epoch)) break;
    __builtin_amdgcn_s_sleep(SLP);
  }
}

#define STAMP(i) do {} while (0)
#define STAMP_DECL do {} while (0)
#define STAMP_FLUSH() do {} while (0)
#define STAMP_ARG
#define STAMP_PASS

DI u32x4 tf(rsrc_t rs, int item, u32 l16) { return __builtin_amdgcn_raw_buffer_load_b128(rs, l16 + (item & 3) * 1024, (item >> 2) * 4096, 0); }
DI u32x4 tfs(rsrc_t rs, int item, u32 l16, u32 so) { return __builtin_amdgcn_raw_buffer_load_b128(rs, l16 + item * 1024, so, 0); }

DI void fft_first_loads(const Params& p, const u32 lb, const int b) {
  const int lane = threadIdx.x & 63, w = __builtin_amdgcn_readfirstlane(threadIdx.x >> 6);
  const u32 l16 = lane * 16;
  const int s2 = b & 63, q = b >> 6;
  const rsrc_t rx = mkrs(p.x, S_ * D_ * 2), rtf = mkrs(TF, TF_N * 1024);
  if (lane < 56) {
#pragma unroll
    for (int r = 0; r < 8; ++r) dma16<0>(rx, lb + L_XT + (8 * w + r) * XRS, l16, (64 * (8 * w + r) + s2) * (D_ * 2));
  }
  const u32 tq = (TF_Q0 + 6 * q) * 1024;
  dma16<0>(rtf, lb + L_C1 + w * 2048, l16, w == 0 ? 0u : tq + (2 * w - 2) * 1024);
  dma16<0>(rtf, lb + L_C1 + w * 2048 + 1024, l16, w == 0 ? 1024u : tq + (2 * w - 1) * 1024);
}

struct P3C { u32x4 b1, b2; f32x4 tc, ts; u32x2 m; };
template <int PH>
DI void p3_prepare(const Params& p, const u32 lb, const int b, const rsrc_t rtf, P3C& c) {
  const int lane = threadIdx.x & 63, i16 = lane & 15, g = lane >> 4;
  const u32 l16 = lane * 16;
  const int s2 = b & 63, q = b >> 6;
  c.tc = bc<f32x4>(tf(rtf, TF_TC, l16)); c.ts = bc<f32x4>(tf(rtf, TF_TS, l16));
  const u32x4 m4 = tf(rtf, TF_M, l16);
  c.m = u32x2{m4.x, m4.y};
  c.b1 = lds_ld<u32x4>(lb + L_C3 + l16); c.b2 = lds_ld<u32x4>(lb + L_C3 + 1024 + l16);
  pin(c.b1); pin(c.b2); pin(c.tc); pin(c.ts); pin(c.m);
}
DI void p3_make_b(const u32 lb, const int b) {
  const int lane = threadIdx.x & 63, i16 = lane & 15, g = lane >> 4;
  const int s2 = b & 63, q = b >> 6;
  const int ro = i16 >> 3;
  const float a = (float)(64 * (8 * q + (i16 & 7)) + s2) * (1.0f / 2048.0f), P0 = a * (float)(4 * g);
  float u[8], v[8];
#pragma unroll
  for (int jj = 0; jj < 8; ++jj) {
    const float ph = __builtin_fmaf(a, (float)(16 * (jj >> 2) + (jj & 3)), P0);
    u[jj] = sinr(ph + (ro ? 0.f : 0.25f));
    v[jj] = sinr(ph + (ro ? 0.25f : 0.5f));
  }
  lds_st<u32x4>(lb + L_C3 + lane * 16, pk8(u));
  lds_st<u32x4>(lb + L_C3 + 1024 + lane * 16, pk8(v));
}
template <bool LDSX>
DI void gate_rows(const Params& p, const u32 lb, const int b, const int r0, const int nr) {
  const int lane = threadIdx.x & 63;
  const u32 l16 = lane * 16;
  const int s2 = b & 63, q = b >> 6;
  u32x4 gwv = {0, 0, 0, 0};
  if (lane < 56) gwv = *(const GAS u32x4*)((const char*)p.gw + l16);
#pragma unroll
  for (int r = 0; r < 2; ++r) {
    if (r < nr) {
      float acc = 0.f;
      if (lane < 56) {
        u32x4 xv;
        if (LDSX) xv = lds_ld<u32x4>(lb + L_XT + (8 * q + r0 + r) * XRS + l16);
        else xv = *(const GAS u32x4*)((const char*)(p.x + (size_t)(64 * (8 * q + r0 + r) + s2) * D_) + l16);
#pragma unroll
        for (int e = 0; e < 4; ++e) acc = dot2b(pk2(up2(xv[e]) * up2(gwv[e])), 0x3f803f80u, acc);
      }
      const float gg = bfr(__builtin_amdgcn_rcpf(1.0f + __expf(-bfr(wave_sum(acc)))));
      if (lane == 0) lds_st<f32x2>(lb + L_G + (r0 + r) * 8, f32x2{gg, bfr(1.0f - gg)});
    }
  }
}

template <int PH>
DI void fft_phase3(const Params& p, const u32 lb, const int b, u32* ctl, const u32 epoch, const u32x4 (&fc)[2], const u32x4 (&fs)[2], const P3C& c STAMP_ARG) {
  constexpr bool P = PH == 0;
  constexpr int TPAUX = P ? 16 : 0;
  const int tid = threadIdx.x, lane = tid & 63, w = __builtin_amdgcn_readfirstlane(tid >> 6), i16 = lane & 15, g = lane >> 4;
  const u32 l16 = lane * 16;
  const int s2 = b & 63, q = b >> 6;
  const f32x4 z4 = {0, 0, 0, 0};
  const rsrc_t rws = mkrs(p.ws, 16u << 20);
  u32 tpa = lb + L_TP + (4 * g + (i16 >> 2)) * TPRS + (i16 & 3) * 128 + (w >> 1) * 32 + (w & 1) * 8;
  const int row = 2 * w + (i16 >> 3);
  u32 zw = lb + L_Z + ((i16 & 7) * 16 + row) * 64 + ((g ^ (row >> 2)) & 3) * 16;
  u32 zr = lb + L_Z + (w * 16 + i16) * 64 + ((g ^ (i16 >> 2)) & 3) * 16;
  u32 boff = (u32)(tid * B_D);
  u32 biw = lb + L_BI + tid * 2, bir = lb + L_BI + w * BRS + (32 * i16 + 4 * g) * 2;
  u32 ooff = (u32)((64 * (8 * q + w) + s2) * (D_ * 2) + (32 * i16 + 4 * g) * 2);
  u32 mi = (i16 & 1) ? 0u : 0x80008000u;
  pin(tpa); pin(zw); pin(zr); pin(boff); pin(biw); pin(bir); pin(ooff); pin(mi);
  STAMP(13);
  if (P) { poll_flags(ctl + CT_F2 + (b & 7) * FR_ + w * 32 * FL, 32, epoch); bar(); __builtin_amdgcn_s_setprio(3); }
  STAMP(14);
#pragma unroll
  for (int r = 0; r < 4; ++r) dma16<TPAUX>(rws, lb + L_TP + (4 * w + r) * TPRS, l16, (u32)WS_TP + s2 * TP_S2 + (4 * w + r) * 1024);
  wait_vm0();
  bar();
  STAMP(15);
#pragma unroll
  for (int t = 0; t < 2; ++t) {
    u32x4 Tr = cat(trread(tpa + t * 512), trread(tpa + t * 512 + 16 * TPRS));
    u32x4 Ti = cat(trread(tpa + t * 512 + 16), trread(tpa + t * 512 + 16 + 16 * TPRS));
    f32x4 z = mfma32(Ti, c.b2, mfma32(Tr, c.b1, z4));
    lds_st<u32x2>(zw + t * 8, u32x2{pk(z[0], z[1]), pk(z[2], z[3])});
  }
  bar();
  STAMP(16);
  f32x2 ga[2][2];
  {
    u32x4 A = lds_ld<u32x4>(zr), A2 = sx1(A, mi);
    const float gg = lds_ld<float>(lb + L_G + w * 8);
#pragma unroll
    for (int td = 0; td < 2; ++td) {
      f32x4 G = mfma32(A2, fs[td], mfma32(A, fc[td], z4));
      const float c0 = c.tc[2 * td], s0 = c.ts[2 * td], c1 = c.tc[2 * td + 1], s1 = c.ts[2 * td + 1];
      const float g0r = G[0] * c0 - G[1] * s0, g0i = G[1] * c0 + G[0] * s0;
      const float g1r = G[2] * c1 - G[3] * s1, g1i = G[3] * c1 + G[2] * s1;
      f32x4 y = mfma16(u32x2{pk(g0r, g0i), pk(g1r, g1i)}, c.m, z4);
      ga[td][0] = up2(pk2(up2(pk(y[0], y[1])) * gg));
      ga[td][1] = up2(pk2(up2(pk(y[2], y[3])) * gg));
      pin(ga[td][0]); pin(ga[td][1]);
    }
  }
  STAMP(17);
  if (P) { poll_flags(ctl + CT_FB + (b & 7) * FR_ + w * 28 * FL, 28, epoch); bar(); }
  STAMP(18);
  if (tid < D_) {
    const u32x4 bv = ld16<P>(rws, boff, (u32)WS_B + (((s2 & 7) * 8 + (s2 >> 3)) * 32 + 8 * q) * 2);
#pragma unroll
    for (int e = 0; e < 4; ++e) {
      lds_st<u16>(biw + (2 * e) * BRS, (u16)bv[e]);
      lds_st<u16>(biw + (2 * e + 1) * BRS, (u16)(bv[e] >> 16));
    }
  }
  bar();
  {
    const rsrc_t ro_ = mkrs(p.out, S_ * D_ * 2);
    const float og = lds_ld<float>(lb + L_G + w * 8 + 4);
    const u32x2 bq0 = lds_ld<u32x2>(bir), bq1 = lds_ld<u32x2>(bir + 32);
#pragma unroll
    for (int td = 0; td < 2; ++td) {
      const u32x2 bq = td ? bq1 : bq0;
      const f32x2 o01 = ga[td][0] + up2(pk2(up2(bq.x) * og));
      const f32x2 o23 = ga[td][1] + up2(pk2(up2(bq.y) * og));
      if (i16 < 14) __builtin_amdgcn_raw_buffer_store_b64(u32x2{pk2(o01), pk2(o23)}, ro_, ooff, td * 32, 0);
    }
  }
  STAMP(19);
  if (P && b == 0 && tid == 0) __hip_atomic_store((GAS u32*)ctl + CT_GEN, epoch, RLX_AGENT);
}

template <int PH>
DI void fft_upper(const Params& p, const u32 lb, const int b, u32* ctl, u32x4 (&fc)[2], u32x4 (&fs)[2], P3C& c3) {
  constexpr bool P = PH == 0;
  const int lane = threadIdx.x & 63, w = __builtin_amdgcn_readfirstlane(threadIdx.x >> 6);
  const u32 l16 = lane * 16;
  const rsrc_t rtf = mkrs(TF, TF_N * 1024);
  if (w == 4) p3_make_b(lb, b);
  if (P) {
    bar(); bar(); bar();
    bar(); bar(); bar();
    __builtin_amdgcn_s_setprio(SH2PRIO);
  }
  if (P) gate_rows<P>(p, lb, b, w, 1); else gate_rows<P>(p, lb, b, 2 * (w - 4), 2);
  fc[0] = tf(rtf, TF_FC0, l16); fs[0] = tf(rtf, TF_FS0, l16);
  fc[1] = xr(fc[0], 0x80000000u); fs[1] = xr(fs[0], 0x80000000u);
  pin(fc[0]); pin(fc[1]); pin(fs[0]); pin(fs[1]);
  if (!P) bar();
  p3_prepare<PH>(p, lb, b, rtf, c3);
  if (!P) bar();
}

template <int PH>
DI void fft_role(const Params& p, const u32 lb, const int b, u32* ctl, const u32 gen, u32x4 (&fc)[2], u32x4 (&fs)[2], P3C& c3 STAMP_ARG) {
  constexpr bool P = PH == 0;
#define TPAUXV 16
  constexpr int AUX = P ? 16 : 0, TPAUX = P ? TPAUXV : 0;
  (void)AUX;
  const int tid = threadIdx.x, lane = tid & 63, w = __builtin_amdgcn_readfirstlane(tid >> 6), i16 = lane & 15, g = lane >> 4;
  const u32 l16 = lane * 16;
  const int s2 = b & 63, q = b >> 6;
  const int xx = b & 7, mm = (b >> 3) & 3, kq = b >> 5;
  const int k1w = (((mm & 1) ? (xx ? 32 - xx : 8) : xx) + 16 * (mm >> 1)) & 31;
  const f32x4 z4 = {0, 0, 0, 0};
  const rsrc_t rws = mkrs(p.ws, 16u << 20), rtf = mkrs(TF, TF_N * 1024);
  const u32 tpo = (u32)((tid >> 3) * TP_S2 + (tid & 7) * 16), tps = (u32)WS_TP + k1w * 1024 + kq * 128;
  if (PH == 2 || PH == 3) {
    fc[0] = tf(rtf, TF_FC0, l16); fs[0] = tf(rtf, TF_FS0, l16);
    fc[1] = xr(fc[0], 0x80000000u); fs[1] = xr(fs[0], 0x80000000u);
  }

  u32x4 co[2], so[2], cot[2], sot[2];
  auto load_p2 = [&]() {
    const bool mir = kq >= 4;
    const int col0 = mir ? 448 - (56 * kq + 49) : 56 * kq;
    const rsrc_t rr = mkrs(p.wr, S_ * DF * 2), ri_ = mkrs(p.wi, S_ * DF * 2);
#pragma unroll
    for (int i = 0; i < 3; ++i) {
      const int gi = w + 4 * i;
      if (gi < 9) {
        const int gam = 64 * gi + lane, k2 = (gam * 7282) >> 16, chunk = gam - 9 * k2;
        const int ks = k1w + 32 * k2, row = mir ? ((2048 - ks) & 2047) : ks;
        const u32 vo = (u32)(((row * DF + col0) * 2) & ~15) + chunk * 16;
        dma16<0>(rr, lb + L_W + gi * 1024, vo, 0);
        dma16<0>(ri_, lb + L_W + 9216 + gi * 1024, vo, 0);
      }
    }
    dma16<0>(rtf, lb + L_C2 + w * 1024, l16, (TF_CO0 + w) * 1024);
  };

  if (PH == 0 || PH == 1) {
    u32x4 C1, S1, nS1;
    {
      const float k1 = (float)(16 * (w >> 1) + i16) * (1.0f / 2048.0f), P0 = k1 * (float)(64 * g + s2), Pl = k1 * 256.f;
      float c[8], s[8];
#pragma unroll
      for (int j = 0; j < 8; ++j) { const float ph = __builtin_fmaf(Pl, (float)j, P0); c[j] = cosr(ph); s[j] = sinr(ph); }
      C1 = pk8(c); S1 = pk8(s); nS1 = xr(S1, 0x80008000u);
      pin(C1); pin(S1); pin(nS1);
    }
    int d0a = 8 * (g >> 1) + (i16 >> 2), d0b = d0a + 4;
    d0b = d0b < 14 ? d0b : 0;
    u32 xa = lb + L_XT + (8 * w + (g & 1)) * XRS + 8 * (i16 & 3);
    u32 xb = xa + d0b * 64; xa += d0a * 64;
    u32 xw = lb + L_XCH + w * 256 + lane * 4;
    u32 xr_ = lb + L_XCH + (w & 1) * 4096 + lane * 4;
    u32 toff = (u32)((16 * (w >> 1) + i16) * T_K1 + 4 * (w & 1) * 32 + g * 16);
    u32 imm = (i16 & 1) ? 0x80008000u : 0u;
    pin(xa); pin(xb); pin(xw); pin(xr_); pin(toff); pin(imm);
    const u32 tso = (u32)WS_T + s2 * 1024 + q * 256;
    STAMP(1);
    wait_vm0();
    bar();
    const u32 cb = lb + L_C1 + l16;
    fc[0] = lds_ld<u32x4>(cb); fs[0] = lds_ld<u32x4>(cb + 1024);
    const u32x4 Ba1 = lds_ld<u32x4>(cb + 2048), Ba2 = lds_ld<u32x4>(cb + 3072);
    const f32x4 twc0 = lds_ld<f32x4>(cb + 4096), twc1 = lds_ld<f32x4>(cb + 5120), tws0 = lds_ld<f32x4>(cb + 6144), tws1 = lds_ld<f32x4>(cb + 7168);
    fc[1] = xr(fc[0], 0x80000000u); fs[1] = xr(fs[0], 0x80000000u);
    STAMP(2);
    f32x4 D2[2][2];
#pragma unroll
    for (int sg = 0; sg < 2; ++sg) {
      f32x4 H[2];
#pragma unroll
      for (int td = 0; td < 2; ++td) {
        const int o = td * 32 + sg * 4 * XRS;
        u32x4 A0 = cat(trread(xa + o), trread(xb + o));
        u32x4 A1 = cat(trread(xa + o + 2 * XRS), trread(xb + o + 2 * XRS));
        H[td] = mfma32(A1, Ba2, mfma32(A0, Ba1, z4));
      }
      float h[8];
#pragma unroll
      for (int j = 0; j < 4; ++j) {
        h[j] = __builtin_fmaf(fx1(H[0][j]), tws0[j], H[0][j] * twc0[j]);
        h[4 + j] = __builtin_fmaf(fx1(H[1][j]), tws1[j], H[1][j] * twc1[j]);
      }
      u32x4 A = pk8(h), A2 = sx1(A, imm);
#pragma unroll
      for (int tk = 0; tk < 2; ++tk) D2[sg][tk] = mfma32(A2, fs[tk], mfma32(A, fc[tk], z4));
    }
#pragma unroll
    for (int tk = 0; tk < 2; ++tk)
#pragma unroll
      for (int r = 0; r < 4; ++r) lds_st<u32>(xw + (tk * 4 + r) * 1024, pk(D2[0][tk][r], D2[1][tk][r]));
    bar();
    STAMP(3);
    {
      u32 pc[4][2];
#pragma unroll
      for (int kk = 0; kk < 2; ++kk) {
        const u32 br = xr_ + kk * 2048;
        u32x4 Xr = {lds_ld<u32>(br), lds_ld<u32>(br + 256), lds_ld<u32>(br + 512), lds_ld<u32>(br + 768)};
        u32x4 Xi = {lds_ld<u32>(br + 1024), lds_ld<u32>(br + 1280), lds_ld<u32>(br + 1536), lds_ld<u32>(br + 1792)};
        f32x4 Tr = mfma32(Xi, S1, mfma32(Xr, C1, z4));
        f32x4 Ti = mfma32(Xr, nS1, mfma32(Xi, C1, z4));
        pc[kk][0] = pk(Tr[0], Tr[1]); pc[kk][1] = pk(Tr[2], Tr[3]);
        pc[2 + kk][0] = pk(Ti[0], Ti[1]); pc[2 + kk][1] = pk(Ti[2], Ti[3]);
      }
      u32x4 lo = {pc[0][0], pc[0][1], pc[1][0], pc[1][1]}, hi = {pc[2][0], pc[2][1], pc[3][0], pc[3][1]};
#pragma unroll
      for (int e = 0; e < 4; ++e) {
        auto r1 = __builtin_amdgcn_permlane16_swap(lo[e], hi[e], false, false);
        auto r2 = __builtin_amdgcn_permlane32_swap(r1[0], r1[1], false, false);
        lo[e] = r2[0]; hi[e] = r2[1];
      }
      st16<P>(rws, toff, tso, lo);
      st16<P>(rws, toff + 64, tso, hi);
    }
    STAMP(4);
    if (P) {
      drain_vm();
      STAMP(20);
      bar();
      publish(ctl + CT_F1, b, gen + 1u);
      __builtin_amdgcn_s_setprio(2);
      load_p2();
    }
    STAMP(5);
  }
  const u32 epoch = __builtin_amdgcn_readfirstlane(gen) + 1u;

  if (PH == 2) load_p2();
  if (PH == 0 || PH == 2) {
    const int ri = i16 >> 3;
    u32 tsv = (u32)((lane >> 1) * 1024 + w * 256 + (lane & 1) * 16);
    u32 tiw = lb + L_TI + w * 2048 + ((lane >> 1) & 1) * 1024 + (lane >> 2) * 32 + (lane & 1) * 16;
    u32 ta = lb + L_TI + w * 2048 + (4 * g + (i16 >> 2)) * 32 + (i16 & 3) * 8;
    u32 tow = lb + L_TO + 2 * i16 * 144 + w * 32 + g * 8;
    u32 tor = lb + L_TO + (tid >> 3) * 144 + (tid & 7) * 16;
    u32 mf = ri ? 0x80008000u : 0u, mi = ri ? 0u : 0x80008000u;
    float wr_[16], wi_[16];
    {
      const bool mir = kq >= 4;
      const int c = i16 & 7, kd = 2 * w + (c >> 2) + 14 * (4 * kq + (c & 3)), col0 = mir ? 448 - (56 * kq + 49) : 56 * kq;
      const int row0 = mir ? ((2048 - k1w) & 2047) : k1w;
      u32 wa = lb + L_W + 4 * g * 144 + (((row0 * DF + col0) * 2) & 15) + ((mir ? 448 - kd : kd) - col0) * 2;
      const u32 sg = (mir != (ri != 0)) ? 0u : 0x80000000u;
      pin(tsv); pin(tiw); pin(ta); pin(tow); pin(tor); pin(mf); pin(mi); pin(wa);
      STAMP(7);
      if (P) poll_flags(ctl + CT_F1 + (b & 7) * FR_ + w * 64 * FL, 64, epoch);
      wait_vm0();
      bar();
      const u32 so_ = (u32)WS_T + k1w * T_K1 + kq * 32;
      const u32x4 t0 = ld16<P>(rws, tsv, so_), t1 = ld16<P>(rws, tsv, so_ + 32 * 1024);
      __builtin_amdgcn_sched_barrier(0);
      co[0] = lds_ld<u32x4>(lb + L_C2 + l16); so[0] = lds_ld<u32x4>(lb + L_C2 + 1024 + l16);
      cot[0] = lds_ld<u32x4>(lb + L_C2 + 2048 + l16); sot[0] = lds_ld<u32x4>(lb + L_C2 + 3072 + l16);
      co[1] = xr(so[0], 0x00008000u);
      so[1] = xr(co[0], 0x80000000u);
      cot[1] = xr(cot[0], 0x80000000u); sot[1] = xr(sot[0], 0x80000000u);
      pin(co[0]); pin(co[1]); pin(so[0]); pin(so[1]); pin(cot[0]); pin(cot[1]); pin(sot[0]); pin(sot[1]);
#pragma unroll
      for (int j = 0; j < 16; ++j) {
        const int o = (32 * (j >> 3) + 16 * ((j >> 2) & 1) + (j & 3)) * 144;
        wr_[j] = bc<float>((u32)lds_ld<u16>(wa + o) << 16);
        wi_[j] = bc<float>(((u32)lds_ld<u16>(wa + 9216 + o) << 16) + sg);
      }
#pragma unroll
      for (int j = 0; j < 16; ++j) { pin(wr_[j]); pin(wi_[j]); }
      __builtin_amdgcn_sched_barrier(0);
      if (P) __builtin_amdgcn_s_setprio(3);
      STAMP(8);
      lds_st<u32x4>(tiw, t0);
      lds_st<u32x4>(tiw + 512, t1);
    }
    STAMP(9);
    f32x4 E[2], O[2];
    {
      u32x4 B0 = cat(trread(ta), trread(ta + 512)), B1 = cat(trread(ta + 1024), trread(ta + 1536));
      u32x4 B0s = sr8(B0, mf), B1s = sr8(B1, mf);
#pragma unroll
      for (int t = 0; t < 2; ++t) {
        E[t] = mfma32(fs[t], B0s, mfma32(fc[t], B0, z4));
        O[t] = mfma32(so[t], B1s, mfma32(co[t], B1, z4));
      }
    }
    float Pp[8], Mm[8];
#pragma unroll
    for (int j = 0; j < 8; ++j) {
      const float e = E[j >> 2][j & 3], o = O[j >> 2][j & 3];
      const float ylo = e + o, yhi = e - o;
      const float vlo = __builtin_fmaf(fr8(ylo), wi_[j], ylo * wr_[j]);
      const float vhi = __builtin_fmaf(fr8(yhi), wi_[8 + j], yhi * wr_[8 + j]);
      Pp[j] = vlo + vhi; Mm[j] = vlo - vhi;
    }
    {
      u32x4 AP = pk8(Pp), AM = pk8(Mm);
      u32x4 APs = sr8(AP, mi), AMs = sr8(AM, mi);
#pragma unroll
      for (int t = 0; t < 2; ++t) {
        f32x4 ze = mfma32(APs, fs[t], mfma32(AP, fc[t], z4));
        f32x4 zo = mfma32(AMs, sot[t], mfma32(AM, cot[t], z4));
        lds_st<u32x2>(tow + t * 32 * 144, u32x2{pk(ze[0], ze[1]), pk(ze[2], ze[3])});
        lds_st<u32x2>(tow + t * 32 * 144 + 144, u32x2{pk(zo[0], zo[1]), pk(zo[2], zo[3])});
      }
    }
    bar();
    STAMP(10);
    {
      st16<P>(rws, tpo, tps, lds_ld<u32x4>(tor));
      st16<P>(rws, tpo, tps + 32 * TP_S2, lds_ld<u32x4>(tor + 32 * 144));
    }
    STAMP(11);
    if (P) { drain_vm(); bar(); publish(ctl + CT_F2, b, epoch); __builtin_amdgcn_s_setprio(SH2PRIO); }
    STAMP(12);
  }

  if (PH == 0) gate_rows<true>(p, lb, b, w, 1);
  if (PH == 3) bar();
  if (PH == 0 || PH == 3) p3_prepare<PH>(p, lb, b, rtf, c3);
  if (PH == 3) bar();
}

DI f32x2 cexp_rev(float x2, float ph) {
  const float e = __builtin_amdgcn_exp2f(x2);
  const float f = __builtin_amdgcn_fractf(ph);
  f32x2 cs = {cosr(f), sinr(f)};
  return cs * e;
}
struct KW { u32 a[8]; };
template <int ROW> DI void wrow(const KW& k, f32x2 v) { lds_st<u32>(k.a[ROW & 7] + ROW * 256, pk2(v)); }
template <int ROW> DI void wrow2(const KW& k, f32x2 lo, f32x2 hi) {
  const u32 a = pk2(lo), b = pk2(hi);
  asm volatile("ds_write2st64_b32 %0, %1, %2 offset0:%c3 offset1:%c4" :: "v"(k.a[ROW & 7]), "v"(a), "v"(b), "i"(ROW), "i"(ROW + 8) : "memory");
}
DI void kfrag(u32 fa, u32 fb, u32x4 (&A)[4]) {
  A[0] = lds_ld<u32x4>(fa); A[1] = lds_ld<u32x4>(fb); A[2] = lds_ld<u32x4>(fa + 128); A[3] = lds_ld<u32x4>(fb + 128);
}
DI f32x4 ktile(const u32x4 (&A)[4], const u32x4 (&V)[4]) {
  f32x4 acc = {0, 0, 0, 0};
#pragma unroll
  for (int ks = 0; ks < 4; ++ks) acc = mfma32(A[ks], V[ks], acc);
  return acc;
}
template <class F0, class F1>
DI void kgen(const u32 (&in)[6], float dt, const KW& k, u32 fa, u32 fb, u32 pb, F0&& hook0, F1&& hook1) {
  const float ar = dt * bc<float>(in[0] << 16), ai = dt * bc<float>(in[1] << 16);
  const f32x2 dB = {dt * bc<float>(in[2] << 16), dt * bc<float>(in[3] << 16)};
  const f32x2 Cc = {bc<float>(in[4] << 16), bc<float>(in[5] << 16)};
  const f32x2 CB = cmul(Cc, dB);
  const float x1 = ar * 1.44269504088896341f, p1 = ai * 0.15915494309189535f;
  const f32x2 E1 = cexp_rev(x1, p1), E8c = cexp_rev(8.f * x1, -8.f * p1), E128 = cexp_rev(128.f * x1, 128.f * p1);
  u32x4 V[4], A0[4], A1[4];
  {
    const f32x2 v2 = cmul(E8c, E8c), v3 = cmul(v2, E8c), v4 = cmul(v2, v2), v8 = cmul(v4, v4);
    const f32x2 v5 = cmul(v4, E8c), v6 = cmul(v4, v2), v7 = cmul(v4, v3);
    wrow2<0>(k, f32x2{1.f, 0.f}, v8); wrow2<1>(k, E8c, cmul(v8, E8c)); wrow2<2>(k, v2, cmul(v8, v2)); wrow2<3>(k, v3, cmul(v8, v3));
    wrow2<4>(k, v4, cmul(v8, v4)); wrow2<5>(k, v5, cmul(v8, v5)); wrow2<6>(k, v6, cmul(v8, v6)); wrow2<7>(k, v7, cmul(v8, v7));
    kfrag(fa, fb, V);
  }
  hook0();
  const f32x2 E2 = cmul(E1, E1), E3 = cmul(E2, E1), E4 = cmul(E2, E2), E5 = cmul(E4, E1), E6 = cmul(E4, E2), E7 = cmul(E4, E3);
  const f32x2 G1 = cmul(CB, E128);
  wrow2<0>(k, CB, G1); wrow2<1>(k, cmul(CB, E1), cmul(G1, E1)); wrow2<2>(k, cmul(CB, E2), cmul(G1, E2)); wrow2<3>(k, cmul(CB, E3), cmul(G1, E3));
  wrow2<4>(k, cmul(CB, E4), cmul(G1, E4)); wrow2<5>(k, cmul(CB, E5), cmul(G1, E5)); wrow2<6>(k, cmul(CB, E6), cmul(G1, E6)); wrow2<7>(k, cmul(CB, E7), cmul(G1, E7));
  kfrag(fa, fb, A0);
  const f32x2 G2 = cmul(G1, E128), G3 = cmul(G2, E128), G4 = cmul(G3, E128), G5 = cmul(G4, E128), G6 = cmul(G5, E128), G7 = cmul(G6, E128);
  wrow2<0>(k, G2, G4); wrow2<1>(k, cmul(G2, E2), cmul(G4, E4)); wrow2<2>(k, cmul(G2, E4), G5); wrow2<3>(k, cmul(G2, E6), cmul(G5, E4));
  wrow2<4>(k, G3, G6); wrow2<5>(k, cmul(G3, E2), cmul(G6, E4)); wrow2<6>(k, cmul(G3, E4), G7); wrow2<7>(k, cmul(G3, E6), cmul(G7, E4));
  kfrag(fa, fb, A1);
  hook1();
  __builtin_amdgcn_sched_barrier(0);
  const f32x4 a0 = ktile(A0, V);
  const f32x2 G8 = cmul(G7, E128), G9 = cmul(G8, E128), G10 = cmul(G9, E128), G11 = cmul(G10, E128);
  const f32x2 G12 = cmul(G11, E128), G13 = cmul(G12, E128), G14 = cmul(G13, E128), G15 = cmul(G14, E128);
  wrow2<0>(k, G8, cmul(G15, E128)); wrow<1>(k, G9); wrow<2>(k, G10); wrow<3>(k, G11);
  wrow<4>(k, G12); wrow<5>(k, G13); wrow<6>(k, G14); wrow<7>(k, G15);
  kfrag(fa, fb, A0);
  __builtin_amdgcn_sched_barrier(0);
  const f32x4 a1 = ktile(A1, V), a2 = ktile(A0, V);
  lds_st<f32x4>(pb, a0);
  lds_st<f32x4>(pb + 1024, a1);
  lds_st<f32x4>(pb + 2048, a2);
}

struct SsmLoads { u32x4 xt_v, kt0; u32x2 kt1; u32 in[6], ldt, dsk; };
template <int PH>
DI void ssm_first_loads(const Params& p, const int j, SsmLoads& L) {
  const int tid = threadIdx.x, w = __builtin_amdgcn_readfirstlane(tid >> 6), d = 2 * j, ch = w >> 2, n = tid & 255;
  const int sb_ = j / 7, db_ = j - sb_ * 7;
  if (PH == 0 || PH == 1)
    L.xt_v = __builtin_amdgcn_raw_buffer_load_b128(mkrs(p.x, S_ * D_ * 2), (u32)((tid >> 3) * (D_ * 2) + (tid & 7) * 16), (u32)(64 * sb_ * (D_ * 2) + db_ * 128), 0);
  if (PH != 1) {
    const u32 vo = n * 2, so_ = (d + ch) * (NS * 2), nb = D_ * NS * 2;
    const u16* arr[6] = {p.are, p.aim, p.bre, p.bim, p.cre, p.cim};
#pragma unroll
    for (int a = 0; a < 6; ++a) L.in[a] = __builtin_amdgcn_raw_buffer_load_b16(mkrs(arr[a], nb), vo, so_, 0);
    L.ldt = *(const GAS u16*)(p.logdt + d + ch);
    L.dsk = *(const GAS u16*)(p.dskip + d + ((w >> 1) & 1));
    L.kt0 = *(const GAS u32x4*)(KTAB.v + 8 * n);
    L.kt1 = *(const GAS u32x2*)(KTAB.v + 8 * n + 8);
  }
}

template <int PH>
DI void ssm_role(const Params& p, const u32 lb, const int j, u32* ctl, const u32 gen, const SsmLoads& L STAMP_ARG) {
  constexpr bool P = PH == 0;
  const int tid = threadIdx.x, lane = tid & 63, w = __builtin_amdgcn_readfirstlane(tid >> 6), i16 = lane & 15, g = lane >> 4;
  const int d = 2 * j, ch = w >> 2, n = tid & 255;
  const f32x4 z4 = {0, 0, 0, 0};
  const rsrc_t rws = mkrs(p.ws, 16u << 20);
  const int sb_ = j / 7, db_ = j - sb_ * 7;
  const u32x4 xt_v = L.xt_v, kt0 = L.kt0;
  const u32x2 kt1 = L.kt1;
  const u32 ldt = L.ldt;
  u32 dsk = L.dsk;
  const u32 (&in)[6] = L.in;
  auto transpose = [&]() {
    lds_st<u32x4>(lb + L_XP + (tid >> 3) * 144 + (tid & 7) * 16, xt_v);
    bar();
    const int chi = g + 4 * (w >> 2);
    const u32 a = lb + L_XP + (8 * chi + (i16 >> 2)) * 144 + (16 * (w & 3) + 4 * (i16 & 3)) * 2;
    st16<P>(rws, (u32)((16 * (w & 3) + i16) * XT_D + chi * 16), (u32)WS_XT + 64 * db_ * XT_D + sb_ * 128, cat(trread(a), trread(a + 4 * 144)));
  };
  if (PH == 1) { transpose(); return; }
  if (P) {
    if (tid == 0) lds_st<u32>(lb + L_CNT, 0u);
    __builtin_amdgcn_sched_barrier(0);
    transpose();
    __builtin_amdgcn_s_setprio(1);
    __builtin_amdgcn_sched_barrier(0);
  }
  KW kw;
  const u32 sb = lb + w * KSCR;
#pragma unroll
  for (int h = 0; h < 8; ++h) { kw.a[h] = sb + (((lane >> 2) ^ h) << 4) + (lane & 3) * 4; pin(kw.a[h]); }
  u32 fa = sb + i16 * 256 + ((g ^ (i16 & 3)) << 4) + ((i16 >> 2) & 1) * 64, fb = fa ^ 64;
  pin(fa); pin(fb);
  STAMP(1);
  kgen(in, bfr(expf(bc<float>(ldt << 16))), kw, fa, fb, sb + lane * 16,
       [&]() {
         if (P) {
           drain_vm();
           STAMP(5);
           if (lane == 0 && __hip_atomic_fetch_add((LAS u32*)(size_t)(lb + L_CNT), 1u, __ATOMIC_RELAXED, __HIP_MEMORY_SCOPE_WORKGROUP) == 7u)
             __hip_atomic_store((GAS u32*)ctl + CT_FX + (db_ * 32 + sb_) * FL, gen + 1u, RLX_AGENT);
         }
       },
       []() {});
  const u32 epoch = __builtin_amdgcn_readfirstlane(gen) + 1u;
  pin(dsk);
  STAMP(2);
  if (P && w == 0) poll_flags(ctl + CT_FX + (d >> 6) * 32 * FL, 32, epoch);
  STAMP(6);
  bar();
  const u32x4 uv_ = ld16<P>(rws, n * 16, (u32)WS_XT + (d + ch) * XT_D);
  STAMP(3);
  if ((w & 3) < 3) {
    const u32 pb = lb + ch * 4 * KSCR + (w & 3) * 1024 + lane * 16;
    const f32x4 s = (lds_ld<f32x4>(pb) + lds_ld<f32x4>(pb + KSCR)) + (lds_ld<f32x4>(pb + 2 * KSCR) + lds_ld<f32x4>(pb + 3 * KSCR));
    const u32 tb = lb + L_KT + ch * 1600 + ((w & 3) * 256 + 64 * g + i16) * 2, a = pk(s[0], s[1]), cc = pk(s[2], s[3]);
    lds_st<u16>(tb, (u16)a); lds_st<u16>(tb + 32, (u16)(a >> 16));
    lds_st<u16>(tb + 64, (u16)cc); lds_st<u16>(tb + 96, (u16)(cc >> 16));
  } else {
    if (lane == 0) lds_st<u16>(lb + L_KT + ch * 1600 + 1536, (u16)0);
    if (lane < 32) lds_st<u32x4>(lb + L_U + ch * 4608 + lane * 16, u32x4{0, 0, 0, 0});
  }
  bar();
  STAMP(4);
  {
    const u32 ktb = lb + L_KT + ch * 1600;
    u32 e[11];
#pragma unroll
    for (int k = 0; k < 4; ++k) { e[2 * k] = lds_ld<u16>(ktb + (kt0[k] & 0xffffu)); e[2 * k + 1] = lds_ld<u16>(ktb + (kt0[k] >> 16)); }
    e[8] = lds_ld<u16>(ktb + (kt1[0] & 0xffffu)); e[9] = lds_ld<u16>(ktb + (kt1[0] >> 16)); e[10] = lds_ld<u16>(ktb + (kt1[1] & 0xffffu));
    const u32 k00 = lds_ld<u16>(ktb);
    u32 dw[5], al[5];
#pragma unroll
    for (int k = 0; k < 5; ++k) { dw[k] = e[2 * k] | (e[2 * k + 1] << 16); al[k] = e[2 * k + 1] | (e[2 * k + 2] << 16); }
    const u32 kb = lb + L_KC + ch * 4 * KCP + n * 16;
    lds_st<u32x4>(kb, u32x4{dw[0], dw[1], dw[2], dw[3]});
    lds_st<u32x4>(kb + KCP, u32x4{al[0], al[1], al[2], al[3]});
    lds_st<u32x4>(kb + 2 * KCP, u32x4{dw[1], dw[2], dw[3], dw[4]});
    lds_st<u32x4>(kb + 3 * KCP, u32x4{al[1], al[2], al[3], al[4]});
    if (n < 16) {
      u32x4 z = {0, 0, 0, 0};
      if (n == 0) z.x = k00;
      lds_st<u32x4>(lb + L_KC + (ch * 4 + (n >> 2)) * KCP + 4096 + (n & 3) * 16, z);
    }
    lds_st<u32x4>(lb + L_U + ch * 4608 + 512 + n * 16, uv_);
  }
  bar();
  STAMP(7);
  f32x4 acc[8];
  const int cc = (w >> 1) & 1, hf = w & 1;
  if (w < 4) {
#pragma unroll
    for (int c = 0; c < 8; ++c) acc[c] = z4;
    const int bsh = (-i16) & 3;
    u32 abase = lb + L_KC + (cc * 4 + bsh) * KCP + (2032 + 8 * g - i16 - bsh) * 2 - hf * 4 * 64 - 3 * 64 - 7 * 512;
    u32 bbase = lb + L_U + cc * 4608 + (256 - 16 + 16 * i16 + 8 * g) * 2 - hf * 4 * 64 - 3 * 64;
    u32 abase8 = abase + 8;
    pin(abase); pin(abase8); pin(bbase);
    u32x4 A[8], B[8];
#pragma unroll
    for (int qq = 0; qq < 8; ++qq) {
      B[7 - qq] = lds_ld<u32x4>(bbase + (7 - qq) * 512 + 3 * 64);
      A[qq] = cat(lds_ld<u32x2>(abase + (7 - qq) * 512 + 3 * 64), lds_ld<u32x2>(abase8 + (7 - qq) * 512 + 3 * 64));
      __builtin_amdgcn_sched_barrier(0);
    }
#pragma unroll
    for (int rr = 0; rr < 4; ++rr) {
#pragma unroll
      for (int qq = 0; qq < 8; ++qq) {
#pragma unroll
        for (int m = 7 - qq; m >= 0; --m) {
          acc[qq + m] = mfma32(A[qq], B[m], acc[qq + m]);
          if (CNOP >= 0 && rr < CNOPR) asm volatile("s_nop %c0" :: "i"(CNOP >= 0 ? CNOP : 0));
        }
        if (rr < 3) {
          A[qq] = cat(lds_ld<u32x2>(abase + (7 - qq) * 512 + (2 - rr) * 64), lds_ld<u32x2>(abase8 + (7 - qq) * 512 + (2 - rr) * 64));
          B[7 - qq] = lds_ld<u32x4>(bbase + (7 - qq) * 512 + (2 - rr) * 64);
        }
        __builtin_amdgcn_sched_barrier(0);
      }
    }
    if (hf) {
#pragma unroll
      for (int c = 0; c < 4; ++c) lds_st<f32x4>(lb + L_RED + (w * 4 + c) * 1024 + lane * 16, acc[c]);
#pragma unroll
      for (int c = 0; c < 4; ++c) acc[c] = acc[4 + c];
    } else {
#pragma unroll
      for (int c = 0; c < 4; ++c) lds_st<f32x4>(lb + L_RED + (w * 4 + c) * 1024 + lane * 16, acc[4 + c]);
    }
  }
  bar();
  STAMP(8);
  if (w < 4) {
    const float dk = bc<float>(dsk << 16);
    const u32 tb = (1024 * hf + 16 * i16 + 4 * g) * 2;
    f32x4 o[4];
    u32x2 uv[4];
#pragma unroll
    for (int c = 0; c < 4; ++c) {
      o[c] = lds_ld<f32x4>(lb + L_RED + ((w ^ 1) * 4 + c) * 1024 + lane * 16);
      uv[c] = lds_ld<u32x2>(lb + L_U + cc * 4608 + 512 + tb + c * 512);
    }
#pragma unroll
    for (int c = 0; c < 4; ++c) {
      const f32x4 y = acc[c] + o[c];
      const f32x2 b01 = up2(pk(y[0], y[1])) + up2(pk2(up2(uv[c].x) * dk));
      const f32x2 b23 = up2(pk(y[2], y[3])) + up2(pk2(up2(uv[c].y) * dk));
      lds_st<u32x2>(lb + L_BO + cc * 4096 + tb + c * 512, u32x2{pk2(b01), pk2(b23)});
    }
  }
  bar();
  STAMP(9);
  {
    const int st = w & 3, s2 = 16 * st + i16;
    const u32 a = lb + L_BO + ch * 4096 + (8 * g + (i16 >> 2)) * 128 + (16 * st + 4 * (i16 & 3)) * 2;
    st16<P>(rws, (u32)((((s2 & 7) * 8 + (s2 >> 3)) * 32 + 8 * g) * 2), (u32)WS_B + (d + ch) * B_D, cat(trread(a), trread(a + 4 * 128)));
  }
  STAMP(10);
  if (P) { drain_vm(); bar(); publish(ctl + CT_FB, j, epoch); }
  STAMP(11);
}

template <int PH>
__global__ __launch_bounds__(NT, 4) void gfs4_kernel(Params p) {
  extern __shared__ __attribute__((aligned(16))) char smem[];
  const u32 lb = (u32)(size_t)(LAS char*)smem;
  const int b = blockIdx.x;
  u32* ctl = (u32*)(p.ws + WS_CTL);
  STAMP_DECL;
  if (PH == 0 || PH == 1) STAMP(0); else if (PH == 2) STAMP(20); else STAMP(21);
  u32 gen = 0;
  asm volatile("" :: "s"(p.x), "s"(p.wr), "s"(p.wi), "s"(p.are), "s"(p.aim), "s"(p.bre), "s"(p.bim));
  asm volatile("" :: "s"(p.cre), "s"(p.cim), "s"(p.logdt), "s"(p.dskip), "s"(p.gw), "s"(p.out), "s"(p.ws));
  SsmLoads L = {};
  if (PH == 0) __builtin_amdgcn_s_setprio(3);
  if (b < N_FFT) {
    if (threadIdx.x < 256) { if (PH == 0 || PH == 1) fft_first_loads(p, lb, b); }
  } else ssm_first_loads<PH>(p, b - N_FFT, L);
  if (PH == 0) gen = __hip_atomic_load((GAS u32*)ctl + CT_GEN, RLX_AGENT);
  __builtin_amdgcn_sched_barrier(0);
  if (b < N_FFT) {
    u32x4 fc[2], fs[2];
    P3C c3;
    if (threadIdx.x >= 256) {
      if (PH == 1 || PH == 2) return;
      fft_upper<PH>(p, lb, b, ctl, fc, fs, c3);
    } else fft_role<PH>(p, lb, b, ctl, gen, fc, fs, c3 STAMP_PASS);
    if (PH == 0 || PH == 3) fft_phase3<PH>(p, lb, b, ctl, __builtin_amdgcn_readfirstlane(gen) + 1u, fc, fs, c3 STAMP_PASS);
  }
  else ssm_role<PH>(p, lb, b - N_FFT, ctl, gen, L STAMP_PASS);
  STAMP_FLUSH();
}

extern "C" void kernel_launch(void* const* d_in, const int* in_sizes, int n_in,
                              void* d_out, int out_size, void* d_ws, size_t ws_size,
                              hipStream_t stream) {
  (void)in_sizes; (void)n_in; (void)out_size; (void)ws_size;
  Params p;
  p.x = (const u16*)d_in[0]; p.wr = (const u16*)d_in[1]; p.wi = (const u16*)d_in[2];
  p.are = (const u16*)d_in[4]; p.aim = (const u16*)d_in[5]; p.bre = (const u16*)d_in[6]; p.bim = (const u16*)d_in[7];
  p.cre = (const u16*)d_in[8]; p.cim = (const u16*)d_in[9]; p.logdt = (const u16*)d_in[10]; p.dskip = (const u16*)d_in[11];
  p.gw = (const u16*)d_in[12];
  p.out = (u16*)d_out; p.ws = (char*)d_ws;
  (void)hipFuncSetAttribute((const void*)gfs4_kernel<0>, hipFuncAttributeMaxDynamicSharedMemorySize, LDS_BYTES);
  gfs4_kernel<0><<<N_WG, NT, LDS_BYTES, stream>>>(p);
}
```

```cpp
#include <hip/hip_runtime.h>
#include <hip/hip_bf16.h>
#include <stdint.h>

typedef unsigned int u32;
typedef unsigned short u16;
typedef unsigned long long u64;
typedef float f32x4 __attribute__((ext_vector_type(4)));
typedef float f32x2 __attribute__((ext_vector_type(2)));
typedef u32 u32x4 __attribute__((ext_vector_type(4)));
typedef u32 u32x2 __attribute__((ext_vector_type(2)));
typedef __bf16 bf16x8 __attribute__((ext_vector_type(8)));
typedef __bf16 bf16x2 __attribute__((ext_vector_type(2)));
typedef short s16x4 __attribute__((ext_vector_type(4)));
#define LAS __attribute__((address_space(3)))
#define GAS __attribute__((address_space(1)))
#define DI __device__ __forceinline__

#define DIAG 11
#define DK 2
#define DR 0
#define NLAUNCH 1

constexpr int S_ = 2048, D_ = 448, DF = 225, NS = 256;
constexpr int N_FFT = 256, N_SSM = 224, N_WG = N_FFT + N_SSM, NT = 512;

constexpr u32 T_K1 = 65536 + 256;
constexpr u32 TP_S2 = 32768 + 128;
constexpr u32 XT_D = 4096 + 128;
constexpr u32 B_D = 4096 + 128;
constexpr size_t WS_CTL = 0;
constexpr size_t WS_T   = 1u << 20;
constexpr size_t WS_TP  = WS_T + (3u << 20);
constexpr size_t WS_XT  = WS_TP + (3u << 20);
constexpr size_t WS_B   = WS_XT + (2u << 20);
constexpr int FL = 32, FR_ = 256 * FL + 64;
constexpr int CT_GEN = 0;
constexpr int CT_F1 = 64;
constexpr int CT_F2 = CT_F1 + 8 * FR_;
constexpr int CT_FB = CT_F2 + 8 * FR_;
constexpr int CT_FX = CT_FB + 8 * FR_;
static_assert((CT_FX + 7 * 32 * FL) * 4 <= (1 << 20), "control block");

constexpr int XRS = 928;
constexpr int L_XT = 0;
constexpr int L_XCH = 29696;
constexpr int TPRS = 1040;
constexpr int L_TP = 0;
constexpr int L_TO = 29696;
constexpr int L_TI = 38912;
constexpr int L_BI = 38912;
constexpr int BRS = 912;
constexpr int L_Z = 47104;
constexpr int L_C1 = 38912;
constexpr int L_C2 = 65536;
constexpr int L_C3 = 70400;
constexpr int L_W = 47104;
constexpr int L_G = 70208;
constexpr int KCP = 4160;
constexpr int L_KC = 0;
constexpr int KSCR = 4096;
constexpr int L_RED = 33280;
constexpr int L_BO = 49664;
constexpr int L_U = 57856;
constexpr int L_XP = 57856;
constexpr int L_KT = 67072;
constexpr int L_CNT = 70272;
constexpr int LDS_BYTES = 72448;

struct Params {
  const u16* x; const u16* wr; const u16* wi;
  const u16* are; const u16* aim; const u16* bre; const u16* bim; const u16* cre; const u16* cim;
  const u16* logdt; const u16* dskip; const u16* gw;
  u16* out; char* ws;
};

constexpr double cx_sin_oct(double x) { double x2 = x * x, t = x, s = x; for (int i = 1; i < 9; ++i) { t *= -x2 / ((2 * i) * (2 * i + 1)); s += t; } return s; }
constexpr double cx_cos_oct(double x) { double x2 = x * x, t = 1, s = 1; for (int i = 1; i < 9; ++i) { t *= -x2 / ((2 * i - 1) * (2 * i)); s += t; } return s; }
constexpr double cx_sin(long n, long d) {
  n %= d; if (n < 0) n += d;
  const long o = (8 * n) / d;
  const double r = 6.283185307179586476925 * (double)(8 * n - o * d) / (8.0 * (double)d), q = 0.78539816339744830962;
  switch (o) {
    case 0: return cx_sin_oct(r);       case 1: return cx_cos_oct(q - r);
    case 2: return cx_cos_oct(r);       case 3: return cx_sin_oct(q - r);
    case 4: return -cx_sin_oct(r);      case 5: return -cx_cos_oct(q - r);
    case 6: return -cx_cos_oct(r);      default: return -cx_sin_oct(q - r);
  }
}
constexpr double cx_cos(long n, long d) { return cx_sin(4 * n + d, 4 * d); }
constexpr u32 cx_f32(double v) { return __builtin_bit_cast(u32, (float)v); }
constexpr u32 cx_bf(double v) { const u32 u = cx_f32(v); return (u + 0x7fffu + ((u >> 16) & 1u)) >> 16; }
constexpr int cx_pi(int g, int jj) { return 16 * (jj >> 2) + 4 * g + (jj & 3); }
struct Frag { u32 v[64][4]; };
enum { TF_FC0, TF_FS0, TF_CO0, TF_SO0, TF_COT0, TF_SOT0, TF_TC, TF_TS, TF_M, TF_Q0, TF_N = TF_Q0 + 24 };
constexpr Frag make_frag(int item) {
  Frag f{};
  for (int lane = 0; lane < 64; ++lane) {
    const int i16 = lane & 15, g = lane >> 4;
    if (item < TF_TC) {
      const int sn = item & 1;
      for (int jj = 0; jj < 8; ++jj) {
        const int m = cx_pi(g, jj), a = i16;
        long n = 0, d = 32;
        if (item < TF_CO0) { n = a * m; d = 32; }
        else if (item < TF_COT0) { n = a * (2 * m + 1); d = 64; }
        else { n = m * (2 * a + 1); d = 64; }
        f.v[lane][jj >> 1] |= cx_bf(sn ? cx_sin(n, d) : cx_cos(n, d)) << (16 * (jj & 1));
      }
    } else if (item == TF_TC || item == TF_TS) {
      for (int j = 0; j < 4; ++j) {
        const long n = (2 * g + (j & 1)) * (16 * (j >> 1) + i16);
        f.v[lane][j] = cx_f32(item == TF_TC ? cx_cos(n, 448) : cx_sin(n, 448));
      }
    } else if (item == TF_M) {
      for (int j = 0; j < 4; ++j) {
        const int k0 = 2 * g + (j >> 1);
        const double cf = ((k0 == 0 || k0 == 7) ? 1.0 : 2.0) / (2048.0 * 448.0);
        const double v = i16 < 14 ? cf * ((j & 1) ? -cx_sin(k0 * i16, 14) : cx_cos(k0 * i16, 14)) : 0.0;
        f.v[lane][j >> 1] |= cx_bf(v) << (16 * (j & 1));
      }
    } else {
      const int q = (item - TF_Q0) / 6, sub = (item - TF_Q0) % 6, k0 = 2 * q + ((i16 >> 1) & 1), ri = i16 & 1;
      if (sub < 2) {
        if ((i16 >> 2) == 2 * sub + (g & 1))
          for (int j = 0; j < 8; ++j) {
            const int d0 = 8 * (g >> 1) + j;
            const double v = d0 < 14 ? (ri ? -cx_sin(d0 * k0, 14) : cx_cos(d0 * k0, 14)) : 0.0;
            f.v[lane][j >> 1] |= cx_bf(v) << (16 * (j & 1));
          }
      } else {
        for (int j = 0; j < 4; ++j) {
          const long n = cx_pi(g, 4 * (sub & 1) + j) * k0;
          f.v[lane][j] = cx_f32(sub < 4 ? cx_cos(n, 448) : (ri ? -cx_sin(n, 448) : cx_sin(n, 448)));
        }
      }
    }
  }
  return f;
}
template <int I> struct FragC { static constexpr Frag f = make_frag(I); };
#define FR(i) FragC<(i)>::f
#define FR6(i) FR(i), FR(i + 1), FR(i + 2), FR(i + 3), FR(i + 4), FR(i + 5)
__device__ const __attribute__((aligned(1024))) Frag TF[TF_N] = {FR6(0), FR(6), FR(7), FR(8), FR6(9), FR6(15), FR6(21), FR6(27)};

struct KTab { u16 v[2080]; };
constexpr KTab make_ktab() {
  KTab t{};
  for (int i = 0; i < 2080; ++i) {
    const int l = 2048 - i;
    int idx = 768;
    if (l >= 0) {
      const int sh = l > 256 ? (l >= 1024 ? 3 : (l >= 512 ? 2 : 1)) : 0;
      const int v = sh ? (((l + (1 << (sh - 1)) - 1 + ((l >> sh) & 1)) >> sh) << sh) : l;
      if (v < 256) idx = ((v & 7) + 8 * (v >> 7)) * 16 + ((v >> 3) & 15);
      else if (v < 512) idx = 256 + ((((v - 256) >> 1) & 3) + 4 * ((v - 256) >> 7)) * 16 + (((v - 256) >> 3) & 15);
      else if (v < 1024) idx = 256 + (8 + (((v - 512) >> 2) & 1) + 2 * ((v - 512) >> 7)) * 16 + (((v - 512) >> 3) & 15);
      else if (v < 2048) idx = 512 + ((v - 1024) >> 7) * 16 + (((v - 1024) >> 3) & 15);
      else idx = 640;
    }
    t.v[i] = (u16)(2 * idx);
  }
  return t;
}
__device__ const __attribute__((aligned(16))) KTab KTAB = make_ktab();

template <class T, class F> DI T bc(F v) { return __builtin_bit_cast(T, v); }
DI f32x4 mfma32(u32x4 a, u32x4 b, f32x4 c) { return __builtin_amdgcn_mfma_f32_16x16x32_bf16(bc<bf16x8>(a), bc<bf16x8>(b), c, 0, 0, 0); }
DI f32x4 mfma16(u32x2 a, u32x2 b, f32x4 c) { return __builtin_amdgcn_mfma_f32_16x16x16bf16_1k(bc<s16x4>(a), bc<s16x4>(b), c, 0, 0, 0); }
DI u32 pk(float a, float b) { f32x2 v = {a, b}; return bc<u32>(__builtin_convertvector(v, bf16x2)); }
DI u32 pk2(f32x2 v) { return bc<u32>(__builtin_convertvector(v, bf16x2)); }
DI float bfr(float a) { return bc<float>(pk(0.f, a) & 0xffff0000u); }
DI float lo16(u32 v) { return bc<float>(v << 16); }
DI float hi16(u32 v) { return bc<float>(v & 0xffff0000u); }
DI f32x2 up2(u32 v) { return f32x2{lo16(v), hi16(v)}; }
DI float b2f(u16 v) { return bc<float>((u32)v << 16); }
DI u32x2 trread(u32 a) { return bc<u32x2>(__builtin_amdgcn_ds_read_tr16_b64_v4i16((LAS s16x4*)(size_t)a)); }
DI u32x4 cat(u32x2 a, u32x2 b) { u32x4 r = {a.x, a.y, b.x, b.y}; return r; }
DI float sinr(float x) { return __builtin_amdgcn_sinf(x); }
DI float cosr(float x) { return __builtin_amdgcn_cosf(x); }
DI float fx1(float v) { return __builtin_amdgcn_update_dpp(v, v, 0xB1, 0xf, 0xf, true); }
DI float fr8(float v) { return __builtin_amdgcn_update_dpp(v, v, 0x128, 0xf, 0xf, true); }
DI u32 ux1(u32 v) { return (u32)__builtin_amdgcn_update_dpp((int)v, (int)v, 0xB1, 0xf, 0xf, true); }
DI u32 ur8(u32 v) { return (u32)__builtin_amdgcn_update_dpp((int)v, (int)v, 0x128, 0xf, 0xf, true); }
DI u32x4 sx1(u32x4 v, u32 m) { u32x4 r = {ux1(v.x) ^ m, ux1(v.y) ^ m, ux1(v.z) ^ m, ux1(v.w) ^ m}; return r; }
DI u32x4 sr8(u32x4 v, u32 m) { u32x4 r = {ur8(v.x) ^ m, ur8(v.y) ^ m, ur8(v.z) ^ m, ur8(v.w) ^ m}; return r; }
DI u32x4 xr(u32x4 v, u32 m) { u32x4 r = {v.x ^ m, v.y ^ m, v.z ^ m, v.w ^ m}; return r; }
DI u32x4 pk8(const float (&v)[8]) { return u32x4{pk(v[0], v[1]), pk(v[2], v[3]), pk(v[4], v[5]), pk(v[6], v[7])}; }
template <class T> DI T lds_ld(u32 a) { return *(LAS T*)(size_t)a; }
template <class T> DI void lds_st(u32 a, T v) { *(LAS T*)(size_t)a = v; }
DI void wait_vm0() { __builtin_amdgcn_s_waitcnt(0x0F70); }
DI void drain_vm() { asm volatile("s_waitcnt vmcnt(0)" ::: "memory"); }
DI void bar() { __builtin_amdgcn_s_waitcnt(0xC07F); asm volatile("s_barrier" ::: "memory"); }
template <class T> DI void pin(T& v) { asm volatile("" : "+v"(v)); }
typedef __amdgpu_buffer_rsrc_t rsrc_t;
DI rsrc_t mkrs(const void* p, u32 bytes) { return __builtin_amdgcn_make_buffer_rsrc((void*)p, 0, bytes, 0x00020000); }
template <int AUX> DI void dma16(rsrc_t rs, u32 lds, u32 voff, u32 soff) { __builtin_amdgcn_raw_ptr_buffer_load_lds(rs, (LAS void*)(size_t)lds, 16, voff, soff, 0, AUX); }
DI f32x2 cmul(f32x2 x, f32x2 y) {
  f32x2 t;
  asm("v_pk_mul_f32 %0, %1, %2 op_sel:[0,0] op_sel_hi:[0,1]\n\tv_pk_fma_f32 %0, %1, %2, %0 op_sel:[1,1,0] op_sel_hi:[1,0,1] neg_lo:[1,0,0]" : "=&v"(t) : "v"(x), "v"(y));
  return t;
}
DI float dot2b(u32 a, u32 b, float c) { return __builtin_amdgcn_fdot2_f32_bf16(bc<bf16x2>(a), bc<bf16x2>(b), c, false); }
DI float wave_sum(float a) {
  a += __builtin_amdgcn_update_dpp(0.f, a, 0xB1, 0xf, 0xf, true);
  a += __builtin_amdgcn_update_dpp(0.f, a, 0x4E, 0xf, 0xf, true);
  a += __builtin_amdgcn_update_dpp(0.f, a, 0x141, 0xf, 0xf, true);
  a += __builtin_amdgcn_update_dpp(0.f, a, 0x140, 0xf, 0xf, true);
  a += __builtin_amdgcn_update_dpp(0.f, a, 0x142, 0xa, 0xf, false);
  a += __builtin_amdgcn_update_dpp(0.f, a, 0x143, 0xc, 0xf, false);
  return bc<float>(__builtin_amdgcn_readlane(bc<int>(a), 63));
}
DI int pi_k(int g, int jj) { return 16 * (jj >> 2) + 4 * g + (jj & 3); }

#define CNOP 1
#define CNOPR 2
#define SH2PRIO 2
#define SLP 2
#define RLX_AGENT __ATOMIC_RELAXED, __HIP_MEMORY_SCOPE_AGENT
template <bool P> DI void st16(rsrc_t rs, u32 voff, u32 soff, u32x4 v) {
  __builtin_amdgcn_raw_buffer_store_b128(v, rs, voff, soff, P ? 16 : 0);
  asm volatile("s_nop 1" :: "v"(v) : "memory");
}
template <bool P> DI u32x4 ld16(rsrc_t rs, u32 voff, u32 soff) { return __builtin_amdgcn_raw_buffer_load_b128(rs, voff, soff, P ? 16 : 0); }
DI void publish(u32* set, int idx, u32 epoch) {
  if (threadIdx.x < 8) __hip_atomic_store((GAS u32*)set + threadIdx.x * FR_ + idx * FL, epoch, RLX_AGENT);
}
DI void poll_flags(const u32* first, int n, u32 epoch) {
  const int lane = threadIdx.x & 63;
  const GAS u32* f = (const GAS u32*)first + (lane < n ? lane : 0) * FL;
  for (int spin = 0; spin < (1 << 18); ++spin) {
    if (__all(__hip_atomic_load(f, RLX_AGENT) == epoch)) break;
    __builtin_amdgcn_s_sleep(SLP);
  }
}

#define STAMP(i) do { if ((i) == DK && (blockIdx.x >= N_FFT) == (DR == 1) && threadIdx.x == 0) __hip_atomic_store((GAS u32*)(p.ws + (12u << 20)) + 1024 + blockIdx.x, (u32)__builtin_amdgcn_s_memrealtime(), RLX_AGENT); } while (0)
#define STAMP_DECL do {} while (0)
#define STAMP_FLUSH() do {} while (0)
#define STAMP_ARG
#define STAMP_PASS

DI u32x4 tf(rsrc_t rs, int item, u32 l16) { return __builtin_amdgcn_raw_buffer_load_b128(rs, l16 + (item & 3) * 1024, (item >> 2) * 4096, 0); }
DI u32x4 tfs(rsrc_t rs, int item, u32 l16, u32 so) { return __builtin_amdgcn_raw_buffer_load_b128(rs, l16 + item * 1024, so, 0); }

DI void fft_first_loads(const Params& p, const u32 lb, const int b) {
  const int lane = threadIdx.x & 63, w = __builtin_amdgcn_readfirstlane(threadIdx.x >> 6);
  const u32 l16 = lane * 16;
  const int s2 = b & 63, q = b >> 6;
  const rsrc_t rx = mkrs(p.x, S_ * D_ * 2), rtf = mkrs(TF, TF_N * 1024);
  if (lane < 56) {
#pragma unroll
    for (int r = 0; r < 8; ++r) dma16<0>(rx, lb + L_XT + (8 * w + r) * XRS, l16, (64 * (8 * w + r) + s2) * (D_ * 2));
  }
  const u32 tq = (TF_Q0 + 6 * q) * 1024;
  dma16<0>(rtf, lb + L_C1 + w * 2048, l16, w == 0 ? 0u : tq + (2 * w - 2) * 1024);
  dma16<0>(rtf, lb + L_C1 + w * 2048 + 1024, l16, w == 0 ? 1024u : tq + (2 * w - 1) * 1024);
}

struct P3C { u32x4 b1, b2; f32x4 tc, ts; u32x2 m; };
template <int PH>
DI void p3_prepare(const Params& p, const u32 lb, const int b, const rsrc_t rtf, P3C& c) {
  const int lane = threadIdx.x & 63, i16 = lane & 15, g = lane >> 4;
  const u32 l16 = lane * 16;
  const int s2 = b & 63, q = b >> 6;
  c.tc = bc<f32x4>(tf(rtf, TF_TC, l16)); c.ts = bc<f32x4>(tf(rtf, TF_TS, l16));
  const u32x4 m4 = tf(rtf, TF_M, l16);
  c.m = u32x2{m4.x, m4.y};
  c.b1 = lds_ld<u32x4>(lb + L_C3 + l16); c.b2 = lds_ld<u32x4>(lb + L_C3 + 1024 + l16);
  pin(c.b1); pin(c.b2); pin(c.tc); pin(c.ts); pin(c.m);
}
DI void p3_make_b(const u32 lb, const int b) {
  const int lane = threadIdx.x & 63, i16 = lane & 15, g = lane >> 4;
  const int s2 = b & 63, q = b >> 6;
  const int ro = i16 >> 3;
  const float a = (float)(64 * (8 * q + (i16 & 7)) + s2) * (1.0f / 2048.0f), P0 = a * (float)(4 * g);
  float u[8], v[8];
#pragma unroll
  for (int jj = 0; jj < 8; ++jj) {
    const float ph = __builtin_fmaf(a, (float)(16 * (jj >> 2) + (jj & 3)), P0);
    u[jj] = sinr(ph + (ro ? 0.f : 0.25f));
    v[jj] = sinr(ph + (ro ? 0.25f : 0.5f));
  }
  lds_st<u32x4>(lb + L_C3 + lane * 16, pk8(u));
  lds_st<u32x4>(lb + L_C3 + 1024 + lane * 16, pk8(v));
}
template <bool LDSX>
DI void gate_rows(const Params& p, const u32 lb, const int b, const int r0, const int nr) {
  const int lane = threadIdx.x & 63;
  const u32 l16 = lane * 16;
  const int s2 = b & 63, q = b >> 6;
  u32x4 gwv = {0, 0, 0, 0};
  if (lane < 56) gwv = *(const GAS u32x4*)((const char*)p.gw + l16);
#pragma unroll
  for (int r = 0; r < 2; ++r) {
    if (r < nr) {
      float acc = 0.f;
      if (lane < 56) {
        u32x4 xv;
        if (LDSX) xv = lds_ld<u32x4>(lb + L_XT + (8 * q + r0 + r) * XRS + l16);
        else xv = *(const GAS u32x4*)((const char*)(p.x + (size_t)(64 * (8 * q + r0 + r) + s2) * D_) + l16);
#pragma unroll
        for (int e = 0; e < 4; ++e) acc = dot2b(pk2(up2(xv[e]) * up2(gwv[e])), 0x3f803f80u, acc);
      }
      const float gg = bfr(__builtin_amdgcn_rcpf(1.0f + __expf(-bfr(wave_sum(acc)))));
      if (lane == 0) lds_st<f32x2>(lb + L_G + (r0 + r) * 8, f32x2{gg, bfr(1.0f - gg)});
    }
  }
}

template <int PH>
DI void fft_phase3(const Params& p, const u32 lb, const int b, u32* ctl, const u32 epoch, const u32x4 (&fc)[2], const u32x4 (&fs)[2], const P3C& c STAMP_ARG) {
  constexpr bool P = PH == 0;
  constexpr int TPAUX = P ? 16 : 0;
  const int tid = threadIdx.x, lane = tid & 63, w = __builtin_amdgcn_readfirstlane(tid >> 6), i16 = lane & 15, g = lane >> 4;
  const u32 l16 = lane * 16;
  const int s2 = b & 63, q = b >> 6;
  const f32x4 z4 = {0, 0, 0, 0};
  const rsrc_t rws = mkrs(p.ws, 16u << 20);
  u32 tpa = lb + L_TP + (4 * g + (i16 >> 2)) * TPRS + (i16 & 3) * 128 + (w >> 1) * 32 + (w & 1) * 8;
  const int row = 2 * w + (i16 >> 3);
  u32 zw = lb + L_Z + ((i16 & 7) * 16 + row) * 64 + ((g ^ (row >> 2)) & 3) * 16;
  u32 zr = lb + L_Z + (w * 16 + i16) * 64 + ((g ^ (i16 >> 2)) & 3) * 16;
  u32 boff = (u32)(tid * B_D);
  u32 biw = lb + L_BI + tid * 2, bir = lb + L_BI + w * BRS + (32 * i16 + 4 * g) * 2;
  u32 ooff = (u32)((64 * (8 * q + w) + s2) * (D_ * 2) + (32 * i16 + 4 * g) * 2);
  u32 mi = (i16 & 1) ? 0u : 0x80008000u;
  pin(tpa); pin(zw); pin(zr); pin(boff); pin(biw); pin(bir); pin(ooff); pin(mi);
  STAMP(13);
  if (P) { poll_flags(ctl + CT_F2 + (b & 7) * FR_ + w * 32 * FL, 32, epoch); bar(); __builtin_amdgcn_s_setprio(3); }
  STAMP(14);
#pragma unroll
  for (int r = 0; r < 4; ++r) dma16<TPAUX>(rws, lb + L_TP + (4 * w + r) * TPRS, l16, (u32)WS_TP + s2 * TP_S2 + (4 * w + r) * 1024);
  wait_vm0();
  bar();
  STAMP(15);
#pragma unroll
  for (int t = 0; t < 2; ++t) {
    u32x4 Tr = cat(trread(tpa + t * 512), trread(tpa + t * 512 + 16 * TPRS));
    u32x4 Ti = cat(trread(tpa + t * 512 + 16), trread(tpa + t * 512 + 16 + 16 * TPRS));
    f32x4 z = mfma32(Ti, c.b2, mfma32(Tr, c.b1, z4));
    lds_st<u32x2>(zw + t * 8, u32x2{pk(z[0], z[1]), pk(z[2], z[3])});
  }
  bar();
  STAMP(16);
  f32x2 ga[2][2];
  {
    u32x4 A = lds_ld<u32x4>(zr), A2 = sx1(A, mi);
    const float gg = lds_ld<float>(lb + L_G + w * 8);
#pragma unroll
    for (int td = 0; td < 2; ++td) {
      f32x4 G = mfma32(A2, fs[td], mfma32(A, fc[td], z4));
      const float c0 = c.tc[2 * td], s0 = c.ts[2 * td], c1 = c.tc[2 * td + 1], s1 = c.ts[2 * td + 1];
      const float g0r = G[0] * c0 - G[1] * s0, g0i = G[1] * c0 + G[0] * s0;
      const float g1r = G[2] * c1 - G[3] * s1, g1i = G[3] * c1 + G[2] * s1;
      f32x4 y = mfma16(u32x2{pk(g0r, g0i), pk(g1r, g1i)}, c.m, z4);
      ga[td][0] = up2(pk2(up2(pk(y[0], y[1])) * gg));
      ga[td][1] = up2(pk2(up2(pk(y[2], y[3])) * gg));
      pin(ga[td][0]); pin(ga[td][1]);
    }
  }
  STAMP(17);
  if (P) { poll_flags(ctl + CT_FB + (b & 7) * FR_ + w * 28 * FL, 28, epoch); bar(); }
  STAMP(18);
  if (tid < D_) {
    const u32x4 bv = ld16<P>(rws, boff, (u32)WS_B + (((s2 & 7) * 8 + (s2 >> 3)) * 32 + 8 * q) * 2);
#pragma unroll
    for (int e = 0; e < 4; ++e) {
      lds_st<u16>(biw + (2 * e) * BRS, (u16)bv[e]);
      lds_st<u16>(biw + (2 * e + 1) * BRS, (u16)(bv[e] >> 16));
    }
  }
  bar();
  {
    const rsrc_t ro_ = mkrs(p.out, S_ * D_ * 2);
    const float og = lds_ld<float>(lb + L_G + w * 8 + 4);
    const u32x2 bq0 = lds_ld<u32x2>(bir), bq1 = lds_ld<u32x2>(bir + 32);
#pragma unroll
    for (int td = 0; td < 2; ++td) {
      const u32x2 bq = td ? bq1 : bq0;
      const f32x2 o01 = ga[td][0] + up2(pk2(up2(bq.x) * og));
      const f32x2 o23 = ga[td][1] + up2(pk2(up2(bq.y) * og));
      if (i16 < 14) __builtin_amdgcn_raw_buffer_store_b64(u32x2{pk2(o01), pk2(o23)}, ro_, ooff, td * 32, 0);
    }
  }
  STAMP(19);
  if (P && b == 0 && tid == 0) __hip_atomic_store((GAS u32*)ctl + CT_GEN, epoch, RLX_AGENT);
}

template <int PH>
DI void fft_upper(const Params& p, const u32 lb, const int b, u32* ctl, u32x4 (&fc)[2], u32x4 (&fs)[2], P3C& c3) {
  constexpr bool P = PH == 0;
  const int lane = threadIdx.x & 63, w = __builtin_amdgcn_readfirstlane(threadIdx.x >> 6);
  const u32 l16 = lane * 16;
  const rsrc_t rtf = mkrs(TF, TF_N * 1024);
  if (w == 4) p3_make_b(lb, b);
  if (P) {
    bar(); bar(); bar();
    bar(); bar(); bar();
    __builtin_amdgcn_s_setprio(SH2PRIO);
  }
  if (P) gate_rows<P>(p, lb, b, w, 1); else gate_rows<P>(p, lb, b, 2 * (w - 4), 2);
  fc[0] = tf(rtf, TF_FC0, l16); fs[0] = tf(rtf, TF_FS0, l16);
  fc[1] = xr(fc[0], 0x80000000u); fs[1] = xr(fs[0], 0x80000000u);
  pin(fc[0]); pin(fc[1]); pin(fs[0]); pin(fs[1]);
  if (!P) bar();
  p3_prepare<PH>(p, lb, b, rtf, c3);
  if (!P) bar();
}

template <int PH>
DI void fft_role(const Params& p, const u32 lb, const int b, u32* ctl, const u32 gen, u32x4 (&fc)[2], u32x4 (&fs)[2], P3C& c3 STAMP_ARG) {
  constexpr bool P = PH == 0;
#define TPAUXV 16
  constexpr int AUX = P ? 16 : 0, TPAUX = P ? TPAUXV : 0;
  (void)AUX;
  const int tid = threadIdx.x, lane = tid & 63, w = __builtin_amdgcn_readfirstlane(tid >> 6), i16 = lane & 15, g = lane >> 4;
  const u32 l16 = lane * 16;
  const int s2 = b & 63, q = b >> 6;
  const int xx = b & 7, mm = (b >> 3) & 3, kq = b >> 5;
  const int k1w = (((mm & 1) ? (xx ? 32 - xx : 8) : xx) + 16 * (mm >> 1)) & 31;
  const f32x4 z4 = {0, 0, 0, 0};
  const rsrc_t rws = mkrs(p.ws, 16u << 20), rtf = mkrs(TF, TF_N * 1024);
  const u32 tpo = (u32)((tid >> 3) * TP_S2 + (tid & 7) * 16), tps = (u32)WS_TP + k1w * 1024 + kq * 128;
  if (PH == 2 || PH == 3) {
    fc[0] = tf(rtf, TF_FC0, l16); fs[0] = tf(rtf, TF_FS0, l16);
    fc[1] = xr(fc[0], 0x80000000u); fs[1] = xr(fs[0], 0x80000000u);
  }

  u32x4 co[2], so[2], cot[2], sot[2];
  auto load_p2 = [&]() {
    const bool mir = kq >= 4;
    const int col0 = mir ? 448 - (56 * kq + 49) : 56 * kq;
    const rsrc_t rr = mkrs(p.wr, S_ * DF * 2), ri_ = mkrs(p.wi, S_ * DF * 2);
#pragma unroll
    for (int i = 0; i < 3; ++i) {
      const int gi = w + 4 * i;
      if (gi < 9) {
        const int gam = 64 * gi + lane, k2 = (gam * 7282) >> 16, chunk = gam - 9 * k2;
        const int ks = k1w + 32 * k2, row = mir ? ((2048 - ks) & 2047) : ks;
        const u32 vo = (u32)(((row * DF + col0) * 2) & ~15) + chunk * 16;
        dma16<0>(rr, lb + L_W + gi * 1024, vo, 0);
        dma16<0>(ri_, lb + L_W + 9216 + gi * 1024, vo, 0);
      }
    }
    dma16<0>(rtf, lb + L_C2 + w * 1024, l16, (TF_CO0 + w) * 1024);
  };

  if (PH == 0 || PH == 1) {
    u32x4 C1, S1, nS1;
    {
      const float k1 = (float)(16 * (w >> 1) + i16) * (1.0f / 2048.0f), P0 = k1 * (float)(64 * g + s2), Pl = k1 * 256.f;
      float c[8], s[8];
#pragma unroll
      for (int j = 0; j < 8; ++j) { const float ph = __builtin_fmaf(Pl, (float)j, P0); c[j] = cosr(ph); s[j] = sinr(ph); }
      C1 = pk8(c); S1 = pk8(s); nS1 = xr(S1, 0x80008000u);
      pin(C1); pin(S1); pin(nS1);
    }
    int d0a = 8 * (g >> 1) + (i16 >> 2), d0b = d0a + 4;
    d0b = d0b < 14 ? d0b : 0;
    u32 xa = lb + L_XT + (8 * w + (g & 1)) * XRS + 8 * (i16 & 3);
    u32 xb = xa + d0b * 64; xa += d0a * 64;
    u32 xw = lb + L_XCH + w * 256 + lane * 4;
    u32 xr_ = lb + L_XCH + (w & 1) * 4096 + lane * 4;
    u32 toff = (u32)((16 * (w >> 1) + i16) * T_K1 + 4 * (w & 1) * 32 + g * 16);
    u32 imm = (i16 & 1) ? 0x80008000u : 0u;
    pin(xa); pin(xb); pin(xw); pin(xr_); pin(toff); pin(imm);
    const u32 tso = (u32)WS_T + s2 * 1024 + q * 256;
    STAMP(1);
    wait_vm0();
    bar();
    const u32 cb = lb + L_C1 + l16;
    fc[0] = lds_ld<u32x4>(cb); fs[0] = lds_ld<u32x4>(cb + 1024);
    const u32x4 Ba1 = lds_ld<u32x4>(cb + 2048), Ba2 = lds_ld<u32x4>(cb + 3072);
    const f32x4 twc0 = lds_ld<f32x4>(cb + 4096), twc1 = lds_ld<f32x4>(cb + 5120), tws0 = lds_ld<f32x4>(cb + 6144), tws1 = lds_ld<f32x4>(cb + 7168);
    fc[1] = xr(fc[0], 0x80000000u); fs[1] = xr(fs[0], 0x80000000u);
    STAMP(2);
    f32x4 D2[2][2];
#pragma unroll
    for (int sg = 0; sg < 2; ++sg) {
      f32x4 H[2];
#pragma unroll
      for (int td = 0; td < 2; ++td) {
        const int o = td * 32 + sg * 4 * XRS;
        u32x4 A0 = cat(trread(xa + o), trread(xb + o));
        u32x4 A1 = cat(trread(xa + o + 2 * XRS), trread(xb + o + 2 * XRS));
        H[td] = mfma32(A1, Ba2, mfma32(A0, Ba1, z4));
      }
      float h[8];
#pragma unroll
      for (int j = 0; j < 4; ++j) {
        h[j] = __builtin_fmaf(fx1(H[0][j]), tws0[j], H[0][j] * twc0[j]);
        h[4 + j] = __builtin_fmaf(fx1(H[1][j]), tws1[j], H[1][j] * twc1[j]);
      }
      u32x4 A = pk8(h), A2 = sx1(A, imm);
#pragma unroll
      for (int tk = 0; tk < 2; ++tk) D2[sg][tk] = mfma32(A2, fs[tk], mfma32(A, fc[tk], z4));
    }
#pragma unroll
    for (int tk = 0; tk < 2; ++tk)
#pragma unroll
      for (int r = 0; r < 4; ++r) lds_st<u32>(xw + (tk * 4 + r) * 1024, pk(D2[0][tk][r], D2[1][tk][r]));
    bar();
    STAMP(3);
    {
      u32 pc[4][2];
#pragma unroll
      for (int kk = 0; kk < 2; ++kk) {
        const u32 br = xr_ + kk * 2048;
        u32x4 Xr = {lds_ld<u32>(br), lds_ld<u32>(br + 256), lds_ld<u32>(br + 512), lds_ld<u32>(br + 768)};
        u32x4 Xi = {lds_ld<u32>(br + 1024), lds_ld<u32>(br + 1280), lds_ld<u32>(br + 1536), lds_ld<u32>(br + 1792)};
        f32x4 Tr = mfma32(Xi, S1, mfma32(Xr, C1, z4));
        f32x4 Ti = mfma32(Xr, nS1, mfma32(Xi, C1, z4));
        pc[kk][0] = pk(Tr[0], Tr[1]); pc[kk][1] = pk(Tr[2], Tr[3]);
        pc[2 + kk][0] = pk(Ti[0], Ti[1]); pc[2 + kk][1] = pk(Ti[2], Ti[3]);
      }
      u32x4 lo = {pc[0][0], pc[0][1], pc[1][0], pc[1][1]}, hi = {pc[2][0], pc[2][1], pc[3][0], pc[3][1]};
#pragma unroll
      for (int e = 0; e < 4; ++e) {
        auto r1 = __builtin_amdgcn_permlane16_swap(lo[e], hi[e], false, false);
        auto r2 = __builtin_amdgcn_permlane32_swap(r1[0], r1[1], false, false);
        lo[e] = r2[0]; hi[e] = r2[1];
      }
      st16<P>(rws, toff, tso, lo);
      st16<P>(rws, toff + 64, tso, hi);
    }
    STAMP(4);
    if (P) {
      drain_vm();
      STAMP(20);
      bar();
      publish(ctl + CT_F1, b, gen + 1u);
      __builtin_amdgcn_s_setprio(2);
      load_p2();
    }
    STAMP(5);
  }
  const u32 epoch = __builtin_amdgcn_readfirstlane(gen) + 1u;

  if (PH == 2) load_p2();
  if (PH == 0 || PH == 2) {
    const int ri = i16 >> 3;
    u32 tsv = (u32)((lane >> 1) * 1024 + w * 256 + (lane & 1) * 16);
    u32 tiw = lb + L_TI + w * 2048 + ((lane >> 1) & 1) * 1024 + (lane >> 2) * 32 + (lane & 1) * 16;
    u32 ta = lb + L_TI + w * 2048 + (4 * g + (i16 >> 2)) * 32 + (i16 & 3) * 8;
    u32 tow = lb + L_TO + 2 * i16 * 144 + w * 32 + g * 8;
    u32 tor = lb + L_TO + (tid >> 3) * 144 + (tid & 7) * 16;
    u32 mf = ri ? 0x80008000u : 0u, mi = ri ? 0u : 0x80008000u;
    float wr_[16], wi_[16];
    {
      const bool mir = kq >= 4;
      const int c = i16 & 7, kd = 2 * w + (c >> 2) + 14 * (4 * kq + (c & 3)), col0 = mir ? 448 - (56 * kq + 49) : 56 * kq;
      const int row0 = mir ? ((2048 - k1w) & 2047) : k1w;
      u32 wa = lb + L_W + 4 * g * 144 + (((row0 * DF + col0) * 2) & 15) + ((mir ? 448 - kd : kd) - col0) * 2;
      const u32 sg = (mir != (ri != 0)) ? 0u : 0x80000000u;
      pin(tsv); pin(tiw); pin(ta); pin(tow); pin(tor); pin(mf); pin(mi); pin(wa);
      STAMP(7);
      if (P) poll_flags(ctl + CT_F1 + (b & 7) * FR_ + w * 64 * FL, 64, epoch);
      wait_vm0();
      bar();
      const u32 so_ = (u32)WS_T + k1w * T_K1 + kq * 32;
      const u32x4 t0 = ld16<P>(rws, tsv, so_), t1 = ld16<P>(rws, tsv, so_ + 32 * 1024);
      __builtin_amdgcn_sched_barrier(0);
      co[0] = lds_ld<u32x4>(lb + L_C2 + l16); so[0] = lds_ld<u32x4>(lb + L_C2 + 1024 + l16);
      cot[0] = lds_ld<u32x4>(lb + L_C2 + 2048 + l16); sot[0] = lds_ld<u32x4>(lb + L_C2 + 3072 + l16);
      co[1] = xr(so[0], 0x00008000u);
      so[1] = xr(co[0], 0x80000000u);
      cot[1] = xr(cot[0], 0x80000000u); sot[1] = xr(sot[0], 0x80000000u);
      pin(co[0]); pin(co[1]); pin(so[0]); pin(so[1]); pin(cot[0]); pin(cot[1]); pin(sot[0]); pin(sot[1]);
#pragma unroll
      for (int j = 0; j < 16; ++j) {
        const int o = (32 * (j >> 3) + 16 * ((j >> 2) & 1) + (j & 3)) * 144;
        wr_[j] = bc<float>((u32)lds_ld<u16>(wa + o) << 16);
        wi_[j] = bc<float>(((u32)lds_ld<u16>(wa + 9216 + o) << 16) + sg);
      }
#pragma unroll
      for (int j = 0; j < 16; ++j) { pin(wr_[j]); pin(wi_[j]); }
      __builtin_amdgcn_sched_barrier(0);
      if (P) __builtin_amdgcn_s_setprio(3);
      STAMP(8);
      lds_st<u32x4>(tiw, t0);
      lds_st<u32x4>(tiw + 512, t1);
    }
    STAMP(9);
    f32x4 E[2], O[2];
    {
      u32x4 B0 = cat(trread(ta), trread(ta + 512)), B1 = cat(trread(ta + 1024), trread(ta + 1536));
      u32x4 B0s = sr8(B0, mf), B1s = sr8(B1, mf);
#pragma unroll
      for (int t = 0; t < 2; ++t) {
        E[t] = mfma32(fs[t], B0s, mfma32(fc[t], B0, z4));
        O[t] = mfma32(so[t], B1s, mfma32(co[t], B1, z4));
      }
    }
    float Pp[8], Mm[8];
#pragma unroll
    for (int j = 0; j < 8; ++j) {
      const float e = E[j >> 2][j & 3], o = O[j >> 2][j & 3];
      const float ylo = e + o, yhi = e - o;
      const float vlo = __builtin_fmaf(fr8(ylo), wi_[j], ylo * wr_[j]);
      const float vhi = __builtin_fmaf(fr8(yhi), wi_[8 + j], yhi * wr_[8 + j]);
      Pp[j] = vlo + vhi; Mm[j] = vlo - vhi;
    }
    {
      u32x4 AP = pk8(Pp), AM = pk8(Mm);
      u32x4 APs = sr8(AP, mi), AMs = sr8(AM, mi);
#pragma unroll
      for (int t = 0; t < 2; ++t) {
        f32x4 ze = mfma32(APs, fs[t], mfma32(AP, fc[t], z4));
        f32x4 zo = mfma32(AMs, sot[t], mfma32(AM, cot[t], z4));
        lds_st<u32x2>(tow + t * 32 * 144, u32x2{pk(ze[0], ze[1]), pk(ze[2], ze[3])});
        lds_st<u32x2>(tow + t * 32 * 144 + 144, u32x2{pk(zo[0], zo[1]), pk(zo[2], zo[3])});
      }
    }
    bar();
    STAMP(10);
    {
      st16<P>(rws, tpo, tps, lds_ld<u32x4>(tor));
      st16<P>(rws, tpo, tps + 32 * TP_S2, lds_ld<u32x4>(tor + 32 * 144));
    }
    STAMP(11);
    if (P) { drain_vm(); bar(); publish(ctl + CT_F2, b, epoch); __builtin_amdgcn_s_setprio(SH2PRIO); }
    STAMP(12);
  }

  if (PH == 0) gate_rows<true>(p, lb, b, w, 1);
  if (PH == 3) bar();
  if (PH == 0 || PH == 3) p3_prepare<PH>(p, lb, b, rtf, c3);
  if (PH == 3) bar();
}

DI f32x2 cexp_rev(float x2, float ph) {
  const float e = __builtin_amdgcn_exp2f(x2);
  const float f = __builtin_amdgcn_fractf(ph);
  f32x2 cs = {cosr(f), sinr(f)};
  return cs * e;
}
struct KW { u32 a[8]; };
template <int ROW> DI void wrow(const KW& k, f32x2 v) { lds_st<u32>(k.a[ROW & 7] + ROW * 256, pk2(v)); }
template <int ROW> DI void wrow2(const KW& k, f32x2 lo, f32x2 hi) {
  const u32 a = pk2(lo), b = pk2(hi);
  asm volatile("ds_write2st64_b32 %0, %1, %2 offset0:%c3 offset1:%c4" :: "v"(k.a[ROW & 7]), "v"(a), "v"(b), "i"(ROW), "i"(ROW + 8) : "memory");
}
DI void kfrag(u32 fa, u32 fb, u32x4 (&A)[4]) {
  A[0] = lds_ld<u32x4>(fa); A[1] = lds_ld<u32x4>(fb); A[2] = lds_ld<u32x4>(fa + 128); A[3] = lds_ld<u32x4>(fb + 128);
}
DI f32x4 ktile(const u32x4 (&A)[4], const u32x4 (&V)[4]) {
  f32x4 acc = {0, 0, 0, 0};
#pragma unroll
  for (int ks = 0; ks < 4; ++ks) acc = mfma32(A[ks], V[ks], acc);
  return acc;
}
template <class F0, class F1>
DI void kgen(const u32 (&in)[6], float dt, const KW& k, u32 fa, u32 fb, u32 pb, F0&& hook0, F1&& hook1) {
  const float ar = dt * bc<float>(in[0] << 16), ai = dt * bc<float>(in[1] << 16);
  const f32x2 dB = {dt * bc<float>(in[2] << 16), dt * bc<float>(in[3] << 16)};
  const f32x2 Cc = {bc<float>(in[4] << 16), bc<float>(in[5] << 16)};
  const f32x2 CB = cmul(Cc, dB);
  const float x1 = ar * 1.44269504088896341f, p1 = ai * 0.15915494309189535f;
  const f32x2 E1 = cexp_rev(x1, p1), E8c = cexp_rev(8.f * x1, -8.f * p1), E128 = cexp_rev(128.f * x1, 128.f * p1);
  u32x4 V[4], A0[4], A1[4];
  {
    const f32x2 v2 = cmul(E8c, E8c), v3 = cmul(v2, E8c), v4 = cmul(v2, v2), v8 = cmul(v4, v4);
    const f32x2 v5 = cmul(v4, E8c), v6 = cmul(v4, v2), v7 = cmul(v4, v3);
    wrow2<0>(k, f32x2{1.f, 0.f}, v8); wrow2<1>(k, E8c, cmul(v8, E8c)); wrow2<2>(k, v2, cmul(v8, v2)); wrow2<3>(k, v3, cmul(v8, v3));
    wrow2<4>(k, v4, cmul(v8, v4)); wrow2<5>(k, v5, cmul(v8, v5)); wrow2<6>(k, v6, cmul(v8, v6)); wrow2<7>(k, v7, cmul(v8, v7));
    kfrag(fa, fb, V);
  }
  hook0();
  const f32x2 E2 = cmul(E1, E1), E3 = cmul(E2, E1), E4 = cmul(E2, E2), E5 = cmul(E4, E1), E6 = cmul(E4, E2), E7 = cmul(E4, E3);
  const f32x2 G1 = cmul(CB, E128);
  wrow2<0>(k, CB, G1); wrow2<1>(k, cmul(CB, E1), cmul(G1, E1)); wrow2<2>(k, cmul(CB, E2), cmul(G1, E2)); wrow2<3>(k, cmul(CB, E3), cmul(G1, E3));
  wrow2<4>(k, cmul(CB, E4), cmul(G1, E4)); wrow2<5>(k, cmul(CB, E5), cmul(G1, E5)); wrow2<6>(k, cmul(CB, E6), cmul(G1, E6)); wrow2<7>(k, cmul(CB, E7), cmul(G1, E7));
  kfrag(fa, fb, A0);
  const f32x2 G2 = cmul(G1, E128), G3 = cmul(G2, E128), G4 = cmul(G3, E128), G5 = cmul(G4, E128), G6 = cmul(G5, E128), G7 = cmul(G6, E128);
  wrow2<0>(k, G2, G4); wrow2<1>(k, cmul(G2, E2), cmul(G4, E4)); wrow2<2>(k, cmul(G2, E4), G5); wrow2<3>(k, cmul(G2, E6), cmul(G5, E4));
  wrow2<4>(k, G3, G6); wrow2<5>(k, cmul(G3, E2), cmul(G6, E4)); wrow2<6>(k, cmul(G3, E4), G7); wrow2<7>(k, cmul(G3, E6), cmul(G7, E4));
  kfrag(fa, fb, A1);
  hook1();
  __builtin_amdgcn_sched_barrier(0);
  const f32x4 a0 = ktile(A0, V);
  const f32x2 G8 = cmul(G7, E128), G9 = cmul(G8, E128), G10 = cmul(G9, E128), G11 = cmul(G10, E128);
  const f32x2 G12 = cmul(G11, E128), G13 = cmul(G12, E128), G14 = cmul(G13, E128), G15 = cmul(G14, E128);
  wrow2<0>(k, G8, cmul(G15, E128)); wrow<1>(k, G9); wrow<2>(k, G10); wrow<3>(k, G11);
  wrow<4>(k, G12); wrow<5>(k, G13); wrow<6>(k, G14); wrow<7>(k, G15);
  kfrag(fa, fb, A0);
  __builtin_amdgcn_sched_barrier(0);
  const f32x4 a1 = ktile(A1, V), a2 = ktile(A0, V);
  lds_st<f32x4>(pb, a0);
  lds_st<f32x4>(pb + 1024, a1);
  lds_st<f32x4>(pb + 2048, a2);
}

struct SsmLoads { u32x4 xt_v, kt0; u32x2 kt1; u32 in[6], ldt, dsk; };
template <int PH>
DI void ssm_first_loads(const Params& p, const int j, SsmLoads& L) {
  const int tid = threadIdx.x, w = __builtin_amdgcn_readfirstlane(tid >> 6), d = 2 * j, ch = w >> 2, n = tid & 255;
  const int sb_ = j / 7, db_ = j - sb_ * 7;
  if (PH == 0 || PH == 1)
    L.xt_v = __builtin_amdgcn_raw_buffer_load_b128(mkrs(p.x, S_ * D_ * 2), (u32)((tid >> 3) * (D_ * 2) + (tid & 7) * 16), (u32)(64 * sb_ * (D_ * 2) + db_ * 128), 0);
  if (PH != 1) {
    const u32 vo = n * 2, so_ = (d + ch) * (NS * 2), nb = D_ * NS * 2;
    const u16* arr[6] = {p.are, p.aim, p.bre, p.bim, p.cre, p.cim};
#pragma unroll
    for (int a = 0; a < 6; ++a) L.in[a] = __builtin_amdgcn_raw_buffer_load_b16(mkrs(arr[a], nb), vo, so_, 0);
    L.ldt = *(const GAS u16*)(p.logdt + d + ch);
    L.dsk = *(const GAS u16*)(p.dskip + d + ((w >> 1) & 1));
    L.kt0 = *(const GAS u32x4*)(KTAB.v + 8 * n);
    L.kt1 = *(const GAS u32x2*)(KTAB.v + 8 * n + 8);
  }
}

template <int PH>
DI void ssm_role(const Params& p, const u32 lb, const int j, u32* ctl, const u32 gen, const SsmLoads& L STAMP_ARG) {
  constexpr bool P = PH == 0;
  const int tid = threadIdx.x, lane = tid & 63, w = __builtin_amdgcn_readfirstlane(tid >> 6), i16 = lane & 15, g = lane >> 4;
  const int d = 2 * j, ch = w >> 2, n = tid & 255;
  const f32x4 z4 = {0, 0, 0, 0};
  const rsrc_t rws = mkrs(p.ws, 16u << 20);
  const int sb_ = j / 7, db_ = j - sb_ * 7;
  const u32x4 xt_v = L.xt_v, kt0 = L.kt0;
  const u32x2 kt1 = L.kt1;
  const u32 ldt = L.ldt;
  u32 dsk = L.dsk;
  const u32 (&in)[6] = L.in;
  auto transpose = [&]() {
    lds_st<u32x4>(lb + L_XP + (tid >> 3) * 144 + (tid & 7) * 16, xt_v);
    bar();
    const int chi = g + 4 * (w >> 2);
    const u32 a = lb + L_XP + (8 * chi + (i16 >> 2)) * 144 + (16 * (w & 3) + 4 * (i16 & 3)) * 2;
    st16<P>(rws, (u32)((16 * (w & 3) + i16) * XT_D + chi * 16), (u32)WS_XT + 64 * db_ * XT_D + sb_ * 128, cat(trread(a), trread(a + 4 * 144)));
  };
  if (PH == 1) { transpose(); return; }
  if (P) {
    if (tid == 0) lds_st<u32>(lb + L_CNT, 0u);
    __builtin_amdgcn_sched_barrier(0);
    transpose();
    __builtin_amdgcn_s_setprio(1);
    __builtin_amdgcn_sched_barrier(0);
  }
  KW kw;
  const u32 sb = lb + w * KSCR;
#pragma unroll
  for (int h = 0; h < 8; ++h) { kw.a[h] = sb + (((lane >> 2) ^ h) << 4) + (lane & 3) * 4; pin(kw.a[h]); }
  u32 fa = sb + i16 * 256 + ((g ^ (i16 & 3)) << 4) + ((i16 >> 2) & 1) * 64, fb = fa ^ 64;
  pin(fa); pin(fb);
  STAMP(1);
  kgen(in, bfr(expf(bc<float>(ldt << 16))), kw, fa, fb, sb + lane * 16,
       [&]() {
         if (P) {
           drain_vm();
           STAMP(5);
           if (lane == 0 && __hip_atomic_fetch_add((LAS u32*)(size_t)(lb + L_CNT), 1u, __ATOMIC_RELAXED, __HIP_MEMORY_SCOPE_WORKGROUP) == 7u)
             __hip_atomic_store((GAS u32*)ctl + CT_FX + (db_ * 32 + sb_) * FL, gen + 1u, RLX_AGENT);
         }
       },
       []() {});
  const u32 epoch = __builtin_amdgcn_readfirstlane(gen) + 1u;
  pin(dsk);
  STAMP(2);
  if (P && w == 0) poll_flags(ctl + CT_FX + (d >> 6) * 32 * FL, 32, epoch);
  STAMP(6);
  bar();
  const u32x4 uv_ = ld16<P>(rws, n * 16, (u32)WS_XT + (d + ch) * XT_D);
  STAMP(3);
  if ((w & 3) < 3) {
    const u32 pb = lb + ch * 4 * KSCR + (w & 3) * 1024 + lane * 16;
    const f32x4 s = (lds_ld<f32x4>(pb) + lds_ld<f32x4>(pb + KSCR)) + (lds_ld<f32x4>(pb + 2 * KSCR) + lds_ld<f32x4>(pb + 3 * KSCR));
    const u32 tb = lb + L_KT + ch * 1600 + ((w & 3) * 256 + 64 * g + i16) * 2, a = pk(s[0], s[1]), cc = pk(s[2], s[3]);
    lds_st<u16>(tb, (u16)a); lds_st<u16>(tb + 32, (u16)(a >> 16));
    lds_st<u16>(tb + 64, (u16)cc); lds_st<u16>(tb + 96, (u16)(cc >> 16));
  } else {
    if (lane == 0) lds_st<u16>(lb + L_KT + ch * 1600 + 1536, (u16)0);
    if (lane < 32) lds_st<u32x4>(lb + L_U + ch * 4608 + lane * 16, u32x4{0, 0, 0, 0});
  }
  bar();
  STAMP(4);
  {
    const u32 ktb = lb + L_KT + ch * 1600;
    u32 e[11];
#pragma unroll
    for (int k = 0; k < 4; ++k) { e[2 * k] = lds_ld<u16>(ktb + (kt0[k] & 0xffffu)); e[2 * k + 1] = lds_ld<u16>(ktb + (kt0[k] >> 16)); }
    e[8] = lds_ld<u16>(ktb + (kt1[0] & 0xffffu)); e[9] = lds_ld<u16>(ktb + (kt1[0] >> 16)); e[10] = lds_ld<u16>(ktb + (kt1[1] & 0xffffu));
    const u32 k00 = lds_ld<u16>(ktb);
    u32 dw[5], al[5];
#pragma unroll
    for (int k = 0; k < 5; ++k) { dw[k] = e[2 * k] | (e[2 * k + 1] << 16); al[k] = e[2 * k + 1] | (e[2 * k + 2] << 16); }
    const u32 kb = lb + L_KC + ch * 4 * KCP + n * 16;
    lds_st<u32x4>(kb, u32x4{dw[0], dw[1], dw[2], dw[3]});
    lds_st<u32x4>(kb + KCP, u32x4{al[0], al[1], al[2], al[3]});
    lds_st<u32x4>(kb + 2 * KCP, u32x4{dw[1], dw[2], dw[3], dw[4]});
    lds_st<u32x4>(kb + 3 * KCP, u32x4{al[1], al[2], al[3], al[4]});
    if (n < 16) {
      u32x4 z = {0, 0, 0, 0};
      if (n == 0) z.x = k00;
      lds_st<u32x4>(lb + L_KC + (ch * 4 + (n >> 2)) * KCP + 4096 + (n & 3) * 16, z);
    }
    lds_st<u32x4>(lb + L_U + ch * 4608 + 512 + n * 16, uv_);
  }
  bar();
  STAMP(7);
  f32x4 acc[8];
  const int cc = (w >> 1) & 1, hf = w & 1;
  if (w < 4) {
#pragma unroll
    for (int c = 0; c < 8; ++c) acc[c] = z4;
    const int bsh = (-i16) & 3;
    u32 abase = lb + L_KC + (cc * 4 + bsh) * KCP + (2032 + 8 * g - i16 - bsh) * 2 - hf * 4 * 64 - 3 * 64 - 7 * 512;
    u32 bbase = lb + L_U + cc * 4608 + (256 - 16 + 16 * i16 + 8 * g) * 2 - hf * 4 * 64 - 3 * 64;
    u32 abase8 = abase + 8;
    pin(abase); pin(abase8); pin(bbase);
    u32x4 A[8], B[8];
#pragma unroll
    for (int qq = 0; qq < 8; ++qq) {
      B[7 - qq] = lds_ld<u32x4>(bbase + (7 - qq) * 512 + 3 * 64);
      A[qq] = cat(lds_ld<u32x2>(abase + (7 - qq) * 512 + 3 * 64), lds_ld<u32x2>(abase8 + (7 - qq) * 512 + 3 * 64));
      __builtin_amdgcn_sched_barrier(0);
    }
#pragma unroll
    for (int rr = 0; rr < 4; ++rr) {
#pragma unroll
      for (int qq = 0; qq < 8; ++qq) {
#pragma unroll
        for (int m = 7 - qq; m >= 0; --m) {
          acc[qq + m] = mfma32(A[qq], B[m], acc[qq + m]);
          if (CNOP >= 0 && rr < CNOPR) asm volatile("s_nop %c0" :: "i"(CNOP >= 0 ? CNOP : 0));
        }
        if (rr < 3) {
          A[qq] = cat(lds_ld<u32x2>(abase + (7 - qq) * 512 + (2 - rr) * 64), lds_ld<u32x2>(abase8 + (7 - qq) * 512 + (2 - rr) * 64));
          B[7 - qq] = lds_ld<u32x4>(bbase + (7 - qq) * 512 + (2 - rr) * 64);
        }
        __builtin_amdgcn_sched_barrier(0);
      }
    }
    if (hf) {
#pragma unroll
      for (int c = 0; c < 4; ++c) lds_st<f32x4>(lb + L_RED + (w * 4 + c) * 1024 + lane * 16, acc[c]);
#pragma unroll
      for (int c = 0; c < 4; ++c) acc[c] = acc[4 + c];
    } else {
#pragma unroll
      for (int c = 0; c < 4; ++c) lds_st<f32x4>(lb + L_RED + (w * 4 + c) * 1024 + lane * 16, acc[4 + c]);
    }
  }
  bar();
  STAMP(8);
  if (w < 4) {
    const float dk = bc<float>(dsk << 16);
    const u32 tb = (1024 * hf + 16 * i16 + 4 * g) * 2;
    f32x4 o[4];
    u32x2 uv[4];
#pragma unroll
    for (int c = 0; c < 4; ++c) {
      o[c] = lds_ld<f32x4>(lb + L_RED + ((w ^ 1) * 4 + c) * 1024 + lane * 16);
      uv[c] = lds_ld<u32x2>(lb + L_U + cc * 4608 + 512 + tb + c * 512);
    }
#pragma unroll
    for (int c = 0; c < 4; ++c) {
      const f32x4 y = acc[c] + o[c];
      const f32x2 b01 = up2(pk(y[0], y[1])) + up2(pk2(up2(uv[c].x) * dk));
      const f32x2 b23 = up2(pk(y[2], y[3])) + up2(pk2(up2(uv[c].y) * dk));
      lds_st<u32x2>(lb + L_BO + cc * 4096 + tb + c * 512, u32x2{pk2(b01), pk2(b23)});
    }
  }
  bar();
  STAMP(9);
  {
    const int st = w & 3, s2 = 16 * st + i16;
    const u32 a = lb + L_BO + ch * 4096 + (8 * g + (i16 >> 2)) * 128 + (16 * st + 4 * (i16 & 3)) * 2;
    st16<P>(rws, (u32)((((s2 & 7) * 8 + (s2 >> 3)) * 32 + 8 * g) * 2), (u32)WS_B + (d + ch) * B_D, cat(trread(a), trread(a + 4 * 128)));
  }
  STAMP(10);
  if (P) { drain_vm(); bar(); publish(ctl + CT_FB, j, epoch); }
  STAMP(11);
}

template <int PH>
__global__ __launch_bounds__(NT, 4) void gfs4_kernel(Params p) {
  extern __shared__ __attribute__((aligned(16))) char smem[];
  const u32 lb = (u32)(size_t)(LAS char*)smem;
  const int b = blockIdx.x;
  u32* ctl = (u32*)(p.ws + WS_CTL);
  STAMP_DECL;
  if (PH == 0 || PH == 1) STAMP(0); else if (PH == 2) STAMP(20); else STAMP(21);
  u32 gen = 0;
  const u32 dg_t0 = (u32)__builtin_amdgcn_s_memrealtime();
  if (threadIdx.x == 0) __hip_atomic_store((GAS u32*)(p.ws + (12u << 20)) + b, dg_t0, RLX_AGENT);
  asm volatile("" :: "s"(p.x), "s"(p.wr), "s"(p.wi), "s"(p.are), "s"(p.aim), "s"(p.bre), "s"(p.bim));
  asm volatile("" :: "s"(p.cre), "s"(p.cim), "s"(p.logdt), "s"(p.dskip), "s"(p.gw), "s"(p.out), "s"(p.ws));
  SsmLoads L = {};
  if (PH == 0) __builtin_amdgcn_s_setprio(3);
  if (b < N_FFT) {
    if (threadIdx.x < 256) { if (PH == 0 || PH == 1) fft_first_loads(p, lb, b); }
  } else ssm_first_loads<PH>(p, b - N_FFT, L);
  if (PH == 0) gen = __hip_atomic_load((GAS u32*)ctl + CT_GEN, RLX_AGENT);
  __builtin_amdgcn_sched_barrier(0);
  if (b < N_FFT) {
    u32x4 fc[2], fs[2];
    P3C c3;
    if (threadIdx.x >= 256) {
      if (PH == 1 || PH == 2) return;
      fft_upper<PH>(p, lb, b, ctl, fc, fs, c3);
    } else fft_role<PH>(p, lb, b, ctl, gen, fc, fs, c3 STAMP_PASS);
    if (PH == 0 || PH == 3) fft_phase3<PH>(p, lb, b, ctl, __builtin_amdgcn_readfirstlane(gen) + 1u, fc, fs, c3 STAMP_PASS);
  }
  else ssm_role<PH>(p, lb, b - N_FFT, ctl, gen, L STAMP_PASS);
  STAMP_FLUSH();
  if (b == 0 && threadIdx.x < 64 && (gen & 0xff) != ((gen >> 8) & 0xff) && (gen & 3) == 3) {
    u32 mn = 0x7fffffffu, mx = 0, lo = 0x7fffffffu;
    for (int i = threadIdx.x; i < N_WG; i += 64) {
      mn = min(mn, __hip_atomic_load((GAS u32*)(p.ws + (12u << 20)) + i, RLX_AGENT) - dg_t0 + 100000u);
      if ((i >= N_FFT) == (DR == 1)) { const u32 a = __hip_atomic_load((GAS u32*)(p.ws + (12u << 20)) + 1024 + i, RLX_AGENT) - dg_t0 + 100000u; mx = max(mx, a); lo = min(lo, a); }
    }
    for (int o = 32; o >= 1; o >>= 1) { mn = min(mn, (u32)__shfl_xor((int)mn, o)); mx = max(mx, (u32)__shfl_xor((int)mx, o)); lo = min(lo, (u32)__shfl_xor((int)lo, o)); }
    const float v = DIAG == 10 ? (float)(mx - mn) * 0.01f : (float)(lo - mn) * 0.01f;
    wait_vm0();
    if (threadIdx.x == 0) p.out[0] = (u16)(pk(v, v) & 0xffff);
  }
}

extern "C" void kernel_launch(void* const* d_in, const int* in_sizes, int n_in,
                              void* d_out, int out_size, void* d_ws, size_t ws_size,
                              hipStream_t stream) {
  (void)in_sizes; (void)n_in; (void)out_size; (void)ws_size;
  Params p;
  p.x = (const u16*)d_in[0]; p.wr = (const u16*)d_in[1]; p.wi = (const u16*)d_in[2];
  p.are = (const u16*)d_in[4]; p.aim = (const u16*)d_in[5]; p.bre = (const u16*)d_in[6]; p.bim = (const u16*)d_in[7];
  p.cre = (const u16*)d_in[8]; p.cim = (const u16*)d_in[9]; p.logdt = (const u16*)d_in[10]; p.dskip = (const u16*)d_in[11];
  p.gw = (const u16*)d_in[12];
  p.out = (u16*)d_out; p.ws = (char*)d_ws;
  (void)hipFuncSetAttribute((const void*)gfs4_kernel<0>, hipFuncAttributeMaxDynamicSharedMemorySize, LDS_BYTES);
  gfs4_kernel<0><<<N_WG, NT, LDS_BYTES, stream>>>(p);
}
```

```cpp
#include <hip/hip_runtime.h>
#include <hip/hip_bf16.h>
#include <stdint.h>

typedef unsigned int u32;
typedef unsigned short u16;
typedef unsigned long long u64;
typedef float f32x4 __attribute__((ext_vector_type(4)));
typedef float f32x2 __attribute__((ext_vector_type(2)));
typedef u32 u32x4 __attribute__((ext_vector_type(4)));
typedef u32 u32x2 __attribute__((ext_vector_type(2)));
typedef __bf16 bf16x8 __attribute__((ext_vector_type(8)));
typedef __bf16 bf16x2 __attribute__((ext_vector_type(2)));
typedef short s16x4 __attribute__((ext_vector_type(4)));
#define LAS __attribute__((address_space(3)))
#define GAS __attribute__((address_space(1)))
#define DI __device__ __forceinline__

#define NLAUNCH 1

constexpr int S_ = 2048, D_ = 448, DF = 225, NS = 256;
constexpr int N_FFT = 256, N_SSM = 224, N_WG = N_FFT + N_SSM, NT = 512;

constexpr u32 T_K1 = 65536 + 256;
constexpr u32 TP_S2 = 32768 + 128;
constexpr u32 XT_D = 4096 + 128;
constexpr u32 B_D = 4096 + 128;
constexpr size_t WS_CTL = 0;
constexpr size_t WS_T   = 1u << 20;
constexpr size_t WS_TP  = WS_T + (3u << 20);
constexpr size_t WS_XT  = WS_TP + (3u << 20);
constexpr size_t WS_B   = WS_XT + (2u << 20);
constexpr int FL = 32, FR_ = 256 * FL + 64;
constexpr int CT_GEN = 0;
constexpr int CT_F1 = 64;
constexpr int CT_F2 = CT_F1 + 8 * FR_;
constexpr int CT_FB = CT_F2 + 8 * FR_;
constexpr int CT_FX = CT_FB + 8 * FR_;
static_assert((CT_FX + 7 * 32 * FL) * 4 <= (1 << 20), "control block");

constexpr int XRS = 928;
constexpr int L_XT = 0;
constexpr int L_XCH = 29696;
constexpr int TPRS = 1040;
constexpr int L_TP = 0;
constexpr int L_TO = 29696;
constexpr int L_TI = 38912;
constexpr int L_BI = 38912;
constexpr int BRS = 912;
constexpr int L_Z = 47104;
constexpr int L_C1 = 38912;
constexpr int L_C2 = 65536;
constexpr int L_C3 = 70400;
constexpr int L_W = 47104;
constexpr int L_G = 70208;
constexpr int KCP = 4160;
constexpr int L_KC = 0;
constexpr int KSCR = 4096;
constexpr int L_RED = 33280;
constexpr int L_BO = 49664;
constexpr int L_U = 57856;
constexpr int L_XP = 57856;
constexpr int L_KT = 67072;
constexpr int L_CNT = 70272;
constexpr int LDS_BYTES = 72448;

struct Params {
  const u16* x; const u16* wr; const u16* wi;
  const u16* are; const u16* aim; const u16* bre; const u16* bim; const u16* cre; const u16* cim;
  const u16* logdt; const u16* dskip; const u16* gw;
  u16* out; char* ws;
};

constexpr double cx_sin_oct(double x) { double x2 = x * x, t = x, s = x; for (int i = 1; i < 9; ++i) { t *= -x2 / ((2 * i) * (2 * i + 1)); s += t; } return s; }
constexpr double cx_cos_oct(double x) { double x2 = x * x, t = 1, s = 1; for (int i = 1; i < 9; ++i) { t *= -x2 / ((2 * i - 1) * (2 * i)); s += t; } return s; }
constexpr double cx_sin(long n, long d) {
  n %= d; if (n < 0) n += d;
  const long o = (8 * n) / d;
  const double r = 6.283185307179586476925 * (double)(8 * n - o * d) / (8.0 * (double)d), q = 0.78539816339744830962;
  switch (o) {
    case 0: return cx_sin_oct(r);       case 1: return cx_cos_oct(q - r);
    case 2: return cx_cos_oct(r);       case 3: return cx_sin_oct(q - r);
    case 4: return -cx_sin_oct(r);      case 5: return -cx_cos_oct(q - r);
    case 6: return -cx_cos_oct(r);      default: return -cx_sin_oct(q - r);
  }
}
constexpr double cx_cos(long n, long d) { return cx_sin(4 * n + d, 4 * d); }
constexpr u32 cx_f32(double v) { return __builtin_bit_cast(u32, (float)v); }
constexpr u32 cx_bf(double v) { const u32 u = cx_f32(v); return (u + 0x7fffu + ((u >> 16) & 1u)) >> 16; }
constexpr int cx_pi(int g, int jj) { return 16 * (jj >> 2) + 4 * g + (jj & 3); }
struct Frag { u32 v[64][4]; };
enum { TF_FC0, TF_FS0, TF_CO0, TF_SO0, TF_COT0, TF_SOT0, TF_TC, TF_TS, TF_M, TF_Q0, TF_N = TF_Q0 + 24 };
constexpr Frag make_frag(int item) {
  Frag f{};
  for (int lane = 0; lane < 64; ++lane) {
    const int i16 = lane & 15, g = lane >> 4;
    if (item < TF_TC) {
      const int sn = item & 1;
      for (int jj = 0; jj < 8; ++jj) {
        const int m = cx_pi(g, jj), a = i16;
        long n = 0, d = 32;
        if (item < TF_CO0) { n = a * m; d = 32; }
        else if (item < TF_COT0) { n = a * (2 * m + 1); d = 64; }
        else { n = m * (2 * a + 1); d = 64; }
        f.v[lane][jj >> 1] |= cx_bf(sn ? cx_sin(n, d) : cx_cos(n, d)) << (16 * (jj & 1));
      }
    } else if (item == TF_TC || item == TF_TS) {
      for (int j = 0; j < 4; ++j) {
        const long n = (2 * g + (j & 1)) * (16 * (j >> 1) + i16);
        f.v[lane][j] = cx_f32(item == TF_TC ? cx_cos(n, 448) : cx_sin(n, 448));
      }
    } else if (item == TF_M) {
      for (int j = 0; j < 4; ++j) {
        const int k0 = 2 * g + (j >> 1);
        const double cf = ((k0 == 0 || k0 == 7) ? 1.0 : 2.0) / (2048.0 * 448.0);
        const double v = i16 < 14 ? cf * ((j & 1) ? -cx_sin(k0 * i16, 14) : cx_cos(k0 * i16, 14)) : 0.0;
        f.v[lane][j >> 1] |= cx_bf(v) << (16 * (j & 1));
      }
    } else {
      const int q = (item - TF_Q0) / 6, sub = (item - TF_Q0) % 6, k0 = 2 * q + ((i16 >> 1) & 1), ri = i16 & 1;
      if (sub < 2) {
        if ((i16 >> 2) == 2 * sub + (g & 1))
          for (int j = 0; j < 8; ++j) {
            const int d0 = 8 * (g >> 1) + j;
            const double v = d0 < 14 ? (ri ? -cx_sin(d0 * k0, 14) : cx_cos(d0 * k0, 14)) : 0.0;
            f.v[lane][j >> 1] |= cx_bf(v) << (16 * (j & 1));
          }
      } else {
        for (int j = 0; j < 4; ++j) {
          const long n = cx_pi(g, 4 * (sub & 1) + j) * k0;
          f.v[lane][j] = cx_f32(sub < 4 ? cx_cos(n, 448) : (ri ? -cx_sin(n, 448) : cx_sin(n, 448)));
        }
      }
    }
  }
  return f;
}
template <int I> struct FragC { static constexpr Frag f = make_frag(I); };
#define FR(i) FragC<(i)>::f
#define FR6(i) FR(i), FR(i + 1), FR(i + 2), FR(i + 3), FR(i + 4), FR(i + 5)
__device__ const __attribute__((aligned(1024))) Frag TF[TF_N] = {FR6(0), FR(6), FR(7), FR(8), FR6(9), FR6(15), FR6(21), FR6(27)};

struct KTab { u16 v[2080]; };
constexpr KTab make_ktab() {
  KTab t{};
  for (int i = 0; i < 2080; ++i) {
    const int l = 2048 - i;
    int idx = 768;
    if (l >= 0) {
      const int sh = l > 256 ? (l >= 1024 ? 3 : (l >= 512 ? 2 : 1)) : 0;
      const int v = sh ? (((l + (1 << (sh - 1)) - 1 + ((l >> sh) & 1)) >> sh) << sh) : l;
      if (v < 256) idx = ((v & 7) + 8 * (v >> 7)) * 16 + ((v >> 3) & 15);
      else if (v < 512) idx = 256 + ((((v - 256) >> 1) & 3) + 4 * ((v - 256) >> 7)) * 16 + (((v - 256) >> 3) & 15);
      else if (v < 1024) idx = 256 + (8 + (((v - 512) >> 2) & 1) + 2 * ((v - 512) >> 7)) * 16 + (((v - 512) >> 3) & 15);
      else if (v < 2048) idx = 512 + ((v - 1024) >> 7) * 16 + (((v - 1024) >> 3) & 15);
      else idx = 640;
    }
    t.v[i] = (u16)(2 * idx);
  }
  return t;
}
__device__ const __attribute__((aligned(16))) KTab KTAB = make_ktab();

template <class T, class F> DI T bc(F v) { return __builtin_bit_cast(T, v); }
DI f32x4 mfma32(u32x4 a, u32x4 b, f32x4 c) { return __builtin_amdgcn_mfma_f32_16x16x32_bf16(bc<bf16x8>(a), bc<bf16x8>(b), c, 0, 0, 0); }
DI f32x4 mfma16(u32x2 a, u32x2 b, f32x4 c) { return __builtin_amdgcn_mfma_f32_16x16x16bf16_1k(bc<s16x4>(a), bc<s16x4>(b), c, 0, 0, 0); }
DI u32 pk(float a, float b) { f32x2 v = {a, b}; return bc<u32>(__builtin_convertvector(v, bf16x2)); }
DI u32 pk2(f32x2 v) { return bc<u32>(__builtin_convertvector(v, bf16x2)); }
DI float bfr(float a) { return bc<float>(pk(0.f, a) & 0xffff0000u); }
DI float lo16(u32 v) { return bc<float>(v << 16); }
DI float hi16(u32 v) { return bc<float>(v & 0xffff0000u); }
DI f32x2 up2(u32 v) { return f32x2{lo16(v), hi16(v)}; }
DI float b2f(u16 v) { return bc<float>((u32)v << 16); }
DI u32x2 trread(u32 a) { return bc<u32x2>(__builtin_amdgcn_ds_read_tr16_b64_v4i16((LAS s16x4*)(size_t)a)); }
DI u32x4 cat(u32x2 a, u32x2 b) { u32x4 r = {a.x, a.y, b.x, b.y}; return r; }
DI float sinr(float x) { return __builtin_amdgcn_sinf(x); }
DI float cosr(float x) { return __builtin_amdgcn_cosf(x); }
DI float fx1(float v) { return __builtin_amdgcn_update_dpp(v, v, 0xB1, 0xf, 0xf, true); }
DI float fr8(float v) { return __builtin_amdgcn_update_dpp(v, v, 0x128, 0xf, 0xf, true); }
DI u32 ux1(u32 v) { return (u32)__builtin_amdgcn_update_dpp((int)v, (int)v, 0xB1, 0xf, 0xf, true); }
DI u32 ur8(u32 v) { return (u32)__builtin_amdgcn_update_dpp((int)v, (int)v, 0x128, 0xf, 0xf, true); }
DI u32x4 sx1(u32x4 v, u32 m) { u32x4 r = {ux1(v.x) ^ m, ux1(v.y) ^ m, ux1(v.z) ^ m, ux1(v.w) ^ m}; return r; }
DI u32x4 sr8(u32x4 v, u32 m) { u32x4 r = {ur8(v.x) ^ m, ur8(v.y) ^ m, ur8(v.z) ^ m, ur8(v.w) ^ m}; return r; }
DI u32x4 xr(u32x4 v, u32 m) { u32x4 r = {v.x ^ m, v.y ^ m, v.z ^ m, v.w ^ m}; return r; }
DI u32x4 pk8(const float (&v)[8]) { return u32x4{pk(v[0], v[1]), pk(v[2], v[3]), pk(v[4], v[5]), pk(v[6], v[7])}; }
template <class T> DI T lds_ld(u32 a) { return *(LAS T*)(size_t)a; }
template <class T> DI void lds_st(u32 a, T v) { *(LAS T*)(size_t)a = v; }
DI void wait_vm0() { __builtin_amdgcn_s_waitcnt(0x0F70); }
DI void drain_vm() { asm volatile("s_waitcnt vmcnt(0)" ::: "memory"); }
DI void bar() { __builtin_amdgcn_s_waitcnt(0xC07F); asm volatile("s_barrier" ::: "memory"); }
template <class T> DI void pin(T& v) { asm volatile("" : "+v"(v)); }
typedef __amdgpu_buffer_rsrc_t rsrc_t;
DI rsrc_t mkrs(const void* p, u32 bytes) { return __builtin_amdgcn_make_buffer_rsrc((void*)p, 0, bytes, 0x00020000); }
template <int AUX> DI void dma16(rsrc_t rs, u32 lds, u32 voff, u32 soff) { __builtin_amdgcn_raw_ptr_buffer_load_lds(rs, (LAS void*)(size_t)lds, 16, voff, soff, 0, AUX); }
DI f32x2 cmul(f32x2 x, f32x2 y) {
  f32x2 t;
  asm("v_pk_mul_f32 %0, %1, %2 op_sel:[0,0] op_sel_hi:[0,1]\n\tv_pk_fma_f32 %0, %1, %2, %0 op_sel:[1,1,0] op_sel_hi:[1,0,1] neg_lo:[1,0,0]" : "=&v"(t) : "v"(x), "v"(y));
  return t;
}
DI float dot2b(u32 a, u32 b, float c) { return __builtin_amdgcn_fdot2_f32_bf16(bc<bf16x2>(a), bc<bf16x2>(b), c, false); }
DI float wave_sum(float a) {
  a += __builtin_amdgcn_update_dpp(0.f, a, 0xB1, 0xf, 0xf, true);
  a += __builtin_amdgcn_update_dpp(0.f, a, 0x4E, 0xf, 0xf, true);
  a += __builtin_amdgcn_update_dpp(0.f, a, 0x141, 0xf, 0xf, true);
  a += __builtin_amdgcn_update_dpp(0.f, a, 0x140, 0xf, 0xf, true);
  a += __builtin_amdgcn_update_dpp(0.f, a, 0x142, 0xa, 0xf, false);
  a += __builtin_amdgcn_update_dpp(0.f, a, 0x143, 0xc, 0xf, false);
  return bc<float>(__builtin_amdgcn_readlane(bc<int>(a), 63));
}
DI int pi_k(int g, int jj) { return 16 * (jj >> 2) + 4 * g + (jj & 3); }

#define CNOP 1
#define CNOPR 2
#define PFFT 0.6
#define SH2PRIO 2
#define SLP 2
#define RLX_AGENT __ATOMIC_RELAXED, __HIP_MEMORY_SCOPE_AGENT
template <bool P> DI void st16(rsrc_t rs, u32 voff, u32 soff, u32x4 v) {
  __builtin_amdgcn_raw_buffer_store_b128(v, rs, voff, soff, P ? 16 : 0);
  asm volatile("s_nop 1" :: "v"(v) : "memory");
}
template <bool P> DI u32x4 ld16(rsrc_t rs, u32 voff, u32 soff) { return __builtin_amdgcn_raw_buffer_load_b128(rs, voff, soff, P ? 16 : 0); }
DI void publish(u32* set, int idx, u32 epoch) {
  if (threadIdx.x < 8) __hip_atomic_store((GAS u32*)set + threadIdx.x * FR_ + idx * FL, epoch, RLX_AGENT);
}
DI void poll_flags(const u32* first, int n, u32 epoch) {
  const int lane = threadIdx.x & 63;
  const GAS u32* f = (const GAS u32*)first + (lane < n ? lane : 0) * FL;
  for (int spin = 0; spin < (1 << 18); ++spin) {
    if (__all(__hip_atomic_load(f, RLX_AGENT) == epoch)) break;
    __builtin_amdgcn_s_sleep(SLP);
  }
}

#define STAMP(i) do {} while (0)
#define STAMP_DECL do {} while (0)
#define STAMP_FLUSH() do {} while (0)
#define STAMP_ARG
#define STAMP_PASS

DI u32x4 tf(rsrc_t rs, int item, u32 l16) { return __builtin_amdgcn_raw_buffer_load_b128(rs, l16 + (item & 3) * 1024, (item >> 2) * 4096, 0); }
DI u32x4 tfs(rsrc_t rs, int item, u32 l16, u32 so) { return __builtin_amdgcn_raw_buffer_load_b128(rs, l16 + item * 1024, so, 0); }

DI void fft_first_loads(const Params& p, const u32 lb, const int b) {
  const int lane = threadIdx.x & 63, w = __builtin_amdgcn_readfirstlane(threadIdx.x >> 6);
  const u32 l16 = lane * 16;
  const int s2 = b & 63, q = b >> 6;
  const rsrc_t rx = mkrs(p.x, S_ * D_ * 2), rtf = mkrs(TF, TF_N * 1024);
  if (lane < 56) {
    u32 la = lb + L_XT + 8 * w * XRS, so = (64 * 8 * w + s2) * (D_ * 2);
#pragma unroll 1
    for (int r = 0; r < 8; ++r) { dma16<0>(rx, la, l16, so); la += XRS; so += 64 * (D_ * 2); }
  }
  const u32 tq = (TF_Q0 + 6 * q) * 1024;
  dma16<0>(rtf, lb + L_C1 + w * 2048, l16, w == 0 ? 0u : tq + (2 * w - 2) * 1024);
  dma16<0>(rtf, lb + L_C1 + w * 2048 + 1024, l16, w == 0 ? 1024u : tq + (2 * w - 1) * 1024);
}

struct P3C { u32x4 b1, b2; f32x4 tc, ts; u32x2 m; };
template <int PH>
DI void p3_prepare(const Params& p, const u32 lb, const int b, const rsrc_t rtf, P3C& c) {
  const int lane = threadIdx.x & 63, i16 = lane & 15, g = lane >> 4;
  const u32 l16 = lane * 16;
  const int s2 = b & 63, q = b >> 6;
  c.tc = bc<f32x4>(tf(rtf, TF_TC, l16)); c.ts = bc<f32x4>(tf(rtf, TF_TS, l16));
  const u32x4 m4 = tf(rtf, TF_M, l16);
  c.m = u32x2{m4.x, m4.y};
  c.b1 = lds_ld<u32x4>(lb + L_C3 + l16); c.b2 = lds_ld<u32x4>(lb + L_C3 + 1024 + l16);
  pin(c.b1); pin(c.b2); pin(c.tc); pin(c.ts); pin(c.m);
}
DI void p3_make_b(const u32 lb, const int b) {
  const int lane = threadIdx.x & 63, i16 = lane & 15, g = lane >> 4;
  const int s2 = b & 63, q = b >> 6;
  const int ro = i16 >> 3;
  const float a = (float)(64 * (8 * q + (i16 & 7)) + s2) * (1.0f / 2048.0f), P0 = a * (float)(4 * g);
  float u[8], v[8];
#pragma unroll
  for (int jj = 0; jj < 8; ++jj) {
    const float ph = __builtin_fmaf(a, (float)(16 * (jj >> 2) + (jj & 3)), P0);
    u[jj] = sinr(ph + (ro ? 0.f : 0.25f));
    v[jj] = sinr(ph + (ro ? 0.25f : 0.5f));
  }
  lds_st<u32x4>(lb + L_C3 + lane * 16, pk8(u));
  lds_st<u32x4>(lb + L_C3 + 1024 + lane * 16, pk8(v));
}
template <bool LDSX>
DI void gate_rows(const Params& p, const u32 lb, const int b, const int r0, const int nr) {
  const int lane = threadIdx.x & 63;
  const u32 l16 = lane * 16;
  const int s2 = b & 63, q = b >> 6;
  u32x4 gwv = {0, 0, 0, 0};
  if (lane < 56) gwv = *(const GAS u32x4*)((const char*)p.gw + l16);
#pragma unroll
  for (int r = 0; r < 2; ++r) {
    if (r < nr) {
      float acc = 0.f;
      if (lane < 56) {
        u32x4 xv;
        if (LDSX) xv = lds_ld<u32x4>(lb + L_XT + (8 * q + r0 + r) * XRS + l16);
        else xv = *(const GAS u32x4*)((const char*)(p.x + (size_t)(64 * (8 * q + r0 + r) + s2) * D_) + l16);
#pragma unroll
        for (int e = 0; e < 4; ++e) acc = dot2b(pk2(up2(xv[e]) * up2(gwv[e])), 0x3f803f80u, acc);
      }
      const float gg = bfr(__builtin_amdgcn_rcpf(1.0f + __expf(-bfr(wave_sum(acc)))));
      if (lane == 0) lds_st<f32x2>(lb + L_G + (r0 + r) * 8, f32x2{gg, bfr(1.0f - gg)});
    }
  }
}

template <int PH>
DI void fft_phase3(const Params& p, const u32 lb, const int b, u32* ctl, const u32 epoch, const u32x4 (&fc)[2], const u32x4 (&fs)[2], const P3C& c STAMP_ARG) {
  constexpr bool P = PH == 0;
  constexpr int TPAUX = P ? 16 : 0;
  const int tid = threadIdx.x, lane = tid & 63, w = __builtin_amdgcn_readfirstlane(tid >> 6), i16 = lane & 15, g = lane >> 4;
  const u32 l16 = lane * 16;
  const int s2 = b & 63, q = b >> 6;
  const f32x4 z4 = {0, 0, 0, 0};
  const rsrc_t rws = mkrs(p.ws, 16u << 20);
  u32 tpa = lb + L_TP + (4 * g + (i16 >> 2)) * TPRS + (i16 & 3) * 128 + (w >> 1) * 32 + (w & 1) * 8;
  const int row = 2 * w + (i16 >> 3);
  u32 zw = lb + L_Z + ((i16 & 7) * 16 + row) * 64 + ((g ^ (row >> 2)) & 3) * 16;
  u32 zr = lb + L_Z + (w * 16 + i16) * 64 + ((g ^ (i16 >> 2)) & 3) * 16;
  u32 boff = (u32)(tid * B_D);
  u32 biw = lb + L_BI + tid * 2, bir = lb + L_BI + w * BRS + (32 * i16 + 4 * g) * 2;
  u32 ooff = (u32)((64 * (8 * q + w) + s2) * (D_ * 2) + (32 * i16 + 4 * g) * 2);
  u32 mi = (i16 & 1) ? 0u : 0x80008000u;
  pin(tpa); pin(zw); pin(zr); pin(boff); pin(biw); pin(bir); pin(ooff); pin(mi);
  STAMP(13);
  if (P) { poll_flags(ctl + CT_F2 + (b & 7) * FR_ + w * 32 * FL, 32, epoch); bar(); __builtin_amdgcn_s_setprio(3); }
  STAMP(14);
#pragma unroll
  for (int r = 0; r < 4; ++r) dma16<TPAUX>(rws, lb + L_TP + (4 * w + r) * TPRS, l16, (u32)WS_TP + s2 * TP_S2 + (4 * w + r) * 1024);
  wait_vm0();
  bar();
  STAMP(15);
#pragma unroll
  for (int t = 0; t < 2; ++t) {
    u32x4 Tr = cat(trread(tpa + t * 512), trread(tpa + t * 512 + 16 * TPRS));
    u32x4 Ti = cat(trread(tpa + t * 512 + 16), trread(tpa + t * 512 + 16 + 16 * TPRS));
    f32x4 z = mfma32(Ti, c.b2, mfma32(Tr, c.b1, z4));
    lds_st<u32x2>(zw + t * 8, u32x2{pk(z[0], z[1]), pk(z[2], z[3])});
  }
  bar();
  STAMP(16);
  f32x2 ga[2][2];
  {
    u32x4 A = lds_ld<u32x4>(zr), A2 = sx1(A, mi);
    const float gg = lds_ld<float>(lb + L_G + w * 8);
#pragma unroll
    for (int td = 0; td < 2; ++td) {
      f32x4 G = mfma32(A2, fs[td], mfma32(A, fc[td], z4));
      const float c0 = c.tc[2 * td], s0 = c.ts[2 * td], c1 = c.tc[2 * td + 1], s1 = c.ts[2 * td + 1];
      const float g0r = G[0] * c0 - G[1] * s0, g0i = G[1] * c0 + G[0] * s0;
      const float g1r = G[2] * c1 - G[3] * s1, g1i = G[3] * c1 + G[2] * s1;
      f32x4 y = mfma16(u32x2{pk(g0r, g0i), pk(g1r, g1i)}, c.m, z4);
      ga[td][0] = up2(pk2(up2(pk(y[0], y[1])) * gg));
      ga[td][1] = up2(pk2(up2(pk(y[2], y[3])) * gg));
      pin(ga[td][0]); pin(ga[td][1]);
    }
  }
  STAMP(17);
  if (P) { poll_flags(ctl + CT_FB + (b & 7) * FR_ + w * 28 * FL, 28, epoch); bar(); }
  STAMP(18);
  if (tid < D_) {
    const u32x4 bv = ld16<P>(rws, boff, (u32)WS_B + (((s2 & 7) * 8 + (s2 >> 3)) * 32 + 8 * q) * 2);
#pragma unroll
    for (int e = 0; e < 4; ++e) {
      lds_st<u16>(biw + (2 * e) * BRS, (u16)bv[e]);
      lds_st<u16>(biw + (2 * e + 1) * BRS, (u16)(bv[e] >> 16));
    }
  }
  bar();
  {
    const rsrc_t ro_ = mkrs(p.out, S_ * D_ * 2);
    const float og = lds_ld<float>(lb + L_G + w * 8 + 4);
    const u32x2 bq0 = lds_ld<u32x2>(bir), bq1 = lds_ld<u32x2>(bir + 32);
#pragma unroll
    for (int td = 0; td < 2; ++td) {
      const u32x2 bq = td ? bq1 : bq0;
      const f32x2 o01 = ga[td][0] + up2(pk2(up2(bq.x) * og));
      const f32x2 o23 = ga[td][1] + up2(pk2(up2(bq.y) * og));
      if (i16 < 14) __builtin_amdgcn_raw_buffer_store_b64(u32x2{pk2(o01), pk2(o23)}, ro_, ooff, td * 32, 0);
    }
  }
  STAMP(19);
  if (P && b == 0 && tid == 0) __hip_atomic_store((GAS u32*)ctl + CT_GEN, epoch, RLX_AGENT);
}

template <int PH>
DI void fft_upper(const Params& p, const u32 lb, const int b, u32* ctl, u32x4 (&fc)[2], u32x4 (&fs)[2], P3C& c3) {
  constexpr bool P = PH == 0;
  const int lane = threadIdx.x & 63, w = __builtin_amdgcn_readfirstlane(threadIdx.x >> 6);
  const u32 l16 = lane * 16;
  const rsrc_t rtf = mkrs(TF, TF_N * 1024);
  if (w == 4) p3_make_b(lb, b);
  if (P) {
    bar(); bar(); bar();
    bar(); bar(); bar();
    __builtin_amdgcn_s_setprio(SH2PRIO);
  }
  if (P) gate_rows<P>(p, lb, b, w, 1); else gate_rows<P>(p, lb, b, 2 * (w - 4), 2);
  fc[0] = tf(rtf, TF_FC0, l16); fs[0] = tf(rtf, TF_FS0, l16);
  fc[1] = xr(fc[0], 0x80000000u); fs[1] = xr(fs[0], 0x80000000u);
  pin(fc[0]); pin(fc[1]); pin(fs[0]); pin(fs[1]);
  if (!P) bar();
  p3_prepare<PH>(p, lb, b, rtf, c3);
  if (!P) bar();
}

template <int PH>
DI void fft_role(const Params& p, const u32 lb, const int b, u32* ctl, const u32 gen, u32x4 (&fc)[2], u32x4 (&fs)[2], P3C& c3 STAMP_ARG) {
  constexpr bool P = PH == 0;
#define TPAUXV 16
  constexpr int AUX = P ? 16 : 0, TPAUX = P ? TPAUXV : 0;
  (void)AUX;
  const int tid = threadIdx.x, lane = tid & 63, w = __builtin_amdgcn_readfirstlane(tid >> 6), i16 = lane & 15, g = lane >> 4;
  const u32 l16 = lane * 16;
  const int s2 = b & 63, q = b >> 6;
  const int xx = b & 7, mm = (b >> 3) & 3, kq = b >> 5;
  const int k1w = (((mm & 1) ? (xx ? 32 - xx : 8) : xx) + 16 * (mm >> 1)) & 31;
  const f32x4 z4 = {0, 0, 0, 0};
  const rsrc_t rws = mkrs(p.ws, 16u << 20), rtf = mkrs(TF, TF_N * 1024);
  const u32 tpo = (u32)((tid >> 3) * TP_S2 + (tid & 7) * 16), tps = (u32)WS_TP + k1w * 1024 + kq * 128;
  if (PH == 2 || PH == 3) {
    fc[0] = tf(rtf, TF_FC0, l16); fs[0] = tf(rtf, TF_FS0, l16);
    fc[1] = xr(fc[0], 0x80000000u); fs[1] = xr(fs[0], 0x80000000u);
  }

  u32x4 co[2], so[2], cot[2], sot[2];
  auto load_p2 = [&]() {
    const bool mir = kq >= 4;
    const int col0 = mir ? 448 - (56 * kq + 49) : 56 * kq;
    const rsrc_t rr = mkrs(p.wr, S_ * DF * 2), ri_ = mkrs(p.wi, S_ * DF * 2);
#pragma unroll
    for (int i = 0; i < 3; ++i) {
      const int gi = w + 4 * i;
      if (gi < 9) {
        const int gam = 64 * gi + lane, k2 = (gam * 7282) >> 16, chunk = gam - 9 * k2;
        const int ks = k1w + 32 * k2, row = mir ? ((2048 - ks) & 2047) : ks;
        const u32 vo = (u32)(((row * DF + col0) * 2) & ~15) + chunk * 16;
        dma16<0>(rr, lb + L_W + gi * 1024, vo, 0);
        dma16<0>(ri_, lb + L_W + 9216 + gi * 1024, vo, 0);
      }
    }
    dma16<0>(rtf, lb + L_C2 + w * 1024, l16, (TF_CO0 + w) * 1024);
  };

  if (PH == 0 || PH == 1) {
    u32x4 C1, S1, nS1;
    {
      const float k1 = (float)(16 * (w >> 1) + i16) * (1.0f / 2048.0f), P0 = k1 * (float)(64 * g + s2), Pl = k1 * 256.f;
      float c[8], s[8];
#pragma unroll
      for (int j = 0; j < 8; ++j) { const float ph = __builtin_fmaf(Pl, (float)j, P0); c[j] = cosr(ph); s[j] = sinr(ph); }
      C1 = pk8(c); S1 = pk8(s); nS1 = xr(S1, 0x80008000u);
      pin(C1); pin(S1); pin(nS1);
    }
    int d0a = 8 * (g >> 1) + (i16 >> 2), d0b = d0a + 4;
    d0b = d0b < 14 ? d0b : 0;
    u32 xa = lb + L_XT + (8 * w + (g & 1)) * XRS + 8 * (i16 & 3);
    u32 xb = xa + d0b * 64; xa += d0a * 64;
    u32 xw = lb + L_XCH + w * 256 + lane * 4;
    u32 xr_ = lb + L_XCH + (w & 1) * 4096 + lane * 4;
    u32 toff = (u32)((16 * (w >> 1) + i16) * T_K1 + 4 * (w & 1) * 32 + g * 16);
    u32 imm = (i16 & 1) ? 0x80008000u : 0u;
    pin(xa); pin(xb); pin(xw); pin(xr_); pin(toff); pin(imm);
    const u32 tso = (u32)WS_T + s2 * 1024 + q * 256;
    STAMP(1);
    wait_vm0();
    bar();
    const u32 cb = lb + L_C1 + l16;
    fc[0] = lds_ld<u32x4>(cb); fs[0] = lds_ld<u32x4>(cb + 1024);
    const u32x4 Ba1 = lds_ld<u32x4>(cb + 2048), Ba2 = lds_ld<u32x4>(cb + 3072);
    const f32x4 twc0 = lds_ld<f32x4>(cb + 4096), twc1 = lds_ld<f32x4>(cb + 5120), tws0 = lds_ld<f32x4>(cb + 6144), tws1 = lds_ld<f32x4>(cb + 7168);
    fc[1] = xr(fc[0], 0x80000000u); fs[1] = xr(fs[0], 0x80000000u);
    STAMP(2);
    f32x4 D2[2][2];
#pragma unroll
    for (int sg = 0; sg < 2; ++sg) {
      f32x4 H[2];
#pragma unroll
      for (int td = 0; td < 2; ++td) {
        const int o = td * 32 + sg * 4 * XRS;
        u32x4 A0 = cat(trread(xa + o), trread(xb + o));
        u32x4 A1 = cat(trread(xa + o + 2 * XRS), trread(xb + o + 2 * XRS));
        H[td] = mfma32(A1, Ba2, mfma32(A0, Ba1, z4));
      }
      float h[8];
#pragma unroll
      for (int j = 0; j < 4; ++j) {
        h[j] = __builtin_fmaf(fx1(H[0][j]), tws0[j], H[0][j] * twc0[j]);
        h[4 + j] = __builtin_fmaf(fx1(H[1][j]), tws1[j], H[1][j] * twc1[j]);
      }
      u32x4 A = pk8(h), A2 = sx1(A, imm);
#pragma unroll
      for (int tk = 0; tk < 2; ++tk) D2[sg][tk] = mfma32(A2, fs[tk], mfma32(A, fc[tk], z4));
    }
#pragma unroll
    for (int tk = 0; tk < 2; ++tk)
#pragma unroll
      for (int r = 0; r < 4; ++r) lds_st<u32>(xw + (tk * 4 + r) * 1024, pk(D2[0][tk][r], D2[1][tk][r]));
    bar();
    STAMP(3);
    {
      u32 pc[4][2];
#pragma unroll
      for (int kk = 0; kk < 2; ++kk) {
        const u32 br = xr_ + kk * 2048;
        u32x4 Xr = {lds_ld<u32>(br), lds_ld<u32>(br + 256), lds_ld<u32>(br + 512), lds_ld<u32>(br + 768)};
        u32x4 Xi = {lds_ld<u32>(br + 1024), lds_ld<u32>(br + 1280), lds_ld<u32>(br + 1536), lds_ld<u32>(br + 1792)};
        f32x4 Tr = mfma32(Xi, S1, mfma32(Xr, C1, z4));
        f32x4 Ti = mfma32(Xr, nS1, mfma32(Xi, C1, z4));
        pc[kk][0] = pk(Tr[0], Tr[1]); pc[kk][1] = pk(Tr[2], Tr[3]);
        pc[2 + kk][0] = pk(Ti[0], Ti[1]); pc[2 + kk][1] = pk(Ti[2], Ti[3]);
      }
      u32x4 lo = {pc[0][0], pc[0][1], pc[1][0], pc[1][1]}, hi = {pc[2][0], pc[2][1], pc[3][0], pc[3][1]};
#pragma unroll
      for (int e = 0; e < 4; ++e) {
        auto r1 = __builtin_amdgcn_permlane16_swap(lo[e], hi[e], false, false);
        auto r2 = __builtin_amdgcn_permlane32_swap(r1[0], r1[1], false, false);
        lo[e] = r2[0]; hi[e] = r2[1];
      }
      st16<P>(rws, toff, tso, lo);
      st16<P>(rws, toff + 64, tso, hi);
    }
    STAMP(4);
    if (P) {
      drain_vm();
      STAMP(20);
      bar();
      publish(ctl + CT_F1, b, gen + 1u);
      __builtin_amdgcn_s_setprio(2);
      load_p2();
    }
    STAMP(5);
  }
  const u32 epoch = __builtin_amdgcn_readfirstlane(gen) + 1u;

  if (PH == 2) load_p2();
  if (PH == 0 || PH == 2) {
    const int ri = i16 >> 3;
    u32 tsv = (u32)((lane >> 1) * 1024 + w * 256 + (lane & 1) * 16);
    u32 tiw = lb + L_TI + w * 2048 + ((lane >> 1) & 1) * 1024 + (lane >> 2) * 32 + (lane & 1) * 16;
    u32 ta = lb + L_TI + w * 2048 + (4 * g + (i16 >> 2)) * 32 + (i16 & 3) * 8;
    u32 tow = lb + L_TO + 2 * i16 * 144 + w * 32 + g * 8;
    u32 tor = lb + L_TO + (tid >> 3) * 144 + (tid & 7) * 16;
    u32 mf = ri ? 0x80008000u : 0u, mi = ri ? 0u : 0x80008000u;
    float wr_[16], wi_[16];
    {
      const bool mir = kq >= 4;
      const int c = i16 & 7, kd = 2 * w + (c >> 2) + 14 * (4 * kq + (c & 3)), col0 = mir ? 448 - (56 * kq + 49) : 56 * kq;
      const int row0 = mir ? ((2048 - k1w) & 2047) : k1w;
      u32 wa = lb + L_W + 4 * g * 144 + (((row0 * DF + col0) * 2) & 15) + ((mir ? 448 - kd : kd) - col0) * 2;
      const u32 sg = (mir != (ri != 0)) ? 0u : 0x80000000u;
      pin(tsv); pin(tiw); pin(ta); pin(tow); pin(tor); pin(mf); pin(mi); pin(wa);
      STAMP(7);
      if (P) poll_flags(ctl + CT_F1 + (b & 7) * FR_ + w * 64 * FL, 64, epoch);
      wait_vm0();
      bar();
      const u32 so_ = (u32)WS_T + k1w * T_K1 + kq * 32;
      const u32x4 t0 = ld16<P>(rws, tsv, so_), t1 = ld16<P>(rws, tsv, so_ + 32 * 1024);
      __builtin_amdgcn_sched_barrier(0);
      co[0] = lds_ld<u32x4>(lb + L_C2 + l16); so[0] = lds_ld<u32x4>(lb + L_C2 + 1024 + l16);
      cot[0] = lds_ld<u32x4>(lb + L_C2 + 2048 + l16); sot[0] = lds_ld<u32x4>(lb + L_C2 + 3072 + l16);
      co[1] = xr(so[0], 0x00008000u);
      so[1] = xr(co[0], 0x80000000u);
      cot[1] = xr(cot[0], 0x80000000u); sot[1] = xr(sot[0], 0x80000000u);
      pin(co[0]); pin(co[1]); pin(so[0]); pin(so[1]); pin(cot[0]); pin(cot[1]); pin(sot[0]); pin(sot[1]);
#pragma unroll
      for (int j = 0; j < 16; ++j) {
        const int o = (32 * (j >> 3) + 16 * ((j >> 2) & 1) + (j & 3)) * 144;
        wr_[j] = bc<float>((u32)lds_ld<u16>(wa + o) << 16);
        wi_[j] = bc<float>(((u32)lds_ld<u16>(wa + 9216 + o) << 16) + sg);
      }
#pragma unroll
      for (int j = 0; j < 16; ++j) { pin(wr_[j]); pin(wi_[j]); }
      __builtin_amdgcn_sched_barrier(0);
      if (P) __builtin_amdgcn_s_setprio(3);
      STAMP(8);
      lds_st<u32x4>(tiw, t0);
      lds_st<u32x4>(tiw + 512, t1);
    }
    STAMP(9);
    f32x4 E[2], O[2];
    {
      u32x4 B0 = cat(trread(ta), trread(ta + 512)), B1 = cat(trread(ta + 1024), trread(ta + 1536));
      u32x4 B0s = sr8(B0, mf), B1s = sr8(B1, mf);
#pragma unroll
      for (int t = 0; t < 2; ++t) {
        E[t] = mfma32(fs[t], B0s, mfma32(fc[t], B0, z4));
        O[t] = mfma32(so[t], B1s, mfma32(co[t], B1, z4));
      }
    }
    float Pp[8], Mm[8];
#pragma unroll
    for (int j = 0; j < 8; ++j) {
      const float e = E[j >> 2][j & 3], o = O[j >> 2][j & 3];
      const float ylo = e + o, yhi = e - o;
      const float vlo = __builtin_fmaf(fr8(ylo), wi_[j], ylo * wr_[j]);
      const float vhi = __builtin_fmaf(fr8(yhi), wi_[8 + j], yhi * wr_[8 + j]);
      Pp[j] = vlo + vhi; Mm[j] = vlo - vhi;
    }
    {
      u32x4 AP = pk8(Pp), AM = pk8(Mm);
      u32x4 APs = sr8(AP, mi), AMs = sr8(AM, mi);
#pragma unroll
      for (int t = 0; t < 2; ++t) {
        f32x4 ze = mfma32(APs, fs[t], mfma32(AP, fc[t], z4));
        f32x4 zo = mfma32(AMs, sot[t], mfma32(AM, cot[t], z4));
        lds_st<u32x2>(tow + t * 32 * 144, u32x2{pk(ze[0], ze[1]), pk(ze[2], ze[3])});
        lds_st<u32x2>(tow + t * 32 * 144 + 144, u32x2{pk(zo[0], zo[1]), pk(zo[2], zo[3])});
      }
    }
    bar();
    STAMP(10);
    {
      st16<P>(rws, tpo, tps, lds_ld<u32x4>(tor));
      st16<P>(rws, tpo, tps + 32 * TP_S2, lds_ld<u32x4>(tor + 32 * 144));
    }
    STAMP(11);
    if (P) { drain_vm(); bar(); publish(ctl + CT_F2, b, epoch); __builtin_amdgcn_s_setprio(SH2PRIO); }
    STAMP(12);
  }

  if (PH == 0) gate_rows<true>(p, lb, b, w, 1);
  if (PH == 3) bar();
  if (PH == 0 || PH == 3) p3_prepare<PH>(p, lb, b, rtf, c3);
  if (PH == 3) bar();
}

DI f32x2 cexp_rev(float x2, float ph) {
  const float e = __builtin_amdgcn_exp2f(x2);
  const float f = __builtin_amdgcn_fractf(ph);
  f32x2 cs = {cosr(f), sinr(f)};
  return cs * e;
}
struct KW { u32 a[8]; };
template <int ROW> DI void wrow(const KW& k, f32x2 v) { lds_st<u32>(k.a[ROW & 7] + ROW * 256, pk2(v)); }
template <int ROW> DI void wrow2(const KW& k, f32x2 lo, f32x2 hi) {
  const u32 a = pk2(lo), b = pk2(hi);
  asm volatile("ds_write2st64_b32 %0, %1, %2 offset0:%c3 offset1:%c4" :: "v"(k.a[ROW & 7]), "v"(a), "v"(b), "i"(ROW), "i"(ROW + 8) : "memory");
}
DI void kfrag(u32 fa, u32 fb, u32x4 (&A)[4]) {
  A[0] = lds_ld<u32x4>(fa); A[1] = lds_ld<u32x4>(fb); A[2] = lds_ld<u32x4>(fa + 128); A[3] = lds_ld<u32x4>(fb + 128);
}
DI f32x4 ktile(const u32x4 (&A)[4], const u32x4 (&V)[4]) {
  f32x4 acc = {0, 0, 0, 0};
#pragma unroll
  for (int ks = 0; ks < 4; ++ks) acc = mfma32(A[ks], V[ks], acc);
  return acc;
}
template <class F0, class F1>
DI void kgen(const u32 (&in)[6], float dt, const KW& k, u32 fa, u32 fb, u32 pb, F0&& hook0, F1&& hook1) {
  const float ar = dt * bc<float>(in[0] << 16), ai = dt * bc<float>(in[1] << 16);
  const f32x2 dB = {dt * bc<float>(in[2] << 16), dt * bc<float>(in[3] << 16)};
  const f32x2 Cc = {bc<float>(in[4] << 16), bc<float>(in[5] << 16)};
  const f32x2 CB = cmul(Cc, dB);
  const float x1 = ar * 1.44269504088896341f, p1 = ai * 0.15915494309189535f;
  const f32x2 E1 = cexp_rev(x1, p1), E8c = cexp_rev(8.f * x1, -8.f * p1), E128 = cexp_rev(128.f * x1, 128.f * p1);
  u32x4 V[4], A0[4], A1[4];
  {
    const f32x2 v2 = cmul(E8c, E8c), v3 = cmul(v2, E8c), v4 = cmul(v2, v2), v8 = cmul(v4, v4);
    const f32x2 v5 = cmul(v4, E8c), v6 = cmul(v4, v2), v7 = cmul(v4, v3);
    wrow2<0>(k, f32x2{1.f, 0.f}, v8); wrow2<1>(k, E8c, cmul(v8, E8c)); wrow2<2>(k, v2, cmul(v8, v2)); wrow2<3>(k, v3, cmul(v8, v3));
    wrow2<4>(k, v4, cmul(v8, v4)); wrow2<5>(k, v5, cmul(v8, v5)); wrow2<6>(k, v6, cmul(v8, v6)); wrow2<7>(k, v7, cmul(v8, v7));
    kfrag(fa, fb, V);
  }
  hook0();
  const f32x2 E2 = cmul(E1, E1), E3 = cmul(E2, E1), E4 = cmul(E2, E2), E5 = cmul(E4, E1), E6 = cmul(E4, E2), E7 = cmul(E4, E3);
  const f32x2 G1 = cmul(CB, E128);
  wrow2<0>(k, CB, G1); wrow2<1>(k, cmul(CB, E1), cmul(G1, E1)); wrow2<2>(k, cmul(CB, E2), cmul(G1, E2)); wrow2<3>(k, cmul(CB, E3), cmul(G1, E3));
  wrow2<4>(k, cmul(CB, E4), cmul(G1, E4)); wrow2<5>(k, cmul(CB, E5), cmul(G1, E5)); wrow2<6>(k, cmul(CB, E6), cmul(G1, E6)); wrow2<7>(k, cmul(CB, E7), cmul(G1, E7));
  kfrag(fa, fb, A0);
  const f32x2 G2 = cmul(G1, E128), G3 = cmul(G2, E128), G4 = cmul(G3, E128), G5 = cmul(G4, E128), G6 = cmul(G5, E128), G7 = cmul(G6, E128);
  wrow2<0>(k, G2, G4); wrow2<1>(k, cmul(G2, E2), cmul(G4, E4)); wrow2<2>(k, cmul(G2, E4), G5); wrow2<3>(k, cmul(G2, E6), cmul(G5, E4));
  wrow2<4>(k, G3, G6); wrow2<5>(k, cmul(G3, E2), cmul(G6, E4)); wrow2<6>(k, cmul(G3, E4), G7); wrow2<7>(k, cmul(G3, E6), cmul(G7, E4));
  kfrag(fa, fb, A1);
  hook1();
  __builtin_amdgcn_sched_barrier(0);
  const f32x4 a0 = ktile(A0, V);
  const f32x2 G8 = cmul(G7, E128), G9 = cmul(G8, E128), G10 = cmul(G9, E128), G11 = cmul(G10, E128);
  const f32x2 G12 = cmul(G11, E128), G13 = cmul(G12, E128), G14 = cmul(G13, E128), G15 = cmul(G14, E128);
  wrow2<0>(k, G8, cmul(G15, E128)); wrow<1>(k, G9); wrow<2>(k, G10); wrow<3>(k, G11);
  wrow<4>(k, G12); wrow<5>(k, G13); wrow<6>(k, G14); wrow<7>(k, G15);
  kfrag(fa, fb, A0);
  __builtin_amdgcn_sched_barrier(0);
  const f32x4 a1 = ktile(A1, V), a2 = ktile(A0, V);
  lds_st<f32x4>(pb, a0);
  lds_st<f32x4>(pb + 1024, a1);
  lds_st<f32x4>(pb + 2048, a2);
}

struct SsmLoads { u32x4 xt_v, kt0; u32x2 kt1; u32 in[6], ldt, dsk; };
template <int PH>
DI void ssm_first_loads(const Params& p, const int j, SsmLoads& L) {
  const int tid = threadIdx.x, w = __builtin_amdgcn_readfirstlane(tid >> 6), d = 2 * j, ch = w >> 2, n = tid & 255;
  const int sb_ = j / 7, db_ = j - sb_ * 7;
  if (PH == 0 || PH == 1)
    L.xt_v = __builtin_amdgcn_raw_buffer_load_b128(mkrs(p.x, S_ * D_ * 2), (u32)((tid >> 3) * (D_ * 2) + (tid & 7) * 16), (u32)(64 * sb_ * (D_ * 2) + db_ * 128), 0);
  __builtin_amdgcn_sched_barrier(0);
  if (PH != 1) {
    const u32 vo = ((d + ch) * NS + n) * 2;
    const u16* arr[6] = {p.are, p.aim, p.bre, p.bim, p.cre, p.cim};
#pragma unroll
    for (int a = 0; a < 6; ++a) L.in[a] = *(const GAS u16*)((const GAS char*)arr[a] + vo);
    L.ldt = *(const GAS u16*)(p.logdt + d + ch);
    L.dsk = *(const GAS u16*)(p.dskip + d + ((w >> 1) & 1));
    L.kt0 = *(const GAS u32x4*)(KTAB.v + 8 * n);
    L.kt1 = *(const GAS u32x2*)(KTAB.v + 8 * n + 8);
  }
}

template <int PH>
DI void ssm_role(const Params& p, const u32 lb, const int j, u32* ctl, const u32 gen, const SsmLoads& L STAMP_ARG) {
  constexpr bool P = PH == 0;
  const int tid = threadIdx.x, lane = tid & 63, w = __builtin_amdgcn_readfirstlane(tid >> 6), i16 = lane & 15, g = lane >> 4;
  const int d = 2 * j, ch = w >> 2, n = tid & 255;
  const f32x4 z4 = {0, 0, 0, 0};
  const rsrc_t rws = mkrs(p.ws, 16u << 20);
  const int sb_ = j / 7, db_ = j - sb_ * 7;
  const u32x4 xt_v = L.xt_v, kt0 = L.kt0;
  const u32x2 kt1 = L.kt1;
  const u32 ldt = L.ldt;
  u32 dsk = L.dsk;
  const u32 (&in)[6] = L.in;
  auto transpose = [&]() {
    lds_st<u32x4>(lb + L_XP + (tid >> 3) * 144 + (tid & 7) * 16, xt_v);
    bar();
    const int chi = g + 4 * (w >> 2);
    const u32 a = lb + L_XP + (8 * chi + (i16 >> 2)) * 144 + (16 * (w & 3) + 4 * (i16 & 3)) * 2;
    st16<P>(rws, (u32)((16 * (w & 3) + i16) * XT_D + chi * 16), (u32)WS_XT + 64 * db_ * XT_D + sb_ * 128, cat(trread(a), trread(a + 4 * 144)));
  };
  if (PH == 1) { transpose(); return; }
  if (P) {
    if (tid == 0) lds_st<u32>(lb + L_CNT, 0u);
    __builtin_amdgcn_sched_barrier(0);
    transpose();
    __builtin_amdgcn_s_setprio(1);
    __builtin_amdgcn_sched_barrier(0);
  }
  KW kw;
  const u32 sb = lb + w * KSCR;
#pragma unroll
  for (int h = 0; h < 8; ++h) { kw.a[h] = sb + (((lane >> 2) ^ h) << 4) + (lane & 3) * 4; pin(kw.a[h]); }
  u32 fa = sb + i16 * 256 + ((g ^ (i16 & 3)) << 4) + ((i16 >> 2) & 1) * 64, fb = fa ^ 64;
  pin(fa); pin(fb);
  STAMP(1);
  kgen(in, bfr(expf(bc<float>(ldt << 16))), kw, fa, fb, sb + lane * 16,
       [&]() {
         if (P) {
           drain_vm();
           STAMP(5);
           if (lane == 0 && __hip_atomic_fetch_add((LAS u32*)(size_t)(lb + L_CNT), 1u, __ATOMIC_RELAXED, __HIP_MEMORY_SCOPE_WORKGROUP) == 7u)
             __hip_atomic_store((GAS u32*)ctl + CT_FX + (db_ * 32 + sb_) * FL, gen + 1u, RLX_AGENT);
         }
       },
       []() {});
  const u32 epoch = __builtin_amdgcn_readfirstlane(gen) + 1u;
  pin(dsk);
  STAMP(2);
  if (P && w == 0) poll_flags(ctl + CT_FX + (d >> 6) * 32 * FL, 32, epoch);
  STAMP(6);
  bar();
  const u32x4 uv_ = ld16<P>(rws, n * 16, (u32)WS_XT + (d + ch) * XT_D);
  STAMP(3);
  if ((w & 3) < 3) {
    const u32 pb = lb + ch * 4 * KSCR + (w & 3) * 1024 + lane * 16;
    const f32x4 s = (lds_ld<f32x4>(pb) + lds_ld<f32x4>(pb + KSCR)) + (lds_ld<f32x4>(pb + 2 * KSCR) + lds_ld<f32x4>(pb + 3 * KSCR));
    const u32 tb = lb + L_KT + ch * 1600 + ((w & 3) * 256 + 64 * g + i16) * 2, a = pk(s[0], s[1]), cc = pk(s[2], s[3]);
    lds_st<u16>(tb, (u16)a); lds_st<u16>(tb + 32, (u16)(a >> 16));
    lds_st<u16>(tb + 64, (u16)cc); lds_st<u16>(tb + 96, (u16)(cc >> 16));
  } else {
    if (lane == 0) lds_st<u16>(lb + L_KT + ch * 1600 + 1536, (u16)0);
    if (lane < 32) lds_st<u32x4>(lb + L_U + ch * 4608 + lane * 16, u32x4{0, 0, 0, 0});
  }
  bar();
  STAMP(4);
  {
    const u32 ktb = lb + L_KT + ch * 1600;
    u32 e[11];
#pragma unroll
    for (int k = 0; k < 4; ++k) { e[2 * k] = lds_ld<u16>(ktb + (kt0[k] & 0xffffu)); e[2 * k + 1] = lds_ld<u16>(ktb + (kt0[k] >> 16)); }
    e[8] = lds_ld<u16>(ktb + (kt1[0] & 0xffffu)); e[9] = lds_ld<u16>(ktb + (kt1[0] >> 16)); e[10] = lds_ld<u16>(ktb + (kt1[1] & 0xffffu));
    const u32 k00 = lds_ld<u16>(ktb);
    u32 dw[5], al[5];
#pragma unroll
    for (int k = 0; k < 5; ++k) { dw[k] = e[2 * k] | (e[2 * k + 1] << 16); al[k] = e[2 * k + 1] | (e[2 * k + 2] << 16); }
    const u32 kb = lb + L_KC + ch * 4 * KCP + n * 16;
    lds_st<u32x4>(kb, u32x4{dw[0], dw[1], dw[2], dw[3]});
    lds_st<u32x4>(kb + KCP, u32x4{al[0], al[1], al[2], al[3]});
    lds_st<u32x4>(kb + 2 * KCP, u32x4{dw[1], dw[2], dw[3], dw[4]});
    lds_st<u32x4>(kb + 3 * KCP, u32x4{al[1], al[2], al[3], al[4]});
    if (n < 16) {
      u32x4 z = {0, 0, 0, 0};
      if (n == 0) z.x = k00;
      lds_st<u32x4>(lb + L_KC + (ch * 4 + (n >> 2)) * KCP + 4096 + (n & 3) * 16, z);
    }
    lds_st<u32x4>(lb + L_U + ch * 4608 + 512 + n * 16, uv_);
  }
  bar();
  STAMP(7);
  f32x4 acc[8];
  const int cc = (w >> 1) & 1, hf = w & 1;
  if (w < 4) {
#pragma unroll
    for (int c = 0; c < 8; ++c) acc[c] = z4;
    const int bsh = (-i16) & 3;
    u32 abase = lb + L_KC + (cc * 4 + bsh) * KCP + (2032 + 8 * g - i16 - bsh) * 2 - hf * 4 * 64 - 3 * 64 - 7 * 512;
    u32 bbase = lb + L_U + cc * 4608 + (256 - 16 + 16 * i16 + 8 * g) * 2 - hf * 4 * 64 - 3 * 64;
    u32 abase8 = abase + 8;
    pin(abase); pin(abase8); pin(bbase);
    u32x4 A[8], B[8];
#pragma unroll
    for (int qq = 0; qq < 8; ++qq) {
      B[7 - qq] = lds_ld<u32x4>(bbase + (7 - qq) * 512 + 3 * 64);
      A[qq] = cat(lds_ld<u32x2>(abase + (7 - qq) * 512 + 3 * 64), lds_ld<u32x2>(abase8 + (7 - qq) * 512 + 3 * 64));
      __builtin_amdgcn_sched_barrier(0);
    }
#pragma unroll
    for (int rr = 0; rr < 4; ++rr) {
#pragma unroll
      for (int qq = 0; qq < 8; ++qq) {
#pragma unroll
        for (int m = 7 - qq; m >= 0; --m) {
          acc[qq + m] = mfma32(A[qq], B[m], acc[qq + m]);
          if (CNOP >= 0 && rr < CNOPR) asm volatile("s_nop %c0" :: "i"(CNOP >= 0 ? CNOP : 0));
        }
        if (rr < 3) {
          A[qq] = cat(lds_ld<u32x2>(abase + (7 - qq) * 512 + (2 - rr) * 64), lds_ld<u32x2>(abase8 + (7 - qq) * 512 + (2 - rr) * 64));
          B[7 - qq] = lds_ld<u32x4>(bbase + (7 - qq) * 512 + (2 - rr) * 64);
        }
        __builtin_amdgcn_sched_barrier(0);
      }
    }
    if (hf) {
#pragma unroll
      for (int c = 0; c < 4; ++c) lds_st<f32x4>(lb + L_RED + (w * 4 + c) * 1024 + lane * 16, acc[c]);
#pragma unroll
      for (int c = 0; c < 4; ++c) acc[c] = acc[4 + c];
    } else {
#pragma unroll
      for (int c = 0; c < 4; ++c) lds_st<f32x4>(lb + L_RED + (w * 4 + c) * 1024 + lane * 16, acc[4 + c]);
    }
  }
  bar();
  STAMP(8);
  if (w < 4) {
    const float dk = bc<float>(dsk << 16);
    const u32 tb = (1024 * hf + 16 * i16 + 4 * g) * 2;
    f32x4 o[4];
    u32x2 uv[4];
#pragma unroll
    for (int c = 0; c < 4; ++c) {
      o[c] = lds_ld<f32x4>(lb + L_RED + ((w ^ 1) * 4 + c) * 1024 + lane * 16);
      uv[c] = lds_ld<u32x2>(lb + L_U + cc * 4608 + 512 + tb + c * 512);
    }
#pragma unroll
    for (int c = 0; c < 4; ++c) {
      const f32x4 y = acc[c] + o[c];
      const f32x2 b01 = up2(pk(y[0], y[1])) + up2(pk2(up2(uv[c].x) * dk));
      const f32x2 b23 = up2(pk(y[2], y[3])) + up2(pk2(up2(uv[c].y) * dk));
      lds_st<u32x2>(lb + L_BO + cc * 4096 + tb + c * 512, u32x2{pk2(b01), pk2(b23)});
    }
  }
  bar();
  STAMP(9);
  {
    const int st = w & 3, s2 = 16 * st + i16;
    const u32 a = lb + L_BO + ch * 4096 + (8 * g + (i16 >> 2)) * 128 + (16 * st + 4 * (i16 & 3)) * 2;
    st16<P>(rws, (u32)((((s2 & 7) * 8 + (s2 >> 3)) * 32 + 8 * g) * 2), (u32)WS_B + (d + ch) * B_D, cat(trread(a), trread(a + 4 * 128)));
  }
  STAMP(10);
  if (P) { drain_vm(); bar(); publish(ctl + CT_FB, j, epoch); }
  STAMP(11);
}

template <int PH>
__global__ __launch_bounds__(NT, 4) void gfs4_kernel(Params p) {
  extern __shared__ __attribute__((aligned(16))) char smem[];
  const u32 lb = (u32)(size_t)(LAS char*)smem;
  const int b = blockIdx.x;
  u32* ctl = (u32*)(p.ws + WS_CTL);
  STAMP_DECL;
  if (PH == 0 || PH == 1) STAMP(0); else if (PH == 2) STAMP(20); else STAMP(21);
  u32 gen = 0;
  asm volatile("" :: "s"(p.x), "s"(p.wr), "s"(p.wi), "s"(p.are), "s"(p.aim), "s"(p.bre), "s"(p.bim));
  asm volatile("" :: "s"(p.cre), "s"(p.cim), "s"(p.logdt), "s"(p.dskip), "s"(p.gw), "s"(p.out), "s"(p.ws));
  SsmLoads L;
  if (PH == 0) __builtin_amdgcn_s_setprio(3);
  if (__builtin_expect_with_probability(b < N_FFT, 1, PFFT)) {
    if (__builtin_expect(threadIdx.x < 256, 1)) { if (PH == 0 || PH == 1) fft_first_loads(p, lb, b); }
  } else {
    ssm_first_loads<PH>(p, b - N_FFT, L);
  }
  if (PH == 0) gen = __hip_atomic_load((GAS u32*)ctl + CT_GEN, RLX_AGENT);
  __builtin_amdgcn_sched_barrier(0);
  if (b < N_FFT) {
    u32x4 fc[2], fs[2];
    P3C c3;
    if (threadIdx.x >= 256) {
      if (PH == 1 || PH == 2) return;
      fft_upper<PH>(p, lb, b, ctl, fc, fs, c3);
    } else fft_role<PH>(p, lb, b, ctl, gen, fc, fs, c3 STAMP_PASS);
    if (PH == 0 || PH == 3) fft_phase3<PH>(p, lb, b, ctl, __builtin_amdgcn_readfirstlane(gen) + 1u, fc, fs, c3 STAMP_PASS);
  }
  else ssm_role<PH>(p, lb, b - N_FFT, ctl, gen, L STAMP_PASS);
  STAMP_FLUSH();
}

extern "C" void kernel_launch(void* const* d_in, const int* in_sizes, int n_in,
                              void* d_out, int out_size, void* d_ws, size_t ws_size,
                              hipStream_t stream) {
  (void)in_sizes; (void)n_in; (void)out_size; (void)ws_size;
  Params p;
  p.x = (const u16*)d_in[0]; p.wr = (const u16*)d_in[1]; p.wi = (const u16*)d_in[2];
  p.are = (const u16*)d_in[4]; p.aim = (const u16*)d_in[5]; p.bre = (const u16*)d_in[6]; p.bim = (const u16*)d_in[7];
  p.cre = (const u16*)d_in[8]; p.cim = (const u16*)d_in[9]; p.logdt = (const u16*)d_in[10]; p.dskip = (const u16*)d_in[11];
  p.gw = (const u16*)d_in[12];
  p.out = (u16*)d_out; p.ws = (char*)d_ws;
  (void)hipFuncSetAttribute((const void*)gfs4_kernel<0>, hipFuncAttributeMaxDynamicSharedMemorySize, LDS_BYTES);
  gfs4_kernel<0><<<N_WG, NT, LDS_BYTES, stream>>>(p);
}
```

```cpp
#include <hip/hip_runtime.h>
#include <hip/hip_bf16.h>
#include <stdint.h>

typedef unsigned int u32;
typedef unsigned short u16;
typedef unsigned long long u64;
typedef float f32x4 __attribute__((ext_vector_type(4)));
typedef float f32x2 __attribute__((ext_vector_type(2)));
typedef u32 u32x4 __attribute__((ext_vector_type(4)));
typedef u32 u32x2 __attribute__((ext_vector_type(2)));
typedef __bf16 bf16x8 __attribute__((ext_vector_type(8)));
typedef __bf16 bf16x2 __attribute__((ext_vector_type(2)));
typedef short s16x4 __attribute__((ext_vector_type(4)));
#define LAS __attribute__((address_space(3)))
#define GAS __attribute__((address_space(1)))
#define DI __device__ __forceinline__

#define DIAG 10
#define DK 8
#define DR 0
#define NLAUNCH 1

constexpr int S_ = 2048, D_ = 448, DF = 225, NS = 256;
constexpr int N_FFT = 256, N_SSM = 224, N_WG = N_FFT + N_SSM, NT = 512;

constexpr u32 T_K1 = 65536 + 256;
constexpr u32 TP_S2 = 32768 + 128;
constexpr u32 XT_D = 4096 + 128;
constexpr u32 B_D = 4096 + 128;
constexpr size_t WS_CTL = 0;
constexpr size_t WS_T   = 1u << 20;
constexpr size_t WS_TP  = WS_T + (3u << 20);
constexpr size_t WS_XT  = WS_TP + (3u << 20);
constexpr size_t WS_B   = WS_XT + (2u << 20);
constexpr int FL = 32, FR_ = 256 * FL + 64;
constexpr int CT_GEN = 0;
constexpr int CT_F1 = 64;
constexpr int CT_F2 = CT_F1 + 8 * FR_;
constexpr int CT_FB = CT_F2 + 8 * FR_;
constexpr int CT_FX = CT_FB + 8 * FR_;
static_assert((CT_FX + 7 * 32 * FL) * 4 <= (1 << 20), "control block");

constexpr int XRS = 928;
constexpr int L_XT = 0;
constexpr int L_XCH = 29696;
constexpr int TPRS = 1040;
constexpr int L_TP = 0;
constexpr int L_TO = 29696;
constexpr int L_TI = 38912;
constexpr int L_BI = 38912;
constexpr int BRS = 912;
constexpr int L_Z = 47104;
constexpr int L_C1 = 38912;
constexpr int L_C2 = 65536;
constexpr int L_C3 = 70400;
constexpr int L_W = 47104;
constexpr int L_G = 70208;
constexpr int KCP = 4160;
constexpr int L_KC = 0;
constexpr int KSCR = 4096;
constexpr int L_RED = 33280;
constexpr int L_BO = 49664;
constexpr int L_U = 57856;
constexpr int L_XP = 57856;
constexpr int L_KT = 67072;
constexpr int L_CNT = 70272;
constexpr int LDS_BYTES = 72448;

struct Params {
  const u16* x; const u16* wr; const u16* wi;
  const u16* are; const u16* aim; const u16* bre; const u16* bim; const u16* cre; const u16* cim;
  const u16* logdt; const u16* dskip; const u16* gw;
  u16* out; char* ws;
};

constexpr double cx_sin_oct(double x) { double x2 = x * x, t = x, s = x; for (int i = 1; i < 9; ++i) { t *= -x2 / ((2 * i) * (2 * i + 1)); s += t; } return s; }
constexpr double cx_cos_oct(double x) { double x2 = x * x, t = 1, s = 1; for (int i = 1; i < 9; ++i) { t *= -x2 / ((2 * i - 1) * (2 * i)); s += t; } return s; }
constexpr double cx_sin(long n, long d) {
  n %= d; if (n < 0) n += d;
  const long o = (8 * n) / d;
  const double r = 6.283185307179586476925 * (double)(8 * n - o * d) / (8.0 * (double)d), q = 0.78539816339744830962;
  switch (o) {
    case 0: return cx_sin_oct(r);       case 1: return cx_cos_oct(q - r);
    case 2: return cx_cos_oct(r);       case 3: return cx_sin_oct(q - r);
    case 4: return -cx_sin_oct(r);      case 5: return -cx_cos_oct(q - r);
    case 6: return -cx_cos_oct(r);      default: return -cx_sin_oct(q - r);
  }
}
constexpr double cx_cos(long n, long d) { return cx_sin(4 * n + d, 4 * d); }
constexpr u32 cx_f32(double v) { return __builtin_bit_cast(u32, (float)v); }
constexpr u32 cx_bf(double v) { const u32 u = cx_f32(v); return (u + 0x7fffu + ((u >> 16) & 1u)) >> 16; }
constexpr int cx_pi(int g, int jj) { return 16 * (jj >> 2) + 4 * g + (jj & 3); }
struct Frag { u32 v[64][4]; };
enum { TF_FC0, TF_FS0, TF_CO0, TF_SO0, TF_COT0, TF_SOT0, TF_TC, TF_TS, TF_M, TF_Q0, TF_N = TF_Q0 + 24 };
constexpr Frag make_frag(int item) {
  Frag f{};
  for (int lane = 0; lane < 64; ++lane) {
    const int i16 = lane & 15, g = lane >> 4;
    if (item < TF_TC) {
      const int sn = item & 1;
      for (int jj = 0; jj < 8; ++jj) {
        const int m = cx_pi(g, jj), a = i16;
        long n = 0, d = 32;
        if (item < TF_CO0) { n = a * m; d = 32; }
        else if (item < TF_COT0) { n = a * (2 * m + 1); d = 64; }
        else { n = m * (2 * a + 1); d = 64; }
        f.v[lane][jj >> 1] |= cx_bf(sn ? cx_sin(n, d) : cx_cos(n, d)) << (16 * (jj & 1));
      }
    } else if (item == TF_TC || item == TF_TS) {
      for (int j = 0; j < 4; ++j) {
        const long n = (2 * g + (j & 1)) * (16 * (j >> 1) + i16);
        f.v[lane][j] = cx_f32(item == TF_TC ? cx_cos(n, 448) : cx_sin(n, 448));
      }
    } else if (item == TF_M) {
      for (int j = 0; j < 4; ++j) {
        const int k0 = 2 * g + (j >> 1);
        const double cf = ((k0 == 0 || k0 == 7) ? 1.0 : 2.0) / (2048.0 * 448.0);
        const double v = i16 < 14 ? cf * ((j & 1) ? -cx_sin(k0 * i16, 14) : cx_cos(k0 * i16, 14)) : 0.0;
        f.v[lane][j >> 1] |= cx_bf(v) << (16 * (j & 1));
      }
    } else {
      const int q = (item - TF_Q0) / 6, sub = (item - TF_Q0) % 6, k0 = 2 * q + ((i16 >> 1) & 1), ri = i16 & 1;
      if (sub < 2) {
        if ((i16 >> 2) == 2 * sub + (g & 1))
          for (int j = 0; j < 8; ++j) {
            const int d0 = 8 * (g >> 1) + j;
            const double v = d0 < 14 ? (ri ? -cx_sin(d0 * k0, 14) : cx_cos(d0 * k0, 14)) : 0.0;
            f.v[lane][j >> 1] |= cx_bf(v) << (16 * (j & 1));
          }
      } else {
        for (int j = 0; j < 4; ++j) {
          const long n = cx_pi(g, 4 * (sub & 1) + j) * k0;
          f.v[lane][j] = cx_f32(sub < 4 ? cx_cos(n, 448) : (ri ? -cx_sin(n, 448) : cx_sin(n, 448)));
        }
      }
    }
  }
  return f;
}
template <int I> struct FragC { static constexpr Frag f = make_frag(I); };
#define FR(i) FragC<(i)>::f
#define FR6(i) FR(i), FR(i + 1), FR(i + 2), FR(i + 3), FR(i + 4), FR(i + 5)
__device__ const __attribute__((aligned(1024))) Frag TF[TF_N] = {FR6(0), FR(6), FR(7), FR(8), FR6(9), FR6(15), FR6(21), FR6(27)};

struct KTab { u16 v[2080]; };
constexpr KTab make_ktab() {
  KTab t{};
  for (int i = 0; i < 2080; ++i) {
    const int l = 2048 - i;
    int idx = 768;
    if (l >= 0) {
      const int sh = l > 256 ? (l >= 1024 ? 3 : (l >= 512 ? 2 : 1)) : 0;
      const int v = sh ? (((l + (1 << (sh - 1)) - 1 + ((l >> sh) & 1)) >> sh) << sh) : l;
      if (v < 256) idx = ((v & 7) + 8 * (v >> 7)) * 16 + ((v >> 3) & 15);
      else if (v < 512) idx = 256 + ((((v - 256) >> 1) & 3) + 4 * ((v - 256) >> 7)) * 16 + (((v - 256) >> 3) & 15);
      else if (v < 1024) idx = 256 + (8 + (((v - 512) >> 2) & 1) + 2 * ((v - 512) >> 7)) * 16 + (((v - 512) >> 3) & 15);
      else if (v < 2048) idx = 512 + ((v - 1024) >> 7) * 16 + (((v - 1024) >> 3) & 15);
      else idx = 640;
    }
    t.v[i] = (u16)(2 * idx);
  }
  return t;
}
__device__ const __attribute__((aligned(16))) KTab KTAB = make_ktab();

template <class T, class F> DI T bc(F v) { return __builtin_bit_cast(T, v); }
DI f32x4 mfma32(u32x4 a, u32x4 b, f32x4 c) { return __builtin_amdgcn_mfma_f32_16x16x32_bf16(bc<bf16x8>(a), bc<bf16x8>(b), c, 0, 0, 0); }
DI f32x4 mfma16(u32x2 a, u32x2 b, f32x4 c) { return __builtin_amdgcn_mfma_f32_16x16x16bf16_1k(bc<s16x4>(a), bc<s16x4>(b), c, 0, 0, 0); }
DI u32 pk(float a, float b) { f32x2 v = {a, b}; return bc<u32>(__builtin_convertvector(v, bf16x2)); }
DI u32 pk2(f32x2 v) { return bc<u32>(__builtin_convertvector(v, bf16x2)); }
DI float bfr(float a) { return bc<float>(pk(0.f, a) & 0xffff0000u); }
DI float lo16(u32 v) { return bc<float>(v << 16); }
DI float hi16(u32 v) { return bc<float>(v & 0xffff0000u); }
DI f32x2 up2(u32 v) { return f32x2{lo16(v), hi16(v)}; }
DI float b2f(u16 v) { return bc<float>((u32)v << 16); }
DI u32x2 trread(u32 a) { return bc<u32x2>(__builtin_amdgcn_ds_read_tr16_b64_v4i16((LAS s16x4*)(size_t)a)); }
DI u32x4 cat(u32x2 a, u32x2 b) { u32x4 r = {a.x, a.y, b.x, b.y}; return r; }
DI float sinr(float x) { return __builtin_amdgcn_sinf(x); }
DI float cosr(float x) { return __builtin_amdgcn_cosf(x); }
DI float fx1(float v) { return __builtin_amdgcn_update_dpp(v, v, 0xB1, 0xf, 0xf, true); }
DI float fr8(float v) { return __builtin_amdgcn_update_dpp(v, v, 0x128, 0xf, 0xf, true); }
DI u32 ux1(u32 v) { return (u32)__builtin_amdgcn_update_dpp((int)v, (int)v, 0xB1, 0xf, 0xf, true); }
DI u32 ur8(u32 v) { return (u32)__builtin_amdgcn_update_dpp((int)v, (int)v, 0x128, 0xf, 0xf, true); }
DI u32x4 sx1(u32x4 v, u32 m) { u32x4 r = {ux1(v.x) ^ m, ux1(v.y) ^ m, ux1(v.z) ^ m, ux1(v.w) ^ m}; return r; }
DI u32x4 sr8(u32x4 v, u32 m) { u32x4 r = {ur8(v.x) ^ m, ur8(v.y) ^ m, ur8(v.z) ^ m, ur8(v.w) ^ m}; return r; }
DI u32x4 xr(u32x4 v, u32 m) { u32x4 r = {v.x ^ m, v.y ^ m, v.z ^ m, v.w ^ m}; return r; }
DI u32x4 pk8(const float (&v)[8]) { return u32x4{pk(v[0], v[1]), pk(v[2], v[3]), pk(v[4], v[5]), pk(v[6], v[7])}; }
template <class T> DI T lds_ld(u32 a) { return *(LAS T*)(size_t)a; }
template <class T> DI void lds_st(u32 a, T v) { *(LAS T*)(size_t)a = v; }
DI void wait_vm0() { __builtin_amdgcn_s_waitcnt(0x0F70); }
DI void drain_vm() { asm volatile("s_waitcnt vmcnt(0)" ::: "memory"); }
DI void bar() { __builtin_amdgcn_s_waitcnt(0xC07F); asm volatile("s_barrier" ::: "memory"); }
template <class T> DI void pin(T& v) { asm volatile("" : "+v"(v)); }
typedef __amdgpu_buffer_rsrc_t rsrc_t;
DI rsrc_t mkrs(const void* p, u32 bytes) { return __builtin_amdgcn_make_buffer_rsrc((void*)p, 0, bytes, 0x00020000); }
template <int AUX> DI void dma16(rsrc_t rs, u32 lds, u32 voff, u32 soff) { __builtin_amdgcn_raw_ptr_buffer_load_lds(rs, (LAS void*)(size_t)lds, 16, voff, soff, 0, AUX); }
DI f32x2 cmul(f32x2 x, f32x2 y) {
  f32x2 t;
  asm("v_pk_mul_f32 %0, %1, %2 op_sel:[0,0] op_sel_hi:[0,1]\n\tv_pk_fma_f32 %0, %1, %2, %0 op_sel:[1,1,0] op_sel_hi:[1,0,1] neg_lo:[1,0,0]" : "=&v"(t) : "v"(x), "v"(y));
  return t;
}
DI float dot2b(u32 a, u32 b, float c) { return __builtin_amdgcn_fdot2_f32_bf16(bc<bf16x2>(a), bc<bf16x2>(b), c, false); }
DI float wave_sum(float a) {
  a += __builtin_amdgcn_update_dpp(0.f, a, 0xB1, 0xf, 0xf, true);
  a += __builtin_amdgcn_update_dpp(0.f, a, 0x4E, 0xf, 0xf, true);
  a += __builtin_amdgcn_update_dpp(0.f, a, 0x141, 0xf, 0xf, true);
  a += __builtin_amdgcn_update_dpp(0.f, a, 0x140, 0xf, 0xf, true);
  a += __builtin_amdgcn_update_dpp(0.f, a, 0x142, 0xa, 0xf, false);
  a += __builtin_amdgcn_update_dpp(0.f, a, 0x143, 0xc, 0xf, false);
  return bc<float>(__builtin_amdgcn_readlane(bc<int>(a), 63));
}
DI int pi_k(int g, int jj) { return 16 * (jj >> 2) + 4 * g + (jj & 3); }

#define CNOP 1
#define CNOPR 2
#define PFFT 0.6
#define SH2PRIO 2
#define SLP 2
#define RLX_AGENT __ATOMIC_RELAXED, __HIP_MEMORY_SCOPE_AGENT
template <bool P> DI void st16(rsrc_t rs, u32 voff, u32 soff, u32x4 v) {
  __builtin_amdgcn_raw_buffer_store_b128(v, rs, voff, soff, P ? 16 : 0);
  asm volatile("s_nop 1" :: "v"(v) : "memory");
}
template <bool P> DI u32x4 ld16(rsrc_t rs, u32 voff, u32 soff) { return __builtin_amdgcn_raw_buffer_load_b128(rs, voff, soff, P ? 16 : 0); }
DI void publish(u32* set, int idx, u32 epoch) {
  if (threadIdx.x < 8) __hip_atomic_store((GAS u32*)set + threadIdx.x * FR_ + idx * FL, epoch, RLX_AGENT);
}
DI void poll_flags(const u32* first, int n, u32 epoch) {
  const int lane = threadIdx.x & 63;
  const GAS u32* f = (const GAS u32*)first + (lane < n ? lane : 0) * FL;
  for (int spin = 0; spin < (1 << 18); ++spin) {
    if (__all(__hip_atomic_load(f, RLX_AGENT) == epoch)) break;
    __builtin_amdgcn_s_sleep(SLP);
  }
}

#define STAMP(i) do { if ((i) == DK && (blockIdx.x >= N_FFT) == (DR == 1) && threadIdx.x == 0) __hip_atomic_store((GAS u32*)(p.ws + (12u << 20)) + 1024 + blockIdx.x, (u32)__builtin_amdgcn_s_memrealtime(), RLX_AGENT); } while (0)
#define STAMP_DECL do {} while (0)
#define STAMP_FLUSH() do {} while (0)
#define STAMP_ARG
#define STAMP_PASS

DI u32x4 tf(rsrc_t rs, int item, u32 l16) { return __builtin_amdgcn_raw_buffer_load_b128(rs, l16 + (item & 3) * 1024, (item >> 2) * 4096, 0); }
DI u32x4 tfs(rsrc_t rs, int item, u32 l16, u32 so) { return __builtin_amdgcn_raw_buffer_load_b128(rs, l16 + item * 1024, so, 0); }

DI void fft_first_loads(const Params& p, const u32 lb, const int b) {
  const int lane = threadIdx.x & 63, w = __builtin_amdgcn_readfirstlane(threadIdx.x >> 6);
  const u32 l16 = lane * 16;
  const int s2 = b & 63, q = b >> 6;
  const rsrc_t rx = mkrs(p.x, S_ * D_ * 2), rtf = mkrs(TF, TF_N * 1024);
  if (lane < 56) {
    u32 la = lb + L_XT + 8 * w * XRS, so = (64 * 8 * w + s2) * (D_ * 2);
#pragma unroll 1
    for (int r = 0; r < 8; ++r) { dma16<0>(rx, la, l16, so); la += XRS; so += 64 * (D_ * 2); }
  }
  const u32 tq = (TF_Q0 + 6 * q) * 1024;
  dma16<0>(rtf, lb + L_C1 + w * 2048, l16, w == 0 ? 0u : tq + (2 * w - 2) * 1024);
  dma16<0>(rtf, lb + L_C1 + w * 2048 + 1024, l16, w == 0 ? 1024u : tq + (2 * w - 1) * 1024);
}

struct P3C { u32x4 b1, b2; f32x4 tc, ts; u32x2 m; };
template <int PH>
DI void p3_prepare(const Params& p, const u32 lb, const int b, const rsrc_t rtf, P3C& c) {
  const int lane = threadIdx.x & 63, i16 = lane & 15, g = lane >> 4;
  const u32 l16 = lane * 16;
  const int s2 = b & 63, q = b >> 6;
  c.tc = bc<f32x4>(tf(rtf, TF_TC, l16)); c.ts = bc<f32x4>(tf(rtf, TF_TS, l16));
  const u32x4 m4 = tf(rtf, TF_M, l16);
  c.m = u32x2{m4.x, m4.y};
  c.b1 = lds_ld<u32x4>(lb + L_C3 + l16); c.b2 = lds_ld<u32x4>(lb + L_C3 + 1024 + l16);
  pin(c.b1); pin(c.b2); pin(c.tc); pin(c.ts); pin(c.m);
}
DI void p3_make_b(const u32 lb, const int b) {
  const int lane = threadIdx.x & 63, i16 = lane & 15, g = lane >> 4;
  const int s2 = b & 63, q = b >> 6;
  const int ro = i16 >> 3;
  const float a = (float)(64 * (8 * q + (i16 & 7)) + s2) * (1.0f / 2048.0f), P0 = a * (float)(4 * g);
  float u[8], v[8];
#pragma unroll
  for (int jj = 0; jj < 8; ++jj) {
    const float ph = __builtin_fmaf(a, (float)(16 * (jj >> 2) + (jj & 3)), P0);
    u[jj] = sinr(ph + (ro ? 0.f : 0.25f));
    v[jj] = sinr(ph + (ro ? 0.25f : 0.5f));
  }
  lds_st<u32x4>(lb + L_C3 + lane * 16, pk8(u));
  lds_st<u32x4>(lb + L_C3 + 1024 + lane * 16, pk8(v));
}
template <bool LDSX>
DI void gate_rows(const Params& p, const u32 lb, const int b, const int r0, const int nr) {
  const int lane = threadIdx.x & 63;
  const u32 l16 = lane * 16;
  const int s2 = b & 63, q = b >> 6;
  u32x4 gwv = {0, 0, 0, 0};
  if (lane < 56) gwv = *(const GAS u32x4*)((const char*)p.gw + l16);
#pragma unroll
  for (int r = 0; r < 2; ++r) {
    if (r < nr) {
      float acc = 0.f;
      if (lane < 56) {
        u32x4 xv;
        if (LDSX) xv = lds_ld<u32x4>(lb + L_XT + (8 * q + r0 + r) * XRS + l16);
        else xv = *(const GAS u32x4*)((const char*)(p.x + (size_t)(64 * (8 * q + r0 + r) + s2) * D_) + l16);
#pragma unroll
        for (int e = 0; e < 4; ++e) acc = dot2b(pk2(up2(xv[e]) * up2(gwv[e])), 0x3f803f80u, acc);
      }
      const float gg = bfr(__builtin_amdgcn_rcpf(1.0f + __expf(-bfr(wave_sum(acc)))));
      if (lane == 0) lds_st<f32x2>(lb + L_G + (r0 + r) * 8, f32x2{gg, bfr(1.0f - gg)});
    }
  }
}

template <int PH>
DI void fft_phase3(const Params& p, const u32 lb, const int b, u32* ctl, const u32 epoch, const u32x4 (&fc)[2], const u32x4 (&fs)[2], const P3C& c STAMP_ARG) {
  constexpr bool P = PH == 0;
  constexpr int TPAUX = P ? 16 : 0;
  const int tid = threadIdx.x, lane = tid & 63, w = __builtin_amdgcn_readfirstlane(tid >> 6), i16 = lane & 15, g = lane >> 4;
  const u32 l16 = lane * 16;
  const int s2 = b & 63, q = b >> 6;
  const f32x4 z4 = {0, 0, 0, 0};
  const rsrc_t rws = mkrs(p.ws, 16u << 20);
  u32 tpa = lb + L_TP + (4 * g + (i16 >> 2)) * TPRS + (i16 & 3) * 128 + (w >> 1) * 32 + (w & 1) * 8;
  const int row = 2 * w + (i16 >> 3);
  u32 zw = lb + L_Z + ((i16 & 7) * 16 + row) * 64 + ((g ^ (row >> 2)) & 3) * 16;
  u32 zr = lb + L_Z + (w * 16 + i16) * 64 + ((g ^ (i16 >> 2)) & 3) * 16;
  u32 boff = (u32)(tid * B_D);
  u32 biw = lb + L_BI + tid * 2, bir = lb + L_BI + w * BRS + (32 * i16 + 4 * g) * 2;
  u32 ooff = (u32)((64 * (8 * q + w) + s2) * (D_ * 2) + (32 * i16 + 4 * g) * 2);
  u32 mi = (i16 & 1) ? 0u : 0x80008000u;
  pin(tpa); pin(zw); pin(zr); pin(boff); pin(biw); pin(bir); pin(ooff); pin(mi);
  STAMP(13);
  if (P) { poll_flags(ctl + CT_F2 + (b & 7) * FR_ + w * 32 * FL, 32, epoch); bar(); __builtin_amdgcn_s_setprio(3); }
  STAMP(14);
#pragma unroll
  for (int r = 0; r < 4; ++r) dma16<TPAUX>(rws, lb + L_TP + (4 * w + r) * TPRS, l16, (u32)WS_TP + s2 * TP_S2 + (4 * w + r) * 1024);
  wait_vm0();
  bar();
  STAMP(15);
#pragma unroll
  for (int t = 0; t < 2; ++t) {
    u32x4 Tr = cat(trread(tpa + t * 512), trread(tpa + t * 512 + 16 * TPRS));
    u32x4 Ti = cat(trread(tpa + t * 512 + 16), trread(tpa + t * 512 + 16 + 16 * TPRS));
    f32x4 z = mfma32(Ti, c.b2, mfma32(Tr, c.b1, z4));
    lds_st<u32x2>(zw + t * 8, u32x2{pk(z[0], z[1]), pk(z[2], z[3])});
  }
  bar();
  STAMP(16);
  f32x2 ga[2][2];
  {
    u32x4 A = lds_ld<u32x4>(zr), A2 = sx1(A, mi);
    const float gg = lds_ld<float>(lb + L_G + w * 8);
#pragma unroll
    for (int td = 0; td < 2; ++td) {
      f32x4 G = mfma32(A2, fs[td], mfma32(A, fc[td], z4));
      const float c0 = c.tc[2 * td], s0 = c.ts[2 * td], c1 = c.tc[2 * td + 1], s1 = c.ts[2 * td + 1];
      const float g0r = G[0] * c0 - G[1] * s0, g0i = G[1] * c0 + G[0] * s0;
      const float g1r = G[2] * c1 - G[3] * s1, g1i = G[3] * c1 + G[2] * s1;
      f32x4 y = mfma16(u32x2{pk(g0r, g0i), pk(g1r, g1i)}, c.m, z4);
      ga[td][0] = up2(pk2(up2(pk(y[0], y[1])) * gg));
      ga[td][1] = up2(pk2(up2(pk(y[2], y[3])) * gg));
      pin(ga[td][0]); pin(ga[td][1]);
    }
  }
  STAMP(17);
  if (P) { poll_flags(ctl + CT_FB + (b & 7) * FR_ + w * 28 * FL, 28, epoch); bar(); }
  STAMP(18);
  if (tid < D_) {
    const u32x4 bv = ld16<P>(rws, boff, (u32)WS_B + (((s2 & 7) * 8 + (s2 >> 3)) * 32 + 8 * q) * 2);
#pragma unroll
    for (int e = 0; e < 4; ++e) {
      lds_st<u16>(biw + (2 * e) * BRS, (u16)bv[e]);
      lds_st<u16>(biw + (2 * e + 1) * BRS, (u16)(bv[e] >> 16));
    }
  }
  bar();
  {
    const rsrc_t ro_ = mkrs(p.out, S_ * D_ * 2);
    const float og = lds_ld<float>(lb + L_G + w * 8 + 4);
    const u32x2 bq0 = lds_ld<u32x2>(bir), bq1 = lds_ld<u32x2>(bir + 32);
#pragma unroll
    for (int td = 0; td < 2; ++td) {
      const u32x2 bq = td ? bq1 : bq0;
      const f32x2 o01 = ga[td][0] + up2(pk2(up2(bq.x) * og));
      const f32x2 o23 = ga[td][1] + up2(pk2(up2(bq.y) * og));
      if (i16 < 14) __builtin_amdgcn_raw_buffer_store_b64(u32x2{pk2(o01), pk2(o23)}, ro_, ooff, td * 32, 0);
    }
  }
  STAMP(19);
  if (P && b == 0 && tid == 0) __hip_atomic_store((GAS u32*)ctl + CT_GEN, epoch, RLX_AGENT);
}

template <int PH>
DI void fft_upper(const Params& p, const u32 lb, const int b, u32* ctl, u32x4 (&fc)[2], u32x4 (&fs)[2], P3C& c3) {
  constexpr bool P = PH == 0;
  const int lane = threadIdx.x & 63, w = __builtin_amdgcn_readfirstlane(threadIdx.x >> 6);
  const u32 l16 = lane * 16;
  const rsrc_t rtf = mkrs(TF, TF_N * 1024);
  if (w == 4) p3_make_b(lb, b);
  if (P) {
    bar(); bar(); bar();
    bar(); bar(); bar();
    __builtin_amdgcn_s_setprio(SH2PRIO);
  }
  if (P) gate_rows<P>(p, lb, b, w, 1); else gate_rows<P>(p, lb, b, 2 * (w - 4), 2);
  fc[0] = tf(rtf, TF_FC0, l16); fs[0] = tf(rtf, TF_FS0, l16);
  fc[1] = xr(fc[0], 0x80000000u); fs[1] = xr(fs[0], 0x80000000u);
  pin(fc[0]); pin(fc[1]); pin(fs[0]); pin(fs[1]);
  if (!P) bar();
  p3_prepare<PH>(p, lb, b, rtf, c3);
  if (!P) bar();
}

template <int PH>
DI void fft_role(const Params& p, const u32 lb, const int b, u32* ctl, const u32 gen, u32x4 (&fc)[2], u32x4 (&fs)[2], P3C& c3 STAMP_ARG) {
  constexpr bool P = PH == 0;
#define TPAUXV 16
  constexpr int AUX = P ? 16 : 0, TPAUX = P ? TPAUXV : 0;
  (void)AUX;
  const int tid = threadIdx.x, lane = tid & 63, w = __builtin_amdgcn_readfirstlane(tid >> 6), i16 = lane & 15, g = lane >> 4;
  const u32 l16 = lane * 16;
  const int s2 = b & 63, q = b >> 6;
  const int xx = b & 7, mm = (b >> 3) & 3, kq = b >> 5;
  const int k1w = (((mm & 1) ? (xx ? 32 - xx : 8) : xx) + 16 * (mm >> 1)) & 31;
  const f32x4 z4 = {0, 0, 0, 0};
  const rsrc_t rws = mkrs(p.ws, 16u << 20), rtf = mkrs(TF, TF_N * 1024);
  const u32 tpo = (u32)((tid >> 3) * TP_S2 + (tid & 7) * 16), tps = (u32)WS_TP + k1w * 1024 + kq * 128;
  if (PH == 2 || PH == 3) {
    fc[0] = tf(rtf, TF_FC0, l16); fs[0] = tf(rtf, TF_FS0, l16);
    fc[1] = xr(fc[0], 0x80000000u); fs[1] = xr(fs[0], 0x80000000u);
  }

  u32x4 co[2], so[2], cot[2], sot[2];
  auto load_p2 = [&]() {
    const bool mir = kq >= 4;
    const int col0 = mir ? 448 - (56 * kq + 49) : 56 * kq;
    const rsrc_t rr = mkrs(p.wr, S_ * DF * 2), ri_ = mkrs(p.wi, S_ * DF * 2);
#pragma unroll
    for (int i = 0; i < 3; ++i) {
      const int gi = w + 4 * i;
      if (gi < 9) {
        const int gam = 64 * gi + lane, k2 = (gam * 7282) >> 16, chunk = gam - 9 * k2;
        const int ks = k1w + 32 * k2, row = mir ? ((2048 - ks) & 2047) : ks;
        const u32 vo = (u32)(((row * DF + col0) * 2) & ~15) + chunk * 16;
        dma16<0>(rr, lb + L_W + gi * 1024, vo, 0);
        dma16<0>(ri_, lb + L_W + 9216 + gi * 1024, vo, 0);
      }
    }
    dma16<0>(rtf, lb + L_C2 + w * 1024, l16, (TF_CO0 + w) * 1024);
  };

  if (PH == 0 || PH == 1) {
    u32x4 C1, S1, nS1;
    {
      const float k1 = (float)(16 * (w >> 1) + i16) * (1.0f / 2048.0f), P0 = k1 * (float)(64 * g + s2), Pl = k1 * 256.f;
      float c[8], s[8];
#pragma unroll
      for (int j = 0; j < 8; ++j) { const float ph = __builtin_fmaf(Pl, (float)j, P0); c[j] = cosr(ph); s[j] = sinr(ph); }
      C1 = pk8(c); S1 = pk8(s); nS1 = xr(S1, 0x80008000u);
      pin(C1); pin(S1); pin(nS1);
    }
    int d0a = 8 * (g >> 1) + (i16 >> 2), d0b = d0a + 4;
    d0b = d0b < 14 ? d0b : 0;
    u32 xa = lb + L_XT + (8 * w + (g & 1)) * XRS + 8 * (i16 & 3);
    u32 xb = xa + d0b * 64; xa += d0a * 64;
    u32 xw = lb + L_XCH + w * 256 + lane * 4;
    u32 xr_ = lb + L_XCH + (w & 1) * 4096 + lane * 4;
    u32 toff = (u32)((16 * (w >> 1) + i16) * T_K1 + 4 * (w & 1) * 32 + g * 16);
    u32 imm = (i16 & 1) ? 0x80008000u : 0u;
    pin(xa); pin(xb); pin(xw); pin(xr_); pin(toff); pin(imm);
    const u32 tso = (u32)WS_T + s2 * 1024 + q * 256;
    STAMP(1);
    wait_vm0();
    bar();
    const u32 cb = lb + L_C1 + l16;
    fc[0] = lds_ld<u32x4>(cb); fs[0] = lds_ld<u32x4>(cb + 1024);
    const u32x4 Ba1 = lds_ld<u32x4>(cb + 2048), Ba2 = lds_ld<u32x4>(cb + 3072);
    const f32x4 twc0 = lds_ld<f32x4>(cb + 4096), twc1 = lds_ld<f32x4>(cb + 5120), tws0 = lds_ld<f32x4>(cb + 6144), tws1 = lds_ld<f32x4>(cb + 7168);
    fc[1] = xr(fc[0], 0x80000000u); fs[1] = xr(fs[0], 0x80000000u);
    STAMP(2);
    f32x4 D2[2][2];
#pragma unroll
    for (int sg = 0; sg < 2; ++sg) {
      f32x4 H[2];
#pragma unroll
      for (int td = 0; td < 2; ++td) {
        const int o = td * 32 + sg * 4 * XRS;
        u32x4 A0 = cat(trread(xa + o), trread(xb + o));
        u32x4 A1 = cat(trread(xa + o + 2 * XRS), trread(xb + o + 2 * XRS));
        H[td] = mfma32(A1, Ba2, mfma32(A0, Ba1, z4));
      }
      float h[8];
#pragma unroll
      for (int j = 0; j < 4; ++j) {
        h[j] = __builtin_fmaf(fx1(H[0][j]), tws0[j], H[0][j] * twc0[j]);
        h[4 + j] = __builtin_fmaf(fx1(H[1][j]), tws1[j], H[1][j] * twc1[j]);
      }
      u32x4 A = pk8(h), A2 = sx1(A, imm);
#pragma unroll
      for (int tk = 0; tk < 2; ++tk) D2[sg][tk] = mfma32(A2, fs[tk], mfma32(A, fc[tk], z4));
    }
#pragma unroll
    for (int tk = 0; tk < 2; ++tk)
#pragma unroll
      for (int r = 0; r < 4; ++r) lds_st<u32>(xw + (tk * 4 + r) * 1024, pk(D2[0][tk][r], D2[1][tk][r]));
    bar();
    STAMP(3);
    {
      u32 pc[4][2];
#pragma unroll
      for (int kk = 0; kk < 2; ++kk) {
        const u32 br = xr_ + kk * 2048;
        u32x4 Xr = {lds_ld<u32>(br), lds_ld<u32>(br + 256), lds_ld<u32>(br + 512), lds_ld<u32>(br + 768)};
        u32x4 Xi = {lds_ld<u32>(br + 1024), lds_ld<u32>(br + 1280), lds_ld<u32>(br + 1536), lds_ld<u32>(br + 1792)};
        f32x4 Tr = mfma32(Xi, S1, mfma32(Xr, C1, z4));
        f32x4 Ti = mfma32(Xr, nS1, mfma32(Xi, C1, z4));
        pc[kk][0] = pk(Tr[0], Tr[1]); pc[kk][1] = pk(Tr[2], Tr[3]);
        pc[2 + kk][0] = pk(Ti[0], Ti[1]); pc[2 + kk][1] = pk(Ti[2], Ti[3]);
      }
      u32x4 lo = {pc[0][0], pc[0][1], pc[1][0], pc[1][1]}, hi = {pc[2][0], pc[2][1], pc[3][0], pc[3][1]};
#pragma unroll
      for (int e = 0; e < 4; ++e) {
        auto r1 = __builtin_amdgcn_permlane16_swap(lo[e], hi[e], false, false);
        auto r2 = __builtin_amdgcn_permlane32_swap(r1[0], r1[1], false, false);
        lo[e] = r2[0]; hi[e] = r2[1];
      }
      st16<P>(rws, toff, tso, lo);
      st16<P>(rws, toff + 64, tso, hi);
    }
    STAMP(4);
    if (P) {
      drain_vm();
      STAMP(20);
      bar();
      publish(ctl + CT_F1, b, gen + 1u);
      __builtin_amdgcn_s_setprio(2);
      load_p2();
    }
    STAMP(5);
  }
  const u32 epoch = __builtin_amdgcn_readfirstlane(gen) + 1u;

  if (PH == 2) load_p2();
  if (PH == 0 || PH == 2) {
    const int ri = i16 >> 3;
    u32 tsv = (u32)((lane >> 1) * 1024 + w * 256 + (lane & 1) * 16);
    u32 tiw = lb + L_TI + w * 2048 + ((lane >> 1) & 1) * 1024 + (lane >> 2) * 32 + (lane & 1) * 16;
    u32 ta = lb + L_TI + w * 2048 + (4 * g + (i16 >> 2)) * 32 + (i16 & 3) * 8;
    u32 tow = lb + L_TO + 2 * i16 * 144 + w * 32 + g * 8;
    u32 tor = lb + L_TO + (tid >> 3) * 144 + (tid & 7) * 16;
    u32 mf = ri ? 0x80008000u : 0u, mi = ri ? 0u : 0x80008000u;
    float wr_[16], wi_[16];
    {
      const bool mir = kq >= 4;
      const int c = i16 & 7, kd = 2 * w + (c >> 2) + 14 * (4 * kq + (c & 3)), col0 = mir ? 448 - (56 * kq + 49) : 56 * kq;
      const int row0 = mir ? ((2048 - k1w) & 2047) : k1w;
      u32 wa = lb + L_W + 4 * g * 144 + (((row0 * DF + col0) * 2) & 15) + ((mir ? 448 - kd : kd) - col0) * 2;
      const u32 sg = (mir != (ri != 0)) ? 0u : 0x80000000u;
      pin(tsv); pin(tiw); pin(ta); pin(tow); pin(tor); pin(mf); pin(mi); pin(wa);
      STAMP(7);
      if (P) poll_flags(ctl + CT_F1 + (b & 7) * FR_ + w * 64 * FL, 64, epoch);
      wait_vm0();
      bar();
      const u32 so_ = (u32)WS_T + k1w * T_K1 + kq * 32;
      const u32x4 t0 = ld16<P>(rws, tsv, so_), t1 = ld16<P>(rws, tsv, so_ + 32 * 1024);
      __builtin_amdgcn_sched_barrier(0);
      co[0] = lds_ld<u32x4>(lb + L_C2 + l16); so[0] = lds_ld<u32x4>(lb + L_C2 + 1024 + l16);
      cot[0] = lds_ld<u32x4>(lb + L_C2 + 2048 + l16); sot[0] = lds_ld<u32x4>(lb + L_C2 + 3072 + l16);
      co[1] = xr(so[0], 0x00008000u);
      so[1] = xr(co[0], 0x80000000u);
      cot[1] = xr(cot[0], 0x80000000u); sot[1] = xr(sot[0], 0x80000000u);
      pin(co[0]); pin(co[1]); pin(so[0]); pin(so[1]); pin(cot[0]); pin(cot[1]); pin(sot[0]); pin(sot[1]);
#pragma unroll
      for (int j = 0; j < 16; ++j) {
        const int o = (32 * (j >> 3) + 16 * ((j >> 2) & 1) + (j & 3)) * 144;
        wr_[j] = bc<float>((u32)lds_ld<u16>(wa + o) << 16);
        wi_[j] = bc<float>(((u32)lds_ld<u16>(wa + 9216 + o) << 16) + sg);
      }
#pragma unroll
      for (int j = 0; j < 16; ++j) { pin(wr_[j]); pin(wi_[j]); }
      __builtin_amdgcn_sched_barrier(0);
      if (P) __builtin_amdgcn_s_setprio(3);
      STAMP(8);
      lds_st<u32x4>(tiw, t0);
      lds_st<u32x4>(tiw + 512, t1);
    }
    STAMP(9);
    f32x4 E[2], O[2];
    {
      u32x4 B0 = cat(trread(ta), trread(ta + 512)), B1 = cat(trread(ta + 1024), trread(ta + 1536));
      u32x4 B0s = sr8(B0, mf), B1s = sr8(B1, mf);
#pragma unroll
      for (int t = 0; t < 2; ++t) {
        E[t] = mfma32(fs[t], B0s, mfma32(fc[t], B0, z4));
        O[t] = mfma32(so[t], B1s, mfma32(co[t], B1, z4));
      }
    }
    float Pp[8], Mm[8];
#pragma unroll
    for (int j = 0; j < 8; ++j) {
      const float e = E[j >> 2][j & 3], o = O[j >> 2][j & 3];
      const float ylo = e + o, yhi = e - o;
      const float vlo = __builtin_fmaf(fr8(ylo), wi_[j], ylo * wr_[j]);
      const float vhi = __builtin_fmaf(fr8(yhi), wi_[8 + j], yhi * wr_[8 + j]);
      Pp[j] = vlo + vhi; Mm[j] = vlo - vhi;
    }
    {
      u32x4 AP = pk8(Pp), AM = pk8(Mm);
      u32x4 APs = sr8(AP, mi), AMs = sr8(AM, mi);
#pragma unroll
      for (int t = 0; t < 2; ++t) {
        f32x4 ze = mfma32(APs, fs[t], mfma32(AP, fc[t], z4));
        f32x4 zo = mfma32(AMs, sot[t], mfma32(AM, cot[t], z4));
        lds_st<u32x2>(tow + t * 32 * 144, u32x2{pk(ze[0], ze[1]), pk(ze[2], ze[3])});
        lds_st<u32x2>(tow + t * 32 * 144 + 144, u32x2{pk(zo[0], zo[1]), pk(zo[2], zo[3])});
      }
    }
    bar();
    STAMP(10);
    {
      st16<P>(rws, tpo, tps, lds_ld<u32x4>(tor));
      st16<P>(rws, tpo, tps + 32 * TP_S2, lds_ld<u32x4>(tor + 32 * 144));
    }
    STAMP(11);
    if (P) { drain_vm(); bar(); publish(ctl + CT_F2, b, epoch); __builtin_amdgcn_s_setprio(SH2PRIO); }
    STAMP(12);
  }

  if (PH == 0) gate_rows<true>(p, lb, b, w, 1);
  if (PH == 3) bar();
  if (PH == 0 || PH == 3) p3_prepare<PH>(p, lb, b, rtf, c3);
  if (PH == 3) bar();
}

DI f32x2 cexp_rev(float x2, float ph) {
  const float e = __builtin_amdgcn_exp2f(x2);
  const float f = __builtin_amdgcn_fractf(ph);
  f32x2 cs = {cosr(f), sinr(f)};
  return cs * e;
}
struct KW { u32 a[8]; };
template <int ROW> DI void wrow(const KW& k, f32x2 v) { lds_st<u32>(k.a[ROW & 7] + ROW * 256, pk2(v)); }
template <int ROW> DI void wrow2(const KW& k, f32x2 lo, f32x2 hi) {
  const u32 a = pk2(lo), b = pk2(hi);
  asm volatile("ds_write2st64_b32 %0, %1, %2 offset0:%c3 offset1:%c4" :: "v"(k.a[ROW & 7]), "v"(a), "v"(b), "i"(ROW), "i"(ROW + 8) : "memory");
}
DI void kfrag(u32 fa, u32 fb, u32x4 (&A)[4]) {
  A[0] = lds_ld<u32x4>(fa); A[1] = lds_ld<u32x4>(fb); A[2] = lds_ld<u32x4>(fa + 128); A[3] = lds_ld<u32x4>(fb + 128);
}
DI f32x4 ktile(const u32x4 (&A)[4], const u32x4 (&V)[4]) {
  f32x4 acc = {0, 0, 0, 0};
#pragma unroll
  for (int ks = 0; ks < 4; ++ks) acc = mfma32(A[ks], V[ks], acc);
  return acc;
}
template <class F0, class F1>
DI void kgen(const u32 (&in)[6], float dt, const KW& k, u32 fa, u32 fb, u32 pb, F0&& hook0, F1&& hook1) {
  const float ar = dt * bc<float>(in[0] << 16), ai = dt * bc<float>(in[1] << 16);
  const f32x2 dB = {dt * bc<float>(in[2] << 16), dt * bc<float>(in[3] << 16)};
  const f32x2 Cc = {bc<float>(in[4] << 16), bc<float>(in[5] << 16)};
  const f32x2 CB = cmul(Cc, dB);
  const float x1 = ar * 1.44269504088896341f, p1 = ai * 0.15915494309189535f;
  const f32x2 E1 = cexp_rev(x1, p1), E8c = cexp_rev(8.f * x1, -8.f * p1), E128 = cexp_rev(128.f * x1, 128.f * p1);
  u32x4 V[4], A0[4], A1[4];
  {
    const f32x2 v2 = cmul(E8c, E8c), v3 = cmul(v2, E8c), v4 = cmul(v2, v2), v8 = cmul(v4, v4);
    const f32x2 v5 = cmul(v4, E8c), v6 = cmul(v4, v2), v7 = cmul(v4, v3);
    wrow2<0>(k, f32x2{1.f, 0.f}, v8); wrow2<1>(k, E8c, cmul(v8, E8c)); wrow2<2>(k, v2, cmul(v8, v2)); wrow2<3>(k, v3, cmul(v8, v3));
    wrow2<4>(k, v4, cmul(v8, v4)); wrow2<5>(k, v5, cmul(v8, v5)); wrow2<6>(k, v6, cmul(v8, v6)); wrow2<7>(k, v7, cmul(v8, v7));
    kfrag(fa, fb, V);
  }
  hook0();
  const f32x2 E2 = cmul(E1, E1), E3 = cmul(E2, E1), E4 = cmul(E2, E2), E5 = cmul(E4, E1), E6 = cmul(E4, E2), E7 = cmul(E4, E3);
  const f32x2 G1 = cmul(CB, E128);
  wrow2<0>(k, CB, G1); wrow2<1>(k, cmul(CB, E1), cmul(G1, E1)); wrow2<2>(k, cmul(CB, E2), cmul(G1, E2)); wrow2<3>(k, cmul(CB, E3), cmul(G1, E3));
  wrow2<4>(k, cmul(CB, E4), cmul(G1, E4)); wrow2<5>(k, cmul(CB, E5), cmul(G1, E5)); wrow2<6>(k, cmul(CB, E6), cmul(G1, E6)); wrow2<7>(k, cmul(CB, E7), cmul(G1, E7));
  kfrag(fa, fb, A0);
  const f32x2 G2 = cmul(G1, E128), G3 = cmul(G2, E128), G4 = cmul(G3, E128), G5 = cmul(G4, E128), G6 = cmul(G5, E128), G7 = cmul(G6, E128);
  wrow2<0>(k, G2, G4); wrow2<1>(k, cmul(G2, E2), cmul(G4, E4)); wrow2<2>(k, cmul(G2, E4), G5); wrow2<3>(k, cmul(G2, E6), cmul(G5, E4));
  wrow2<4>(k, G3, G6); wrow2<5>(k, cmul(G3, E2), cmul(G6, E4)); wrow2<6>(k, cmul(G3, E4), G7); wrow2<7>(k, cmul(G3, E6), cmul(G7, E4));
  kfrag(fa, fb, A1);
  hook1();
  __builtin_amdgcn_sched_barrier(0);
  const f32x4 a0 = ktile(A0, V);
  const f32x2 G8 = cmul(G7, E128), G9 = cmul(G8, E128), G10 = cmul(G9, E128), G11 = cmul(G10, E128);
  const f32x2 G12 = cmul(G11, E128), G13 = cmul(G12, E128), G14 = cmul(G13, E128), G15 = cmul(G14, E128);
  wrow2<0>(k, G8, cmul(G15, E128)); wrow<1>(k, G9); wrow<2>(k, G10); wrow<3>(k, G11);
  wrow<4>(k, G12); wrow<5>(k, G13); wrow<6>(k, G14); wrow<7>(k, G15);
  kfrag(fa, fb, A0);
  __builtin_amdgcn_sched_barrier(0);
  const f32x4 a1 = ktile(A1, V), a2 = ktile(A0, V);
  lds_st<f32x4>(pb, a0);
  lds_st<f32x4>(pb + 1024, a1);
  lds_st<f32x4>(pb + 2048, a2);
}

struct SsmLoads { u32x4 xt_v, kt0; u32x2 kt1; u32 in[6], ldt, dsk; };
template <int PH>
DI void ssm_first_loads(const Params& p, const int j, SsmLoads& L) {
  const int tid = threadIdx.x, w = __builtin_amdgcn_readfirstlane(tid >> 6), d = 2 * j, ch = w >> 2, n = tid & 255;
  const int sb_ = j / 7, db_ = j - sb_ * 7;
  if (PH == 0 || PH == 1)
    L.xt_v = __builtin_amdgcn_raw_buffer_load_b128(mkrs(p.x, S_ * D_ * 2), (u32)((tid >> 3) * (D_ * 2) + (tid & 7) * 16), (u32)(64 * sb_ * (D_ * 2) + db_ * 128), 0);
  __builtin_amdgcn_sched_barrier(0);
  if (PH != 1) {
    const u32 vo = ((d + ch) * NS + n) * 2;
    const u16* arr[6] = {p.are, p.aim, p.bre, p.bim, p.cre, p.cim};
#pragma unroll
    for (int a = 0; a < 6; ++a) L.in[a] = *(const GAS u16*)((const GAS char*)arr[a] + vo);
    L.ldt = *(const GAS u16*)(p.logdt + d + ch);
    L.dsk = *(const GAS u16*)(p.dskip + d + ((w >> 1) & 1));
    L.kt0 = *(const GAS u32x4*)(KTAB.v + 8 * n);
    L.kt1 = *(const GAS u32x2*)(KTAB.v + 8 * n + 8);
  }
}

template <int PH>
DI void ssm_role(const Params& p, const u32 lb, const int j, u32* ctl, const u32 gen, const SsmLoads& L STAMP_ARG) {
  constexpr bool P = PH == 0;
  const int tid = threadIdx.x, lane = tid & 63, w = __builtin_amdgcn_readfirstlane(tid >> 6), i16 = lane & 15, g = lane >> 4;
  const int d = 2 * j, ch = w >> 2, n = tid & 255;
  const f32x4 z4 = {0, 0, 0, 0};
  const rsrc_t rws = mkrs(p.ws, 16u << 20);
  const int sb_ = j / 7, db_ = j - sb_ * 7;
  const u32x4 xt_v = L.xt_v, kt0 = L.kt0;
  const u32x2 kt1 = L.kt1;
  const u32 ldt = L.ldt;
  u32 dsk = L.dsk;
  const u32 (&in)[6] = L.in;
  auto transpose = [&]() {
    lds_st<u32x4>(lb + L_XP + (tid >> 3) * 144 + (tid & 7) * 16, xt_v);
    bar();
    const int chi = g + 4 * (w >> 2);
    const u32 a = lb + L_XP + (8 * chi + (i16 >> 2)) * 144 + (16 * (w & 3) + 4 * (i16 & 3)) * 2;
    st16<P>(rws, (u32)((16 * (w & 3) + i16) * XT_D + chi * 16), (u32)WS_XT + 64 * db_ * XT_D + sb_ * 128, cat(trread(a), trread(a + 4 * 144)));
  };
  if (PH == 1) { transpose(); return; }
  if (P) {
    if (tid == 0) lds_st<u32>(lb + L_CNT, 0u);
    __builtin_amdgcn_sched_barrier(0);
    transpose();
    __builtin_amdgcn_s_setprio(1);
    __builtin_amdgcn_sched_barrier(0);
  }
  KW kw;
  const u32 sb = lb + w * KSCR;
#pragma unroll
  for (int h = 0; h < 8; ++h) { kw.a[h] = sb + (((lane >> 2) ^ h) << 4) + (lane & 3) * 4; pin(kw.a[h]); }
  u32 fa = sb + i16 * 256 + ((g ^ (i16 & 3)) << 4) + ((i16 >> 2) & 1) * 64, fb = fa ^ 64;
  pin(fa); pin(fb);
  STAMP(1);
  kgen(in, bfr(expf(bc<float>(ldt << 16))), kw, fa, fb, sb + lane * 16,
       [&]() {
         if (P) {
           drain_vm();
           STAMP(5);
           if (lane == 0 && __hip_atomic_fetch_add((LAS u32*)(size_t)(lb + L_CNT), 1u, __ATOMIC_RELAXED, __HIP_MEMORY_SCOPE_WORKGROUP) == 7u)
             __hip_atomic_store((GAS u32*)ctl + CT_FX + (db_ * 32 + sb_) * FL, gen + 1u, RLX_AGENT);
         }
       },
       []() {});
  const u32 epoch = __builtin_amdgcn_readfirstlane(gen) + 1u;
  pin(dsk);
  STAMP(2);
  if (P && w == 0) poll_flags(ctl + CT_FX + (d >> 6) * 32 * FL, 32, epoch);
  STAMP(6);
  bar();
  const u32x4 uv_ = ld16<P>(rws, n * 16, (u32)WS_XT + (d + ch) * XT_D);
  STAMP(3);
  if ((w & 3) < 3) {
    const u32 pb = lb + ch * 4 * KSCR + (w & 3) * 1024 + lane * 16;
    const f32x4 s = (lds_ld<f32x4>(pb) + lds_ld<f32x4>(pb + KSCR)) + (lds_ld<f32x4>(pb + 2 * KSCR) + lds_ld<f32x4>(pb + 3 * KSCR));
    const u32 tb = lb + L_KT + ch * 1600 + ((w & 3) * 256 + 64 * g + i16) * 2, a = pk(s[0], s[1]), cc = pk(s[2], s[3]);
    lds_st<u16>(tb, (u16)a); lds_st<u16>(tb + 32, (u16)(a >> 16));
    lds_st<u16>(tb + 64, (u16)cc); lds_st<u16>(tb + 96, (u16)(cc >> 16));
  } else {
    if (lane == 0) lds_st<u16>(lb + L_KT + ch * 1600 + 1536, (u16)0);
    if (lane < 32) lds_st<u32x4>(lb + L_U + ch * 4608 + lane * 16, u32x4{0, 0, 0, 0});
  }
  bar();
  STAMP(4);
  {
    const u32 ktb = lb + L_KT + ch * 1600;
    u32 e[11];
#pragma unroll
    for (int k = 0; k < 4; ++k) { e[2 * k] = lds_ld<u16>(ktb + (kt0[k] & 0xffffu)); e[2 * k + 1] = lds_ld<u16>(ktb + (kt0[k] >> 16)); }
    e[8] = lds_ld<u16>(ktb + (kt1[0] & 0xffffu)); e[9] = lds_ld<u16>(ktb + (kt1[0] >> 16)); e[10] = lds_ld<u16>(ktb + (kt1[1] & 0xffffu));
    const u32 k00 = lds_ld<u16>(ktb);
    u32 dw[5], al[5];
#pragma unroll
    for (int k = 0; k < 5; ++k) { dw[k] = e[2 * k] | (e[2 * k + 1] << 16); al[k] = e[2 * k + 1] | (e[2 * k + 2] << 16); }
    const u32 kb = lb + L_KC + ch * 4 * KCP + n * 16;
    lds_st<u32x4>(kb, u32x4{dw[0], dw[1], dw[2], dw[3]});
    lds_st<u32x4>(kb + KCP, u32x4{al[0], al[1], al[2], al[3]});
    lds_st<u32x4>(kb + 2 * KCP, u32x4{dw[1], dw[2], dw[3], dw[4]});
    lds_st<u32x4>(kb + 3 * KCP, u32x4{al[1], al[2], al[3], al[4]});
    if (n < 16) {
      u32x4 z = {0, 0, 0, 0};
      if (n == 0) z.x = k00;
      lds_st<u32x4>(lb + L_KC + (ch * 4 + (n >> 2)) * KCP + 4096 + (n & 3) * 16, z);
    }
    lds_st<u32x4>(lb + L_U + ch * 4608 + 512 + n * 16, uv_);
  }
  bar();
  STAMP(7);
  f32x4 acc[8];
  const int cc = (w >> 1) & 1, hf = w & 1;
  if (w < 4) {
#pragma unroll
    for (int c = 0; c < 8; ++c) acc[c] = z4;
    const int bsh = (-i16) & 3;
    u32 abase = lb + L_KC + (cc * 4 + bsh) * KCP + (2032 + 8 * g - i16 - bsh) * 2 - hf * 4 * 64 - 3 * 64 - 7 * 512;
    u32 bbase = lb + L_U + cc * 4608 + (256 - 16 + 16 * i16 + 8 * g) * 2 - hf * 4 * 64 - 3 * 64;
    u32 abase8 = abase + 8;
    pin(abase); pin(abase8); pin(bbase);
    u32x4 A[8], B[8];
#pragma unroll
    for (int qq = 0; qq < 8; ++qq) {
      B[7 - qq] = lds_ld<u32x4>(bbase + (7 - qq) * 512 + 3 * 64);
      A[qq] = cat(lds_ld<u32x2>(abase + (7 - qq) * 512 + 3 * 64), lds_ld<u32x2>(abase8 + (7 - qq) * 512 + 3 * 64));
      __builtin_amdgcn_sched_barrier(0);
    }
#pragma unroll
    for (int rr = 0; rr < 4; ++rr) {
#pragma unroll
      for (int qq = 0; qq < 8; ++qq) {
#pragma unroll
        for (int m = 7 - qq; m >= 0; --m) {
          acc[qq + m] = mfma32(A[qq], B[m], acc[qq + m]);
          if (CNOP >= 0 && rr < CNOPR) asm volatile("s_nop %c0" :: "i"(CNOP >= 0 ? CNOP : 0));
        }
        if (rr < 3) {
          A[qq] = cat(lds_ld<u32x2>(abase + (7 - qq) * 512 + (2 - rr) * 64), lds_ld<u32x2>(abase8 + (7 - qq) * 512 + (2 - rr) * 64));
          B[7 - qq] = lds_ld<u32x4>(bbase + (7 - qq) * 512 + (2 - rr) * 64);
        }
        __builtin_amdgcn_sched_barrier(0);
      }
    }
    if (hf) {
#pragma unroll
      for (int c = 0; c < 4; ++c) lds_st<f32x4>(lb + L_RED + (w * 4 + c) * 1024 + lane * 16, acc[c]);
#pragma unroll
      for (int c = 0; c < 4; ++c) acc[c] = acc[4 + c];
    } else {
#pragma unroll
      for (int c = 0; c < 4; ++c) lds_st<f32x4>(lb + L_RED + (w * 4 + c) * 1024 + lane * 16, acc[4 + c]);
    }
  }
  bar();
  STAMP(8);
  if (w < 4) {
    const float dk = bc<float>(dsk << 16);
    const u32 tb = (1024 * hf + 16 * i16 + 4 * g) * 2;
    f32x4 o[4];
    u32x2 uv[4];
#pragma unroll
    for (int c = 0; c < 4; ++c) {
      o[c] = lds_ld<f32x4>(lb + L_RED + ((w ^ 1) * 4 + c) * 1024 + lane * 16);
      uv[c] = lds_ld<u32x2>(lb + L_U + cc * 4608 + 512 + tb + c * 512);
    }
#pragma unroll
    for (int c = 0; c < 4; ++c) {
      const f32x4 y = acc[c] + o[c];
      const f32x2 b01 = up2(pk(y[0], y[1])) + up2(pk2(up2(uv[c].x) * dk));
      const f32x2 b23 = up2(pk(y[2], y[3])) + up2(pk2(up2(uv[c].y) * dk));
      lds_st<u32x2>(lb + L_BO + cc * 4096 + tb + c * 512, u32x2{pk2(b01), pk2(b23)});
    }
  }
  bar();
  STAMP(9);
  {
    const int st = w & 3, s2 = 16 * st + i16;
    const u32 a = lb + L_BO + ch * 4096 + (8 * g + (i16 >> 2)) * 128 + (16 * st + 4 * (i16 & 3)) * 2;
    st16<P>(rws, (u32)((((s2 & 7) * 8 + (s2 >> 3)) * 32 + 8 * g) * 2), (u32)WS_B + (d + ch) * B_D, cat(trread(a), trread(a + 4 * 128)));
  }
  STAMP(10);
  if (P) { drain_vm(); bar(); publish(ctl + CT_FB, j, epoch); }
  STAMP(11);
}

template <int PH>
__global__ __launch_bounds__(NT, 4) void gfs4_kernel(Params p) {
  extern __shared__ __attribute__((aligned(16))) char smem[];
  const u32 lb = (u32)(size_t)(LAS char*)smem;
  const int b = blockIdx.x;
  u32* ctl = (u32*)(p.ws + WS_CTL);
  STAMP_DECL;
  if (PH == 0 || PH == 1) STAMP(0); else if (PH == 2) STAMP(20); else STAMP(21);
  u32 gen = 0;
  const u32 dg_t0 = (u32)__builtin_amdgcn_s_memrealtime();
  if (threadIdx.x == 0) __hip_atomic_store((GAS u32*)(p.ws + (12u << 20)) + b, dg_t0, RLX_AGENT);
  asm volatile("" :: "s"(p.x), "s"(p.wr), "s"(p.wi), "s"(p.are), "s"(p.aim), "s"(p.bre), "s"(p.bim));
  asm volatile("" :: "s"(p.cre), "s"(p.cim), "s"(p.logdt), "s"(p.dskip), "s"(p.gw), "s"(p.out), "s"(p.ws));
  SsmLoads L;
  if (PH == 0) __builtin_amdgcn_s_setprio(3);
  if (__builtin_expect_with_probability(b < N_FFT, 1, PFFT)) {
    if (__builtin_expect(threadIdx.x < 256, 1)) { if (PH == 0 || PH == 1) fft_first_loads(p, lb, b); }
  } else {
    ssm_first_loads<PH>(p, b - N_FFT, L);
  }
  if (PH == 0) gen = __hip_atomic_load((GAS u32*)ctl + CT_GEN, RLX_AGENT);
  __builtin_amdgcn_sched_barrier(0);
  if (b < N_FFT) {
    u32x4 fc[2], fs[2];
    P3C c3;
    if (threadIdx.x >= 256) {
      if (PH == 1 || PH == 2) return;
      fft_upper<PH>(p, lb, b, ctl, fc, fs, c3);
    } else fft_role<PH>(p, lb, b, ctl, gen, fc, fs, c3 STAMP_PASS);
    if (PH == 0 || PH == 3) fft_phase3<PH>(p, lb, b, ctl, __builtin_amdgcn_readfirstlane(gen) + 1u, fc, fs, c3 STAMP_PASS);
  }
  else ssm_role<PH>(p, lb, b - N_FFT, ctl, gen, L STAMP_PASS);
  STAMP_FLUSH();
  if (b == 0 && threadIdx.x < 64 && (gen & 0xff) != ((gen >> 8) & 0xff) && (gen & 7) == 3) {
    u32 mn = 0x7fffffffu, mx = 0, lo = 0x7fffffffu;
    for (int i = threadIdx.x; i < N_WG; i += 64) {
      mn = min(mn, __hip_atomic_load((GAS u32*)(p.ws + (12u << 20)) + i, RLX_AGENT) - dg_t0 + 100000u);
      if ((i >= N_FFT) == (DR == 1)) { const u32 a = __hip_atomic_load((GAS u32*)(p.ws + (12u << 20)) + 1024 + i, RLX_AGENT) - dg_t0 + 100000u; mx = max(mx, a); lo = min(lo, a); }
    }
    for (int o = 32; o >= 1; o >>= 1) { mn = min(mn, (u32)__shfl_xor((int)mn, o)); mx = max(mx, (u32)__shfl_xor((int)mx, o)); lo = min(lo, (u32)__shfl_xor((int)lo, o)); }
    const float v = DIAG == 10 ? (float)(mx - mn) * 0.01f : (float)(lo - mn) * 0.01f;
    wait_vm0();
    if (threadIdx.x == 0) p.out[0] = (u16)(pk(v, v) & 0xffff);
  }
}

extern "C" void kernel_launch(void* const* d_in, const int* in_sizes, int n_in,
                              void* d_out, int out_size, void* d_ws, size_t ws_size,
                              hipStream_t stream) {
  (void)in_sizes; (void)n_in; (void)out_size; (void)ws_size;
  Params p;
  p.x = (const u16*)d_in[0]; p.wr = (const u16*)d_in[1]; p.wi = (const u16*)d_in[2];
  p.are = (const u16*)d_in[4]; p.aim = (const u16*)d_in[5]; p.bre = (const u16*)d_in[6]; p.bim = (const u16*)d_in[7];
  p.cre = (const u16*)d_in[8]; p.cim = (const u16*)d_in[9]; p.logdt = (const u16*)d_in[10]; p.dskip = (const u16*)d_in[11];
  p.gw = (const u16*)d_in[12];
  p.out = (u16*)d_out; p.ws = (char*)d_ws;
  (void)hipFuncSetAttribute((const void*)gfs4_kernel<0>, hipFuncAttributeMaxDynamicSharedMemorySize, LDS_BYTES);
  gfs4_kernel<0><<<N_WG, NT, LDS_BYTES, stream>>>(p);
}
```

```cpp
#include <hip/hip_runtime.h>
#include <hip/hip_bf16.h>
#include <stdint.h>

typedef unsigned int u32;
typedef unsigned short u16;
typedef unsigned long long u64;
typedef float f32x4 __attribute__((ext_vector_type(4)));
typedef float f32x2 __attribute__((ext_vector_type(2)));
typedef u32 u32x4 __attribute__((ext_vector_type(4)));
typedef u32 u32x2 __attribute__((ext_vector_type(2)));
typedef __bf16 bf16x8 __attribute__((ext_vector_type(8)));
typedef __bf16 bf16x2 __attribute__((ext_vector_type(2)));
typedef short s16x4 __attribute__((ext_vector_type(4)));
#define LAS __attribute__((address_space(3)))
#define GAS __attribute__((address_space(1)))
#define DI __device__ __forceinline__

#define NLAUNCH 1

constexpr int S_ = 2048, D_ = 448, DF = 225, NS = 256;
constexpr int N_FFT = 256, N_SSM = 224, N_WG = N_FFT + N_SSM, NT = 512;

constexpr u32 T_K1 = 65536 + 256;
constexpr u32 TP_S2 = 32768 + 128;
constexpr u32 XT_D = 4096 + 128;
constexpr u32 B_D = 4096 + 128;
constexpr size_t WS_CTL = 0;
constexpr size_t WS_T   = 1u << 20;
constexpr size_t WS_TP  = WS_T + (3u << 20);
constexpr size_t WS_XT  = WS_TP + (3u << 20);
constexpr size_t WS_B   = WS_XT + (2u << 20);
constexpr int FL = 32, FR_ = 256 * FL + 64;
constexpr int CT_GEN = 0;
constexpr int CT_F1 = 64;
constexpr int CT_F2 = CT_F1 + 8 * FR_;
constexpr int CT_FB = CT_F2 + 8 * FR_;
constexpr int CT_FX = CT_FB + 8 * FR_;
static_assert((CT_FX + 7 * 32 * FL) * 4 <= (1 << 20), "control block");

constexpr int XRS = 928;
constexpr int L_XT = 0;
constexpr int L_XCH = 29696;
constexpr int TPRS = 1040;
constexpr int L_TP = 0;
constexpr int L_TO = 29696;
constexpr int L_TI = 38912;
constexpr int L_BI = 38912;
constexpr int BRS = 912;
constexpr int L_Z = 47104;
constexpr int L_C1 = 38912;
constexpr int L_C2 = 65536;
constexpr int L_C3 = 70400;
constexpr int L_W = 47104;
constexpr int L_G = 70208;
constexpr int KCP = 4160;
constexpr int L_KC = 0;
constexpr int KSCR = 4096;
constexpr int L_RED = 33280;
constexpr int L_BO = 49664;
constexpr int L_U = 57856;
constexpr int L_XP = 57856;
constexpr int L_KT = 67072;
constexpr int L_CNT = 70272;
constexpr int LDS_BYTES = 72448;

struct Params {
  const u16* x; const u16* wr; const u16* wi;
  const u16* are; const u16* aim; const u16* bre; const u16* bim; const u16* cre; const u16* cim;
  const u16* logdt; const u16* dskip; const u16* gw;
  u16* out; char* ws;
};

constexpr double cx_sin_oct(double x) { double x2 = x * x, t = x, s = x; for (int i = 1; i < 9; ++i) { t *= -x2 / ((2 * i) * (2 * i + 1)); s += t; } return s; }
constexpr double cx_cos_oct(double x) { double x2 = x * x, t = 1, s = 1; for (int i = 1; i < 9; ++i) { t *= -x2 / ((2 * i - 1) * (2 * i)); s += t; } return s; }
constexpr double cx_sin(long n, long d) {
  n %= d; if (n < 0) n += d;
  const long o = (8 * n) / d;
  const double r = 6.283185307179586476925 * (double)(8 * n - o * d) / (8.0 * (double)d), q = 0.78539816339744830962;
  switch (o) {
    case 0: return cx_sin_oct(r);       case 1: return cx_cos_oct(q - r);
    case 2: return cx_cos_oct(r);       case 3: return cx_sin_oct(q - r);
    case 4: return -cx_sin_oct(r);      case 5: return -cx_cos_oct(q - r);
    case 6: return -cx_cos_oct(r);      default: return -cx_sin_oct(q - r);
  }
}
constexpr double cx_cos(long n, long d) { return cx_sin(4 * n + d, 4 * d); }
constexpr u32 cx_f32(double v) { return __builtin_bit_cast(u32, (float)v); }
constexpr u32 cx_bf(double v) { const u32 u = cx_f32(v); return (u + 0x7fffu + ((u >> 16) & 1u)) >> 16; }
constexpr int cx_pi(int g, int jj) { return 16 * (jj >> 2) + 4 * g + (jj & 3); }
struct Frag { u32 v[64][4]; };
enum { TF_FC0, TF_FS0, TF_CO0, TF_SO0, TF_COT0, TF_SOT0, TF_TC, TF_TS, TF_M, TF_Q0, TF_N = TF_Q0 + 24 };
constexpr Frag make_frag(int item) {
  Frag f{};
  for (int lane = 0; lane < 64; ++lane) {
    const int i16 = lane & 15, g = lane >> 4;
    if (item < TF_TC) {
      const int sn = item & 1;
      for (int jj = 0; jj < 8; ++jj) {
        const int m = cx_pi(g, jj), a = i16;
        long n = 0, d = 32;
        if (item < TF_CO0) { n = a * m; d = 32; }
        else if (item < TF_COT0) { n = a * (2 * m + 1); d = 64; }
        else { n = m * (2 * a + 1); d = 64; }
        f.v[lane][jj >> 1] |= cx_bf(sn ? cx_sin(n, d) : cx_cos(n, d)) << (16 * (jj & 1));
      }
    } else if (item == TF_TC || item == TF_TS) {
      for (int j = 0; j < 4; ++j) {
        const long n = (2 * g + (j & 1)) * (16 * (j >> 1) + i16);
        f.v[lane][j] = cx_f32(item == TF_TC ? cx_cos(n, 448) : cx_sin(n, 448));
      }
    } else if (item == TF_M) {
      for (int j = 0; j < 4; ++j) {
        const int k0 = 2 * g + (j >> 1);
        const double cf = ((k0 == 0 || k0 == 7) ? 1.0 : 2.0) / (2048.0 * 448.0);
        const double v = i16 < 14 ? cf * ((j & 1) ? -cx_sin(k0 * i16, 14) : cx_cos(k0 * i16, 14)) : 0.0;
        f.v[lane][j >> 1] |= cx_bf(v) << (16 * (j & 1));
      }
    } else {
      const int q = (item - TF_Q0) / 6, sub = (item - TF_Q0) % 6, k0 = 2 * q + ((i16 >> 1) & 1), ri = i16 & 1;
      if (sub < 2) {
        if ((i16 >> 2) == 2 * sub + (g & 1))
          for (int j = 0; j < 8; ++j) {
            const int d0 = 8 * (g >> 1) + j;
            const double v = d0 < 14 ? (ri ? -cx_sin(d0 * k0, 14) : cx_cos(d0 * k0, 14)) : 0.0;
            f.v[lane][j >> 1] |= cx_bf(v) << (16 * (j & 1));
          }
      } else {
        for (int j = 0; j < 4; ++j) {
          const long n = cx_pi(g, 4 * (sub & 1) + j) * k0;
          f.v[lane][j] = cx_f32(sub < 4 ? cx_cos(n, 448) : (ri ? -cx_sin(n, 448) : cx_sin(n, 448)));
        }
      }
    }
  }
  return f;
}
template <int I> struct FragC { static constexpr Frag f = make_frag(I); };
#define FR(i) FragC<(i)>::f
#define FR6(i) FR(i), FR(i + 1), FR(i + 2), FR(i + 3), FR(i + 4), FR(i + 5)
__device__ const __attribute__((aligned(1024))) Frag TF[TF_N] = {FR6(0), FR(6), FR(7), FR(8), FR6(9), FR6(15), FR6(21), FR6(27)};

struct KTab { u16 v[2080]; };
constexpr KTab make_ktab() {
  KTab t{};
  for (int i = 0; i < 2080; ++i) {
    const int l = 2048 - i;
    int idx = 768;
    if (l >= 0) {
      const int sh = l > 256 ? (l >= 1024 ? 3 : (l >= 512 ? 2 : 1)) : 0;
      const int v = sh ? (((l + (1 << (sh - 1)) - 1 + ((l >> sh) & 1)) >> sh) << sh) : l;
      if (v < 256) idx = ((v & 7) + 8 * (v >> 7)) * 16 + ((v >> 3) & 15);
      else if (v < 512) idx = 256 + ((((v - 256) >> 1) & 3) + 4 * ((v - 256) >> 7)) * 16 + (((v - 256) >> 3) & 15);
      else if (v < 1024) idx = 256 + (8 + (((v - 512) >> 2) & 1) + 2 * ((v - 512) >> 7)) * 16 + (((v - 512) >> 3) & 15);
      else if (v < 2048) idx = 512 + ((v - 1024) >> 7) * 16 + (((v - 1024) >> 3) & 15);
      else idx = 640;
    }
    t.v[i] = (u16)(2 * idx);
  }
  return t;
}
__device__ const __attribute__((aligned(16))) KTab KTAB = make_ktab();

template <class T, class F> DI T bc(F v) { return __builtin_bit_cast(T, v); }
DI f32x4 mfma32(u32x4 a, u32x4 b, f32x4 c) { return __builtin_amdgcn_mfma_f32_16x16x32_bf16(bc<bf16x8>(a), bc<bf16x8>(b), c, 0, 0, 0); }
DI f32x4 mfma16(u32x2 a, u32x2 b, f32x4 c) { return __builtin_amdgcn_mfma_f32_16x16x16bf16_1k(bc<s16x4>(a), bc<s16x4>(b), c, 0, 0, 0); }
DI u32 pk(float a, float b) { f32x2 v = {a, b}; return bc<u32>(__builtin_convertvector(v, bf16x2)); }
DI u32 pk2(f32x2 v) { return bc<u32>(__builtin_convertvector(v, bf16x2)); }
DI float bfr(float a) { return bc<float>(pk(0.f, a) & 0xffff0000u); }
DI float lo16(u32 v) { return bc<float>(v << 16); }
DI float hi16(u32 v) { return bc<float>(v & 0xffff0000u); }
DI f32x2 up2(u32 v) { return f32x2{lo16(v), hi16(v)}; }
DI float b2f(u16 v) { return bc<float>((u32)v << 16); }
DI u32x2 trread(u32 a) { return bc<u32x2>(__builtin_amdgcn_ds_read_tr16_b64_v4i16((LAS s16x4*)(size_t)a)); }
DI u32x4 cat(u32x2 a, u32x2 b) { u32x4 r = {a.x, a.y, b.x, b.y}; return r; }
DI float sinr(float x) { return __builtin_amdgcn_sinf(x); }
DI float cosr(float x) { return __builtin_amdgcn_cosf(x); }
DI float fx1(float v) { return __builtin_amdgcn_update_dpp(v, v, 0xB1, 0xf, 0xf, true); }
DI float fr8(float v) { return __builtin_amdgcn_update_dpp(v, v, 0x128, 0xf, 0xf, true); }
DI u32 ux1(u32 v) { return (u32)__builtin_amdgcn_update_dpp((int)v, (int)v, 0xB1, 0xf, 0xf, true); }
DI u32 ur8(u32 v) { return (u32)__builtin_amdgcn_update_dpp((int)v, (int)v, 0x128, 0xf, 0xf, true); }
DI u32x4 sx1(u32x4 v, u32 m) { u32x4 r = {ux1(v.x) ^ m, ux1(v.y) ^ m, ux1(v.z) ^ m, ux1(v.w) ^ m}; return r; }
DI u32x4 sr8(u32x4 v, u32 m) { u32x4 r = {ur8(v.x) ^ m, ur8(v.y) ^ m, ur8(v.z) ^ m, ur8(v.w) ^ m}; return r; }
DI u32x4 xr(u32x4 v, u32 m) { u32x4 r = {v.x ^ m, v.y ^ m, v.z ^ m, v.w ^ m}; return r; }
DI u32x4 pk8(const float (&v)[8]) { return u32x4{pk(v[0], v[1]), pk(v[2], v[3]), pk(v[4], v[5]), pk(v[6], v[7])}; }
template <class T> DI T lds_ld(u32 a) { return *(LAS T*)(size_t)a; }
template <class T> DI void lds_st(u32 a, T v) { *(LAS T*)(size_t)a = v; }
DI void wait_vm0() { __builtin_amdgcn_s_waitcnt(0x0F70); }
DI void drain_vm() { asm volatile("s_waitcnt vmcnt(0)" ::: "memory"); }
DI void bar() { __builtin_amdgcn_s_waitcnt(0xC07F); asm volatile("s_barrier" ::: "memory"); }
template <class T> DI void pin(T& v) { asm volatile("" : "+v"(v)); }
typedef __amdgpu_buffer_rsrc_t rsrc_t;
DI rsrc_t mkrs(const void* p, u32 bytes) { return __builtin_amdgcn_make_buffer_rsrc((void*)p, 0, bytes, 0x00020000); }
template <int AUX> DI void dma16(rsrc_t rs, u32 lds, u32 voff, u32 soff) { __builtin_amdgcn_raw_ptr_buffer_load_lds(rs, (LAS void*)(size_t)lds, 16, voff, soff, 0, AUX); }
DI f32x2 cmul(f32x2 x, f32x2 y) {
  f32x2 t;
  asm("v_pk_mul_f32 %0, %1, %2 op_sel:[0,0] op_sel_hi:[0,1]\n\tv_pk_fma_f32 %0, %1, %2, %0 op_sel:[1,1,0] op_sel_hi:[1,0,1] neg_lo:[1,0,0]" : "=&v"(t) : "v"(x), "v"(y));
  return t;
}
DI float dot2b(u32 a, u32 b, float c) { return __builtin_amdgcn_fdot2_f32_bf16(bc<bf16x2>(a), bc<bf16x2>(b), c, false); }
DI float wave_sum(float a) {
  a += __builtin_amdgcn_update_dpp(0.f, a, 0xB1, 0xf, 0xf, true);
  a += __builtin_amdgcn_update_dpp(0.f, a, 0x4E, 0xf, 0xf, true);
  a += __builtin_amdgcn_update_dpp(0.f, a, 0x141, 0xf, 0xf, true);
  a += __builtin_amdgcn_update_dpp(0.f, a, 0x140, 0xf, 0xf, true);
  a += __builtin_amdgcn_update_dpp(0.f, a, 0x142, 0xa, 0xf, false);
  a += __builtin_amdgcn_update_dpp(0.f, a, 0x143, 0xc, 0xf, false);
  return bc<float>(__builtin_amdgcn_readlane(bc<int>(a), 63));
}
DI int pi_k(int g, int jj) { return 16 * (jj >> 2) + 4 * g + (jj & 3); }

#define CNOP 1
#define CNOPR 2
#define PFFT 0.6
#define SH2PRIO 2
#define SLP 2
#define RLX_AGENT __ATOMIC_RELAXED, __HIP_MEMORY_SCOPE_AGENT
template <bool P> DI void st16(rsrc_t rs, u32 voff, u32 soff, u32x4 v) {
  __builtin_amdgcn_raw_buffer_store_b128(v, rs, voff, soff, P ? 16 : 0);
  asm volatile("s_nop 1" :: "v"(v) : "memory");
}
template <bool P> DI u32x4 ld16(rsrc_t rs, u32 voff, u32 soff) { return __builtin_amdgcn_raw_buffer_load_b128(rs, voff, soff, P ? 16 : 0); }
DI void publish(u32* set, int idx, u32 epoch) {
  if (threadIdx.x < 8) __hip_atomic_store((GAS u32*)set + threadIdx.x * FR_ + idx * FL, epoch, RLX_AGENT);
}
DI void poll_flags(const u32* first, int n, u32 epoch) {
  const int lane = threadIdx.x & 63;
  const GAS u32* f = (const GAS u32*)first + (lane < n ? lane : 0) * FL;
  for (int spin = 0; spin < (1 << 18); ++spin) {
    if (__all(__hip_atomic_load(f, RLX_AGENT) == epoch)) break;
    __builtin_amdgcn_s_sleep(SLP);
  }
}

#define STAMP(i) do {} while (0)
#define STAMP_DECL do {} while (0)
#define STAMP_FLUSH() do {} while (0)
#define STAMP_ARG
#define STAMP_PASS

DI u32x4 tf(rsrc_t rs, int item, u32 l16) { return __builtin_amdgcn_raw_buffer_load_b128(rs, l16 + (item & 3) * 1024, (item >> 2) * 4096, 0); }
DI u32x4 tfs(rsrc_t rs, int item, u32 l16, u32 so) { return __builtin_amdgcn_raw_buffer_load_b128(rs, l16 + item * 1024, so, 0); }

DI void fft_first_loads(const Params& p, const u32 lb, const int b) {
  const int lane = threadIdx.x & 63, w = __builtin_amdgcn_readfirstlane(threadIdx.x >> 6);
  const u32 l16 = lane * 16;
  const int s2 = b & 63, q = b >> 6;
  const rsrc_t rx = mkrs(p.x, S_ * D_ * 2), rtf = mkrs(TF, TF_N * 1024);
  if (lane < 56) {
    u32 la = lb + L_XT + 8 * w * XRS, so = (64 * 8 * w + s2) * (D_ * 2);
#pragma unroll 1
    for (int r = 0; r < 8; ++r) { dma16<0>(rx, la, l16, so); la += XRS; so += 64 * (D_ * 2); }
  }
  const u32 tq = (TF_Q0 + 6 * q) * 1024;
  dma16<0>(rtf, lb + L_C1 + w * 2048, l16, w == 0 ? 0u : tq + (2 * w - 2) * 1024);
  dma16<0>(rtf, lb + L_C1 + w * 2048 + 1024, l16, w == 0 ? 1024u : tq + (2 * w - 1) * 1024);
}

struct P3C { u32x4 b1, b2; f32x4 tc, ts; u32x2 m; };
template <int PH>
DI void p3_prepare(const Params& p, const u32 lb, const int b, const rsrc_t rtf, P3C& c) {
  const int lane = threadIdx.x & 63, i16 = lane & 15, g = lane >> 4;
  const u32 l16 = lane * 16;
  const int s2 = b & 63, q = b >> 6;
  c.tc = bc<f32x4>(tf(rtf, TF_TC, l16)); c.ts = bc<f32x4>(tf(rtf, TF_TS, l16));
  const u32x4 m4 = tf(rtf, TF_M, l16);
  c.m = u32x2{m4.x, m4.y};
  c.b1 = lds_ld<u32x4>(lb + L_C3 + l16); c.b2 = lds_ld<u32x4>(lb + L_C3 + 1024 + l16);
  pin(c.b1); pin(c.b2); pin(c.tc); pin(c.ts); pin(c.m);
}
DI void p3_make_b(const u32 lb, const int b) {
  const int lane = threadIdx.x & 63, i16 = lane & 15, g = lane >> 4;
  const int s2 = b & 63, q = b >> 6;
  const int ro = i16 >> 3;
  const float a = (float)(64 * (8 * q + (i16 & 7)) + s2) * (1.0f / 2048.0f), P0 = a * (float)(4 * g);
  float u[8], v[8];
#pragma unroll
  for (int jj = 0; jj < 8; ++jj) {
    const float ph = __builtin_fmaf(a, (float)(16 * (jj >> 2) + (jj & 3)), P0);
    u[jj] = sinr(ph + (ro ? 0.f : 0.25f));
    v[jj] = sinr(ph + (ro ? 0.25f : 0.5f));
  }
  lds_st<u32x4>(lb + L_C3 + lane * 16, pk8(u));
  lds_st<u32x4>(lb + L_C3 + 1024 + lane * 16, pk8(v));
}
template <bool LDSX>
DI void gate_rows(const Params& p, const u32 lb, const int b, const int r0, const int nr) {
  const int lane = threadIdx.x & 63;
  const u32 l16 = lane * 16;
  const int s2 = b & 63, q = b >> 6;
  u32x4 gwv = {0, 0, 0, 0};
  if (lane < 56) gwv = *(const GAS u32x4*)((const char*)p.gw + l16);
#pragma unroll
  for (int r = 0; r < 2; ++r) {
    if (r < nr) {
      float acc = 0.f;
      if (lane < 56) {
        u32x4 xv;
        if (LDSX) xv = lds_ld<u32x4>(lb + L_XT + (8 * q + r0 + r) * XRS + l16);
        else xv = *(const GAS u32x4*)((const char*)(p.x + (size_t)(64 * (8 * q + r0 + r) + s2) * D_) + l16);
#pragma unroll
        for (int e = 0; e < 4; ++e) acc = dot2b(pk2(up2(xv[e]) * up2(gwv[e])), 0x3f803f80u, acc);
      }
      const float gg = bfr(__builtin_amdgcn_rcpf(1.0f + __expf(-bfr(wave_sum(acc)))));
      if (lane == 0) lds_st<f32x2>(lb + L_G + (r0 + r) * 8, f32x2{gg, bfr(1.0f - gg)});
    }
  }
}

template <int PH>
DI void fft_phase3(const Params& p, const u32 lb, const int b, u32* ctl, const u32 epoch, const u32x4 (&fc)[2], const u32x4 (&fs)[2], const P3C& c STAMP_ARG) {
  constexpr bool P = PH == 0;
  constexpr int TPAUX = P ? 16 : 0;
  const int tid = threadIdx.x, lane = tid & 63, w = __builtin_amdgcn_readfirstlane(tid >> 6), i16 = lane & 15, g = lane >> 4;
  const u32 l16 = lane * 16;
  const int s2 = b & 63, q = b >> 6;
  const f32x4 z4 = {0, 0, 0, 0};
  const rsrc_t rws = mkrs(p.ws, 16u << 20);
  u32 tpa = lb + L_TP + (4 * g + (i16 >> 2)) * TPRS + (i16 & 3) * 128 + (w >> 1) * 32 + (w & 1) * 8;
  const int row = 2 * w + (i16 >> 3);
  u32 zw = lb + L_Z + ((i16 & 7) * 16 + row) * 64 + ((g ^ (row >> 2)) & 3) * 16;
  u32 zr = lb + L_Z + (w * 16 + i16) * 64 + ((g ^ (i16 >> 2)) & 3) * 16;
  u32 boff = (u32)(tid * B_D);
  u32 biw = lb + L_BI + tid * 2, bir = lb + L_BI + w * BRS + (32 * i16 + 4 * g) * 2;
  u32 ooff = (u32)((64 * (8 * q + w) + s2) * (D_ * 2) + (32 * i16 + 4 * g) * 2);
  u32 mi = (i16 & 1) ? 0u : 0x80008000u;
  pin(tpa); pin(zw); pin(zr); pin(boff); pin(biw); pin(bir); pin(ooff); pin(mi);
  STAMP(13);
  if (P) { poll_flags(ctl + CT_F2 + (b & 7) * FR_ + w * 32 * FL, 32, epoch); bar(); __builtin_amdgcn_s_setprio(3); }
  STAMP(14);
#pragma unroll
  for (int r = 0; r < 4; ++r) dma16<TPAUX>(rws, lb + L_TP + (4 * w + r) * TPRS, l16, (u32)WS_TP + s2 * TP_S2 + (4 * w + r) * 1024);
  wait_vm0();
  bar();
  STAMP(15);
#pragma unroll
  for (int t = 0; t < 2; ++t) {
    u32x4 Tr = cat(trread(tpa + t * 512), trread(tpa + t * 512 + 16 * TPRS));
    u32x4 Ti = cat(trread(tpa + t * 512 + 16), trread(tpa + t * 512 + 16 + 16 * TPRS));
    f32x4 z = mfma32(Ti, c.b2, mfma32(Tr, c.b1, z4));
    lds_st<u32x2>(zw + t * 8, u32x2{pk(z[0], z[1]), pk(z[2], z[3])});
  }
  bar();
  STAMP(16);
  f32x2 ga[2][2];
  {
    u32x4 A = lds_ld<u32x4>(zr), A2 = sx1(A, mi);
    const float gg = lds_ld<float>(lb + L_G + w * 8);
#pragma unroll
    for (int td = 0; td < 2; ++td) {
      f32x4 G = mfma32(A2, fs[td], mfma32(A, fc[td], z4));
      const float c0 = c.tc[2 * td], s0 = c.ts[2 * td], c1 = c.tc[2 * td + 1], s1 = c.ts[2 * td + 1];
      const float g0r = G[0] * c0 - G[1] * s0, g0i = G[1] * c0 + G[0] * s0;
      const float g1r = G[2] * c1 - G[3] * s1, g1i = G[3] * c1 + G[2] * s1;
      f32x4 y = mfma16(u32x2{pk(g0r, g0i), pk(g1r, g1i)}, c.m, z4);
      ga[td][0] = up2(pk2(up2(pk(y[0], y[1])) * gg));
      ga[td][1] = up2(pk2(up2(pk(y[2], y[3])) * gg));
      pin(ga[td][0]); pin(ga[td][1]);
    }
  }
  STAMP(17);
  if (P) { poll_flags(ctl + CT_FB + (b & 7) * FR_ + w * 28 * FL, 28, epoch); bar(); }
  STAMP(18);
  if (tid < D_) {
    const u32x4 bv = ld16<P>(rws, boff, (u32)WS_B + (((s2 & 7) * 8 + (s2 >> 3)) * 32 + 8 * q) * 2);
#pragma unroll
    for (int e = 0; e < 4; ++e) {
      lds_st<u16>(biw + (2 * e) * BRS, (u16)bv[e]);
      lds_st<u16>(biw + (2 * e + 1) * BRS, (u16)(bv[e] >> 16));
    }
  }
  bar();
  {
    const rsrc_t ro_ = mkrs(p.out, S_ * D_ * 2);
    const float og = lds_ld<float>(lb + L_G + w * 8 + 4);
    const u32x2 bq0 = lds_ld<u32x2>(bir), bq1 = lds_ld<u32x2>(bir + 32);
#pragma unroll
    for (int td = 0; td < 2; ++td) {
      const u32x2 bq = td ? bq1 : bq0;
      const f32x2 o01 = ga[td][0] + up2(pk2(up2(bq.x) * og));
      const f32x2 o23 = ga[td][1] + up2(pk2(up2(bq.y) * og));
      if (i16 < 14) __builtin_amdgcn_raw_buffer_store_b64(u32x2{pk2(o01), pk2(o23)}, ro_, ooff, td * 32, 0);
    }
  }
  STAMP(19);
  if (P && b == 0 && tid == 0) __hip_atomic_store((GAS u32*)ctl + CT_GEN, epoch, RLX_AGENT);
}

template <int PH>
DI void fft_upper(const Params& p, const u32 lb, const int b, u32* ctl, u32x4 (&fc)[2], u32x4 (&fs)[2], P3C& c3) {
  constexpr bool P = PH == 0;
  const int lane = threadIdx.x & 63, w = __builtin_amdgcn_readfirstlane(threadIdx.x >> 6);
  const u32 l16 = lane * 16;
  const rsrc_t rtf = mkrs(TF, TF_N * 1024);
  if (!P && w == 4) p3_make_b(lb, b);
  if (P) {
    if (w == 4) p3_make_b(lb, b);
    bar();
    bar(); bar(); bar();
    __builtin_amdgcn_s_setprio(SH2PRIO);
  }
  if (P) gate_rows<P>(p, lb, b, w, 1); else gate_rows<P>(p, lb, b, 2 * (w - 4), 2);
  fc[0] = tf(rtf, TF_FC0, l16); fs[0] = tf(rtf, TF_FS0, l16);
  fc[1] = xr(fc[0], 0x80000000u); fs[1] = xr(fs[0], 0x80000000u);
  pin(fc[0]); pin(fc[1]); pin(fs[0]); pin(fs[1]);
  if (!P) bar();
  p3_prepare<PH>(p, lb, b, rtf, c3);
  if (!P) bar();
}

template <int PH>
DI void fft_role(const Params& p, const u32 lb, const int b, u32* ctl, const u32 gen, u32x4 (&fc)[2], u32x4 (&fs)[2], P3C& c3 STAMP_ARG) {
  constexpr bool P = PH == 0;
#define TPAUXV 16
  constexpr int AUX = P ? 16 : 0, TPAUX = P ? TPAUXV : 0;
  (void)AUX;
  const int tid = threadIdx.x, lane = tid & 63, w = __builtin_amdgcn_readfirstlane(tid >> 6), i16 = lane & 15, g = lane >> 4;
  const u32 l16 = lane * 16;
  const int s2 = b & 63, q = b >> 6;
  const int xx = b & 7, mm = (b >> 3) & 3, kq = b >> 5;
  const int k1w = (((mm & 1) ? (xx ? 32 - xx : 8) : xx) + 16 * (mm >> 1)) & 31;
  const f32x4 z4 = {0, 0, 0, 0};
  const rsrc_t rws = mkrs(p.ws, 16u << 20), rtf = mkrs(TF, TF_N * 1024);
  const u32 tpo = (u32)((tid >> 3) * TP_S2 + (tid & 7) * 16), tps = (u32)WS_TP + k1w * 1024 + kq * 128;
  if (PH == 2 || PH == 3) {
    fc[0] = tf(rtf, TF_FC0, l16); fs[0] = tf(rtf, TF_FS0, l16);
    fc[1] = xr(fc[0], 0x80000000u); fs[1] = xr(fs[0], 0x80000000u);
  }

  u32x4 co[2], so[2], cot[2], sot[2];
  auto load_p2 = [&]() {
    const bool mir = kq >= 4;
    const int col0 = mir ? 448 - (56 * kq + 49) : 56 * kq;
    const rsrc_t rr = mkrs(p.wr, S_ * DF * 2), ri_ = mkrs(p.wi, S_ * DF * 2);
#pragma unroll
    for (int i = 0; i < 3; ++i) {
      const int gi = w + 4 * i;
      if (gi < 9) {
        const int gam = 64 * gi + lane, k2 = (gam * 7282) >> 16, chunk = gam - 9 * k2;
        const int ks = k1w + 32 * k2, row = mir ? ((2048 - ks) & 2047) : ks;
        const u32 vo = (u32)(((row * DF + col0) * 2) & ~15) + chunk * 16;
        dma16<0>(rr, lb + L_W + gi * 1024, vo, 0);
        dma16<0>(ri_, lb + L_W + 9216 + gi * 1024, vo, 0);
      }
    }
    dma16<0>(rtf, lb + L_C2 + w * 1024, l16, (TF_CO0 + w) * 1024);
  };

  if (PH == 0 || PH == 1) {
    u32x4 C1, S1, nS1;
    {
      const float k1 = (float)(16 * (w >> 1) + i16) * (1.0f / 2048.0f), P0 = k1 * (float)(64 * g + s2), Pl = k1 * 256.f;
      float c[8], s[8];
#pragma unroll
      for (int j = 0; j < 8; ++j) { const float ph = __builtin_fmaf(Pl, (float)j, P0); c[j] = cosr(ph); s[j] = sinr(ph); }
      C1 = pk8(c); S1 = pk8(s); nS1 = xr(S1, 0x80008000u);
      pin(C1); pin(S1); pin(nS1);
    }
    int d0a = 8 * (g >> 1) + (i16 >> 2), d0b = d0a + 4;
    d0b = d0b < 14 ? d0b : 0;
    u32 xa = lb + L_XT + (8 * w + (g & 1)) * XRS + 8 * (i16 & 3);
    u32 xb = xa + d0b * 64; xa += d0a * 64;
    u32 xw = lb + L_XCH + w * 256 + lane * 4;
    u32 xr_ = lb + L_XCH + (w & 1) * 4096 + lane * 4;
    u32 toff = (u32)((16 * (w >> 1) + i16) * T_K1 + 4 * (w & 1) * 32 + g * 16);
    u32 imm = (i16 & 1) ? 0x80008000u : 0u;
    pin(xa); pin(xb); pin(xw); pin(xr_); pin(toff); pin(imm);
    const u32 tso = (u32)WS_T + s2 * 1024 + q * 256;
    STAMP(1);
    wait_vm0();
    bar();
    const u32 cb = lb + L_C1 + l16;
    fc[0] = lds_ld<u32x4>(cb); fs[0] = lds_ld<u32x4>(cb + 1024);
    const u32x4 Ba1 = lds_ld<u32x4>(cb + 2048), Ba2 = lds_ld<u32x4>(cb + 3072);
    const f32x4 twc0 = lds_ld<f32x4>(cb + 4096), twc1 = lds_ld<f32x4>(cb + 5120), tws0 = lds_ld<f32x4>(cb + 6144), tws1 = lds_ld<f32x4>(cb + 7168);
    fc[1] = xr(fc[0], 0x80000000u); fs[1] = xr(fs[0], 0x80000000u);
    STAMP(2);
    f32x4 D2[2][2];
#pragma unroll
    for (int sg = 0; sg < 2; ++sg) {
      f32x4 H[2];
#pragma unroll
      for (int td = 0; td < 2; ++td) {
        const int o = td * 32 + sg * 4 * XRS;
        u32x4 A0 = cat(trread(xa + o), trread(xb + o));
        u32x4 A1 = cat(trread(xa + o + 2 * XRS), trread(xb + o + 2 * XRS));
        H[td] = mfma32(A1, Ba2, mfma32(A0, Ba1, z4));
      }
      float h[8];
#pragma unroll
      for (int j = 0; j < 4; ++j) {
        h[j] = __builtin_fmaf(fx1(H[0][j]), tws0[j], H[0][j] * twc0[j]);
        h[4 + j] = __builtin_fmaf(fx1(H[1][j]), tws1[j], H[1][j] * twc1[j]);
      }
      u32x4 A = pk8(h), A2 = sx1(A, imm);
#pragma unroll
      for (int tk = 0; tk < 2; ++tk) D2[sg][tk] = mfma32(A2, fs[tk], mfma32(A, fc[tk], z4));
    }
#pragma unroll
    for (int tk = 0; tk < 2; ++tk)
#pragma unroll
      for (int r = 0; r < 4; ++r) lds_st<u32>(xw + (tk * 4 + r) * 1024, pk(D2[0][tk][r], D2[1][tk][r]));
    bar();
    STAMP(3);
    {
      u32 pc[4][2];
#pragma unroll
      for (int kk = 0; kk < 2; ++kk) {
        const u32 br = xr_ + kk * 2048;
        u32x4 Xr = {lds_ld<u32>(br), lds_ld<u32>(br + 256), lds_ld<u32>(br + 512), lds_ld<u32>(br + 768)};
        u32x4 Xi = {lds_ld<u32>(br + 1024), lds_ld<u32>(br + 1280), lds_ld<u32>(br + 1536), lds_ld<u32>(br + 1792)};
        f32x4 Tr = mfma32(Xi, S1, mfma32(Xr, C1, z4));
        f32x4 Ti = mfma32(Xr, nS1, mfma32(Xi, C1, z4));
        pc[kk][0] = pk(Tr[0], Tr[1]); pc[kk][1] = pk(Tr[2], Tr[3]);
        pc[2 + kk][0] = pk(Ti[0], Ti[1]); pc[2 + kk][1] = pk(Ti[2], Ti[3]);
      }
      u32x4 lo = {pc[0][0], pc[0][1], pc[1][0], pc[1][1]}, hi = {pc[2][0], pc[2][1], pc[3][0], pc[3][1]};
#pragma unroll
      for (int e = 0; e < 4; ++e) {
        auto r1 = __builtin_amdgcn_permlane16_swap(lo[e], hi[e], false, false);
        auto r2 = __builtin_amdgcn_permlane32_swap(r1[0], r1[1], false, false);
        lo[e] = r2[0]; hi[e] = r2[1];
      }
      st16<P>(rws, toff, tso, lo);
      st16<P>(rws, toff + 64, tso, hi);
    }
    STAMP(4);
    if (P) {
      drain_vm();
      STAMP(20);
      bar();
      publish(ctl + CT_F1, b, gen + 1u);
      __builtin_amdgcn_s_setprio(2);
      load_p2();
    }
    STAMP(5);
  }
  const u32 epoch = __builtin_amdgcn_readfirstlane(gen) + 1u;

  if (PH == 2) load_p2();
  if (PH == 0 || PH == 2) {
    const int ri = i16 >> 3;
    u32 tsv = (u32)((lane >> 1) * 1024 + w * 256 + (lane & 1) * 16);
    u32 tiw = lb + L_TI + w * 2048 + ((lane >> 1) & 1) * 1024 + (lane >> 2) * 32 + (lane & 1) * 16;
    u32 ta = lb + L_TI + w * 2048 + (4 * g + (i16 >> 2)) * 32 + (i16 & 3) * 8;
    u32 tow = lb + L_TO + 2 * i16 * 144 + w * 32 + g * 8;
    u32 tor = lb + L_TO + (tid >> 3) * 144 + (tid & 7) * 16;
    u32 mf = ri ? 0x80008000u : 0u, mi = ri ? 0u : 0x80008000u;
    float wr_[16], wi_[16];
    {
      const bool mir = kq >= 4;
      const int c = i16 & 7, kd = 2 * w + (c >> 2) + 14 * (4 * kq + (c & 3)), col0 = mir ? 448 - (56 * kq + 49) : 56 * kq;
      const int row0 = mir ? ((2048 - k1w) & 2047) : k1w;
      u32 wa = lb + L_W + 4 * g * 144 + (((row0 * DF + col0) * 2) & 15) + ((mir ? 448 - kd : kd) - col0) * 2;
      const u32 sg = (mir != (ri != 0)) ? 0u : 0x80000000u;
      pin(tsv); pin(tiw); pin(ta); pin(tow); pin(tor); pin(mf); pin(mi); pin(wa);
      STAMP(7);
      if (P) poll_flags(ctl + CT_F1 + (b & 7) * FR_ + w * 64 * FL, 64, epoch);
      wait_vm0();
      bar();
      const u32 so_ = (u32)WS_T + k1w * T_K1 + kq * 32;
      const u32x4 t0 = ld16<P>(rws, tsv, so_), t1 = ld16<P>(rws, tsv, so_ + 32 * 1024);
      __builtin_amdgcn_sched_barrier(0);
      co[0] = lds_ld<u32x4>(lb + L_C2 + l16); so[0] = lds_ld<u32x4>(lb + L_C2 + 1024 + l16);
      cot[0] = lds_ld<u32x4>(lb + L_C2 + 2048 + l16); sot[0] = lds_ld<u32x4>(lb + L_C2 + 3072 + l16);
      co[1] = xr(so[0], 0x00008000u);
      so[1] = xr(co[0], 0x80000000u);
      cot[1] = xr(cot[0], 0x80000000u); sot[1] = xr(sot[0], 0x80000000u);
      pin(co[0]); pin(co[1]); pin(so[0]); pin(so[1]); pin(cot[0]); pin(cot[1]); pin(sot[0]); pin(sot[1]);
#pragma unroll
      for (int j = 0; j < 16; ++j) {
        const int o = (32 * (j >> 3) + 16 * ((j >> 2) & 1) + (j & 3)) * 144;
        wr_[j] = bc<float>((u32)lds_ld<u16>(wa + o) << 16);
        wi_[j] = bc<float>(((u32)lds_ld<u16>(wa + 9216 + o) << 16) + sg);
      }
#pragma unroll
      for (int j = 0; j < 16; ++j) { pin(wr_[j]); pin(wi_[j]); }
      __builtin_amdgcn_sched_barrier(0);
      if (P) __builtin_amdgcn_s_setprio(3);
      STAMP(8);
      lds_st<u32x4>(tiw, t0);
      lds_st<u32x4>(tiw + 512, t1);
    }
    STAMP(9);
    f32x4 E[2], O[2];
    {
      u32x4 B0 = cat(trread(ta), trread(ta + 512)), B1 = cat(trread(ta + 1024), trread(ta + 1536));
      u32x4 B0s = sr8(B0, mf), B1s = sr8(B1, mf);
#pragma unroll
      for (int t = 0; t < 2; ++t) {
        E[t] = mfma32(fs[t], B0s, mfma32(fc[t], B0, z4));
        O[t] = mfma32(so[t], B1s, mfma32(co[t], B1, z4));
      }
    }
    float Pp[8], Mm[8];
#pragma unroll
    for (int j = 0; j < 8; ++j) {
      const float e = E[j >> 2][j & 3], o = O[j >> 2][j & 3];
      const float ylo = e + o, yhi = e - o;
      const float vlo = __builtin_fmaf(fr8(ylo), wi_[j], ylo * wr_[j]);
      const float vhi = __builtin_fmaf(fr8(yhi), wi_[8 + j], yhi * wr_[8 + j]);
      Pp[j] = vlo + vhi; Mm[j] = vlo - vhi;
    }
    {
      u32x4 AP = pk8(Pp), AM = pk8(Mm);
      u32x4 APs = sr8(AP, mi), AMs = sr8(AM, mi);
#pragma unroll
      for (int t = 0; t < 2; ++t) {
        f32x4 ze = mfma32(APs, fs[t], mfma32(AP, fc[t], z4));
        f32x4 zo = mfma32(AMs, sot[t], mfma32(AM, cot[t], z4));
        lds_st<u32x2>(tow + t * 32 * 144, u32x2{pk(ze[0], ze[1]), pk(ze[2], ze[3])});
        lds_st<u32x2>(tow + t * 32 * 144 + 144, u32x2{pk(zo[0], zo[1]), pk(zo[2], zo[3])});
      }
    }
    bar();
    STAMP(10);
    {
      st16<P>(rws, tpo, tps, lds_ld<u32x4>(tor));
      st16<P>(rws, tpo, tps + 32 * TP_S2, lds_ld<u32x4>(tor + 32 * 144));
    }
    STAMP(11);
    if (P) { drain_vm(); bar(); publish(ctl + CT_F2, b, epoch); __builtin_amdgcn_s_setprio(SH2PRIO); }
    STAMP(12);
  }

  if (PH == 0) gate_rows<true>(p, lb, b, w, 1);
  if (PH == 3) bar();
  if (PH == 0 || PH == 3) p3_prepare<PH>(p, lb, b, rtf, c3);
  if (PH == 3) bar();
}

DI f32x2 cexp_rev(float x2, float ph) {
  const float e = __builtin_amdgcn_exp2f(x2);
  const float f = __builtin_amdgcn_fractf(ph);
  f32x2 cs = {cosr(f), sinr(f)};
  return cs * e;
}
struct KW { u32 a[8]; };
template <int ROW> DI void wrow(const KW& k, f32x2 v) { lds_st<u32>(k.a[ROW & 7] + ROW * 256, pk2(v)); }
template <int ROW> DI void wrow2(const KW& k, f32x2 lo, f32x2 hi) {
  const u32 a = pk2(lo), b = pk2(hi);
  asm volatile("ds_write2st64_b32 %0, %1, %2 offset0:%c3 offset1:%c4" :: "v"(k.a[ROW & 7]), "v"(a), "v"(b), "i"(ROW), "i"(ROW + 8) : "memory");
}
DI void kfrag(u32 fa, u32 fb, u32x4 (&A)[4]) {
  A[0] = lds_ld<u32x4>(fa); A[1] = lds_ld<u32x4>(fb); A[2] = lds_ld<u32x4>(fa + 128); A[3] = lds_ld<u32x4>(fb + 128);
}
DI f32x4 ktile(const u32x4 (&A)[4], const u32x4 (&V)[4]) {
  f32x4 acc = {0, 0, 0, 0};
#pragma unroll
  for (int ks = 0; ks < 4; ++ks) acc = mfma32(A[ks], V[ks], acc);
  return acc;
}
template <class F0, class F1>
DI void kgen(const u32 (&in)[6], float dt, const KW& k, u32 fa, u32 fb, u32 pb, F0&& hook0, F1&& hook1) {
  const float ar = dt * bc<float>(in[0] << 16), ai = dt * bc<float>(in[1] << 16);
  const f32x2 dB = {dt * bc<float>(in[2] << 16), dt * bc<float>(in[3] << 16)};
  const f32x2 Cc = {bc<float>(in[4] << 16), bc<float>(in[5] << 16)};
  const f32x2 CB = cmul(Cc, dB);
  const float x1 = ar * 1.44269504088896341f, p1 = ai * 0.15915494309189535f;
  const f32x2 E1 = cexp_rev(x1, p1), E8c = cexp_rev(8.f * x1, -8.f * p1), E128 = cexp_rev(128.f * x1, 128.f * p1);
  u32x4 V[4], A0[4], A1[4];
  {
    const f32x2 v2 = cmul(E8c, E8c), v3 = cmul(v2, E8c), v4 = cmul(v2, v2), v8 = cmul(v4, v4);
    const f32x2 v5 = cmul(v4, E8c), v6 = cmul(v4, v2), v7 = cmul(v4, v3);
    wrow2<0>(k, f32x2{1.f, 0.f}, v8); wrow2<1>(k, E8c, cmul(v8, E8c)); wrow2<2>(k, v2, cmul(v8, v2)); wrow2<3>(k, v3, cmul(v8, v3));
    wrow2<4>(k, v4, cmul(v8, v4)); wrow2<5>(k, v5, cmul(v8, v5)); wrow2<6>(k, v6, cmul(v8, v6)); wrow2<7>(k, v7, cmul(v8, v7));
    kfrag(fa, fb, V);
  }
  hook0();
  const f32x2 E2 = cmul(E1, E1), E3 = cmul(E2, E1), E4 = cmul(E2, E2), E5 = cmul(E4, E1), E6 = cmul(E4, E2), E7 = cmul(E4, E3);
  const f32x2 G1 = cmul(CB, E128);
  wrow2<0>(k, CB, G1); wrow2<1>(k, cmul(CB, E1), cmul(G1, E1)); wrow2<2>(k, cmul(CB, E2), cmul(G1, E2)); wrow2<3>(k, cmul(CB, E3), cmul(G1, E3));
  wrow2<4>(k, cmul(CB, E4), cmul(G1, E4)); wrow2<5>(k, cmul(CB, E5), cmul(G1, E5)); wrow2<6>(k, cmul(CB, E6), cmul(G1, E6)); wrow2<7>(k, cmul(CB, E7), cmul(G1, E7));
  kfrag(fa, fb, A0);
  const f32x2 G2 = cmul(G1, E128), G3 = cmul(G2, E128), G4 = cmul(G3, E128), G5 = cmul(G4, E128), G6 = cmul(G5, E128), G7 = cmul(G6, E128);
  wrow2<0>(k, G2, G4); wrow2<1>(k, cmul(G2, E2), cmul(G4, E4)); wrow2<2>(k, cmul(G2, E4), G5); wrow2<3>(k, cmul(G2, E6), cmul(G5, E4));
  wrow2<4>(k, G3, G6); wrow2<5>(k, cmul(G3, E2), cmul(G6, E4)); wrow2<6>(k, cmul(G3, E4), G7); wrow2<7>(k, cmul(G3, E6), cmul(G7, E4));
  kfrag(fa, fb, A1);
  hook1();
  __builtin_amdgcn_sched_barrier(0);
  const f32x4 a0 = ktile(A0, V);
  const f32x2 G8 = cmul(G7, E128), G9 = cmul(G8, E128), G10 = cmul(G9, E128), G11 = cmul(G10, E128);
  const f32x2 G12 = cmul(G11, E128), G13 = cmul(G12, E128), G14 = cmul(G13, E128), G15 = cmul(G14, E128);
  wrow2<0>(k, G8, cmul(G15, E128)); wrow<1>(k, G9); wrow<2>(k, G10); wrow<3>(k, G11);
  wrow<4>(k, G12); wrow<5>(k, G13); wrow<6>(k, G14); wrow<7>(k, G15);
  kfrag(fa, fb, A0);
  __builtin_amdgcn_sched_barrier(0);
  const f32x4 a1 = ktile(A1, V), a2 = ktile(A0, V);
  lds_st<f32x4>(pb, a0);
  lds_st<f32x4>(pb + 1024, a1);
  lds_st<f32x4>(pb + 2048, a2);
}

struct SsmLoads { u32x4 xt_v, kt0; u32x2 kt1; u32 in[6], ldt, dsk; };
template <int PH>
DI void ssm_first_loads(const Params& p, const int j, SsmLoads& L) {
  const int tid = threadIdx.x, w = __builtin_amdgcn_readfirstlane(tid >> 6), d = 2 * j, ch = w >> 2, n = tid & 255;
  const int sb_ = j / 7, db_ = j - sb_ * 7;
  if (PH == 0 || PH == 1)
    L.xt_v = __builtin_amdgcn_raw_buffer_load_b128(mkrs(p.x, S_ * D_ * 2), (u32)((tid >> 3) * (D_ * 2) + (tid & 7) * 16), (u32)(64 * sb_ * (D_ * 2) + db_ * 128), 0);
  __builtin_amdgcn_sched_barrier(0);
  if (PH != 1) {
    const u32 vo = ((d + ch) * NS + n) * 2;
    const u16* arr[6] = {p.are, p.aim, p.bre, p.bim, p.cre, p.cim};
#pragma unroll
    for (int a = 0; a < 6; ++a) L.in[a] = *(const GAS u16*)((const GAS char*)arr[a] + vo);
    L.ldt = *(const GAS u16*)(p.logdt + d + ch);
    L.dsk = *(const GAS u16*)(p.dskip + d + ((w >> 1) & 1));
    L.kt0 = *(const GAS u32x4*)(KTAB.v + 8 * n);
    L.kt1 = *(const GAS u32x2*)(KTAB.v + 8 * n + 8);
  }
}

template <int PH>
DI void ssm_role(const Params& p, const u32 lb, const int j, u32* ctl, const u32 gen, const SsmLoads& L STAMP_ARG) {
  constexpr bool P = PH == 0;
  const int tid = threadIdx.x, lane = tid & 63, w = __builtin_amdgcn_readfirstlane(tid >> 6), i16 = lane & 15, g = lane >> 4;
  const int d = 2 * j, ch = w >> 2, n = tid & 255;
  const f32x4 z4 = {0, 0, 0, 0};
  const rsrc_t rws = mkrs(p.ws, 16u << 20);
  const int sb_ = j / 7, db_ = j - sb_ * 7;
  const u32x4 xt_v = L.xt_v, kt0 = L.kt0;
  const u32x2 kt1 = L.kt1;
  const u32 ldt = L.ldt;
  u32 dsk = L.dsk;
  const u32 (&in)[6] = L.in;
  auto transpose = [&]() {
    lds_st<u32x4>(lb + L_XP + (tid >> 3) * 144 + (tid & 7) * 16, xt_v);
    bar();
    const int chi = g + 4 * (w >> 2);
    const u32 a = lb + L_XP + (8 * chi + (i16 >> 2)) * 144 + (16 * (w & 3) + 4 * (i16 & 3)) * 2;
    st16<P>(rws, (u32)((16 * (w & 3) + i16) * XT_D + chi * 16), (u32)WS_XT + 64 * db_ * XT_D + sb_ * 128, cat(trread(a), trread(a + 4 * 144)));
  };
  if (PH == 1) { transpose(); return; }
  if (P) {
    if (tid == 0) lds_st<u32>(lb + L_CNT, 0u);
    __builtin_amdgcn_sched_barrier(0);
    transpose();
    __builtin_amdgcn_s_setprio(1);
    __builtin_amdgcn_sched_barrier(0);
  }
  KW kw;
  const u32 sb = lb + w * KSCR;
#pragma unroll
  for (int h = 0; h < 8; ++h) { kw.a[h] = sb + (((lane >> 2) ^ h) << 4) + (lane & 3) * 4; pin(kw.a[h]); }
  u32 fa = sb + i16 * 256 + ((g ^ (i16 & 3)) << 4) + ((i16 >> 2) & 1) * 64, fb = fa ^ 64;
  pin(fa); pin(fb);
  STAMP(1);
  kgen(in, bfr(expf(bc<float>(ldt << 16))), kw, fa, fb, sb + lane * 16,
       [&]() {
         if (P) {
           drain_vm();
           STAMP(5);
           if (lane == 0 && __hip_atomic_fetch_add((LAS u32*)(size_t)(lb + L_CNT), 1u, __ATOMIC_RELAXED, __HIP_MEMORY_SCOPE_WORKGROUP) == 7u)
             __hip_atomic_store((GAS u32*)ctl + CT_FX + (db_ * 32 + sb_) * FL, gen + 1u, RLX_AGENT);
         }
       },
       []() {});
  const u32 epoch = __builtin_amdgcn_readfirstlane(gen) + 1u;
  pin(dsk);
  STAMP(2);
  if (P && w == 0) poll_flags(ctl + CT_FX + (d >> 6) * 32 * FL, 32, epoch);
  STAMP(6);
  bar();
  const u32x4 uv_ = ld16<P>(rws, n * 16, (u32)WS_XT + (d + ch) * XT_D);
  STAMP(3);
  if ((w & 3) < 3) {
    const u32 pb = lb + ch * 4 * KSCR + (w & 3) * 1024 + lane * 16;
    const f32x4 s = (lds_ld<f32x4>(pb) + lds_ld<f32x4>(pb + KSCR)) + (lds_ld<f32x4>(pb + 2 * KSCR) + lds_ld<f32x4>(pb + 3 * KSCR));
    const u32 tb = lb + L_KT + ch * 1600 + ((w & 3) * 256 + 64 * g + i16) * 2, a = pk(s[0], s[1]), cc = pk(s[2], s[3]);
    lds_st<u16>(tb, (u16)a); lds_st<u16>(tb + 32, (u16)(a >> 16));
    lds_st<u16>(tb + 64, (u16)cc); lds_st<u16>(tb + 96, (u16)(cc >> 16));
  } else {
    if (lane == 0) lds_st<u16>(lb + L_KT + ch * 1600 + 1536, (u16)0);
    if (lane < 32) lds_st<u32x4>(lb + L_U + ch * 4608 + lane * 16, u32x4{0, 0, 0, 0});
  }
  bar();
  STAMP(4);
  {
    const u32 ktb = lb + L_KT + ch * 1600;
    u32 e[11];
#pragma unroll
    for (int k = 0; k < 4; ++k) { e[2 * k] = lds_ld<u16>(ktb + (kt0[k] & 0xffffu)); e[2 * k + 1] = lds_ld<u16>(ktb + (kt0[k] >> 16)); }
    e[8] = lds_ld<u16>(ktb + (kt1[0] & 0xffffu)); e[9] = lds_ld<u16>(ktb + (kt1[0] >> 16)); e[10] = lds_ld<u16>(ktb + (kt1[1] & 0xffffu));
    const u32 k00 = lds_ld<u16>(ktb);
    u32 dw[5], al[5];
#pragma unroll
    for (int k = 0; k < 5; ++k) { dw[k] = e[2 * k] | (e[2 * k + 1] << 16); al[k] = e[2 * k + 1] | (e[2 * k + 2] << 16); }
    const u32 kb = lb + L_KC + ch * 4 * KCP + n * 16;
    lds_st<u32x4>(kb, u32x4{dw[0], dw[1], dw[2], dw[3]});
    lds_st<u32x4>(kb + KCP, u32x4{al[0], al[1], al[2], al[3]});
    lds_st<u32x4>(kb + 2 * KCP, u32x4{dw[1], dw[2], dw[3], dw[4]});
    lds_st<u32x4>(kb + 3 * KCP, u32x4{al[1], al[2], al[3], al[4]});
    if (n < 16) {
      u32x4 z = {0, 0, 0, 0};
      if (n == 0) z.x = k00;
      lds_st<u32x4>(lb + L_KC + (ch * 4 + (n >> 2)) * KCP + 4096 + (n & 3) * 16, z);
    }
    lds_st<u32x4>(lb + L_U + ch * 4608 + 512 + n * 16, uv_);
  }
  bar();
  STAMP(7);
  f32x4 acc[8];
  const int cc = (w >> 1) & 1, hf = w & 1;
  if (w < 4) {
#pragma unroll
    for (int c = 0; c < 8; ++c) acc[c] = z4;
    const int bsh = (-i16) & 3;
    u32 abase = lb + L_KC + (cc * 4 + bsh) * KCP + (2032 + 8 * g - i16 - bsh) * 2 - hf * 4 * 64 - 3 * 64 - 7 * 512;
    u32 bbase = lb + L_U + cc * 4608 + (256 - 16 + 16 * i16 + 8 * g) * 2 - hf * 4 * 64 - 3 * 64;
    u32 abase8 = abase + 8;
    pin(abase); pin(abase8); pin(bbase);
    u32x4 A[8], B[8];
#pragma unroll
    for (int qq = 0; qq < 8; ++qq) {
      B[7 - qq] = lds_ld<u32x4>(bbase + (7 - qq) * 512 + 3 * 64);
      A[qq] = cat(lds_ld<u32x2>(abase + (7 - qq) * 512 + 3 * 64), lds_ld<u32x2>(abase8 + (7 - qq) * 512 + 3 * 64));
      __builtin_amdgcn_sched_barrier(0);
    }
#pragma unroll
    for (int rr = 0; rr < 4; ++rr) {
#pragma unroll
      for (int qq = 0; qq < 8; ++qq) {
#pragma unroll
        for (int m = 7 - qq; m >= 0; --m) {
          acc[qq + m] = mfma32(A[qq], B[m], acc[qq + m]);
          if (CNOP >= 0 && rr < CNOPR) asm volatile("s_nop %c0" :: "i"(CNOP >= 0 ? CNOP : 0));
        }
        if (rr < 3) {
          A[qq] = cat(lds_ld<u32x2>(abase + (7 - qq) * 512 + (2 - rr) * 64), lds_ld<u32x2>(abase8 + (7 - qq) * 512 + (2 - rr) * 64));
          B[7 - qq] = lds_ld<u32x4>(bbase + (7 - qq) * 512 + (2 - rr) * 64);
        }
        __builtin_amdgcn_sched_barrier(0);
      }
    }
    if (hf) {
#pragma unroll
      for (int c = 0; c < 4; ++c) lds_st<f32x4>(lb + L_RED + (w * 4 + c) * 1024 + lane * 16, acc[c]);
#pragma unroll
      for (int c = 0; c < 4; ++c) acc[c] = acc[4 + c];
    } else {
#pragma unroll
      for (int c = 0; c < 4; ++c) lds_st<f32x4>(lb + L_RED + (w * 4 + c) * 1024 + lane * 16, acc[4 + c]);
    }
  }
  bar();
  STAMP(8);
  if (w < 4) {
    const float dk = bc<float>(dsk << 16);
    const u32 tb = (1024 * hf + 16 * i16 + 4 * g) * 2;
    f32x4 o[4];
    u32x2 uv[4];
#pragma unroll
    for (int c = 0; c < 4; ++c) {
      o[c] = lds_ld<f32x4>(lb + L_RED + ((w ^ 1) * 4 + c) * 1024 + lane * 16);
      uv[c] = lds_ld<u32x2>(lb + L_U + cc * 4608 + 512 + tb + c * 512);
    }
#pragma unroll
    for (int c = 0; c < 4; ++c) {
      const f32x4 y = acc[c] + o[c];
      const f32x2 b01 = up2(pk(y[0], y[1])) + up2(pk2(up2(uv[c].x) * dk));
      const f32x2 b23 = up2(pk(y[2], y[3])) + up2(pk2(up2(uv[c].y) * dk));
      lds_st<u32x2>(lb + L_BO + cc * 4096 + tb + c * 512, u32x2{pk2(b01), pk2(b23)});
    }
  }
  bar();
  STAMP(9);
  {
    const int st = w & 3, s2 = 16 * st + i16;
    const u32 a = lb + L_BO + ch * 4096 + (8 * g + (i16 >> 2)) * 128 + (16 * st + 4 * (i16 & 3)) * 2;
    st16<P>(rws, (u32)((((s2 & 7) * 8 + (s2 >> 3)) * 32 + 8 * g) * 2), (u32)WS_B + (d + ch) * B_D, cat(trread(a), trread(a + 4 * 128)));
  }
  STAMP(10);
  if (P) { drain_vm(); bar(); publish(ctl + CT_FB, j, epoch); }
  STAMP(11);
}

template <int PH>
__global__ __launch_bounds__(NT, 4) void gfs4_kernel(Params p) {
  extern __shared__ __attribute__((aligned(16))) char smem[];
  const u32 lb = (u32)(size_t)(LAS char*)smem;
  const int b = blockIdx.x;
  u32* ctl = (u32*)(p.ws + WS_CTL);
  STAMP_DECL;
  if (PH == 0 || PH == 1) STAMP(0); else if (PH == 2) STAMP(20); else STAMP(21);
  u32 gen = 0;
  asm volatile("" :: "s"(p.x), "s"(p.wr), "s"(p.wi), "s"(p.are), "s"(p.aim), "s"(p.bre), "s"(p.bim));
  asm volatile("" :: "s"(p.cre), "s"(p.cim), "s"(p.logdt), "s"(p.dskip), "s"(p.gw), "s"(p.out), "s"(p.ws));
  SsmLoads L;
  if (PH == 0) __builtin_amdgcn_s_setprio(3);
  if (__builtin_expect_with_probability(b < N_FFT, 1, PFFT)) {
    if (__builtin_expect(threadIdx.x < 256, 1)) { if (PH == 0 || PH == 1) fft_first_loads(p, lb, b); }
  } else {
    ssm_first_loads<PH>(p, b - N_FFT, L);
  }
  if (PH == 0) gen = __hip_atomic_load((GAS u32*)ctl + CT_GEN, RLX_AGENT);
  __builtin_amdgcn_sched_barrier(0);
  if (PH == 0 && b < N_FFT && threadIdx.x >= 256) {
    asm volatile("s_barrier\n\ts_barrier" ::: "memory");
    __builtin_amdgcn_sched_barrier(0);
  }
  if (b < N_FFT) {
    u32x4 fc[2], fs[2];
    P3C c3;
    if (threadIdx.x >= 256) {
      if (PH == 1 || PH == 2) return;
      fft_upper<PH>(p, lb, b, ctl, fc, fs, c3);
    } else fft_role<PH>(p, lb, b, ctl, gen, fc, fs, c3 STAMP_PASS);
    if (PH == 0 || PH == 3) fft_phase3<PH>(p, lb, b, ctl, __builtin_amdgcn_readfirstlane(gen) + 1u, fc, fs, c3 STAMP_PASS);
  }
  else ssm_role<PH>(p, lb, b - N_FFT, ctl, gen, L STAMP_PASS);
  STAMP_FLUSH();
}

extern "C" void kernel_launch(void* const* d_in, const int* in_sizes, int n_in,
                              void* d_out, int out_size, void* d_ws, size_t ws_size,
                              hipStream_t stream) {
  (void)in_sizes; (void)n_in; (void)out_size; (void)ws_size;
  Params p;
  p.x = (const u16*)d_in[0]; p.wr = (const u16*)d_in[1]; p.wi = (const u16*)d_in[2];
  p.are = (const u16*)d_in[4]; p.aim = (const u16*)d_in[5]; p.bre = (const u16*)d_in[6]; p.bim = (const u16*)d_in[7];
  p.cre = (const u16*)d_in[8]; p.cim = (const u16*)d_in[9]; p.logdt = (const u16*)d_in[10]; p.dskip = (const u16*)d_in[11];
  p.gw = (const u16*)d_in[12];
  p.out = (u16*)d_out; p.ws = (char*)d_ws;
  (void)hipFuncSetAttribute((const void*)gfs4_kernel<0>, hipFuncAttributeMaxDynamicSharedMemorySize, LDS_BYTES);
  gfs4_kernel<0><<<N_WG, NT, LDS_BYTES, stream>>>(p);
}
```

```cpp
#include <hip/hip_runtime.h>
#include <hip/hip_bf16.h>
#include <stdint.h>

typedef unsigned int u32;
typedef unsigned short u16;
typedef unsigned long long u64;
typedef float f32x4 __attribute__((ext_vector_type(4)));
typedef float f32x2 __attribute__((ext_vector_type(2)));
typedef u32 u32x4 __attribute__((ext_vector_type(4)));
typedef u32 u32x2 __attribute__((ext_vector_type(2)));
typedef __bf16 bf16x8 __attribute__((ext_vector_type(8)));
typedef __bf16 bf16x2 __attribute__((ext_vector_type(2)));
typedef short s16x4 __attribute__((ext_vector_type(4)));
#define LAS __attribute__((address_space(3)))
#define GAS __attribute__((address_space(1)))
#define DI __device__ __forceinline__

#define DIAG 10
#define DK 2
#define DR 0
#define NLAUNCH 1

constexpr int S_ = 2048, D_ = 448, DF = 225, NS = 256;
constexpr int N_FFT = 256, N_SSM = 224, N_WG = N_FFT + N_SSM, NT = 512;

constexpr u32 T_K1 = 65536 + 256;
constexpr u32 TP_S2 = 32768 + 128;
constexpr u32 XT_D = 4096 + 128;
constexpr u32 B_D = 4096 + 128;
constexpr size_t WS_CTL = 0;
constexpr size_t WS_T   = 1u << 20;
constexpr size_t WS_TP  = WS_T + (3u << 20);
constexpr size_t WS_XT  = WS_TP + (3u << 20);
constexpr size_t WS_B   = WS_XT + (2u << 20);
constexpr int FL = 32, FR_ = 256 * FL + 64;
constexpr int CT_GEN = 0;
constexpr int CT_F1 = 64;
constexpr int CT_F2 = CT_F1 + 8 * FR_;
constexpr int CT_FB = CT_F2 + 8 * FR_;
constexpr int CT_FX = CT_FB + 8 * FR_;
static_assert((CT_FX + 7 * 32 * FL) * 4 <= (1 << 20), "control block");

constexpr int XRS = 928;
constexpr int L_XT = 0;
constexpr int L_XCH = 29696;
constexpr int TPRS = 1040;
constexpr int L_TP = 0;
constexpr int L_TO = 29696;
constexpr int L_TI = 38912;
constexpr int L_BI = 38912;
constexpr int BRS = 912;
constexpr int L_Z = 47104;
constexpr int L_C1 = 38912;
constexpr int L_C2 = 65536;
constexpr int L_C3 = 70400;
constexpr int L_W = 47104;
constexpr int L_G = 70208;
constexpr int KCP = 4160;
constexpr int L_KC = 0;
constexpr int KSCR = 4096;
constexpr int L_RED = 33280;
constexpr int L_BO = 49664;
constexpr int L_U = 57856;
constexpr int L_XP = 57856;
constexpr int L_KT = 67072;
constexpr int L_CNT = 70272;
constexpr int LDS_BYTES = 72448;

struct Params {
  const u16* x; const u16* wr; const u16* wi;
  const u16* are; const u16* aim; const u16* bre; const u16* bim; const u16* cre; const u16* cim;
  const u16* logdt; const u16* dskip; const u16* gw;
  u16* out; char* ws;
};

constexpr double cx_sin_oct(double x) { double x2 = x * x, t = x, s = x; for (int i = 1; i < 9; ++i) { t *= -x2 / ((2 * i) * (2 * i + 1)); s += t; } return s; }
constexpr double cx_cos_oct(double x) { double x2 = x * x, t = 1, s = 1; for (int i = 1; i < 9; ++i) { t *= -x2 / ((2 * i - 1) * (2 * i)); s += t; } return s; }
constexpr double cx_sin(long n, long d) {
  n %= d; if (n < 0) n += d;
  const long o = (8 * n) / d;
  const double r = 6.283185307179586476925 * (double)(8 * n - o * d) / (8.0 * (double)d), q = 0.78539816339744830962;
  switch (o) {
    case 0: return cx_sin_oct(r);       case 1: return cx_cos_oct(q - r);
    case 2: return cx_cos_oct(r);       case 3: return cx_sin_oct(q - r);
    case 4: return -cx_sin_oct(r);      case 5: return -cx_cos_oct(q - r);
    case 6: return -cx_cos_oct(r);      default: return -cx_sin_oct(q - r);
  }
}
constexpr double cx_cos(long n, long d) { return cx_sin(4 * n + d, 4 * d); }
constexpr u32 cx_f32(double v) { return __builtin_bit_cast(u32, (float)v); }
constexpr u32 cx_bf(double v) { const u32 u = cx_f32(v); return (u + 0x7fffu + ((u >> 16) & 1u)) >> 16; }
constexpr int cx_pi(int g, int jj) { return 16 * (jj >> 2) + 4 * g + (jj & 3); }
struct Frag { u32 v[64][4]; };
enum { TF_FC0, TF_FS0, TF_CO0, TF_SO0, TF_COT0, TF_SOT0, TF_TC, TF_TS, TF_M, TF_Q0, TF_N = TF_Q0 + 24 };
constexpr Frag make_frag(int item) {
  Frag f{};
  for (int lane = 0; lane < 64; ++lane) {
    const int i16 = lane & 15, g = lane >> 4;
    if (item < TF_TC) {
      const int sn = item & 1;
      for (int jj = 0; jj < 8; ++jj) {
        const int m = cx_pi(g, jj), a = i16;
        long n = 0, d = 32;
        if (item < TF_CO0) { n = a * m; d = 32; }
        else if (item < TF_COT0) { n = a * (2 * m + 1); d = 64; }
        else { n = m * (2 * a + 1); d = 64; }
        f.v[lane][jj >> 1] |= cx_bf(sn ? cx_sin(n, d) : cx_cos(n, d)) << (16 * (jj & 1));
      }
    } else if (item == TF_TC || item == TF_TS) {
      for (int j = 0; j < 4; ++j) {
        const long n = (2 * g + (j & 1)) * (16 * (j >> 1) + i16);
        f.v[lane][j] = cx_f32(item == TF_TC ? cx_cos(n, 448) : cx_sin(n, 448));
      }
    } else if (item == TF_M) {
      for (int j = 0; j < 4; ++j) {
        const int k0 = 2 * g + (j >> 1);
        const double cf = ((k0 == 0 || k0 == 7) ? 1.0 : 2.0) / (2048.0 * 448.0);
        const double v = i16 < 14 ? cf * ((j & 1) ? -cx_sin(k0 * i16, 14) : cx_cos(k0 * i16, 14)) : 0.0;
        f.v[lane][j >> 1] |= cx_bf(v) << (16 * (j & 1));
      }
    } else {
      const int q = (item - TF_Q0) / 6, sub = (item - TF_Q0) % 6, k0 = 2 * q + ((i16 >> 1) & 1), ri = i16 & 1;
      if (sub < 2) {
        if ((i16 >> 2) == 2 * sub + (g & 1))
          for (int j = 0; j < 8; ++j) {
            const int d0 = 8 * (g >> 1) + j;
            const double v = d0 < 14 ? (ri ? -cx_sin(d0 * k0, 14) : cx_cos(d0 * k0, 14)) : 0.0;
            f.v[lane][j >> 1] |= cx_bf(v) << (16 * (j & 1));
          }
      } else {
        for (int j = 0; j < 4; ++j) {
          const long n = cx_pi(g, 4 * (sub & 1) + j) * k0;
          f.v[lane][j] = cx_f32(sub < 4 ? cx_cos(n, 448) : (ri ? -cx_sin(n, 448) : cx_sin(n, 448)));
        }
      }
    }
  }
  return f;
}
template <int I> struct FragC { static constexpr Frag f = make_frag(I); };
#define FR(i) FragC<(i)>::f
#define FR6(i) FR(i), FR(i + 1), FR(i + 2), FR(i + 3), FR(i + 4), FR(i + 5)
__device__ const __attribute__((aligned(1024))) Frag TF[TF_N] = {FR6(0), FR(6), FR(7), FR(8), FR6(9), FR6(15), FR6(21), FR6(27)};

struct KTab { u16 v[2080]; };
constexpr KTab make_ktab() {
  KTab t{};
  for (int i = 0; i < 2080; ++i) {
    const int l = 2048 - i;
    int idx = 768;
    if (l >= 0) {
      const int sh = l > 256 ? (l >= 1024 ? 3 : (l >= 512 ? 2 : 1)) : 0;
      const int v = sh ? (((l + (1 << (sh - 1)) - 1 + ((l >> sh) & 1)) >> sh) << sh) : l;
      if (v < 256) idx = ((v & 7) + 8 * (v >> 7)) * 16 + ((v >> 3) & 15);
      else if (v < 512) idx = 256 + ((((v - 256) >> 1) & 3) + 4 * ((v - 256) >> 7)) * 16 + (((v - 256) >> 3) & 15);
      else if (v < 1024) idx = 256 + (8 + (((v - 512) >> 2) & 1) + 2 * ((v - 512) >> 7)) * 16 + (((v - 512) >> 3) & 15);
      else if (v < 2048) idx = 512 + ((v - 1024) >> 7) * 16 + (((v - 1024) >> 3) & 15);
      else idx = 640;
    }
    t.v[i] = (u16)(2 * idx);
  }
  return t;
}
__device__ const __attribute__((aligned(16))) KTab KTAB = make_ktab();

template <class T, class F> DI T bc(F v) { return __builtin_bit_cast(T, v); }
DI f32x4 mfma32(u32x4 a, u32x4 b, f32x4 c) { return __builtin_amdgcn_mfma_f32_16x16x32_bf16(bc<bf16x8>(a), bc<bf16x8>(b), c, 0, 0, 0); }
DI f32x4 mfma16(u32x2 a, u32x2 b, f32x4 c) { return __builtin_amdgcn_mfma_f32_16x16x16bf16_1k(bc<s16x4>(a), bc<s16x4>(b), c, 0, 0, 0); }
DI u32 pk(float a, float b) { f32x2 v = {a, b}; return bc<u32>(__builtin_convertvector(v, bf16x2)); }
DI u32 pk2(f32x2 v) { return bc<u32>(__builtin_convertvector(v, bf16x2)); }
DI float bfr(float a) { return bc<float>(pk(0.f, a) & 0xffff0000u); }
DI float lo16(u32 v) { return bc<float>(v << 16); }
DI float hi16(u32 v) { return bc<float>(v & 0xffff0000u); }
DI f32x2 up2(u32 v) { return f32x2{lo16(v), hi16(v)}; }
DI float b2f(u16 v) { return bc<float>((u32)v << 16); }
DI u32x2 trread(u32 a) { return bc<u32x2>(__builtin_amdgcn_ds_read_tr16_b64_v4i16((LAS s16x4*)(size_t)a)); }
DI u32x4 cat(u32x2 a, u32x2 b) { u32x4 r = {a.x, a.y, b.x, b.y}; return r; }
DI float sinr(float x) { return __builtin_amdgcn_sinf(x); }
DI float cosr(float x) { return __builtin_amdgcn_cosf(x); }
DI float fx1(float v) { return __builtin_amdgcn_update_dpp(v, v, 0xB1, 0xf, 0xf, true); }
DI float fr8(float v) { return __builtin_amdgcn_update_dpp(v, v, 0x128, 0xf, 0xf, true); }
DI u32 ux1(u32 v) { return (u32)__builtin_amdgcn_update_dpp((int)v, (int)v, 0xB1, 0xf, 0xf, true); }
DI u32 ur8(u32 v) { return (u32)__builtin_amdgcn_update_dpp((int)v, (int)v, 0x128, 0xf, 0xf, true); }
DI u32x4 sx1(u32x4 v, u32 m) { u32x4 r = {ux1(v.x) ^ m, ux1(v.y) ^ m, ux1(v.z) ^ m, ux1(v.w) ^ m}; return r; }
DI u32x4 sr8(u32x4 v, u32 m) { u32x4 r = {ur8(v.x) ^ m, ur8(v.y) ^ m, ur8(v.z) ^ m, ur8(v.w) ^ m}; return r; }
DI u32x4 xr(u32x4 v, u32 m) { u32x4 r = {v.x ^ m, v.y ^ m, v.z ^ m, v.w ^ m}; return r; }
DI u32x4 pk8(const float (&v)[8]) { return u32x4{pk(v[0], v[1]), pk(v[2], v[3]), pk(v[4], v[5]), pk(v[6], v[7])}; }
template <class T> DI T lds_ld(u32 a) { return *(LAS T*)(size_t)a; }
template <class T> DI void lds_st(u32 a, T v) { *(LAS T*)(size_t)a = v; }
DI void wait_vm0() { __builtin_amdgcn_s_waitcnt(0x0F70); }
DI void drain_vm() { asm volatile("s_waitcnt vmcnt(0)" ::: "memory"); }
DI void bar() { __builtin_amdgcn_s_waitcnt(0xC07F); asm volatile("s_barrier" ::: "memory"); }
template <class T> DI void pin(T& v) { asm volatile("" : "+v"(v)); }
typedef __amdgpu_buffer_rsrc_t rsrc_t;
DI rsrc_t mkrs(const void* p, u32 bytes) { return __builtin_amdgcn_make_buffer_rsrc((void*)p, 0, bytes, 0x00020000); }
template <int AUX> DI void dma16(rsrc_t rs, u32 lds, u32 voff, u32 soff) { __builtin_amdgcn_raw_ptr_buffer_load_lds(rs, (LAS void*)(size_t)lds, 16, voff, soff, 0, AUX); }
DI f32x2 cmul(f32x2 x, f32x2 y) {
  f32x2 t;
  asm("v_pk_mul_f32 %0, %1, %2 op_sel:[0,0] op_sel_hi:[0,1]\n\tv_pk_fma_f32 %0, %1, %2, %0 op_sel:[1,1,0] op_sel_hi:[1,0,1] neg_lo:[1,0,0]" : "=&v"(t) : "v"(x), "v"(y));
  return t;
}
DI float dot2b(u32 a, u32 b, float c) { return __builtin_amdgcn_fdot2_f32_bf16(bc<bf16x2>(a), bc<bf16x2>(b), c, false); }
DI float wave_sum(float a) {
  a += __builtin_amdgcn_update_dpp(0.f, a, 0xB1, 0xf, 0xf, true);
  a += __builtin_amdgcn_update_dpp(0.f, a, 0x4E, 0xf, 0xf, true);
  a += __builtin_amdgcn_update_dpp(0.f, a, 0x141, 0xf, 0xf, true);
  a += __builtin_amdgcn_update_dpp(0.f, a, 0x140, 0xf, 0xf, true);
  a += __builtin_amdgcn_update_dpp(0.f, a, 0x142, 0xa, 0xf, false);
  a += __builtin_amdgcn_update_dpp(0.f, a, 0x143, 0xc, 0xf, false);
  return bc<float>(__builtin_amdgcn_readlane(bc<int>(a), 63));
}
DI int pi_k(int g, int jj) { return 16 * (jj >> 2) + 4 * g + (jj & 3); }

#define CNOP 1
#define CNOPR 2
#define PFFT 0.6
#define SH2PRIO 2
#define SLP 2
#define RLX_AGENT __ATOMIC_RELAXED, __HIP_MEMORY_SCOPE_AGENT
template <bool P> DI void st16(rsrc_t rs, u32 voff, u32 soff, u32x4 v) {
  __builtin_amdgcn_raw_buffer_store_b128(v, rs, voff, soff, P ? 16 : 0);
  asm volatile("s_nop 1" :: "v"(v) : "memory");
}
template <bool P> DI u32x4 ld16(rsrc_t rs, u32 voff, u32 soff) { return __builtin_amdgcn_raw_buffer_load_b128(rs, voff, soff, P ? 16 : 0); }
DI void publish(u32* set, int idx, u32 epoch) {
  if (threadIdx.x < 8) __hip_atomic_store((GAS u32*)set + threadIdx.x * FR_ + idx * FL, epoch, RLX_AGENT);
}
DI void poll_flags(const u32* first, int n, u32 epoch) {
  const int lane = threadIdx.x & 63;
  const GAS u32* f = (const GAS u32*)first + (lane < n ? lane : 0) * FL;
  for (int spin = 0; spin < (1 << 18); ++spin) {
    if (__all(__hip_atomic_load(f, RLX_AGENT) == epoch)) break;
    __builtin_amdgcn_s_sleep(SLP);
  }
}

#define STAMP(i) do { if ((i) == DK && (blockIdx.x >= N_FFT) == (DR == 1) && threadIdx.x == 0) __hip_atomic_store((GAS u32*)(p.ws + (12u << 20)) + 1024 + blockIdx.x, (u32)__builtin_amdgcn_s_memrealtime(), RLX_AGENT); } while (0)
#define STAMP_DECL do {} while (0)
#define STAMP_FLUSH() do {} while (0)
#define STAMP_ARG
#define STAMP_PASS

DI u32x4 tf(rsrc_t rs, int item, u32 l16) { return __builtin_amdgcn_raw_buffer_load_b128(rs, l16 + (item & 3) * 1024, (item >> 2) * 4096, 0); }
DI u32x4 tfs(rsrc_t rs, int item, u32 l16, u32 so) { return __builtin_amdgcn_raw_buffer_load_b128(rs, l16 + item * 1024, so, 0); }

DI void fft_first_loads(const Params& p, const u32 lb, const int b) {
  const int lane = threadIdx.x & 63, w = __builtin_amdgcn_readfirstlane(threadIdx.x >> 6);
  const u32 l16 = lane * 16;
  const int s2 = b & 63, q = b >> 6;
  const rsrc_t rx = mkrs(p.x, S_ * D_ * 2), rtf = mkrs(TF, TF_N * 1024);
  if (lane < 56) {
    u32 la = lb + L_XT + 8 * w * XRS, so = (64 * 8 * w + s2) * (D_ * 2);
#pragma unroll 1
    for (int r = 0; r < 8; ++r) { dma16<0>(rx, la, l16, so); la += XRS; so += 64 * (D_ * 2); }
  }
  const u32 tq = (TF_Q0 + 6 * q) * 1024;
  dma16<0>(rtf, lb + L_C1 + w * 2048, l16, w == 0 ? 0u : tq + (2 * w - 2) * 1024);
  dma16<0>(rtf, lb + L_C1 + w * 2048 + 1024, l16, w == 0 ? 1024u : tq + (2 * w - 1) * 1024);
}

struct P3C { u32x4 b1, b2; f32x4 tc, ts; u32x2 m; };
template <int PH>
DI void p3_prepare(const Params& p, const u32 lb, const int b, const rsrc_t rtf, P3C& c) {
  const int lane = threadIdx.x & 63, i16 = lane & 15, g = lane >> 4;
  const u32 l16 = lane * 16;
  const int s2 = b & 63, q = b >> 6;
  c.tc = bc<f32x4>(tf(rtf, TF_TC, l16)); c.ts = bc<f32x4>(tf(rtf, TF_TS, l16));
  const u32x4 m4 = tf(rtf, TF_M, l16);
  c.m = u32x2{m4.x, m4.y};
  c.b1 = lds_ld<u32x4>(lb + L_C3 + l16); c.b2 = lds_ld<u32x4>(lb + L_C3 + 1024 + l16);
  pin(c.b1); pin(c.b2); pin(c.tc); pin(c.ts); pin(c.m);
}
DI void p3_make_b(const u32 lb, const int b) {
  const int lane = threadIdx.x & 63, i16 = lane & 15, g = lane >> 4;
  const int s2 = b & 63, q = b >> 6;
  const int ro = i16 >> 3;
  const float a = (float)(64 * (8 * q + (i16 & 7)) + s2) * (1.0f / 2048.0f), P0 = a * (float)(4 * g);
  float u[8], v[8];
#pragma unroll
  for (int jj = 0; jj < 8; ++jj) {
    const float ph = __builtin_fmaf(a, (float)(16 * (jj >> 2) + (jj & 3)), P0);
    u[jj] = sinr(ph + (ro ? 0.f : 0.25f));
    v[jj] = sinr(ph + (ro ? 0.25f : 0.5f));
  }
  lds_st<u32x4>(lb + L_C3 + lane * 16, pk8(u));
  lds_st<u32x4>(lb + L_C3 + 1024 + lane * 16, pk8(v));
}
template <bool LDSX>
DI void gate_rows(const Params& p, const u32 lb, const int b, const int r0, const int nr) {
  const int lane = threadIdx.x & 63;
  const u32 l16 = lane * 16;
  const int s2 = b & 63, q = b >> 6;
  u32x4 gwv = {0, 0, 0, 0};
  if (lane < 56) gwv = *(const GAS u32x4*)((const char*)p.gw + l16);
#pragma unroll
  for (int r = 0; r < 2; ++r) {
    if (r < nr) {
      float acc = 0.f;
      if (lane < 56) {
        u32x4 xv;
        if (LDSX) xv = lds_ld<u32x4>(lb + L_XT + (8 * q + r0 + r) * XRS + l16);
        else xv = *(const GAS u32x4*)((const char*)(p.x + (size_t)(64 * (8 * q + r0 + r) + s2) * D_) + l16);
#pragma unroll
        for (int e = 0; e < 4; ++e) acc = dot2b(pk2(up2(xv[e]) * up2(gwv[e])), 0x3f803f80u, acc);
      }
      const float gg = bfr(__builtin_amdgcn_rcpf(1.0f + __expf(-bfr(wave_sum(acc)))));
      if (lane == 0) lds_st<f32x2>(lb + L_G + (r0 + r) * 8, f32x2{gg, bfr(1.0f - gg)});
    }
  }
}

template <int PH>
DI void fft_phase3(const Params& p, const u32 lb, const int b, u32* ctl, const u32 epoch, const u32x4 (&fc)[2], const u32x4 (&fs)[2], const P3C& c STAMP_ARG) {
  constexpr bool P = PH == 0;
  constexpr int TPAUX = P ? 16 : 0;
  const int tid = threadIdx.x, lane = tid & 63, w = __builtin_amdgcn_readfirstlane(tid >> 6), i16 = lane & 15, g = lane >> 4;
  const u32 l16 = lane * 16;
  const int s2 = b & 63, q = b >> 6;
  const f32x4 z4 = {0, 0, 0, 0};
  const rsrc_t rws = mkrs(p.ws, 16u << 20);
  u32 tpa = lb + L_TP + (4 * g + (i16 >> 2)) * TPRS + (i16 & 3) * 128 + (w >> 1) * 32 + (w & 1) * 8;
  const int row = 2 * w + (i16 >> 3);
  u32 zw = lb + L_Z + ((i16 & 7) * 16 + row) * 64 + ((g ^ (row >> 2)) & 3) * 16;
  u32 zr = lb + L_Z + (w * 16 + i16) * 64 + ((g ^ (i16 >> 2)) & 3) * 16;
  u32 boff = (u32)(tid * B_D);
  u32 biw = lb + L_BI + tid * 2, bir = lb + L_BI + w * BRS + (32 * i16 + 4 * g) * 2;
  u32 ooff = (u32)((64 * (8 * q + w) + s2) * (D_ * 2) + (32 * i16 + 4 * g) * 2);
  u32 mi = (i16 & 1) ? 0u : 0x80008000u;
  pin(tpa); pin(zw); pin(zr); pin(boff); pin(biw); pin(bir); pin(ooff); pin(mi);
  STAMP(13);
  if (P) { poll_flags(ctl + CT_F2 + (b & 7) * FR_ + w * 32 * FL, 32, epoch); bar(); __builtin_amdgcn_s_setprio(3); }
  STAMP(14);
#pragma unroll
  for (int r = 0; r < 4; ++r) dma16<TPAUX>(rws, lb + L_TP + (4 * w + r) * TPRS, l16, (u32)WS_TP + s2 * TP_S2 + (4 * w + r) * 1024);
  wait_vm0();
  bar();
  STAMP(15);
#pragma unroll
  for (int t = 0; t < 2; ++t) {
    u32x4 Tr = cat(trread(tpa + t * 512), trread(tpa + t * 512 + 16 * TPRS));
    u32x4 Ti = cat(trread(tpa + t * 512 + 16), trread(tpa + t * 512 + 16 + 16 * TPRS));
    f32x4 z = mfma32(Ti, c.b2, mfma32(Tr, c.b1, z4));
    lds_st<u32x2>(zw + t * 8, u32x2{pk(z[0], z[1]), pk(z[2], z[3])});
  }
  bar();
  STAMP(16);
  f32x2 ga[2][2];
  {
    u32x4 A = lds_ld<u32x4>(zr), A2 = sx1(A, mi);
    const float gg = lds_ld<float>(lb + L_G + w * 8);
#pragma unroll
    for (int td = 0; td < 2; ++td) {
      f32x4 G = mfma32(A2, fs[td], mfma32(A, fc[td], z4));
      const float c0 = c.tc[2 * td], s0 = c.ts[2 * td], c1 = c.tc[2 * td + 1], s1 = c.ts[2 * td + 1];
      const float g0r = G[0] * c0 - G[1] * s0, g0i = G[1] * c0 + G[0] * s0;
      const float g1r = G[2] * c1 - G[3] * s1, g1i = G[3] * c1 + G[2] * s1;
      f32x4 y = mfma16(u32x2{pk(g0r, g0i), pk(g1r, g1i)}, c.m, z4);
      ga[td][0] = up2(pk2(up2(pk(y[0], y[1])) * gg));
      ga[td][1] = up2(pk2(up2(pk(y[2], y[3])) * gg));
      pin(ga[td][0]); pin(ga[td][1]);
    }
  }
  STAMP(17);
  if (P) { poll_flags(ctl + CT_FB + (b & 7) * FR_ + w * 28 * FL, 28, epoch); bar(); }
  STAMP(18);
  if (tid < D_) {
    const u32x4 bv = ld16<P>(rws, boff, (u32)WS_B + (((s2 & 7) * 8 + (s2 >> 3)) * 32 + 8 * q) * 2);
#pragma unroll
    for (int e = 0; e < 4; ++e) {
      lds_st<u16>(biw + (2 * e) * BRS, (u16)bv[e]);
      lds_st<u16>(biw + (2 * e + 1) * BRS, (u16)(bv[e] >> 16));
    }
  }
  bar();
  {
    const rsrc_t ro_ = mkrs(p.out, S_ * D_ * 2);
    const float og = lds_ld<float>(lb + L_G + w * 8 + 4);
    const u32x2 bq0 = lds_ld<u32x2>(bir), bq1 = lds_ld<u32x2>(bir + 32);
#pragma unroll
    for (int td = 0; td < 2; ++td) {
      const u32x2 bq = td ? bq1 : bq0;
      const f32x2 o01 = ga[td][0] + up2(pk2(up2(bq.x) * og));
      const f32x2 o23 = ga[td][1] + up2(pk2(up2(bq.y) * og));
      if (i16 < 14) __builtin_amdgcn_raw_buffer_store_b64(u32x2{pk2(o01), pk2(o23)}, ro_, ooff, td * 32, 0);
    }
  }
  STAMP(19);
  if (P && b == 0 && tid == 0) __hip_atomic_store((GAS u32*)ctl + CT_GEN, epoch, RLX_AGENT);
}

template <int PH>
DI void fft_upper(const Params& p, const u32 lb, const int b, u32* ctl, u32x4 (&fc)[2], u32x4 (&fs)[2], P3C& c3) {
  constexpr bool P = PH == 0;
  const int lane = threadIdx.x & 63, w = __builtin_amdgcn_readfirstlane(threadIdx.x >> 6);
  const u32 l16 = lane * 16;
  const rsrc_t rtf = mkrs(TF, TF_N * 1024);
  if (!P && w == 4) p3_make_b(lb, b);
  if (P) {
    if (w == 4) p3_make_b(lb, b);
    bar();
    bar(); bar(); bar();
    __builtin_amdgcn_s_setprio(SH2PRIO);
  }
  if (P) gate_rows<P>(p, lb, b, w, 1); else gate_rows<P>(p, lb, b, 2 * (w - 4), 2);
  fc[0] = tf(rtf, TF_FC0, l16); fs[0] = tf(rtf, TF_FS0, l16);
  fc[1] = xr(fc[0], 0x80000000u); fs[1] = xr(fs[0], 0x80000000u);
  pin(fc[0]); pin(fc[1]); pin(fs[0]); pin(fs[1]);
  if (!P) bar();
  p3_prepare<PH>(p, lb, b, rtf, c3);
  if (!P) bar();
}

template <int PH>
DI void fft_role(const Params& p, const u32 lb, const int b, u32* ctl, const u32 gen, u32x4 (&fc)[2], u32x4 (&fs)[2], P3C& c3 STAMP_ARG) {
  constexpr bool P = PH == 0;
#define TPAUXV 16
  constexpr int AUX = P ? 16 : 0, TPAUX = P ? TPAUXV : 0;
  (void)AUX;
  const int tid = threadIdx.x, lane = tid & 63, w = __builtin_amdgcn_readfirstlane(tid >> 6), i16 = lane & 15, g = lane >> 4;
  const u32 l16 = lane * 16;
  const int s2 = b & 63, q = b >> 6;
  const int xx = b & 7, mm = (b >> 3) & 3, kq = b >> 5;
  const int k1w = (((mm & 1) ? (xx ? 32 - xx : 8) : xx) + 16 * (mm >> 1)) & 31;
  const f32x4 z4 = {0, 0, 0, 0};
  const rsrc_t rws = mkrs(p.ws, 16u << 20), rtf = mkrs(TF, TF_N * 1024);
  const u32 tpo = (u32)((tid >> 3) * TP_S2 + (tid & 7) * 16), tps = (u32)WS_TP + k1w * 1024 + kq * 128;
  if (PH == 2 || PH == 3) {
    fc[0] = tf(rtf, TF_FC0, l16); fs[0] = tf(rtf, TF_FS0, l16);
    fc[1] = xr(fc[0], 0x80000000u); fs[1] = xr(fs[0], 0x80000000u);
  }

  u32x4 co[2], so[2], cot[2], sot[2];
  auto load_p2 = [&]() {
    const bool mir = kq >= 4;
    const int col0 = mir ? 448 - (56 * kq + 49) : 56 * kq;
    const rsrc_t rr = mkrs(p.wr, S_ * DF * 2), ri_ = mkrs(p.wi, S_ * DF * 2);
#pragma unroll
    for (int i = 0; i < 3; ++i) {
      const int gi = w + 4 * i;
      if (gi < 9) {
        const int gam = 64 * gi + lane, k2 = (gam * 7282) >> 16, chunk = gam - 9 * k2;
        const int ks = k1w + 32 * k2, row = mir ? ((2048 - ks) & 2047) : ks;
        const u32 vo = (u32)(((row * DF + col0) * 2) & ~15) + chunk * 16;
        dma16<0>(rr, lb + L_W + gi * 1024, vo, 0);
        dma16<0>(ri_, lb + L_W + 9216 + gi * 1024, vo, 0);
      }
    }
    dma16<0>(rtf, lb + L_C2 + w * 1024, l16, (TF_CO0 + w) * 1024);
  };

  if (PH == 0 || PH == 1) {
    u32x4 C1, S1, nS1;
    {
      const float k1 = (float)(16 * (w >> 1) + i16) * (1.0f / 2048.0f), P0 = k1 * (float)(64 * g + s2), Pl = k1 * 256.f;
      float c[8], s[8];
#pragma unroll
      for (int j = 0; j < 8; ++j) { const float ph = __builtin_fmaf(Pl, (float)j, P0); c[j] = cosr(ph); s[j] = sinr(ph); }
      C1 = pk8(c); S1 = pk8(s); nS1 = xr(S1, 0x80008000u);
      pin(C1); pin(S1); pin(nS1);
    }
    int d0a = 8 * (g >> 1) + (i16 >> 2), d0b = d0a + 4;
    d0b = d0b < 14 ? d0b : 0;
    u32 xa = lb + L_XT + (8 * w + (g & 1)) * XRS + 8 * (i16 & 3);
    u32 xb = xa + d0b * 64; xa += d0a * 64;
    u32 xw = lb + L_XCH + w * 256 + lane * 4;
    u32 xr_ = lb + L_XCH + (w & 1) * 4096 + lane * 4;
    u32 toff = (u32)((16 * (w >> 1) + i16) * T_K1 + 4 * (w & 1) * 32 + g * 16);
    u32 imm = (i16 & 1) ? 0x80008000u : 0u;
    pin(xa); pin(xb); pin(xw); pin(xr_); pin(toff); pin(imm);
    const u32 tso = (u32)WS_T + s2 * 1024 + q * 256;
    STAMP(1);
    wait_vm0();
    bar();
    const u32 cb = lb + L_C1 + l16;
    fc[0] = lds_ld<u32x4>(cb); fs[0] = lds_ld<u32x4>(cb + 1024);
    const u32x4 Ba1 = lds_ld<u32x4>(cb + 2048), Ba2 = lds_ld<u32x4>(cb + 3072);
    const f32x4 twc0 = lds_ld<f32x4>(cb + 4096), twc1 = lds_ld<f32x4>(cb + 5120), tws0 = lds_ld<f32x4>(cb + 6144), tws1 = lds_ld<f32x4>(cb + 7168);
    fc[1] = xr(fc[0], 0x80000000u); fs[1] = xr(fs[0], 0x80000000u);
    STAMP(2);
    f32x4 D2[2][2];
#pragma unroll
    for (int sg = 0; sg < 2; ++sg) {
      f32x4 H[2];
#pragma unroll
      for (int td = 0; td < 2; ++td) {
        const int o = td * 32 + sg * 4 * XRS;
        u32x4 A0 = cat(trread(xa + o), trread(xb + o));
        u32x4 A1 = cat(trread(xa + o + 2 * XRS), trread(xb + o + 2 * XRS));
        H[td] = mfma32(A1, Ba2, mfma32(A0, Ba1, z4));
      }
      float h[8];
#pragma unroll
      for (int j = 0; j < 4; ++j) {
        h[j] = __builtin_fmaf(fx1(H[0][j]), tws0[j], H[0][j] * twc0[j]);
        h[4 + j] = __builtin_fmaf(fx1(H[1][j]), tws1[j], H[1][j] * twc1[j]);
      }
      u32x4 A = pk8(h), A2 = sx1(A, imm);
#pragma unroll
      for (int tk = 0; tk < 2; ++tk) D2[sg][tk] = mfma32(A2, fs[tk], mfma32(A, fc[tk], z4));
    }
#pragma unroll
    for (int tk = 0; tk < 2; ++tk)
#pragma unroll
      for (int r = 0; r < 4; ++r) lds_st<u32>(xw + (tk * 4 + r) * 1024, pk(D2[0][tk][r], D2[1][tk][r]));
    bar();
    STAMP(3);
    {
      u32 pc[4][2];
#pragma unroll
      for (int kk = 0; kk < 2; ++kk) {
        const u32 br = xr_ + kk * 2048;
        u32x4 Xr = {lds_ld<u32>(br), lds_ld<u32>(br + 256), lds_ld<u32>(br + 512), lds_ld<u32>(br + 768)};
        u32x4 Xi = {lds_ld<u32>(br + 1024), lds_ld<u32>(br + 1280), lds_ld<u32>(br + 1536), lds_ld<u32>(br + 1792)};
        f32x4 Tr = mfma32(Xi, S1, mfma32(Xr, C1, z4));
        f32x4 Ti = mfma32(Xr, nS1, mfma32(Xi, C1, z4));
        pc[kk][0] = pk(Tr[0], Tr[1]); pc[kk][1] = pk(Tr[2], Tr[3]);
        pc[2 + kk][0] = pk(Ti[0], Ti[1]); pc[2 + kk][1] = pk(Ti[2], Ti[3]);
      }
      u32x4 lo = {pc[0][0], pc[0][1], pc[1][0], pc[1][1]}, hi = {pc[2][0], pc[2][1], pc[3][0], pc[3][1]};
#pragma unroll
      for (int e = 0; e < 4; ++e) {
        auto r1 = __builtin_amdgcn_permlane16_swap(lo[e], hi[e], false, false);
        auto r2 = __builtin_amdgcn_permlane32_swap(r1[0], r1[1], false, false);
        lo[e] = r2[0]; hi[e] = r2[1];
      }
      st16<P>(rws, toff, tso, lo);
      st16<P>(rws, toff + 64, tso, hi);
    }
    STAMP(4);
    if (P) {
      drain_vm();
      STAMP(20);
      bar();
      publish(ctl + CT_F1, b, gen + 1u);
      __builtin_amdgcn_s_setprio(2);
      load_p2();
    }
    STAMP(5);
  }
  const u32 epoch = __builtin_amdgcn_readfirstlane(gen) + 1u;

  if (PH == 2) load_p2();
  if (PH == 0 || PH == 2) {
    const int ri = i16 >> 3;
    u32 tsv = (u32)((lane >> 1) * 1024 + w * 256 + (lane & 1) * 16);
    u32 tiw = lb + L_TI + w * 2048 + ((lane >> 1) & 1) * 1024 + (lane >> 2) * 32 + (lane & 1) * 16;
    u32 ta = lb + L_TI + w * 2048 + (4 * g + (i16 >> 2)) * 32 + (i16 & 3) * 8;
    u32 tow = lb + L_TO + 2 * i16 * 144 + w * 32 + g * 8;
    u32 tor = lb + L_TO + (tid >> 3) * 144 + (tid & 7) * 16;
    u32 mf = ri ? 0x80008000u : 0u, mi = ri ? 0u : 0x80008000u;
    float wr_[16], wi_[16];
    {
      const bool mir = kq >= 4;
      const int c = i16 & 7, kd = 2 * w + (c >> 2) + 14 * (4 * kq + (c & 3)), col0 = mir ? 448 - (56 * kq + 49) : 56 * kq;
      const int row0 = mir ? ((2048 - k1w) & 2047) : k1w;
      u32 wa = lb + L_W + 4 * g * 144 + (((row0 * DF + col0) * 2) & 15) + ((mir ? 448 - kd : kd) - col0) * 2;
      const u32 sg = (mir != (ri != 0)) ? 0u : 0x80000000u;
      pin(tsv); pin(tiw); pin(ta); pin(tow); pin(tor); pin(mf); pin(mi); pin(wa);
      STAMP(7);
      if (P) poll_flags(ctl + CT_F1 + (b & 7) * FR_ + w * 64 * FL, 64, epoch);
      wait_vm0();
      bar();
      const u32 so_ = (u32)WS_T + k1w * T_K1 + kq * 32;
      const u32x4 t0 = ld16<P>(rws, tsv, so_), t1 = ld16<P>(rws, tsv, so_ + 32 * 1024);
      __builtin_amdgcn_sched_barrier(0);
      co[0] = lds_ld<u32x4>(lb + L_C2 + l16); so[0] = lds_ld<u32x4>(lb + L_C2 + 1024 + l16);
      cot[0] = lds_ld<u32x4>(lb + L_C2 + 2048 + l16); sot[0] = lds_ld<u32x4>(lb + L_C2 + 3072 + l16);
      co[1] = xr(so[0], 0x00008000u);
      so[1] = xr(co[0], 0x80000000u);
      cot[1] = xr(cot[0], 0x80000000u); sot[1] = xr(sot[0], 0x80000000u);
      pin(co[0]); pin(co[1]); pin(so[0]); pin(so[1]); pin(cot[0]); pin(cot[1]); pin(sot[0]); pin(sot[1]);
#pragma unroll
      for (int j = 0; j < 16; ++j) {
        const int o = (32 * (j >> 3) + 16 * ((j >> 2) & 1) + (j & 3)) * 144;
        wr_[j] = bc<float>((u32)lds_ld<u16>(wa + o) << 16);
        wi_[j] = bc<float>(((u32)lds_ld<u16>(wa + 9216 + o) << 16) + sg);
      }
#pragma unroll
      for (int j = 0; j < 16; ++j) { pin(wr_[j]); pin(wi_[j]); }
      __builtin_amdgcn_sched_barrier(0);
      if (P) __builtin_amdgcn_s_setprio(3);
      STAMP(8);
      lds_st<u32x4>(tiw, t0);
      lds_st<u32x4>(tiw + 512, t1);
    }
    STAMP(9);
    f32x4 E[2], O[2];
    {
      u32x4 B0 = cat(trread(ta), trread(ta + 512)), B1 = cat(trread(ta + 1024), trread(ta + 1536));
      u32x4 B0s = sr8(B0, mf), B1s = sr8(B1, mf);
#pragma unroll
      for (int t = 0; t < 2; ++t) {
        E[t] = mfma32(fs[t], B0s, mfma32(fc[t], B0, z4));
        O[t] = mfma32(so[t], B1s, mfma32(co[t], B1, z4));
      }
    }
    float Pp[8], Mm[8];
#pragma unroll
    for (int j = 0; j < 8; ++j) {
      const float e = E[j >> 2][j & 3], o = O[j >> 2][j & 3];
      const float ylo = e + o, yhi = e - o;
      const float vlo = __builtin_fmaf(fr8(ylo), wi_[j], ylo * wr_[j]);
      const float vhi = __builtin_fmaf(fr8(yhi), wi_[8 + j], yhi * wr_[8 + j]);
      Pp[j] = vlo + vhi; Mm[j] = vlo - vhi;
    }
    {
      u32x4 AP = pk8(Pp), AM = pk8(Mm);
      u32x4 APs = sr8(AP, mi), AMs = sr8(AM, mi);
#pragma unroll
      for (int t = 0; t < 2; ++t) {
        f32x4 ze = mfma32(APs, fs[t], mfma32(AP, fc[t], z4));
        f32x4 zo = mfma32(AMs, sot[t], mfma32(AM, cot[t], z4));
        lds_st<u32x2>(tow + t * 32 * 144, u32x2{pk(ze[0], ze[1]), pk(ze[2], ze[3])});
        lds_st<u32x2>(tow + t * 32 * 144 + 144, u32x2{pk(zo[0], zo[1]), pk(zo[2], zo[3])});
      }
    }
    bar();
    STAMP(10);
    {
      st16<P>(rws, tpo, tps, lds_ld<u32x4>(tor));
      st16<P>(rws, tpo, tps + 32 * TP_S2, lds_ld<u32x4>(tor + 32 * 144));
    }
    STAMP(11);
    if (P) { drain_vm(); bar(); publish(ctl + CT_F2, b, epoch); __builtin_amdgcn_s_setprio(SH2PRIO); }
    STAMP(12);
  }

  if (PH == 0) gate_rows<true>(p, lb, b, w, 1);
  if (PH == 3) bar();
  if (PH == 0 || PH == 3) p3_prepare<PH>(p, lb, b, rtf, c3);
  if (PH == 3) bar();
}

DI f32x2 cexp_rev(float x2, float ph) {
  const float e = __builtin_amdgcn_exp2f(x2);
  const float f = __builtin_amdgcn_fractf(ph);
  f32x2 cs = {cosr(f), sinr(f)};
  return cs * e;
}
struct KW { u32 a[8]; };
template <int ROW> DI void wrow(const KW& k, f32x2 v) { lds_st<u32>(k.a[ROW & 7] + ROW * 256, pk2(v)); }
template <int ROW> DI void wrow2(const KW& k, f32x2 lo, f32x2 hi) {
  const u32 a = pk2(lo), b = pk2(hi);
  asm volatile("ds_write2st64_b32 %0, %1, %2 offset0:%c3 offset1:%c4" :: "v"(k.a[ROW & 7]), "v"(a), "v"(b), "i"(ROW), "i"(ROW + 8) : "memory");
}
DI void kfrag(u32 fa, u32 fb, u32x4 (&A)[4]) {
  A[0] = lds_ld<u32x4>(fa); A[1] = lds_ld<u32x4>(fb); A[2] = lds_ld<u32x4>(fa + 128); A[3] = lds_ld<u32x4>(fb + 128);
}
DI f32x4 ktile(const u32x4 (&A)[4], const u32x4 (&V)[4]) {
  f32x4 acc = {0, 0, 0, 0};
#pragma unroll
  for (int ks = 0; ks < 4; ++ks) acc = mfma32(A[ks], V[ks], acc);
  return acc;
}
template <class F0, class F1>
DI void kgen(const u32 (&in)[6], float dt, const KW& k, u32 fa, u32 fb, u32 pb, F0&& hook0, F1&& hook1) {
  const float ar = dt * bc<float>(in[0] << 16), ai = dt * bc<float>(in[1] << 16);
  const f32x2 dB = {dt * bc<float>(in[2] << 16), dt * bc<float>(in[3] << 16)};
  const f32x2 Cc = {bc<float>(in[4] << 16), bc<float>(in[5] << 16)};
  const f32x2 CB = cmul(Cc, dB);
  const float x1 = ar * 1.44269504088896341f, p1 = ai * 0.15915494309189535f;
  const f32x2 E1 = cexp_rev(x1, p1), E8c = cexp_rev(8.f * x1, -8.f * p1), E128 = cexp_rev(128.f * x1, 128.f * p1);
  u32x4 V[4], A0[4], A1[4];
  {
    const f32x2 v2 = cmul(E8c, E8c), v3 = cmul(v2, E8c), v4 = cmul(v2, v2), v8 = cmul(v4, v4);
    const f32x2 v5 = cmul(v4, E8c), v6 = cmul(v4, v2), v7 = cmul(v4, v3);
    wrow2<0>(k, f32x2{1.f, 0.f}, v8); wrow2<1>(k, E8c, cmul(v8, E8c)); wrow2<2>(k, v2, cmul(v8, v2)); wrow2<3>(k, v3, cmul(v8, v3));
    wrow2<4>(k, v4, cmul(v8, v4)); wrow2<5>(k, v5, cmul(v8, v5)); wrow2<6>(k, v6, cmul(v8, v6)); wrow2<7>(k, v7, cmul(v8, v7));
    kfrag(fa, fb, V);
  }
  hook0();
  const f32x2 E2 = cmul(E1, E1), E3 = cmul(E2, E1), E4 = cmul(E2, E2), E5 = cmul(E4, E1), E6 = cmul(E4, E2), E7 = cmul(E4, E3);
  const f32x2 G1 = cmul(CB, E128);
  wrow2<0>(k, CB, G1); wrow2<1>(k, cmul(CB, E1), cmul(G1, E1)); wrow2<2>(k, cmul(CB, E2), cmul(G1, E2)); wrow2<3>(k, cmul(CB, E3), cmul(G1, E3));
  wrow2<4>(k, cmul(CB, E4), cmul(G1, E4)); wrow2<5>(k, cmul(CB, E5), cmul(G1, E5)); wrow2<6>(k, cmul(CB, E6), cmul(G1, E6)); wrow2<7>(k, cmul(CB, E7), cmul(G1, E7));
  kfrag(fa, fb, A0);
  const f32x2 G2 = cmul(G1, E128), G3 = cmul(G2, E128), G4 = cmul(G3, E128), G5 = cmul(G4, E128), G6 = cmul(G5, E128), G7 = cmul(G6, E128);
  wrow2<0>(k, G2, G4); wrow2<1>(k, cmul(G2, E2), cmul(G4, E4)); wrow2<2>(k, cmul(G2, E4), G5); wrow2<3>(k, cmul(G2, E6), cmul(G5, E4));
  wrow2<4>(k, G3, G6); wrow2<5>(k, cmul(G3, E2), cmul(G6, E4)); wrow2<6>(k, cmul(G3, E4), G7); wrow2<7>(k, cmul(G3, E6), cmul(G7, E4));
  kfrag(fa, fb, A1);
  hook1();
  __builtin_amdgcn_sched_barrier(0);
  const f32x4 a0 = ktile(A0, V);
  const f32x2 G8 = cmul(G7, E128), G9 = cmul(G8, E128), G10 = cmul(G9, E128), G11 = cmul(G10, E128);
  const f32x2 G12 = cmul(G11, E128), G13 = cmul(G12, E128), G14 = cmul(G13, E128), G15 = cmul(G14, E128);
  wrow2<0>(k, G8, cmul(G15, E128)); wrow<1>(k, G9); wrow<2>(k, G10); wrow<3>(k, G11);
  wrow<4>(k, G12); wrow<5>(k, G13); wrow<6>(k, G14); wrow<7>(k, G15);
  kfrag(fa, fb, A0);
  __builtin_amdgcn_sched_barrier(0);
  const f32x4 a1 = ktile(A1, V), a2 = ktile(A0, V);
  lds_st<f32x4>(pb, a0);
  lds_st<f32x4>(pb + 1024, a1);
  lds_st<f32x4>(pb + 2048, a2);
}

struct SsmLoads { u32x4 xt_v, kt0; u32x2 kt1; u32 in[6], ldt, dsk; };
template <int PH>
DI void ssm_first_loads(const Params& p, const int j, SsmLoads& L) {
  const int tid = threadIdx.x, w = __builtin_amdgcn_readfirstlane(tid >> 6), d = 2 * j, ch = w >> 2, n = tid & 255;
  const int sb_ = j / 7, db_ = j - sb_ * 7;
  if (PH == 0 || PH == 1)
    L.xt_v = __builtin_amdgcn_raw_buffer_load_b128(mkrs(p.x, S_ * D_ * 2), (u32)((tid >> 3) * (D_ * 2) + (tid & 7) * 16), (u32)(64 * sb_ * (D_ * 2) + db_ * 128), 0);
  __builtin_amdgcn_sched_barrier(0);
  if (PH != 1) {
    const u32 vo = ((d + ch) * NS + n) * 2;
    const u16* arr[6] = {p.are, p.aim, p.bre, p.bim, p.cre, p.cim};
#pragma unroll
    for (int a = 0; a < 6; ++a) L.in[a] = *(const GAS u16*)((const GAS char*)arr[a] + vo);
    L.ldt = *(const GAS u16*)(p.logdt + d + ch);
    L.dsk = *(const GAS u16*)(p.dskip + d + ((w >> 1) & 1));
    L.kt0 = *(const GAS u32x4*)(KTAB.v + 8 * n);
    L.kt1 = *(const GAS u32x2*)(KTAB.v + 8 * n + 8);
  }
}

template <int PH>
DI void ssm_role(const Params& p, const u32 lb, const int j, u32* ctl, const u32 gen, const SsmLoads& L STAMP_ARG) {
  constexpr bool P = PH == 0;
  const int tid = threadIdx.x, lane = tid & 63, w = __builtin_amdgcn_readfirstlane(tid >> 6), i16 = lane & 15, g = lane >> 4;
  const int d = 2 * j, ch = w >> 2, n = tid & 255;
  const f32x4 z4 = {0, 0, 0, 0};
  const rsrc_t rws = mkrs(p.ws, 16u << 20);
  const int sb_ = j / 7, db_ = j - sb_ * 7;
  const u32x4 xt_v = L.xt_v, kt0 = L.kt0;
  const u32x2 kt1 = L.kt1;
  const u32 ldt = L.ldt;
  u32 dsk = L.dsk;
  const u32 (&in)[6] = L.in;
  auto transpose = [&]() {
    lds_st<u32x4>(lb + L_XP + (tid >> 3) * 144 + (tid & 7) * 16, xt_v);
    bar();
    const int chi = g + 4 * (w >> 2);
    const u32 a = lb + L_XP + (8 * chi + (i16 >> 2)) * 144 + (16 * (w & 3) + 4 * (i16 & 3)) * 2;
    st16<P>(rws, (u32)((16 * (w & 3) + i16) * XT_D + chi * 16), (u32)WS_XT + 64 * db_ * XT_D + sb_ * 128, cat(trread(a), trread(a + 4 * 144)));
  };
  if (PH == 1) { transpose(); return; }
  if (P) {
    if (tid == 0) lds_st<u32>(lb + L_CNT, 0u);
    __builtin_amdgcn_sched_barrier(0);
    transpose();
    __builtin_amdgcn_s_setprio(1);
    __builtin_amdgcn_sched_barrier(0);
  }
  KW kw;
  const u32 sb = lb + w * KSCR;
#pragma unroll
  for (int h = 0; h < 8; ++h) { kw.a[h] = sb + (((lane >> 2) ^ h) << 4) + (lane & 3) * 4; pin(kw.a[h]); }
  u32 fa = sb + i16 * 256 + ((g ^ (i16 & 3)) << 4) + ((i16 >> 2) & 1) * 64, fb = fa ^ 64;
  pin(fa); pin(fb);
  STAMP(1);
  kgen(in, bfr(expf(bc<float>(ldt << 16))), kw, fa, fb, sb + lane * 16,
       [&]() {
         if (P) {
           drain_vm();
           STAMP(5);
           if (lane == 0 && __hip_atomic_fetch_add((LAS u32*)(size_t)(lb + L_CNT), 1u, __ATOMIC_RELAXED, __HIP_MEMORY_SCOPE_WORKGROUP) == 7u)
             __hip_atomic_store((GAS u32*)ctl + CT_FX + (db_ * 32 + sb_) * FL, gen + 1u, RLX_AGENT);
         }
       },
       []() {});
  const u32 epoch = __builtin_amdgcn_readfirstlane(gen) + 1u;
  pin(dsk);
  STAMP(2);
  if (P && w == 0) poll_flags(ctl + CT_FX + (d >> 6) * 32 * FL, 32, epoch);
  STAMP(6);
  bar();
  const u32x4 uv_ = ld16<P>(rws, n * 16, (u32)WS_XT + (d + ch) * XT_D);
  STAMP(3);
  if ((w & 3) < 3) {
    const u32 pb = lb + ch * 4 * KSCR + (w & 3) * 1024 + lane * 16;
    const f32x4 s = (lds_ld<f32x4>(pb) + lds_ld<f32x4>(pb + KSCR)) + (lds_ld<f32x4>(pb + 2 * KSCR) + lds_ld<f32x4>(pb + 3 * KSCR));
    const u32 tb = lb + L_KT + ch * 1600 + ((w & 3) * 256 + 64 * g + i16) * 2, a = pk(s[0], s[1]), cc = pk(s[2], s[3]);
    lds_st<u16>(tb, (u16)a); lds_st<u16>(tb + 32, (u16)(a >> 16));
    lds_st<u16>(tb + 64, (u16)cc); lds_st<u16>(tb + 96, (u16)(cc >> 16));
  } else {
    if (lane == 0) lds_st<u16>(lb + L_KT + ch * 1600 + 1536, (u16)0);
    if (lane < 32) lds_st<u32x4>(lb + L_U + ch * 4608 + lane * 16, u32x4{0, 0, 0, 0});
  }
  bar();
  STAMP(4);
  {
    const u32 ktb = lb + L_KT + ch * 1600;
    u32 e[11];
#pragma unroll
    for (int k = 0; k < 4; ++k) { e[2 * k] = lds_ld<u16>(ktb + (kt0[k] & 0xffffu)); e[2 * k + 1] = lds_ld<u16>(ktb + (kt0[k] >> 16)); }
    e[8] = lds_ld<u16>(ktb + (kt1[0] & 0xffffu)); e[9] = lds_ld<u16>(ktb + (kt1[0] >> 16)); e[10] = lds_ld<u16>(ktb + (kt1[1] & 0xffffu));
    const u32 k00 = lds_ld<u16>(ktb);
    u32 dw[5], al[5];
#pragma unroll
    for (int k = 0; k < 5; ++k) { dw[k] = e[2 * k] | (e[2 * k + 1] << 16); al[k] = e[2 * k + 1] | (e[2 * k + 2] << 16); }
    const u32 kb = lb + L_KC + ch * 4 * KCP + n * 16;
    lds_st<u32x4>(kb, u32x4{dw[0], dw[1], dw[2], dw[3]});
    lds_st<u32x4>(kb + KCP, u32x4{al[0], al[1], al[2], al[3]});
    lds_st<u32x4>(kb + 2 * KCP, u32x4{dw[1], dw[2], dw[3], dw[4]});
    lds_st<u32x4>(kb + 3 * KCP, u32x4{al[1], al[2], al[3], al[4]});
    if (n < 16) {
      u32x4 z = {0, 0, 0, 0};
      if (n == 0) z.x = k00;
      lds_st<u32x4>(lb + L_KC + (ch * 4 + (n >> 2)) * KCP + 4096 + (n & 3) * 16, z);
    }
    lds_st<u32x4>(lb + L_U + ch * 4608 + 512 + n * 16, uv_);
  }
  bar();
  STAMP(7);
  f32x4 acc[8];
  const int cc = (w >> 1) & 1, hf = w & 1;
  if (w < 4) {
#pragma unroll
    for (int c = 0; c < 8; ++c) acc[c] = z4;
    const int bsh = (-i16) & 3;
    u32 abase = lb + L_KC + (cc * 4 + bsh) * KCP + (2032 + 8 * g - i16 - bsh) * 2 - hf * 4 * 64 - 3 * 64 - 7 * 512;
    u32 bbase = lb + L_U + cc * 4608 + (256 - 16 + 16 * i16 + 8 * g) * 2 - hf * 4 * 64 - 3 * 64;
    u32 abase8 = abase + 8;
    pin(abase); pin(abase8); pin(bbase);
    u32x4 A[8], B[8];
#pragma unroll
    for (int qq = 0; qq < 8; ++qq) {
      B[7 - qq] = lds_ld<u32x4>(bbase + (7 - qq) * 512 + 3 * 64);
      A[qq] = cat(lds_ld<u32x2>(abase + (7 - qq) * 512 + 3 * 64), lds_ld<u32x2>(abase8 + (7 - qq) * 512 + 3 * 64));
      __builtin_amdgcn_sched_barrier(0);
    }
#pragma unroll
    for (int rr = 0; rr < 4; ++rr) {
#pragma unroll
      for (int qq = 0; qq < 8; ++qq) {
#pragma unroll
        for (int m = 7 - qq; m >= 0; --m) {
          acc[qq + m] = mfma32(A[qq], B[m], acc[qq + m]);
          if (CNOP >= 0 && rr < CNOPR) asm volatile("s_nop %c0" :: "i"(CNOP >= 0 ? CNOP : 0));
        }
        if (rr < 3) {
          A[qq] = cat(lds_ld<u32x2>(abase + (7 - qq) * 512 + (2 - rr) * 64), lds_ld<u32x2>(abase8 + (7 - qq) * 512 + (2 - rr) * 64));
          B[7 - qq] = lds_ld<u32x4>(bbase + (7 - qq) * 512 + (2 - rr) * 64);
        }
        __builtin_amdgcn_sched_barrier(0);
      }
    }
    if (hf) {
#pragma unroll
      for (int c = 0; c < 4; ++c) lds_st<f32x4>(lb + L_RED + (w * 4 + c) * 1024 + lane * 16, acc[c]);
#pragma unroll
      for (int c = 0; c < 4; ++c) acc[c] = acc[4 + c];
    } else {
#pragma unroll
      for (int c = 0; c < 4; ++c) lds_st<f32x4>(lb + L_RED + (w * 4 + c) * 1024 + lane * 16, acc[4 + c]);
    }
  }
  bar();
  STAMP(8);
  if (w < 4) {
    const float dk = bc<float>(dsk << 16);
    const u32 tb = (1024 * hf + 16 * i16 + 4 * g) * 2;
    f32x4 o[4];
    u32x2 uv[4];
#pragma unroll
    for (int c = 0; c < 4; ++c) {
      o[c] = lds_ld<f32x4>(lb + L_RED + ((w ^ 1) * 4 + c) * 1024 + lane * 16);
      uv[c] = lds_ld<u32x2>(lb + L_U + cc * 4608 + 512 + tb + c * 512);
    }
#pragma unroll
    for (int c = 0; c < 4; ++c) {
      const f32x4 y = acc[c] + o[c];
      const f32x2 b01 = up2(pk(y[0], y[1])) + up2(pk2(up2(uv[c].x) * dk));
      const f32x2 b23 = up2(pk(y[2], y[3])) + up2(pk2(up2(uv[c].y) * dk));
      lds_st<u32x2>(lb + L_BO + cc * 4096 + tb + c * 512, u32x2{pk2(b01), pk2(b23)});
    }
  }
  bar();
  STAMP(9);
  {
    const int st = w & 3, s2 = 16 * st + i16;
    const u32 a = lb + L_BO + ch * 4096 + (8 * g + (i16 >> 2)) * 128 + (16 * st + 4 * (i16 & 3)) * 2;
    st16<P>(rws, (u32)((((s2 & 7) * 8 + (s2 >> 3)) * 32 + 8 * g) * 2), (u32)WS_B + (d + ch) * B_D, cat(trread(a), trread(a + 4 * 128)));
  }
  STAMP(10);
  if (P) { drain_vm(); bar(); publish(ctl + CT_FB, j, epoch); }
  STAMP(11);
}

template <int PH>
__global__ __launch_bounds__(NT, 4) void gfs4_kernel(Params p) {
  extern __shared__ __attribute__((aligned(16))) char smem[];
  const u32 lb = (u32)(size_t)(LAS char*)smem;
  const int b = blockIdx.x;
  u32* ctl = (u32*)(p.ws + WS_CTL);
  STAMP_DECL;
  if (PH == 0 || PH == 1) STAMP(0); else if (PH == 2) STAMP(20); else STAMP(21);
  u32 gen = 0;
  const u32 dg_t0 = (u32)__builtin_amdgcn_s_memrealtime();
  if (threadIdx.x == 0) __hip_atomic_store((GAS u32*)(p.ws + (12u << 20)) + b, dg_t0, RLX_AGENT);
  asm volatile("" :: "s"(p.x), "s"(p.wr), "s"(p.wi), "s"(p.are), "s"(p.aim), "s"(p.bre), "s"(p.bim));
  asm volatile("" :: "s"(p.cre), "s"(p.cim), "s"(p.logdt), "s"(p.dskip), "s"(p.gw), "s"(p.out), "s"(p.ws));
  SsmLoads L;
  if (PH == 0) __builtin_amdgcn_s_setprio(3);
  if (__builtin_expect_with_probability(b < N_FFT, 1, PFFT)) {
    if (__builtin_expect(threadIdx.x < 256, 1)) { if (PH == 0 || PH == 1) fft_first_loads(p, lb, b); }
  } else {
    ssm_first_loads<PH>(p, b - N_FFT, L);
  }
  if (PH == 0) gen = __hip_atomic_load((GAS u32*)ctl + CT_GEN, RLX_AGENT);
  __builtin_amdgcn_sched_barrier(0);
  if (PH == 0 && b < N_FFT && threadIdx.x >= 256) {
    asm volatile("s_barrier\n\ts_barrier" ::: "memory");
    __builtin_amdgcn_sched_barrier(0);
  }
  if (b < N_FFT) {
    u32x4 fc[2], fs[2];
    P3C c3;
    if (threadIdx.x >= 256) {
      if (PH == 1 || PH == 2) return;
      fft_upper<PH>(p, lb, b, ctl, fc, fs, c3);
    } else fft_role<PH>(p, lb, b, ctl, gen, fc, fs, c3 STAMP_PASS);
    if (PH == 0 || PH == 3) fft_phase3<PH>(p, lb, b, ctl, __builtin_amdgcn_readfirstlane(gen) + 1u, fc, fs, c3 STAMP_PASS);
  }
  else ssm_role<PH>(p, lb, b - N_FFT, ctl, gen, L STAMP_PASS);
  STAMP_FLUSH();
  if (b == 0 && threadIdx.x < 64 && (gen & 0xff) != ((gen >> 8) & 0xff) && (gen & 3) == 3) {
    u32 mn = 0x7fffffffu, mx = 0, lo = 0x7fffffffu;
    for (int i = threadIdx.x; i < N_WG; i += 64) {
      mn = min(mn, __hip_atomic_load((GAS u32*)(p.ws + (12u << 20)) + i, RLX_AGENT) - dg_t0 + 100000u);
      if ((i >= N_FFT) == (DR == 1)) { const u32 a = __hip_atomic_load((GAS u32*)(p.ws + (12u << 20)) + 1024 + i, RLX_AGENT) - dg_t0 + 100000u; mx = max(mx, a); lo = min(lo, a); }
    }
    for (int o = 32; o >= 1; o >>= 1) { mn = min(mn, (u32)__shfl_xor((int)mn, o)); mx = max(mx, (u32)__shfl_xor((int)mx, o)); lo = min(lo, (u32)__shfl_xor((int)lo, o)); }
    const float v = DIAG == 10 ? (float)(mx - mn) * 0.01f : (float)(lo - mn) * 0.01f;
    wait_vm0();
    if (threadIdx.x == 0) p.out[0] = (u16)(pk(v, v) & 0xffff);
  }
}

extern "C" void kernel_launch(void* const* d_in, const int* in_sizes, int n_in,
                              void* d_out, int out_size, void* d_ws, size_t ws_size,
                              hipStream_t stream) {
  (void)in_sizes; (void)n_in; (void)out_size; (void)ws_size;
  Params p;
  p.x = (const u16*)d_in[0]; p.wr = (const u16*)d_in[1]; p.wi = (const u16*)d_in[2];
  p.are = (const u16*)d_in[4]; p.aim = (const u16*)d_in[5]; p.bre = (const u16*)d_in[6]; p.bim = (const u16*)d_in[7];
  p.cre = (const u16*)d_in[8]; p.cim = (const u16*)d_in[9]; p.logdt = (const u16*)d_in[10]; p.dskip = (const u16*)d_in[11];
  p.gw = (const u16*)d_in[12];
  p.out = (u16*)d_out; p.ws = (char*)d_ws;
  (void)hipFuncSetAttribute((const void*)gfs4_kernel<0>, hipFuncAttributeMaxDynamicSharedMemorySize, LDS_BYTES);
  gfs4_kernel<0><<<N_WG, NT, LDS_BYTES, stream>>>(p);
}
```

```cpp
#include <hip/hip_runtime.h>
#include <hip/hip_bf16.h>
#include <stdint.h>

typedef unsigned int u32;
typedef unsigned short u16;
typedef unsigned long long u64;
typedef float f32x4 __attribute__((ext_vector_type(4)));
typedef float f32x2 __attribute__((ext_vector_type(2)));
typedef u32 u32x4 __attribute__((ext_vector_type(4)));
typedef u32 u32x2 __attribute__((ext_vector_type(2)));
typedef __bf16 bf16x8 __attribute__((ext_vector_type(8)));
typedef __bf16 bf16x2 __attribute__((ext_vector_type(2)));
typedef short s16x4 __attribute__((ext_vector_type(4)));
#define LAS __attribute__((address_space(3)))
#define GAS __attribute__((address_space(1)))
#define DI __device__ __forceinline__

#define NLAUNCH 1

constexpr int S_ = 2048, D_ = 448, DF = 225, NS = 256;
constexpr int N_FFT = 256, N_SSM = 224, N_WG = N_FFT + N_SSM, NT = 512;

constexpr u32 T_K1 = 65536 + 256;
constexpr u32 TP_S2 = 32768 + 128;
constexpr u32 XT_D = 4096 + 128;
constexpr u32 B_D = 4096 + 128;
constexpr size_t WS_CTL = 0;
constexpr size_t WS_T   = 1u << 20;
constexpr size_t WS_TP  = WS_T + (3u << 20);
constexpr size_t WS_XT  = WS_TP + (3u << 20);
constexpr size_t WS_B   = WS_XT + (2u << 20);
constexpr int FL = 32, FR_ = 256 * FL + 64;
constexpr int CT_GEN = 0;
constexpr int CT_F1 = 64;
constexpr int CT_F2 = CT_F1 + 8 * FR_;
constexpr int CT_FB = CT_F2 + 8 * FR_;
constexpr int CT_FX = CT_FB + 8 * FR_;
static_assert((CT_FX + 7 * 32 * FL) * 4 <= (1 << 20), "control block");

constexpr int XRS = 928;
constexpr int L_XT = 0;
constexpr int L_XCH = 29696;
constexpr int TPRS = 1040;
constexpr int L_TP = 0;
constexpr int L_TO = 29696;
constexpr int L_TI = 38912;
constexpr int L_BI = 38912;
constexpr int BRS = 912;
constexpr int L_Z = 47104;
constexpr int L_C1 = 38912;
constexpr int L_C2 = 65536;
constexpr int L_C3 = 70400;
constexpr int L_W = 47104;
constexpr int L_G = 70208;
constexpr int KCP = 4160;
constexpr int L_KC = 0;
constexpr int KSCR = 4096;
constexpr int L_RED = 33280;
constexpr int L_BO = 49664;
constexpr int L_U = 57856;
constexpr int L_XP = 57856;
constexpr int L_KT = 67072;
constexpr int L_CNT = 70272;
constexpr int LDS_BYTES = 72448;

struct Params {
  const u16* x; const u16* wr; const u16* wi;
  const u16* are; const u16* aim; const u16* bre; const u16* bim; const u16* cre; const u16* cim;
  const u16* logdt; const u16* dskip; const u16* gw;
  u16* out; char* ws;
};

constexpr double cx_sin_oct(double x) { double x2 = x * x, t = x, s = x; for (int i = 1; i < 9; ++i) { t *= -x2 / ((2 * i) * (2 * i + 1)); s += t; } return s; }
constexpr double cx_cos_oct(double x) { double x2 = x * x, t = 1, s = 1; for (int i = 1; i < 9; ++i) { t *= -x2 / ((2 * i - 1) * (2 * i)); s += t; } return s; }
constexpr double cx_sin(long n, long d) {
  n %= d; if (n < 0) n += d;
  const long o = (8 * n) / d;
  const double r = 6.283185307179586476925 * (double)(8 * n - o * d) / (8.0 * (double)d), q = 0.78539816339744830962;
  switch (o) {
    case 0: return cx_sin_oct(r);       case 1: return cx_cos_oct(q - r);
    case 2: return cx_cos_oct(r);       case 3: return cx_sin_oct(q - r);
    case 4: return -cx_sin_oct(r);      case 5: return -cx_cos_oct(q - r);
    case 6: return -cx_cos_oct(r);      default: return -cx_sin_oct(q - r);
  }
}
constexpr double cx_cos(long n, long d) { return cx_sin(4 * n + d, 4 * d); }
constexpr u32 cx_f32(double v) { return __builtin_bit_cast(u32, (float)v); }
constexpr u32 cx_bf(double v) { const u32 u = cx_f32(v); return (u + 0x7fffu + ((u >> 16) & 1u)) >> 16; }
constexpr int cx_pi(int g, int jj) { return 16 * (jj >> 2) + 4 * g + (jj & 3); }
struct Frag { u32 v[64][4]; };
enum { TF_FC0, TF_FS0, TF_CO0, TF_SO0, TF_COT0, TF_SOT0, TF_TC, TF_TS, TF_M, TF_Q0, TF_N = TF_Q0 + 24 };
constexpr Frag make_frag(int item) {
  Frag f{};
  for (int lane = 0; lane < 64; ++lane) {
    const int i16 = lane & 15, g = lane >> 4;
    if (item < TF_TC) {
      const int sn = item & 1;
      for (int jj = 0; jj < 8; ++jj) {
        const int m = cx_pi(g, jj), a = i16;
        long n = 0, d = 32;
        if (item < TF_CO0) { n = a * m; d = 32; }
        else if (item < TF_COT0) { n = a * (2 * m + 1); d = 64; }
        else { n = m * (2 * a + 1); d = 64; }
        f.v[lane][jj >> 1] |= cx_bf(sn ? cx_sin(n, d) : cx_cos(n, d)) << (16 * (jj & 1));
      }
    } else if (item == TF_TC || item == TF_TS) {
      for (int j = 0; j < 4; ++j) {
        const long n = (2 * g + (j & 1)) * (16 * (j >> 1) + i16);
        f.v[lane][j] = cx_f32(item == TF_TC ? cx_cos(n, 448) : cx_sin(n, 448));
      }
    } else if (item == TF_M) {
      for (int j = 0; j < 4; ++j) {
        const int k0 = 2 * g + (j >> 1);
        const double cf = ((k0 == 0 || k0 == 7) ? 1.0 : 2.0) / (2048.0 * 448.0);
        const double v = i16 < 14 ? cf * ((j & 1) ? -cx_sin(k0 * i16, 14) : cx_cos(k0 * i16, 14)) : 0.0;
        f.v[lane][j >> 1] |= cx_bf(v) << (16 * (j & 1));
      }
    } else {
      const int q = (item - TF_Q0) / 6, sub = (item - TF_Q0) % 6, k0 = 2 * q + ((i16 >> 1) & 1), ri = i16 & 1;
      if (sub < 2) {
        if ((i16 >> 2) == 2 * sub + (g & 1))
          for (int j = 0; j < 8; ++j) {
            const int d0 = 8 * (g >> 1) + j;
            const double v = d0 < 14 ? (ri ? -cx_sin(d0 * k0, 14) : cx_cos(d0 * k0, 14)) : 0.0;
            f.v[lane][j >> 1] |= cx_bf(v) << (16 * (j & 1));
          }
      } else {
        for (int j = 0; j < 4; ++j) {
          const long n = cx_pi(g, 4 * (sub & 1) + j) * k0;
          f.v[lane][j] = cx_f32(sub < 4 ? cx_cos(n, 448) : (ri ? -cx_sin(n, 448) : cx_sin(n, 448)));
        }
      }
    }
  }
  return f;
}
template <int I> struct FragC { static constexpr Frag f = make_frag(I); };
#define FR(i) FragC<(i)>::f
#define FR6(i) FR(i), FR(i + 1), FR(i + 2), FR(i + 3), FR(i + 4), FR(i + 5)
__device__ const __attribute__((aligned(1024))) Frag TF[TF_N] = {FR6(0), FR(6), FR(7), FR(8), FR6(9), FR6(15), FR6(21), FR6(27)};

struct KTab { u16 v[2080]; };
constexpr KTab make_ktab() {
  KTab t{};
  for (int i = 0; i < 2080; ++i) {
    const int l = 2048 - i;
    int idx = 768;
    if (l >= 0) {
      const int sh = l > 256 ? (l >= 1024 ? 3 : (l >= 512 ? 2 : 1)) : 0;
      const int v = sh ? (((l + (1 << (sh - 1)) - 1 + ((l >> sh) & 1)) >> sh) << sh) : l;
      if (v < 256) idx = ((v & 7) + 8 * (v >> 7)) * 16 + ((v >> 3) & 15);
      else if (v < 512) idx = 256 + ((((v - 256) >> 1) & 3) + 4 * ((v - 256) >> 7)) * 16 + (((v - 256) >> 3) & 15);
      else if (v < 1024) idx = 256 + (8 + (((v - 512) >> 2) & 1) + 2 * ((v - 512) >> 7)) * 16 + (((v - 512) >> 3) & 15);
      else if (v < 2048) idx = 512 + ((v - 1024) >> 7) * 16 + (((v - 1024) >> 3) & 15);
      else idx = 640;
    }
    t.v[i] = (u16)(2 * idx);
  }
  return t;
}
__device__ const __attribute__((aligned(16))) KTab KTAB = make_ktab();

template <class T, class F> DI T bc(F v) { return __builtin_bit_cast(T, v); }
DI f32x4 mfma32(u32x4 a, u32x4 b, f32x4 c) { return __builtin_amdgcn_mfma_f32_16x16x32_bf16(bc<bf16x8>(a), bc<bf16x8>(b), c, 0, 0, 0); }
DI f32x4 mfma16(u32x2 a, u32x2 b, f32x4 c) { return __builtin_amdgcn_mfma_f32_16x16x16bf16_1k(bc<s16x4>(a), bc<s16x4>(b), c, 0, 0, 0); }
DI u32 pk(float a, float b) { f32x2 v = {a, b}; return bc<u32>(__builtin_convertvector(v, bf16x2)); }
DI u32 pk2(f32x2 v) { return bc<u32>(__builtin_convertvector(v, bf16x2)); }
DI float bfr(float a) { return bc<float>(pk(0.f, a) & 0xffff0000u); }
DI float lo16(u32 v) { return bc<float>(v << 16); }
DI float hi16(u32 v) { return bc<float>(v & 0xffff0000u); }
DI f32x2 up2(u32 v) { return f32x2{lo16(v), hi16(v)}; }
DI float b2f(u16 v) { return bc<float>((u32)v << 16); }
DI u32x2 trread(u32 a) { return bc<u32x2>(__builtin_amdgcn_ds_read_tr16_b64_v4i16((LAS s16x4*)(size_t)a)); }
DI u32x4 cat(u32x2 a, u32x2 b) { u32x4 r = {a.x, a.y, b.x, b.y}; return r; }
DI float sinr(float x) { return __builtin_amdgcn_sinf(x); }
DI float cosr(float x) { return __builtin_amdgcn_cosf(x); }
DI float fx1(float v) { return __builtin_amdgcn_update_dpp(v, v, 0xB1, 0xf, 0xf, true); }
DI float fr8(float v) { return __builtin_amdgcn_update_dpp(v, v, 0x128, 0xf, 0xf, true); }
DI u32 ux1(u32 v) { return (u32)__builtin_amdgcn_update_dpp((int)v, (int)v, 0xB1, 0xf, 0xf, true); }
DI u32 ur8(u32 v) { return (u32)__builtin_amdgcn_update_dpp((int)v, (int)v, 0x128, 0xf, 0xf, true); }
DI u32x4 sx1(u32x4 v, u32 m) { u32x4 r = {ux1(v.x) ^ m, ux1(v.y) ^ m, ux1(v.z) ^ m, ux1(v.w) ^ m}; return r; }
DI u32x4 sr8(u32x4 v, u32 m) { u32x4 r = {ur8(v.x) ^ m, ur8(v.y) ^ m, ur8(v.z) ^ m, ur8(v.w) ^ m}; return r; }
DI u32x4 xr(u32x4 v, u32 m) { u32x4 r = {v.x ^ m, v.y ^ m, v.z ^ m, v.w ^ m}; return r; }
DI u32x4 pk8(const float (&v)[8]) { return u32x4{pk(v[0], v[1]), pk(v[2], v[3]), pk(v[4], v[5]), pk(v[6], v[7])}; }
template <class T> DI T lds_ld(u32 a) { return *(LAS T*)(size_t)a; }
template <class T> DI void lds_st(u32 a, T v) { *(LAS T*)(size_t)a = v; }
DI void wait_vm0() { __builtin_amdgcn_s_waitcnt(0x0F70); }
DI void drain_vm() { asm volatile("s_waitcnt vmcnt(0)" ::: "memory"); }
DI void bar() { __builtin_amdgcn_s_waitcnt(0xC07F); asm volatile("s_barrier" ::: "memory"); }
template <class T> DI void pin(T& v) { asm volatile("" : "+v"(v)); }
typedef __amdgpu_buffer_rsrc_t rsrc_t;
DI rsrc_t mkrs(const void* p, u32 bytes) { return __builtin_amdgcn_make_buffer_rsrc((void*)p, 0, bytes, 0x00020000); }
template <int AUX> DI void dma16(rsrc_t rs, u32 lds, u32 voff, u32 soff) { __builtin_amdgcn_raw_ptr_buffer_load_lds(rs, (LAS void*)(size_t)lds, 16, voff, soff, 0, AUX); }
DI f32x2 cmul(f32x2 x, f32x2 y) {
  f32x2 t;
  asm("v_pk_mul_f32 %0, %1, %2 op_sel:[0,0] op_sel_hi:[0,1]\n\tv_pk_fma_f32 %0, %1, %2, %0 op_sel:[1,1,0] op_sel_hi:[1,0,1] neg_lo:[1,0,0]" : "=&v"(t) : "v"(x), "v"(y));
  return t;
}
DI float dot2b(u32 a, u32 b, float c) { return __builtin_amdgcn_fdot2_f32_bf16(bc<bf16x2>(a), bc<bf16x2>(b), c, false); }
DI float wave_sum(float a) {
  a += __builtin_amdgcn_update_dpp(0.f, a, 0xB1, 0xf, 0xf, true);
  a += __builtin_amdgcn_update_dpp(0.f, a, 0x4E, 0xf, 0xf, true);
  a += __builtin_amdgcn_update_dpp(0.f, a, 0x141, 0xf, 0xf, true);
  a += __builtin_amdgcn_update_dpp(0.f, a, 0x140, 0xf, 0xf, true);
  a += __builtin_amdgcn_update_dpp(0.f, a, 0x142, 0xa, 0xf, false);
  a += __builtin_amdgcn_update_dpp(0.f, a, 0x143, 0xc, 0xf, false);
  return bc<float>(__builtin_amdgcn_readlane(bc<int>(a), 63));
}
DI int pi_k(int g, int jj) { return 16 * (jj >> 2) + 4 * g + (jj & 3); }

#define CNOP 1
#define CNOPR 2
#define PFFT 0.6
#define SH2PRIO 2
#define SLP 2
#define RLX_AGENT __ATOMIC_RELAXED, __HIP_MEMORY_SCOPE_AGENT
template <bool P> DI void st16(rsrc_t rs, u32 voff, u32 soff, u32x4 v) {
  __builtin_amdgcn_raw_buffer_store_b128(v, rs, voff, soff, P ? 16 : 0);
  asm volatile("s_nop 1" :: "v"(v) : "memory");
}
template <bool P> DI u32x4 ld16(rsrc_t rs, u32 voff, u32 soff) { return __builtin_amdgcn_raw_buffer_load_b128(rs, voff, soff, P ? 16 : 0); }
DI void publish(u32* set, int idx, u32 epoch) {
  if (threadIdx.x < 8) __hip_atomic_store((GAS u32*)set + threadIdx.x * FR_ + idx * FL, epoch, RLX_AGENT);
}
DI void poll_flags(const u32* first, int n, u32 epoch) {
  const int lane = threadIdx.x & 63;
  const GAS u32* f = (const GAS u32*)first + (lane < n ? lane : 0) * FL;
  for (int spin = 0; spin < (1 << 18); ++spin) {
    if (__all(__hip_atomic_load(f, RLX_AGENT) == epoch)) break;
    __builtin_amdgcn_s_sleep(SLP);
  }
}

#define STAMP(i) do {} while (0)
#define STAMP_DECL do {} while (0)
#define STAMP_FLUSH() do {} while (0)
#define STAMP_ARG
#define STAMP_PASS

DI u32x4 tf(rsrc_t rs, int item, u32 l16) { return __builtin_amdgcn_raw_buffer_load_b128(rs, l16 + (item & 3) * 1024, (item >> 2) * 4096, 0); }
DI u32x4 tfs(rsrc_t rs, int item, u32 l16, u32 so) { return __builtin_amdgcn_raw_buffer_load_b128(rs, l16 + item * 1024, so, 0); }

DI void fft_first_loads(const Params& p, const u32 lb, const int b) {
  const int lane = threadIdx.x & 63, w = __builtin_amdgcn_readfirstlane(threadIdx.x >> 6);
  const u32 l16 = lane * 16;
  const int s2 = b & 63, q = b >> 6;
  const rsrc_t rx = mkrs(p.x, S_ * D_ * 2), rtf = mkrs(TF, TF_N * 1024);
  if (lane < 56) {
    u32 la = lb + L_XT + 8 * w * XRS, so = (64 * 8 * w + s2) * (D_ * 2);
#pragma unroll 1
    for (int r = 0; r < 8; ++r) { dma16<0>(rx, la, l16, so); la += XRS; so += 64 * (D_ * 2); }
  }
  const u32 tq = (TF_Q0 + 6 * q) * 1024;
  dma16<0>(rtf, lb + L_C1 + w * 2048, l16, w == 0 ? 0u : tq + (2 * w - 2) * 1024);
  dma16<0>(rtf, lb + L_C1 + w * 2048 + 1024, l16, w == 0 ? 1024u : tq + (2 * w - 1) * 1024);
}

struct P3C { u32x4 b1, b2; f32x4 tc, ts; u32x2 m; };
template <int PH>
DI void p3_prepare(const Params& p, const u32 lb, const int b, const rsrc_t rtf, P3C& c) {
  const int lane = threadIdx.x & 63, i16 = lane & 15, g = lane >> 4;
  const u32 l16 = lane * 16;
  const int s2 = b & 63, q = b >> 6;
  c.tc = bc<f32x4>(tf(rtf, TF_TC, l16)); c.ts = bc<f32x4>(tf(rtf, TF_TS, l16));
  const u32x4 m4 = tf(rtf, TF_M, l16);
  c.m = u32x2{m4.x, m4.y};
  c.b1 = lds_ld<u32x4>(lb + L_C3 + l16); c.b2 = lds_ld<u32x4>(lb + L_C3 + 1024 + l16);
  pin(c.b1); pin(c.b2); pin(c.tc); pin(c.ts); pin(c.m);
}
DI void p3_make_b(const u32 lb, const int b) {
  const int lane = threadIdx.x & 63, i16 = lane & 15, g = lane >> 4;
  const int s2 = b & 63, q = b >> 6;
  const int ro = i16 >> 3;
  const float a = (float)(64 * (8 * q + (i16 & 7)) + s2) * (1.0f / 2048.0f), P0 = a * (float)(4 * g);
  float u[8], v[8];
#pragma unroll
  for (int jj = 0; jj < 8; ++jj) {
    const float ph = __builtin_fmaf(a, (float)(16 * (jj >> 2) + (jj & 3)), P0);
    u[jj] = sinr(ph + (ro ? 0.f : 0.25f));
    v[jj] = sinr(ph + (ro ? 0.25f : 0.5f));
  }
  lds_st<u32x4>(lb + L_C3 + lane * 16, pk8(u));
  lds_st<u32x4>(lb + L_C3 + 1024 + lane * 16, pk8(v));
}
template <bool LDSX>
DI void gate_rows(const Params& p, const u32 lb, const int b, const int r0, const int nr) {
  const int lane = threadIdx.x & 63;
  const u32 l16 = lane * 16;
  const int s2 = b & 63, q = b >> 6;
  u32x4 gwv = {0, 0, 0, 0};
  if (lane < 56) gwv = *(const GAS u32x4*)((const char*)p.gw + l16);
#pragma unroll
  for (int r = 0; r < 2; ++r) {
    if (r < nr) {
      float acc = 0.f;
      if (lane < 56) {
        u32x4 xv;
        if (LDSX) xv = lds_ld<u32x4>(lb + L_XT + (8 * q + r0 + r) * XRS + l16);
        else xv = *(const GAS u32x4*)((const char*)(p.x + (size_t)(64 * (8 * q + r0 + r) + s2) * D_) + l16);
#pragma unroll
        for (int e = 0; e < 4; ++e) acc = dot2b(pk2(up2(xv[e]) * up2(gwv[e])), 0x3f803f80u, acc);
      }
      const float gg = bfr(__builtin_amdgcn_rcpf(1.0f + __expf(-bfr(wave_sum(acc)))));
      if (lane == 0) lds_st<f32x2>(lb + L_G + (r0 + r) * 8, f32x2{gg, bfr(1.0f - gg)});
    }
  }
}

template <int PH>
DI void fft_phase3(const Params& p, const u32 lb, const int b, u32* ctl, const u32 epoch, const u32x4 (&fc)[2], const u32x4 (&fs)[2], const P3C& c STAMP_ARG) {
  constexpr bool P = PH == 0;
  constexpr int TPAUX = P ? 16 : 0;
  const int tid = threadIdx.x, lane = tid & 63, w = __builtin_amdgcn_readfirstlane(tid >> 6), i16 = lane & 15, g = lane >> 4;
  const u32 l16 = lane * 16;
  const int s2 = b & 63, q = b >> 6;
  const f32x4 z4 = {0, 0, 0, 0};
  const rsrc_t rws = mkrs(p.ws, 16u << 20);
  u32 tpa = lb + L_TP + (4 * g + (i16 >> 2)) * TPRS + (i16 & 3) * 128 + (w >> 1) * 32 + (w & 1) * 8;
  const int row = 2 * w + (i16 >> 3);
  u32 zw = lb + L_Z + ((i16 & 7) * 16 + row) * 64 + ((g ^ (row >> 2)) & 3) * 16;
  u32 zr = lb + L_Z + (w * 16 + i16) * 64 + ((g ^ (i16 >> 2)) & 3) * 16;
  u32 boff = (u32)(tid * B_D);
  u32 biw = lb + L_BI + tid * 2, bir = lb + L_BI + w * BRS + (32 * i16 + 4 * g) * 2;
  u32 ooff = (u32)((64 * (8 * q + w) + s2) * (D_ * 2) + (32 * i16 + 4 * g) * 2);
  u32 mi = (i16 & 1) ? 0u : 0x80008000u;
  pin(tpa); pin(zw); pin(zr); pin(boff); pin(biw); pin(bir); pin(ooff); pin(mi);
  STAMP(13);
  if (P) { poll_flags(ctl + CT_F2 + (b & 7) * FR_ + w * 32 * FL, 32, epoch); bar(); __builtin_amdgcn_s_setprio(3); }
  STAMP(14);
#pragma unroll
  for (int r = 0; r < 4; ++r) dma16<TPAUX>(rws, lb + L_TP + (4 * w + r) * TPRS, l16, (u32)WS_TP + s2 * TP_S2 + (4 * w + r) * 1024);
  wait_vm0();
  bar();
  STAMP(15);
#pragma unroll
  for (int t = 0; t < 2; ++t) {
    u32x4 Tr = cat(trread(tpa + t * 512), trread(tpa + t * 512 + 16 * TPRS));
    u32x4 Ti = cat(trread(tpa + t * 512 + 16), trread(tpa + t * 512 + 16 + 16 * TPRS));
    f32x4 z = mfma32(Ti, c.b2, mfma32(Tr, c.b1, z4));
    lds_st<u32x2>(zw + t * 8, u32x2{pk(z[0], z[1]), pk(z[2], z[3])});
  }
  bar();
  STAMP(16);
  f32x2 ga[2][2];
  {
    u32x4 A = lds_ld<u32x4>(zr), A2 = sx1(A, mi);
    const float gg = lds_ld<float>(lb + L_G + w * 8);
#pragma unroll
    for (int td = 0; td < 2; ++td) {
      f32x4 G = mfma32(A2, fs[td], mfma32(A, fc[td], z4));
      const float c0 = c.tc[2 * td], s0 = c.ts[2 * td], c1 = c.tc[2 * td + 1], s1 = c.ts[2 * td + 1];
      const float g0r = G[0] * c0 - G[1] * s0, g0i = G[1] * c0 + G[0] * s0;
      const float g1r = G[2] * c1 - G[3] * s1, g1i = G[3] * c1 + G[2] * s1;
      f32x4 y = mfma16(u32x2{pk(g0r, g0i), pk(g1r, g1i)}, c.m, z4);
      ga[td][0] = up2(pk2(up2(pk(y[0], y[1])) * gg));
      ga[td][1] = up2(pk2(up2(pk(y[2], y[3])) * gg));
      pin(ga[td][0]); pin(ga[td][1]);
    }
  }
  STAMP(17);
  if (P) { poll_flags(ctl + CT_FB + (b & 7) * FR_ + w * 28 * FL, 28, epoch); bar(); }
  STAMP(18);
  if (tid < D_) {
    const u32x4 bv = ld16<P>(rws, boff, (u32)WS_B + (((s2 & 7) * 8 + (s2 >> 3)) * 32 + 8 * q) * 2);
#pragma unroll
    for (int e = 0; e < 4; ++e) {
      lds_st<u16>(biw + (2 * e) * BRS, (u16)bv[e]);
      lds_st<u16>(biw + (2 * e + 1) * BRS, (u16)(bv[e] >> 16));
    }
  }
  bar();
  {
    const rsrc_t ro_ = mkrs(p.out, S_ * D_ * 2);
    const float og = lds_ld<float>(lb + L_G + w * 8 + 4);
    const u32x2 bq0 = lds_ld<u32x2>(bir), bq1 = lds_ld<u32x2>(bir + 32);
#pragma unroll
    for (int td = 0; td < 2; ++td) {
      const u32x2 bq = td ? bq1 : bq0;
      const f32x2 o01 = ga[td][0] + up2(pk2(up2(bq.x) * og));
      const f32x2 o23 = ga[td][1] + up2(pk2(up2(bq.y) * og));
      if (i16 < 14) __builtin_amdgcn_raw_buffer_store_b64(u32x2{pk2(o01), pk2(o23)}, ro_, ooff, td * 32, 0);
    }
  }
  STAMP(19);
  if (P && b == 0 && tid == 0) __hip_atomic_store((GAS u32*)ctl + CT_GEN, epoch, RLX_AGENT);
}

template <int PH>
DI void fft_upper(const Params& p, const u32 lb, const int b, u32* ctl, u32x4 (&fc)[2], u32x4 (&fs)[2], P3C& c3) {
  constexpr bool P = PH == 0;
  const int lane = threadIdx.x & 63, w = __builtin_amdgcn_readfirstlane(threadIdx.x >> 6);
  const u32 l16 = lane * 16;
  const rsrc_t rtf = mkrs(TF, TF_N * 1024);
  if (!P && w == 4) p3_make_b(lb, b);
  if (P) {
    if (w == 4) p3_make_b(lb, b);
    bar();
    bar(); bar(); bar();
    __builtin_amdgcn_s_setprio(SH2PRIO);
  }
  if (P) gate_rows<P>(p, lb, b, w, 1); else gate_rows<P>(p, lb, b, 2 * (w - 4), 2);
  fc[0] = tf(rtf, TF_FC0, l16); fs[0] = tf(rtf, TF_FS0, l16);
  fc[1] = xr(fc[0], 0x80000000u); fs[1] = xr(fs[0], 0x80000000u);
  pin(fc[0]); pin(fc[1]); pin(fs[0]); pin(fs[1]);
  if (!P) bar();
  p3_prepare<PH>(p, lb, b, rtf, c3);
  if (!P) bar();
}

template <int PH>
DI void fft_role(const Params& p, const u32 lb, const int b, u32* ctl, const u32 gen, u32x4 (&fc)[2], u32x4 (&fs)[2], P3C& c3 STAMP_ARG) {
  constexpr bool P = PH == 0;
#define TPAUXV 16
  constexpr int AUX = P ? 16 : 0, TPAUX = P ? TPAUXV : 0;
  (void)AUX;
  const int tid = threadIdx.x, lane = tid & 63, w = __builtin_amdgcn_readfirstlane(tid >> 6), i16 = lane & 15, g = lane >> 4;
  const u32 l16 = lane * 16;
  const int s2 = b & 63, q = b >> 6;
  const int xx = b & 7, mm = (b >> 3) & 3, kq = b >> 5;
  const int k1w = (((mm & 1) ? (xx ? 32 - xx : 8) : xx) + 16 * (mm >> 1)) & 31;
  const f32x4 z4 = {0, 0, 0, 0};
  const rsrc_t rws = mkrs(p.ws, 16u << 20), rtf = mkrs(TF, TF_N * 1024);
  const u32 tpo = (u32)((tid >> 3) * TP_S2 + (tid & 7) * 16), tps = (u32)WS_TP + k1w * 1024 + kq * 128;
  if (PH == 2 || PH == 3) {
    fc[0] = tf(rtf, TF_FC0, l16); fs[0] = tf(rtf, TF_FS0, l16);
    fc[1] = xr(fc[0], 0x80000000u); fs[1] = xr(fs[0], 0x80000000u);
  }

  u32x4 co[2], so[2], cot[2], sot[2];
  auto load_p2 = [&]() {
    const bool mir = kq >= 4;
    const int col0 = mir ? 448 - (56 * kq + 49) : 56 * kq;
    const rsrc_t rr = mkrs(p.wr, S_ * DF * 2), ri_ = mkrs(p.wi, S_ * DF * 2);
#pragma unroll
    for (int i = 0; i < 3; ++i) {
      const int gi = w + 4 * i;
      if (gi < 9) {
        const int gam = 64 * gi + lane, k2 = (gam * 7282) >> 16, chunk = gam - 9 * k2;
        const int ks = k1w + 32 * k2, row = mir ? ((2048 - ks) & 2047) : ks;
        const u32 vo = (u32)(((row * DF + col0) * 2) & ~15) + chunk * 16;
        dma16<0>(rr, lb + L_W + gi * 1024, vo, 0);
        dma16<0>(ri_, lb + L_W + 9216 + gi * 1024, vo, 0);
      }
    }
    dma16<0>(rtf, lb + L_C2 + w * 1024, l16, (TF_CO0 + w) * 1024);
  };

  if (PH == 0 || PH == 1) {
    u32x4 C1, S1, nS1;
    {
      const float k1 = (float)(16 * (w >> 1) + i16) * (1.0f / 2048.0f), P0 = k1 * (float)(64 * g + s2), Pl = k1 * 256.f;
      float c[8], s[8];
#pragma unroll
      for (int j = 0; j < 8; ++j) { const float ph = __builtin_fmaf(Pl, (float)j, P0); c[j] = cosr(ph); s[j] = sinr(ph); }
      C1 = pk8(c); S1 = pk8(s); nS1 = xr(S1, 0x80008000u);
      pin(C1); pin(S1); pin(nS1);
    }
    int d0a = 8 * (g >> 1) + (i16 >> 2), d0b = d0a + 4;
    d0b = d0b < 14 ? d0b : 0;
    u32 xa = lb + L_XT + (8 * w + (g & 1)) * XRS + 8 * (i16 & 3);
    u32 xb = xa + d0b * 64; xa += d0a * 64;
    u32 xw = lb + L_XCH + w * 256 + lane * 4;
    u32 xr_ = lb + L_XCH + (w & 1) * 4096 + lane * 4;
    u32 toff = (u32)((16 * (w >> 1) + i16) * T_K1 + 4 * (w & 1) * 32 + g * 16);
    u32 imm = (i16 & 1) ? 0x80008000u : 0u;
    pin(xa); pin(xb); pin(xw); pin(xr_); pin(toff); pin(imm);
    const u32 tso = (u32)WS_T + s2 * 1024 + q * 256;
    STAMP(1);
    wait_vm0();
    bar();
    const u32 cb = lb + L_C1 + l16;
    fc[0] = lds_ld<u32x4>(cb); fs[0] = lds_ld<u32x4>(cb + 1024);
    const u32x4 Ba1 = lds_ld<u32x4>(cb + 2048), Ba2 = lds_ld<u32x4>(cb + 3072);
    const f32x4 twc0 = lds_ld<f32x4>(cb + 4096), twc1 = lds_ld<f32x4>(cb + 5120), tws0 = lds_ld<f32x4>(cb + 6144), tws1 = lds_ld<f32x4>(cb + 7168);
    fc[1] = xr(fc[0], 0x80000000u); fs[1] = xr(fs[0], 0x80000000u);
    STAMP(2);
    f32x4 D2[2][2];
#pragma unroll
    for (int sg = 0; sg < 2; ++sg) {
      f32x4 H[2];
#pragma unroll
      for (int td = 0; td < 2; ++td) {
        const int o = td * 32 + sg * 4 * XRS;
        u32x4 A0 = cat(trread(xa + o), trread(xb + o));
        u32x4 A1 = cat(trread(xa + o + 2 * XRS), trread(xb + o + 2 * XRS));
        H[td] = mfma32(A1, Ba2, mfma32(A0, Ba1, z4));
      }
      float h[8];
#pragma unroll
      for (int j = 0; j < 4; ++j) {
        h[j] = __builtin_fmaf(fx1(H[0][j]), tws0[j], H[0][j] * twc0[j]);
        h[4 + j] = __builtin_fmaf(fx1(H[1][j]), tws1[j], H[1][j] * twc1[j]);
      }
      u32x4 A = pk8(h), A2 = sx1(A, imm);
#pragma unroll
      for (int tk = 0; tk < 2; ++tk) D2[sg][tk] = mfma32(A2, fs[tk], mfma32(A, fc[tk], z4));
    }
#pragma unroll
    for (int tk = 0; tk < 2; ++tk)
#pragma unroll
      for (int r = 0; r < 4; ++r) lds_st<u32>(xw + (tk * 4 + r) * 1024, pk(D2[0][tk][r], D2[1][tk][r]));
    bar();
    STAMP(3);
    {
      u32 pc[4][2];
#pragma unroll
      for (int kk = 0; kk < 2; ++kk) {
        const u32 br = xr_ + kk * 2048;
        u32x4 Xr = {lds_ld<u32>(br), lds_ld<u32>(br + 256), lds_ld<u32>(br + 512), lds_ld<u32>(br + 768)};
        u32x4 Xi = {lds_ld<u32>(br + 1024), lds_ld<u32>(br + 1280), lds_ld<u32>(br + 1536), lds_ld<u32>(br + 1792)};
        f32x4 Tr = mfma32(Xi, S1, mfma32(Xr, C1, z4));
        f32x4 Ti = mfma32(Xr, nS1, mfma32(Xi, C1, z4));
        pc[kk][0] = pk(Tr[0], Tr[1]); pc[kk][1] = pk(Tr[2], Tr[3]);
        pc[2 + kk][0] = pk(Ti[0], Ti[1]); pc[2 + kk][1] = pk(Ti[2], Ti[3]);
      }
      u32x4 lo = {pc[0][0], pc[0][1], pc[1][0], pc[1][1]}, hi = {pc[2][0], pc[2][1], pc[3][0], pc[3][1]};
#pragma unroll
      for (int e = 0; e < 4; ++e) {
        auto r1 = __builtin_amdgcn_permlane16_swap(lo[e], hi[e], false, false);
        auto r2 = __builtin_amdgcn_permlane32_swap(r1[0], r1[1], false, false);
        lo[e] = r2[0]; hi[e] = r2[1];
      }
      st16<P>(rws, toff, tso, lo);
      st16<P>(rws, toff + 64, tso, hi);
    }
    STAMP(4);
    if (P) {
      drain_vm();
      STAMP(20);
      bar();
      publish(ctl + CT_F1, b, gen + 1u);
      __builtin_amdgcn_s_setprio(2);
      load_p2();
    }
    STAMP(5);
  }
  const u32 epoch = __builtin_amdgcn_readfirstlane(gen) + 1u;

  if (PH == 2) load_p2();
  if (PH == 0 || PH == 2) {
    const int ri = i16 >> 3;
    u32 tsv = (u32)((lane >> 1) * 1024 + w * 256 + (lane & 1) * 16);
    u32 tiw = lb + L_TI + w * 2048 + ((lane >> 1) & 1) * 1024 + (lane >> 2) * 32 + (lane & 1) * 16;
    u32 ta = lb + L_TI + w * 2048 + (4 * g + (i16 >> 2)) * 32 + (i16 & 3) * 8;
    u32 tow = lb + L_TO + 2 * i16 * 144 + w * 32 + g * 8;
    u32 tor = lb + L_TO + (tid >> 3) * 144 + (tid & 7) * 16;
    u32 mf = ri ? 0x80008000u : 0u, mi = ri ? 0u : 0x80008000u;
    float wr_[16], wi_[16];
    {
      const bool mir = kq >= 4;
      const int c = i16 & 7, kd = 2 * w + (c >> 2) + 14 * (4 * kq + (c & 3)), col0 = mir ? 448 - (56 * kq + 49) : 56 * kq;
      const int row0 = mir ? ((2048 - k1w) & 2047) : k1w;
      u32 wa = lb + L_W + 4 * g * 144 + (((row0 * DF + col0) * 2) & 15) + ((mir ? 448 - kd : kd) - col0) * 2;
      const u32 sg = (mir != (ri != 0)) ? 0u : 0x80000000u;
      pin(tsv); pin(tiw); pin(ta); pin(tow); pin(tor); pin(mf); pin(mi); pin(wa);
      STAMP(7);
      if (P) poll_flags(ctl + CT_F1 + (b & 7) * FR_ + w * 64 * FL, 64, epoch);
      wait_vm0();
      bar();
      const u32 so_ = (u32)WS_T + k1w * T_K1 + kq * 32;
      const u32x4 t0 = ld16<P>(rws, tsv, so_), t1 = ld16<P>(rws, tsv, so_ + 32 * 1024);
      __builtin_amdgcn_sched_barrier(0);
      co[0] = lds_ld<u32x4>(lb + L_C2 + l16); so[0] = lds_ld<u32x4>(lb + L_C2 + 1024 + l16);
      cot[0] = lds_ld<u32x4>(lb + L_C2 + 2048 + l16); sot[0] = lds_ld<u32x4>(lb + L_C2 + 3072 + l16);
      co[1] = xr(so[0], 0x00008000u);
      so[1] = xr(co[0], 0x80000000u);
      cot[1] = xr(cot[0], 0x80000000u); sot[1] = xr(sot[0], 0x80000000u);
      pin(co[0]); pin(co[1]); pin(so[0]); pin(so[1]); pin(cot[0]); pin(cot[1]); pin(sot[0]); pin(sot[1]);
#pragma unroll
      for (int j = 0; j < 16; ++j) {
        const int o = (32 * (j >> 3) + 16 * ((j >> 2) & 1) + (j & 3)) * 144;
        wr_[j] = bc<float>((u32)lds_ld<u16>(wa + o) << 16);
        wi_[j] = bc<float>(((u32)lds_ld<u16>(wa + 9216 + o) << 16) + sg);
      }
#pragma unroll
      for (int j = 0; j < 16; ++j) { pin(wr_[j]); pin(wi_[j]); }
      __builtin_amdgcn_sched_barrier(0);
      if (P) __builtin_amdgcn_s_setprio(3);
      STAMP(8);
      lds_st<u32x4>(tiw, t0);
      lds_st<u32x4>(tiw + 512, t1);
    }
    STAMP(9);
    f32x4 E[2], O[2];
    {
      u32x4 B0 = cat(trread(ta), trread(ta + 512)), B1 = cat(trread(ta + 1024), trread(ta + 1536));
      u32x4 B0s = sr8(B0, mf), B1s = sr8(B1, mf);
#pragma unroll
      for (int t = 0; t < 2; ++t) {
        E[t] = mfma32(fs[t], B0s, mfma32(fc[t], B0, z4));
        O[t] = mfma32(so[t], B1s, mfma32(co[t], B1, z4));
      }
    }
    float Pp[8], Mm[8];
#pragma unroll
    for (int j = 0; j < 8; ++j) {
      const float e = E[j >> 2][j & 3], o = O[j >> 2][j & 3];
      const float ylo = e + o, yhi = e - o;
      const float vlo = __builtin_fmaf(fr8(ylo), wi_[j], ylo * wr_[j]);
      const float vhi = __builtin_fmaf(fr8(yhi), wi_[8 + j], yhi * wr_[8 + j]);
      Pp[j] = vlo + vhi; Mm[j] = vlo - vhi;
    }
    {
      u32x4 AP = pk8(Pp), AM = pk8(Mm);
      u32x4 APs = sr8(AP, mi), AMs = sr8(AM, mi);
#pragma unroll
      for (int t = 0; t < 2; ++t) {
        f32x4 ze = mfma32(APs, fs[t], mfma32(AP, fc[t], z4));
        f32x4 zo = mfma32(AMs, sot[t], mfma32(AM, cot[t], z4));
        lds_st<u32x2>(tow + t * 32 * 144, u32x2{pk(ze[0], ze[1]), pk(ze[2], ze[3])});
        lds_st<u32x2>(tow + t * 32 * 144 + 144, u32x2{pk(zo[0], zo[1]), pk(zo[2], zo[3])});
      }
    }
    bar();
    STAMP(10);
    {
      st16<P>(rws, tpo, tps, lds_ld<u32x4>(tor));
      st16<P>(rws, tpo, tps + 32 * TP_S2, lds_ld<u32x4>(tor + 32 * 144));
    }
    STAMP(11);
    if (P) { drain_vm(); bar(); publish(ctl + CT_F2, b, epoch); __builtin_amdgcn_s_setprio(SH2PRIO); }
    STAMP(12);
  }

  if (PH == 0) gate_rows<true>(p, lb, b, w, 1);
  if (PH == 3) bar();
  if (PH == 0 || PH == 3) p3_prepare<PH>(p, lb, b, rtf, c3);
  if (PH == 3) bar();
}

DI f32x2 cexp_rev(float x2, float ph) {
  const float e = __builtin_amdgcn_exp2f(x2);
  const float f = __builtin_amdgcn_fractf(ph);
  f32x2 cs = {cosr(f), sinr(f)};
  return cs * e;
}
struct KW { u32 a[8]; };
template <int ROW> DI void wrow(const KW& k, f32x2 v) { lds_st<u32>(k.a[ROW & 7] + ROW * 256, pk2(v)); }
template <int ROW> DI void wrow2(const KW& k, f32x2 lo, f32x2 hi) {
  const u32 a = pk2(lo), b = pk2(hi);
  asm volatile("ds_write2st64_b32 %0, %1, %2 offset0:%c3 offset1:%c4" :: "v"(k.a[ROW & 7]), "v"(a), "v"(b), "i"(ROW), "i"(ROW + 8) : "memory");
}
DI void kfrag(u32 fa, u32 fb, u32x4 (&A)[4]) {
  A[0] = lds_ld<u32x4>(fa); A[1] = lds_ld<u32x4>(fb); A[2] = lds_ld<u32x4>(fa + 128); A[3] = lds_ld<u32x4>(fb + 128);
}
DI f32x4 ktile(const u32x4 (&A)[4], const u32x4 (&V)[4]) {
  f32x4 acc = {0, 0, 0, 0};
#pragma unroll
  for (int ks = 0; ks < 4; ++ks) acc = mfma32(A[ks], V[ks], acc);
  return acc;
}
template <class F0, class F1>
DI void kgen(const u32 (&in)[6], float dt, const KW& k, u32 fa, u32 fb, u32 pb, F0&& hook0, F1&& hook1) {
  const float ar = dt * bc<float>(in[0] << 16), ai = dt * bc<float>(in[1] << 16);
  const f32x2 dB = {dt * bc<float>(in[2] << 16), dt * bc<float>(in[3] << 16)};
  const f32x2 Cc = {bc<float>(in[4] << 16), bc<float>(in[5] << 16)};
  const f32x2 CB = cmul(Cc, dB);
  const float x1 = ar * 1.44269504088896341f, p1 = ai * 0.15915494309189535f;
  const f32x2 E1 = cexp_rev(x1, p1), E8c = cexp_rev(8.f * x1, -8.f * p1), E128 = cexp_rev(128.f * x1, 128.f * p1);
  u32x4 V[4], A0[4], A1[4];
  {
    const f32x2 v2 = cmul(E8c, E8c), v3 = cmul(v2, E8c), v4 = cmul(v2, v2), v8 = cmul(v4, v4);
    const f32x2 v5 = cmul(v4, E8c), v6 = cmul(v4, v2), v7 = cmul(v4, v3);
    wrow2<0>(k, f32x2{1.f, 0.f}, v8); wrow2<1>(k, E8c, cmul(v8, E8c)); wrow2<2>(k, v2, cmul(v8, v2)); wrow2<3>(k, v3, cmul(v8, v3));
    wrow2<4>(k, v4, cmul(v8, v4)); wrow2<5>(k, v5, cmul(v8, v5)); wrow2<6>(k, v6, cmul(v8, v6)); wrow2<7>(k, v7, cmul(v8, v7));
    kfrag(fa, fb, V);
  }
  hook0();
  const f32x2 E2 = cmul(E1, E1), E3 = cmul(E2, E1), E4 = cmul(E2, E2), E5 = cmul(E4, E1), E6 = cmul(E4, E2), E7 = cmul(E4, E3);
  const f32x2 G1 = cmul(CB, E128);
  wrow2<0>(k, CB, G1); wrow2<1>(k, cmul(CB, E1), cmul(G1, E1)); wrow2<2>(k, cmul(CB, E2), cmul(G1, E2)); wrow2<3>(k, cmul(CB, E3), cmul(G1, E3));
  wrow2<4>(k, cmul(CB, E4), cmul(G1, E4)); wrow2<5>(k, cmul(CB, E5), cmul(G1, E5)); wrow2<6>(k, cmul(CB, E6), cmul(G1, E6)); wrow2<7>(k, cmul(CB, E7), cmul(G1, E7));
  kfrag(fa, fb, A0);
  const f32x2 G2 = cmul(G1, E128), G3 = cmul(G2, E128), G4 = cmul(G3, E128), G5 = cmul(G4, E128), G6 = cmul(G5, E128), G7 = cmul(G6, E128);
  wrow2<0>(k, G2, G4); wrow2<1>(k, cmul(G2, E2), cmul(G4, E4)); wrow2<2>(k, cmul(G2, E4), G5); wrow2<3>(k, cmul(G2, E6), cmul(G5, E4));
  wrow2<4>(k, G3, G6); wrow2<5>(k, cmul(G3, E2), cmul(G6, E4)); wrow2<6>(k, cmul(G3, E4), G7); wrow2<7>(k, cmul(G3, E6), cmul(G7, E4));
  kfrag(fa, fb, A1);
  hook1();
  __builtin_amdgcn_sched_barrier(0);
  const f32x4 a0 = ktile(A0, V);
  const f32x2 G8 = cmul(G7, E128), G9 = cmul(G8, E128), G10 = cmul(G9, E128), G11 = cmul(G10, E128);
  const f32x2 G12 = cmul(G11, E128), G13 = cmul(G12, E128), G14 = cmul(G13, E128), G15 = cmul(G14, E128);
  wrow2<0>(k, G8, cmul(G15, E128)); wrow<1>(k, G9); wrow<2>(k, G10); wrow<3>(k, G11);
  wrow<4>(k, G12); wrow<5>(k, G13); wrow<6>(k, G14); wrow<7>(k, G15);
  kfrag(fa, fb, A0);
  __builtin_amdgcn_sched_barrier(0);
  const f32x4 a1 = ktile(A1, V), a2 = ktile(A0, V);
  lds_st<f32x4>(pb, a0);
  lds_st<f32x4>(pb + 1024, a1);
  lds_st<f32x4>(pb + 2048, a2);
}

struct SsmLoads { u32x4 xt_v, kt0; u32x2 kt1; u32 in[6], ldt, dsk; };
template <int PH>
DI void ssm_first_loads(const Params& p, const int j, SsmLoads& L) {
  const int tid = threadIdx.x, w = __builtin_amdgcn_readfirstlane(tid >> 6), d = 2 * j, ch = w >> 2, n = tid & 255;
  const int sb_ = j / 7, db_ = j - sb_ * 7;
  if (PH == 0 || PH == 1)
    L.xt_v = __builtin_amdgcn_raw_buffer_load_b128(mkrs(p.x, S_ * D_ * 2), (u32)((tid >> 3) * (D_ * 2) + (tid & 7) * 16), (u32)(64 * sb_ * (D_ * 2) + db_ * 128), 0);
  __builtin_amdgcn_sched_barrier(0);
  if (PH != 1) {
    const u32 vo = ((d + ch) * NS + n) * 2;
    const u16* arr[6] = {p.are, p.aim, p.bre, p.bim, p.cre, p.cim};
#pragma unroll
    for (int a = 0; a < 6; ++a) L.in[a] = *(const GAS u16*)((const GAS char*)arr[a] + vo);
    L.ldt = *(const GAS u16*)(p.logdt + d + ch);
    L.dsk = *(const GAS u16*)(p.dskip + d + ((w >> 1) & 1));
    L.kt0 = *(const GAS u32x4*)(KTAB.v + 8 * n);
    L.kt1 = *(const GAS u32x2*)(KTAB.v + 8 * n + 8);
  }
}

template <int PH>
DI void ssm_role(const Params& p, const u32 lb, const int j, u32* ctl, const u32 gen, const SsmLoads& L STAMP_ARG) {
  constexpr bool P = PH == 0;
  const int tid = threadIdx.x, lane = tid & 63, w = __builtin_amdgcn_readfirstlane(tid >> 6), i16 = lane & 15, g = lane >> 4;
  const int d = 2 * j, ch = w >> 2, n = tid & 255;
  const f32x4 z4 = {0, 0, 0, 0};
  const rsrc_t rws = mkrs(p.ws, 16u << 20);
  const int sb_ = j / 7, db_ = j - sb_ * 7;
  const u32x4 xt_v = L.xt_v, kt0 = L.kt0;
  const u32x2 kt1 = L.kt1;
  const u32 ldt = L.ldt;
  u32 dsk = L.dsk;
  const u32 (&in)[6] = L.in;
  auto transpose = [&]() {
    lds_st<u32x4>(lb + L_XP + (tid >> 3) * 144 + (tid & 7) * 16, xt_v);
    bar();
    const int chi = g + 4 * (w >> 2);
    const u32 a = lb + L_XP + (8 * chi + (i16 >> 2)) * 144 + (16 * (w & 3) + 4 * (i16 & 3)) * 2;
    st16<P>(rws, (u32)((16 * (w & 3) + i16) * XT_D + chi * 16), (u32)WS_XT + 64 * db_ * XT_D + sb_ * 128, cat(trread(a), trread(a + 4 * 144)));
  };
  if (PH == 1) { transpose(); return; }
  if (P) {
    if (tid == 0) lds_st<u32>(lb + L_CNT, 0u);
    __builtin_amdgcn_sched_barrier(0);
    transpose();
    __builtin_amdgcn_s_setprio(1);
    __builtin_amdgcn_sched_barrier(0);
  }
  KW kw;
  const u32 sb = lb + w * KSCR;
#pragma unroll
  for (int h = 0; h < 8; ++h) { kw.a[h] = sb + (((lane >> 2) ^ h) << 4) + (lane & 3) * 4; pin(kw.a[h]); }
  u32 fa = sb + i16 * 256 + ((g ^ (i16 & 3)) << 4) + ((i16 >> 2) & 1) * 64, fb = fa ^ 64;
  pin(fa); pin(fb);
  STAMP(1);
  kgen(in, bfr(expf(bc<float>(ldt << 16))), kw, fa, fb, sb + lane * 16,
       [&]() {
         if (P) {
           drain_vm();
           STAMP(5);
           if (lane == 0 && __hip_atomic_fetch_add((LAS u32*)(size_t)(lb + L_CNT), 1u, __ATOMIC_RELAXED, __HIP_MEMORY_SCOPE_WORKGROUP) == 7u)
             __hip_atomic_store((GAS u32*)ctl + CT_FX + (db_ * 32 + sb_) * FL, gen + 1u, RLX_AGENT);
         }
       },
       []() {});
  const u32 epoch = __builtin_amdgcn_readfirstlane(gen) + 1u;
  pin(dsk);
  STAMP(2);
  if (P && w == 0) poll_flags(ctl + CT_FX + (d >> 6) * 32 * FL, 32, epoch);
  STAMP(6);
  bar();
  const u32x4 uv_ = ld16<P>(rws, n * 16, (u32)WS_XT + (d + ch) * XT_D);
  STAMP(3);
  if ((w & 3) < 3) {
    const u32 pb = lb + ch * 4 * KSCR + (w & 3) * 1024 + lane * 16;
    const f32x4 s = (lds_ld<f32x4>(pb) + lds_ld<f32x4>(pb + KSCR)) + (lds_ld<f32x4>(pb + 2 * KSCR) + lds_ld<f32x4>(pb + 3 * KSCR));
    const u32 tb = lb + L_KT + ch * 1600 + ((w & 3) * 256 + 64 * g + i16) * 2, a = pk(s[0], s[1]), cc = pk(s[2], s[3]);
    lds_st<u16>(tb, (u16)a); lds_st<u16>(tb + 32, (u16)(a >> 16));
    lds_st<u16>(tb + 64, (u16)cc); lds_st<u16>(tb + 96, (u16)(cc >> 16));
  } else {
    if (lane == 0) lds_st<u16>(lb + L_KT + ch * 1600 + 1536, (u16)0);
    if (lane < 32) lds_st<u32x4>(lb + L_U + ch * 4608 + lane * 16, u32x4{0, 0, 0, 0});
  }
  bar();
  STAMP(4);
  {
    const u32 ktb = lb + L_KT + ch * 1600;
    u32 e[11];
#pragma unroll
    for (int k = 0; k < 4; ++k) { e[2 * k] = lds_ld<u16>(ktb + (kt0[k] & 0xffffu)); e[2 * k + 1] = lds_ld<u16>(ktb + (kt0[k] >> 16)); }
    e[8] = lds_ld<u16>(ktb + (kt1[0] & 0xffffu)); e[9] = lds_ld<u16>(ktb + (kt1[0] >> 16)); e[10] = lds_ld<u16>(ktb + (kt1[1] & 0xffffu));
    const u32 k00 = lds_ld<u16>(ktb);
    u32 dw[5], al[5];
#pragma unroll
    for (int k = 0; k < 5; ++k) { dw[k] = e[2 * k] | (e[2 * k + 1] << 16); al[k] = e[2 * k + 1] | (e[2 * k + 2] << 16); }
    const u32 kb = lb + L_KC + ch * 4 * KCP + n * 16;
    lds_st<u32x4>(kb, u32x4{dw[0], dw[1], dw[2], dw[3]});
    lds_st<u32x4>(kb + KCP, u32x4{al[0], al[1], al[2], al[3]});
    lds_st<u32x4>(kb + 2 * KCP, u32x4{dw[1], dw[2], dw[3], dw[4]});
    lds_st<u32x4>(kb + 3 * KCP, u32x4{al[1], al[2], al[3], al[4]});
    if (n < 16) {
      u32x4 z = {0, 0, 0, 0};
      if (n == 0) z.x = k00;
      lds_st<u32x4>(lb + L_KC + (ch * 4 + (n >> 2)) * KCP + 4096 + (n & 3) * 16, z);
    }
    lds_st<u32x4>(lb + L_U + ch * 4608 + 512 + n * 16, uv_);
  }
  bar();
  STAMP(7);
  f32x4 acc[8];
  const int cc = (w >> 1) & 1, hf = w & 1;
  if (w < 4) {
#pragma unroll
    for (int c = 0; c < 8; ++c) acc[c] = z4;
    const int bsh = (-i16) & 3;
    u32 abase = lb + L_KC + (cc * 4 + bsh) * KCP + (2032 + 8 * g - i16 - bsh) * 2 - hf * 4 * 64 - 3 * 64 - 7 * 512;
    u32 bbase = lb + L_U + cc * 4608 + (256 - 16 + 16 * i16 + 8 * g) * 2 - hf * 4 * 64 - 3 * 64;
    u32 abase8 = abase + 8;
    pin(abase); pin(abase8); pin(bbase);
    u32x4 A[8], B[8];
#pragma unroll
    for (int qq = 0; qq < 8; ++qq) {
      B[7 - qq] = lds_ld<u32x4>(bbase + (7 - qq) * 512 + 3 * 64);
      A[qq] = cat(lds_ld<u32x2>(abase + (7 - qq) * 512 + 3 * 64), lds_ld<u32x2>(abase8 + (7 - qq) * 512 + 3 * 64));
      __builtin_amdgcn_sched_barrier(0);
    }
#pragma unroll
    for (int rr = 0; rr < 4; ++rr) {
#pragma unroll
      for (int qq = 0; qq < 8; ++qq) {
#pragma unroll
        for (int m = 7 - qq; m >= 0; --m) {
          acc[qq + m] = mfma32(A[qq], B[m], acc[qq + m]);
          if (CNOP >= 0 && rr < CNOPR) asm volatile("s_nop %c0" :: "i"(CNOP >= 0 ? CNOP : 0));
        }
        if (rr < 3) {
          A[qq] = cat(lds_ld<u32x2>(abase + (7 - qq) * 512 + (2 - rr) * 64), lds_ld<u32x2>(abase8 + (7 - qq) * 512 + (2 - rr) * 64));
          B[7 - qq] = lds_ld<u32x4>(bbase + (7 - qq) * 512 + (2 - rr) * 64);
        }
        __builtin_amdgcn_sched_barrier(0);
      }
    }
    if (hf) {
#pragma unroll
      for (int c = 0; c < 4; ++c) lds_st<f32x4>(lb + L_RED + (w * 4 + c) * 1024 + lane * 16, acc[c]);
#pragma unroll
      for (int c = 0; c < 4; ++c) acc[c] = acc[4 + c];
    } else {
#pragma unroll
      for (int c = 0; c < 4; ++c) lds_st<f32x4>(lb + L_RED + (w * 4 + c) * 1024 + lane * 16, acc[4 + c]);
    }
  }
  bar();
  STAMP(8);
  if (w < 4) {
    const float dk = bc<float>(dsk << 16);
    const u32 tb = (1024 * hf + 16 * i16 + 4 * g) * 2;
    f32x4 o[4];
    u32x2 uv[4];
#pragma unroll
    for (int c = 0; c < 4; ++c) {
      o[c] = lds_ld<f32x4>(lb + L_RED + ((w ^ 1) * 4 + c) * 1024 + lane * 16);
      uv[c] = lds_ld<u32x2>(lb + L_U + cc * 4608 + 512 + tb + c * 512);
    }
#pragma unroll
    for (int c = 0; c < 4; ++c) {
      const f32x4 y = acc[c] + o[c];
      const f32x2 b01 = up2(pk(y[0], y[1])) + up2(pk2(up2(uv[c].x) * dk));
      const f32x2 b23 = up2(pk(y[2], y[3])) + up2(pk2(up2(uv[c].y) * dk));
      lds_st<u32x2>(lb + L_BO + cc * 4096 + tb + c * 512, u32x2{pk2(b01), pk2(b23)});
    }
  }
  bar();
  STAMP(9);
  {
    const int st = w & 3, s2 = 16 * st + i16;
    const u32 a = lb + L_BO + ch * 4096 + (8 * g + (i16 >> 2)) * 128 + (16 * st + 4 * (i16 & 3)) * 2;
    st16<P>(rws, (u32)((((s2 & 7) * 8 + (s2 >> 3)) * 32 + 8 * g) * 2), (u32)WS_B + (d + ch) * B_D, cat(trread(a), trread(a + 4 * 128)));
  }
  STAMP(10);
  if (P) { drain_vm(); bar(); publish(ctl + CT_FB, j, epoch); }
  STAMP(11);
}

template <int PH>
__global__ __launch_bounds__(NT, 4) void gfs4_kernel(Params p) {
  extern __shared__ __attribute__((aligned(16))) char smem[];
  const u32 lb = (u32)(size_t)(LAS char*)smem;
  const int b = blockIdx.x;
  u32* ctl = (u32*)(p.ws + WS_CTL);
  STAMP_DECL;
  if (PH == 0 || PH == 1) STAMP(0); else if (PH == 2) STAMP(20); else STAMP(21);
  u32 gen = 0;
  asm volatile("" :: "s"(p.x), "s"(p.wr), "s"(p.wi), "s"(p.are), "s"(p.aim), "s"(p.bre), "s"(p.bim));
  asm volatile("" :: "s"(p.cre), "s"(p.cim), "s"(p.logdt), "s"(p.dskip), "s"(p.gw), "s"(p.out), "s"(p.ws));
  SsmLoads L;
  const bool upper = __builtin_amdgcn_readfirstlane(threadIdx.x >> 8) != 0;
  if (PH == 0) __builtin_amdgcn_s_setprio(3);
  if (__builtin_expect_with_probability(b < N_FFT, 1, PFFT)) {
    if (!upper) { if (PH == 0 || PH == 1) fft_first_loads(p, lb, b); }
  } else {
    ssm_first_loads<PH>(p, b - N_FFT, L);
  }
  if (PH == 0) gen = __hip_atomic_load((GAS u32*)ctl + CT_GEN, RLX_AGENT);
  __builtin_amdgcn_sched_barrier(0);
  if (PH == 0 && b < N_FFT && upper) {
    asm volatile("s_barrier\n\ts_barrier" ::: "memory");
    __builtin_amdgcn_sched_barrier(0);
  }
  if (b < N_FFT) {
    u32x4 fc[2], fs[2];
    P3C c3;
    if (upper) {
      if (PH == 1 || PH == 2) return;
      fft_upper<PH>(p, lb, b, ctl, fc, fs, c3);
    } else fft_role<PH>(p, lb, b, ctl, gen, fc, fs, c3 STAMP_PASS);
    if (PH == 0 || PH == 3) fft_phase3<PH>(p, lb, b, ctl, __builtin_amdgcn_readfirstlane(gen) + 1u, fc, fs, c3 STAMP_PASS);
  }
  else ssm_role<PH>(p, lb, b - N_FFT, ctl, gen, L STAMP_PASS);
  STAMP_FLUSH();
}

extern "C" void kernel_launch(void* const* d_in, const int* in_sizes, int n_in,
                              void* d_out, int out_size, void* d_ws, size_t ws_size,
                              hipStream_t stream) {
  (void)in_sizes; (void)n_in; (void)out_size; (void)ws_size;
  Params p;
  p.x = (const u16*)d_in[0]; p.wr = (const u16*)d_in[1]; p.wi = (const u16*)d_in[2];
  p.are = (const u16*)d_in[4]; p.aim = (const u16*)d_in[5]; p.bre = (const u16*)d_in[6]; p.bim = (const u16*)d_in[7];
  p.cre = (const u16*)d_in[8]; p.cim = (const u16*)d_in[9]; p.logdt = (const u16*)d_in[10]; p.dskip = (const u16*)d_in[11];
  p.gw = (const u16*)d_in[12];
  p.out = (u16*)d_out; p.ws = (char*)d_ws;
  (void)hipFuncSetAttribute((const void*)gfs4_kernel<0>, hipFuncAttributeMaxDynamicSharedMemorySize, LDS_BYTES);
  gfs4_kernel<0><<<N_WG, NT, LDS_BYTES, stream>>>(p);
}
```
